```python
import torch

def _softmax(x, axis=-1):
    return torch.softmax(x, dim=axis)
def _gelu(x):
    return torch.nn.functional.gelu(x, approximate="tanh")
def _silu(x):
    return torch.nn.functional.silu(x)
def _sigmoid(x):
    return torch.sigmoid(x)
def _layernorm(x, axis=-1, eps=1e-5):
    m = x.mean(dim=axis, keepdim=True)
    v = x.var(dim=axis, keepdim=True, unbiased=False)
    return (x - m) / torch.sqrt(v + eps)
def _scatter_add(tgt, idx, src, axis=0):
    return tgt.clone().index_add_(axis, idx, src)
def _segment_sum(src, seg, n):
    out = torch.zeros((n,) + src.shape[1:], dtype=src.dtype)
    return out.index_add_(0, seg, src)
def _pad(x, pw):
    flat = tuple(v for pair in reversed(pw) for v in pair)
    return torch.nn.functional.pad(x, flat)

kernel_name = 'concat_heads__mha_block_cached__mha_block_cached__near_ridge'

def setup_inputs():

    _w = (lambda a: (lambda t: t.to(torch.bfloat16) if t.is_floating_point() else t)(torch.as_tensor(a)))
    def _A_setup():
        B, Sq, D = 8, 288, 448
        H, Sk, Dh = 16, 1024, 256
        return {
            "x": _w((lambda *s: torch.randn(*s, dtype=torch.bfloat16) * 0.8)(B, Sq, D)),
            "k_cache": _w((lambda *s: torch.randn(*s, dtype=torch.bfloat16) * 0.8)(B, H, Sk, Dh)),
            "v_cache": _w((lambda *s: torch.randn(*s, dtype=torch.bfloat16) * 0.05)(B, H, Sk, Dh)),
            "wq": _w((lambda *s: torch.randn(*s, dtype=torch.bfloat16) * 0.8)(D, H * Dh) * (D ** -0.5)),
            "wo": _w((lambda *s: torch.randn(*s, dtype=torch.bfloat16) * 0.05)(H * Dh, D) * ((H * Dh) ** -0.5)),
        }
    _A_in = _A_setup()

    def _B_setup():
        B, Sq, D = 8, 288, 448
        H, Sk, Dh = 16, 1024, 256
        return {
            "x": _w((lambda *s: torch.randn(*s, dtype=torch.bfloat16) * 0.8)(B, Sq, D)),
            "k_cache": _w((lambda *s: torch.randn(*s, dtype=torch.bfloat16) * 0.8)(B, H, Sk, Dh)),
            "v_cache": _w((lambda *s: torch.randn(*s, dtype=torch.bfloat16) * 0.05)(B, H, Sk, Dh)),
            "wq": _w((lambda *s: torch.randn(*s, dtype=torch.bfloat16) * 0.8)(D, H * Dh) * (D ** -0.5)),
            "wo": _w((lambda *s: torch.randn(*s, dtype=torch.bfloat16) * 0.05)(H * Dh, D) * ((H * Dh) ** -0.5)),
        }
    _B_in = _B_setup()

    return {
        "A_x": _A_in["x"],
        "A_k_cache": _A_in["k_cache"],
        "A_v_cache": _A_in["v_cache"],
        "A_wq": _A_in["wq"],
        "A_wo": _A_in["wo"],
        "B_x": _B_in["x"],
        "B_k_cache": _B_in["k_cache"],
        "B_v_cache": _B_in["v_cache"],
        "B_wq": _B_in["wq"],
        "B_wo": _B_in["wo"],
    }

def reference(A_x, A_k_cache, A_v_cache, A_wq, A_wo, B_x, B_k_cache, B_v_cache, B_wq, B_wo):
    def _A_ref(x, k_cache, v_cache, wq, wo):
        B, Sq, D = x.shape
        H, Dh = 16, 256
        q = (lambda x, *d: x.permute(*d))((x @ wq).reshape(B, Sq, H, Dh), 0, 2, 1, 3)
        scores = torch.einsum("bhqd,bhkd->bhqk", q, k_cache) * (Dh ** -0.5)
        p = _softmax(scores, axis=-1)
        o = torch.einsum("bhqk,bhkd->bhqd", p, v_cache)
        return (lambda x, *d: x.permute(*d))(o, 0, 2, 1, 3).reshape(B, Sq, H * Dh) @ wo

    def _B_ref(x, k_cache, v_cache, wq, wo):
        B, Sq, D = x.shape
        H, Dh = 16, 256
        q = (lambda x, *d: x.permute(*d))((x @ wq).reshape(B, Sq, H, Dh), 0, 2, 1, 3)
        scores = torch.einsum("bhqd,bhkd->bhqk", q, k_cache) * (Dh ** -0.5)
        p = _softmax(scores, axis=-1)
        o = torch.einsum("bhqk,bhkd->bhqd", p, v_cache)
        return (lambda x, *d: x.permute(*d))(o, 0, 2, 1, 3).reshape(B, Sq, H * Dh) @ wo

    a = _A_ref(A_x, A_k_cache, A_v_cache, A_wq, A_wo)
    b = _B_ref(B_x, B_k_cache, B_v_cache, B_wq, B_wo)
    return torch.concatenate([a, b], axis=-1)
```

```cpp
#include <hip/hip_runtime.h>
#include <hip/hip_bf16.h>
typedef __hip_bfloat16 bf16;
constexpr int B=8, SQ=288, DM=448, H=16, SK=1024, DH=256;
__device__ inline float b2f(unsigned short u){ return __uint_as_float(((unsigned)u)<<16); }
__device__ inline unsigned short f2b(float f){ return __builtin_bit_cast(unsigned short, (__bf16)f); }

__global__ void gemm_naive(const unsigned short* A, const unsigned short* W, unsigned short* C, int M, int N, int K, int ldc, int coff){
  __shared__ float As[16][17], Ws[16][17];
  int tx=threadIdx.x&15, ty=threadIdx.x>>4;
  int row=blockIdx.y*16+ty, col=blockIdx.x*16+tx;
  float acc=0;
  for(int k0=0;k0<K;k0+=16){
    As[ty][tx]=b2f(A[(long)row*K+k0+tx]);
    Ws[ty][tx]=b2f(W[(long)(k0+ty)*N+col]);
    __syncthreads();
    for(int k=0;k<16;++k) acc+=As[ty][k]*Ws[k][tx];
    __syncthreads();
  }
  C[(long)row*ldc+coff+col]=f2b(acc);
}
__global__ void attn_naive(const unsigned short* Q, const unsigned short* Kc, const unsigned short* Vc, unsigned short* O){
  __shared__ float q[DH]; __shared__ float p[SK]; __shared__ float red[256];
  int r=blockIdx.x, h=blockIdx.y, b=blockIdx.z, t=threadIdx.x;
  q[t]=b2f(Q[((long)(b*SQ+r))*(H*DH)+h*DH+t]);
  __syncthreads();
  const unsigned short* Kh=Kc+((long)(b*H+h))*SK*DH; const unsigned short* Vh=Vc+((long)(b*H+h))*SK*DH;
  float mx=-1e30f;
  for(int k=t;k<SK;k+=256){ float s=0; for(int d=0;d<DH;++d) s+=q[d]*b2f(Kh[(long)k*DH+d]); s*=0.0625f; p[k]=s; mx=fmaxf(mx,s);}
  red[t]=mx; __syncthreads();
  for(int o=128;o>0;o>>=1){ if(t<o) red[t]=fmaxf(red[t],red[t+o]); __syncthreads(); }
  mx=red[0]; __syncthreads();
  float sum=0; for(int k=t;k<SK;k+=256){ float e=__expf(p[k]-mx); p[k]=e; sum+=e; }
  red[t]=sum; __syncthreads();
  for(int o=128;o>0;o>>=1){ if(t<o) red[t]+=red[t+o]; __syncthreads(); }
  sum=red[0];
  float acc=0; for(int k=0;k<SK;++k) acc+=p[k]*b2f(Vh[(long)k*DH+t]);
  O[((long)(b*SQ+r))*(H*DH)+h*DH+t]=f2b(acc/sum);
}
extern "C" void kernel_launch(void* const* d_in, const int* in_sizes, int n_in, void* d_out, int out_size, void* d_ws, size_t ws_size, hipStream_t stream){
  unsigned short* ws=(unsigned short*)d_ws;
  long nq=(long)B*SQ*H*DH;
  for(int m=0;m<2;++m){
    const unsigned short* x=(const unsigned short*)d_in[5*m+0];
    const unsigned short* kc=(const unsigned short*)d_in[5*m+1];
    const unsigned short* vc=(const unsigned short*)d_in[5*m+2];
    const unsigned short* wq=(const unsigned short*)d_in[5*m+3];
    const unsigned short* wo=(const unsigned short*)d_in[5*m+4];
    unsigned short* q=ws+(2*m)*nq; unsigned short* o=ws+(2*m+1)*nq;
    gemm_naive<<<dim3(H*DH/16,B*SQ/16),256,0,stream>>>(x,wq,q,B*SQ,H*DH,DM,H*DH,0);
    attn_naive<<<dim3(SQ,H,B),256,0,stream>>>(q,kc,vc,o);
    gemm_naive<<<dim3(DM/16,B*SQ/16),256,0,stream>>>(o,wo,(unsigned short*)d_out,B*SQ,DM,H*DH,2*DM,m*DM);
  }
}
```

```cpp
#include <hip/hip_runtime.h>

__global__ __launch_bounds__(256, 1) void attn_kernel(const char* xa, const char* kca, const char* va, const char* wa, const char* woa,
                                                      const char* xb, const char* kb, const char* vb, const char* wb, const char* wob,
                                                      char* ws, char* out) {
  __shared__ __attribute__((aligned(16))) char lds[163840];
  const unsigned long long ka = (unsigned long long)__builtin_amdgcn_kernarg_segment_ptr();
  const unsigned bid = blockIdx.x + (unsigned)(size_t)lds;
  const unsigned wid = __builtin_amdgcn_readfirstlane(threadIdx.x >> 6);
  asm volatile(
R"ASM(
.macro MA d,a,b
v_mfma_f32_16x16x32_bf16 a[\d:\d+3], v[\a:\a+3], v[\b:\b+3], a[\d:\d+3]
.endm
.macro MA0 d,a,b
v_mfma_f32_16x16x32_bf16 a[\d:\d+3], v[\a:\a+3], v[\b:\b+3], 0
.endm
.macro MV d,a,b
v_mfma_f32_16x16x32_bf16 v[\d:\d+3], v[\a:\a+3], v[\b:\b+3], v[\d:\d+3]
.endm
.macro MV0 d,a,b
v_mfma_f32_16x16x32_bf16 v[\d:\d+3], v[\a:\a+3], v[\b:\b+3], 0
.endm
.macro MG dc,d,ac,a,bc,b
v_mfma_f32_16x16x32_bf16 \dc[\d:\d+3], \ac[\a:\a+3], \bc[\b:\b+3], \dc[\d:\d+3]
.endm
.macro MG0 dc,d,ac,a,bc,b
v_mfma_f32_16x16x32_bf16 \dc[\d:\d+3], \ac[\a:\a+3], \bc[\b:\b+3], 0
.endm
.macro RU d,a,o
ds_read_b64_tr_b16 a[\d:\d+1], v\a offset:\o
.endm
.macro RB d,a,o
ds_read_b128 v[\d:\d+3], v\a offset:\o
.endm
.macro RT d,a,o
ds_read_b64_tr_b16 v[\d:\d+1], v\a offset:\o
.endm
.macro CV d,a,b
v_cvt_pk_bf16_f32 v\d, v\a, v\b
.endm
.macro AR d,a
v_accvgpr_read_b32 v\d, a\a
.endm
s_bfe_u32 s62, %[bid], 0x10002
s_mul_i32 s65, s62, 40
s_load_dwordx8 s[16:23], %[ka], s65
s_add_u32 s65, s65, 32
s_load_dwordx2 s[24:25], %[ka], s65
s_load_dwordx4 s[28:31], %[ka], 80
s_getpc_b64 s[80:81]
Lbase:
s_mov_b32 s77, LW0-Lbase
s_mov_b32 s56, s80
s_and_b32 s57, s81, 0xffff
s_cmp_eq_u32 %[wid], 1
s_cselect_b32 s77, LW1-Lbase, s77
s_cmp_eq_u32 %[wid], 2
s_cselect_b32 s77, LW2-Lbase, s77
s_cmp_eq_u32 %[wid], 3
s_cselect_b32 s77, LW3-Lbase, s77
s_add_u32 s80, s80, s77
s_addc_u32 s81, s81, 0
s_lshr_b32 s61, %[bid], 3
s_and_b32 s63, %[bid], 1
s_lshl_b32 s63, s63, 2
s_lshr_b32 s65, s61, 3
s_add_u32 s63, s63, s65
s_and_b32 s64, %[bid], 2
s_lshl_b32 s64, s64, 2
s_and_b32 s65, s61, 7
s_add_u32 s64, s64, s65
s_lshl_b32 s26, s62, 3
s_add_u32 s26, s26, s63
s_lshl_b32 s26, s26, 4
s_add_u32 s26, s26, s64
s_lshl_b32 s27, s63, 1
s_and_b32 s65, s64, 1
s_add_u32 s27, s27, s65
s_mul_i32 s27, s27, 258048
s_mul_i32 s65, s62, 896
s_add_u32 s27, s27, s65
s_lshl_b32 s78, s61, 2
s_add_u32 s78, s78, %[wid]
s_and_b32 s78, s78, 31
s_lshl_b32 s78, s78, 13
s_mul_i32 s60, s63, 258048
s_lshl_b32 s61, s64, 9
s_setpc_b64 s[80:81]
LW0:
v_mbcnt_lo_u32_b32 v228, -1, 0
v_mbcnt_hi_u32_b32 v228, -1, v228
v_bfe_u32 v231, v228, 2, 3
v_lshrrev_b32 v232, 2, v231
v_sub_u32 v234, 4, v232
v_and_b32 v234, 3, v234
v_and_b32 v233, 3, v228
v_xor_b32 v233, v233, v234
v_lshlrev_b32 v233, 4, v233
v_lshrrev_b32 v234, 5, v228
v_lshl_add_u32 v233, v234, 6, v233
v_and_b32 v234, 3, v231
v_lshl_add_u32 v234, v232, 3, v234
v_add_u32 v234, 0, v234
v_lshl_add_u32 v225, v234, 13, v233
v_add_u32 v234, 0, v231
v_lshl_add_u32 v253, v234, 9, v233
v_add_u32 v234, 0, v231
v_mul_u32_u24 v234, 0x380, v234
v_add_u32 v226, v234, v233
s_and_b32 s77, s26, 1
s_mul_i32 s80, s77, 129024
s_sub_u32 s81, 129024, s80
s_add_u32 s67, s80, 0
s_add_u32 s68, s81, 0
s_add_u32 s69, s80, 28672
s_add_u32 s70, s81, 28672
s_add_u32 s71, s80, 57344
s_add_u32 s72, s81, 57344
s_add_u32 s73, s80, 86016
s_add_u32 s74, s81, 86016
s_add_u32 s75, s80, 114688
s_mov_b32 s76, 0
s_mov_b32 s62, 6
s_mov_b32 s63, 0x3db8aa3b
s_mov_b32 s66, 0xbdb8aa3b
s_mov_b32 s64, 0x41800000
s_mov_b32 s65, 1
s_mov_b32 s50, 3662336
s_mov_b32 s51, 0x20000
s_mov_b32 s54, 258048
s_mov_b32 s55, 0x20000
s_mov_b32 s42, 524288
s_mov_b32 s43, 0x20000
s_mov_b32 s46, 524288
s_mov_b32 s47, 0x20000
s_mov_b32 s58, Lcodeend-Lbase
s_mov_b32 s59, 0x20000
v_lshlrev_b32 v246, 7, v228
buffer_load_dword v247, v246, s[56:59], s78 offen
s_waitcnt lgkmcnt(0)
s_add_u32 s52, s16, s60
s_addc_u32 s53, s17, 0
s_and_b32 s53, s53, 0xffff
s_add_u32 s48, s22, s61
s_addc_u32 s49, s23, 0
s_and_b32 s49, s49, 0xffff
s_mov_b32 m0, 36864
s_nop 0
buffer_load_dwordx4 v226, s[52:55], s67 offen offset:0 lds
s_mov_b32 m0, 40960
s_nop 0
buffer_load_dwordx4 v226, s[52:55], s68 offen offset:0 lds
s_mov_b32 m0, 45056
s_nop 0
buffer_load_dwordx4 v226, s[52:55], s69 offen offset:0 lds
s_mov_b32 m0, 49152
s_nop 0
buffer_load_dwordx4 v226, s[52:55], s70 offen offset:0 lds
s_mov_b32 m0, 53248
s_nop 0
buffer_load_dwordx4 v226, s[52:55], s71 offen offset:0 lds
s_mov_b32 m0, 57344
s_nop 0
buffer_load_dwordx4 v226, s[52:55], s72 offen offset:0 lds
s_mov_b32 m0, 61440
s_nop 0
buffer_load_dwordx4 v226, s[52:55], s73 offen offset:0 lds
s_mov_b32 m0, 65536
s_nop 0
buffer_load_dwordx4 v226, s[52:55], s74 offen offset:0 lds
s_mov_b32 m0, 69632
s_nop 0
buffer_load_dwordx4 v226, s[52:55], s75 offen offset:0 lds
v_add_u32 v226, 0x80, v226
s_mov_b32 m0, 131072
s_nop 0
buffer_load_dwordx4 v225, s[48:51], s76 offen offset:0 lds
s_mov_b32 m0, 132992
s_nop 0
buffer_load_dwordx4 v225, s[48:51], s76 offen offset:128 lds
s_mov_b32 m0, 134912
s_nop 0
buffer_load_dwordx4 v225, s[48:51], s76 offen offset:256 lds
s_mov_b32 m0, 136832
s_nop 0
buffer_load_dwordx4 v225, s[48:51], s76 offen offset:384 lds
s_add_u32 s76, s76, 262144
s_mov_b32 m0, 147456
s_nop 0
buffer_load_dwordx4 v225, s[48:51], s76 offen offset:0 lds
s_mov_b32 m0, 149376
s_nop 0
buffer_load_dwordx4 v225, s[48:51], s76 offen offset:128 lds
s_mov_b32 m0, 151296
s_nop 0
buffer_load_dwordx4 v225, s[48:51], s76 offen offset:256 lds
s_mov_b32 m0, 153216
s_nop 0
buffer_load_dwordx4 v225, s[48:51], s76 offen offset:384 lds
s_add_u32 s76, s76, 262144
s_and_b32 s60, s26, 127
s_lshl_b32 s60, s60, 19
s_add_u32 s40, s18, s60
s_addc_u32 s41, s19, 0
s_and_b32 s41, s41, 0xffff
s_add_u32 s44, s20, s60
s_addc_u32 s45, s21, 0
s_and_b32 s45, s45, 0xffff
s_mov_b32 s60, 0
s_mov_b32 s61, 0
s_lshl_b32 s77, s26, 9
s_and_b32 s77, s77, 0xffffe000
s_add_u32 s77, s77, 100663296
s_add_u32 s38, s28, s77
s_addc_u32 s39, s29, 0
s_and_b32 s77, s26, 15
s_lshl_b32 s36, s77, 8
s_add_u32 s36, s36, 256
s_add_u32 s36, s36, s38
s_addc_u32 s37, s39, 0
s_lshl_b32 s77, s77, 2
s_add_u32 s38, s38, s77
s_addc_u32 s39, s39, 0
v_lshlrev_b32 v208, 2, v228
v_mov_b32 v209, 0
global_load_dword v210, v208, s[36:37] sc1
global_load_dword v211, v209, s[38:39] sc1
s_mov_b32 m0, 0
s_nop 0
buffer_load_dwordx4 v226, s[52:55], s67 offen offset:0 lds
s_mov_b32 m0, 4096
s_nop 0
buffer_load_dwordx4 v226, s[52:55], s68 offen offset:0 lds
s_mov_b32 m0, 8192
s_nop 0
buffer_load_dwordx4 v226, s[52:55], s69 offen offset:0 lds
s_mov_b32 m0, 12288
s_nop 0
buffer_load_dwordx4 v226, s[52:55], s70 offen offset:0 lds
s_mov_b32 m0, 16384
s_nop 0
buffer_load_dwordx4 v226, s[52:55], s71 offen offset:0 lds
s_mov_b32 m0, 20480
s_nop 0
buffer_load_dwordx4 v226, s[52:55], s72 offen offset:0 lds
s_mov_b32 m0, 24576
s_nop 0
buffer_load_dwordx4 v226, s[52:55], s73 offen offset:0 lds
s_mov_b32 m0, 28672
s_nop 0
buffer_load_dwordx4 v226, s[52:55], s74 offen offset:0 lds
s_mov_b32 m0, 32768
s_nop 0
buffer_load_dwordx4 v226, s[52:55], s75 offen offset:0 lds
v_add_u32 v226, 0x80, v226
v_lshrrev_b32 v229, 4, v228
v_and_b32 v230, 15, v228
v_lshlrev_b32 v231, 4, v228
v_add_u32 v252, 0x10000, v231
v_lshrrev_b32 v231, 2, v230
v_sub_u32 v234, 4, v231
v_and_b32 v234, 3, v234
v_xor_b32 v234, v234, v229
v_lshlrev_b32 v234, 4, v234
v_and_b32 v231, 7, v230
v_lshl_add_u32 v234, v231, 6, v234
v_lshrrev_b32 v231, 3, v230
v_lshl_add_u32 v249, v231, 10, v234
v_add_u32 v227, 0x9000, v249
v_lshrrev_b32 v231, 2, v230
v_and_b32 v232, 1, v229
v_lshl_add_u32 v231, v232, 2, v231
v_lshlrev_b32 v231, 6, v231
v_lshrrev_b32 v232, 1, v229
v_lshl_add_u32 v231, v232, 10, v231
v_add_u32 v231, 0x18000, v231
v_sub_u32 v234, 4, v229
v_and_b32 v234, 3, v234
v_and_b32 v232, 3, v228
v_xor_b32 v233, v232, v234
v_lshl_add_u32 v224, v233, 4, v231
v_lshrrev_b32 v233, 1, v232
v_xor_b32 v233, v233, v234
v_lshl_add_u32 v233, v233, 4, v231
v_and_b32 v232, 1, v232
v_lshl_add_u32 v250, v232, 3, v233
v_xor_b32 v251, 32, v250
v_mov_b32 v241, 0
v_mov_b32 v242, 0
v_mov_b32 v243, 0
v_mov_b32 v244, 0
v_mov_b32 v245, 0
v_mov_b32 v248, 0
ds_write_b32 v252, v248 offset:29704
s_waitcnt vmcnt(10)
v_mov_b32 v212, v210
s_waitcnt vmcnt(9)
v_mov_b32 v213, v211
s_nop 0
v_readfirstlane_b32 s79, v213
LN0:
s_add_u32 s79, s79, 0x9e3779b1
v_cmp_eq_u32 vcc, s79, v212
s_cbranch_vccnz LN0
v_mov_b32 v212, s79
global_store_dword v209, v212, s[38:39] sc1
s_waitcnt lgkmcnt(0)
s_barrier
RT 160,224,32768
RT 162,224,40960
RT 164,224,49152
RT 166,224,57344
ds_read_b128 a[0:3], v227 offset:0
ds_read_b128 a[4:7], v227 offset:512
ds_read_b128 a[8:11], v227 offset:2048
ds_read_b128 a[12:15], v227 offset:2560
ds_read_b128 a[16:19], v227 offset:4096
ds_read_b128 a[20:23], v227 offset:4608
ds_read_b128 a[24:27], v227 offset:6144
ds_read_b128 a[28:31], v227 offset:6656
ds_read_b128 a[32:35], v227 offset:32768
ds_read_b128 a[36:39], v227 offset:33280
ds_read_b128 a[40:43], v227 offset:34816
ds_read_b128 a[44:47], v227 offset:35328
RT 168,224,32776
RT 170,224,40968
RT 172,224,49160
RT 174,224,57352
s_waitcnt lgkmcnt(15)
MG0 v,0,v,160,a,0
RT 176,224,33280
s_waitcnt lgkmcnt(15)
MG v,0,v,164,a,4
RT 178,224,41472
s_waitcnt lgkmcnt(15)
MG0 v,32,v,160,a,8
RT 180,224,49664
s_waitcnt lgkmcnt(15)
MG v,32,v,164,a,12
RT 182,224,57856
s_mov_b32 m0, 98304
s_waitcnt lgkmcnt(15)
MG0 v,64,v,160,a,16
buffer_load_dwordx4 v225, s[48:51], s76 offen offset:0 lds
s_waitcnt lgkmcnt(14)
MG v,64,v,164,a,20
s_waitcnt lgkmcnt(13)
MG0 v,96,v,160,a,24
s_waitcnt lgkmcnt(10)
MG v,96,v,164,a,28
s_mov_b32 m0, 100224
MG0 v,128,v,160,a,32
buffer_load_dwordx4 v225, s[48:51], s76 offen offset:128 lds
MG v,128,v,164,a,36
s_waitcnt lgkmcnt(9)
MG0 v,132,v,160,a,40
s_waitcnt lgkmcnt(8)
MG v,132,v,164,a,44
s_waitcnt lgkmcnt(4)
MG0 v,4,v,168,a,0
RT 160,224,33288
MG v,4,v,172,a,4
RT 162,224,41480
MG0 v,36,v,168,a,8
RT 164,224,49672
MG v,36,v,172,a,12
RT 166,224,57864
s_mov_b32 m0, 102144
MG0 v,68,v,168,a,16
buffer_load_dwordx4 v225, s[48:51], s76 offen offset:256 lds
MG v,68,v,172,a,20
MG0 v,100,v,168,a,24
MG v,100,v,172,a,28
s_mov_b32 m0, 104064
MG0 v,136,v,168,a,32
buffer_load_dwordx4 v225, s[48:51], s76 offen offset:384 lds
s_add_u32 s76, s76, 262144
MG v,136,v,172,a,36
MG0 v,140,v,168,a,40
MG v,140,v,172,a,44
s_waitcnt lgkmcnt(4)
MG0 v,8,v,176,a,0
RT 168,224,34816
MG v,8,v,180,a,4
RT 170,224,43008
MG0 v,40,v,176,a,8
RT 172,224,51200
MG v,40,v,180,a,12
RT 174,224,59392
s_mov_b32 m0, 114688
MG0 v,72,v,176,a,16
buffer_load_dwordx4 v225, s[48:51], s76 offen offset:0 lds
MG v,72,v,180,a,20
MG0 v,104,v,176,a,24
MG v,104,v,180,a,28
s_mov_b32 m0, 116608
MG0 v,144,v,176,a,32
buffer_load_dwordx4 v225, s[48:51], s76 offen offset:128 lds
MG v,144,v,180,a,36
MG0 v,148,v,176,a,40
MG v,148,v,180,a,44
s_waitcnt lgkmcnt(4)
MG0 v,12,v,160,a,0
RT 176,224,34824
MG v,12,v,164,a,4
RT 178,224,43016
MG0 v,44,v,160,a,8
RT 180,224,51208
MG v,44,v,164,a,12
RT 182,224,59400
s_mov_b32 m0, 118528
MG0 v,76,v,160,a,16
buffer_load_dwordx4 v225, s[48:51], s76 offen offset:256 lds
MG v,76,v,164,a,20
MG0 v,108,v,160,a,24
MG v,108,v,164,a,28
s_mov_b32 m0, 120448
MG0 v,152,v,160,a,32
buffer_load_dwordx4 v225, s[48:51], s76 offen offset:384 lds
s_add_u32 s76, s76, 262144
MG v,152,v,164,a,36
MG0 v,156,v,160,a,40
MG v,156,v,164,a,44
s_waitcnt lgkmcnt(4)
MG0 v,16,v,168,a,0
RT 160,224,35328
MG v,16,v,172,a,4
RT 162,224,43520
MG0 v,48,v,168,a,8
RT 164,224,51712
MG v,48,v,172,a,12
RT 166,224,59904
MG0 v,80,v,168,a,16
MG v,80,v,172,a,20
MG0 v,112,v,168,a,24
MG v,112,v,172,a,28
s_waitcnt lgkmcnt(4)
MG0 v,20,v,176,a,0
RT 168,224,35336
MG v,20,v,180,a,4
RT 170,224,43528
MG0 v,52,v,176,a,8
RT 172,224,51720
MG v,52,v,180,a,12
RT 174,224,59912
MG0 v,84,v,176,a,16
MG v,84,v,180,a,20
MG0 v,116,v,176,a,24
MG v,116,v,180,a,28
s_waitcnt lgkmcnt(4)
MG0 v,24,v,160,a,0
RT 176,224,36864
MG v,24,v,164,a,4
RT 178,224,45056
MG0 v,56,v,160,a,8
RT 180,224,53248
MG v,56,v,164,a,12
RT 182,224,61440
MG0 v,88,v,160,a,16
MG v,88,v,164,a,20
MG0 v,120,v,160,a,24
s_waitcnt vmcnt(9) lgkmcnt(4)
MG v,120,v,164,a,28
s_barrier
MG0 v,28,v,168,a,0
RT 160,224,36872
MG v,28,v,172,a,4
RT 162,224,45064
MG0 v,60,v,168,a,8
RT 164,224,53256
MG v,60,v,172,a,12
RT 166,224,61448
MG0 v,92,v,168,a,16
ds_read_b128 a[48:51], v249 offset:0
s_mov_b32 m0, 36864
MG v,92,v,172,a,20
buffer_load_dwordx4 v226, s[52:55], s67 offen offset:0 lds
MG0 v,124,v,168,a,24
ds_read_b128 a[52:55], v249 offset:512
MG v,124,v,172,a,28
s_waitcnt lgkmcnt(6)
MG0 a,128,v,176,a,0
RT 168,224,37376
MG a,128,v,180,a,4
RT 170,224,45568
MG0 a,132,v,176,a,8
RT 172,224,53760
MG a,132,v,180,a,12
RT 174,224,61952
MG0 a,136,v,176,a,16
ds_read_b128 a[56:59], v249 offset:2048
s_mov_b32 m0, 40960
MG a,136,v,180,a,20
buffer_load_dwordx4 v226, s[52:55], s68 offen offset:0 lds
MG0 a,140,v,176,a,24
ds_read_b128 a[60:63], v249 offset:2560
MG a,140,v,180,a,28
s_waitcnt lgkmcnt(8)
MG0 a,144,v,160,a,0
RT 176,224,37384
MG a,144,v,164,a,4
RT 178,224,45576
MG0 a,148,v,160,a,8
RT 180,224,53768
MG a,148,v,164,a,12
RT 182,224,61960
MG0 a,152,v,160,a,16
ds_read_b128 a[64:67], v249 offset:4096
s_mov_b32 m0, 45056
MG a,152,v,164,a,20
buffer_load_dwordx4 v226, s[52:55], s69 offen offset:0 lds
MG0 a,156,v,160,a,24
ds_read_b128 a[68:71], v249 offset:4608
MG a,156,v,164,a,28
s_waitcnt lgkmcnt(8)
MG0 a,160,v,168,a,0
RT 160,224,38912
MG a,160,v,172,a,4
RT 162,224,47104
MG0 a,164,v,168,a,8
RT 164,224,55296
MG a,164,v,172,a,12
RT 166,224,63488
MG0 a,168,v,168,a,16
ds_read_b128 a[72:75], v249 offset:6144
s_mov_b32 m0, 49152
MG a,168,v,172,a,20
buffer_load_dwordx4 v226, s[52:55], s70 offen offset:0 lds
MG0 a,172,v,168,a,24
ds_read_b128 a[76:79], v249 offset:6656
MG a,172,v,172,a,28
s_waitcnt lgkmcnt(8)
MG0 a,176,v,176,a,0
RT 168,224,38920
MG a,176,v,180,a,4
RT 170,224,47112
MG0 a,180,v,176,a,8
RT 172,224,55304
MG a,180,v,180,a,12
RT 174,224,63496
MG0 a,184,v,176,a,16
ds_read_b128 a[80:83], v249 offset:32768
s_mov_b32 m0, 53248
MG a,184,v,180,a,20
buffer_load_dwordx4 v226, s[52:55], s71 offen offset:0 lds
MG0 a,188,v,176,a,24
ds_read_b128 a[84:87], v249 offset:33280
MG a,188,v,180,a,28
s_waitcnt lgkmcnt(8)
MG0 a,192,v,160,a,0
RT 176,224,39424
MG a,192,v,164,a,4
RT 178,224,47616
MG0 a,196,v,160,a,8
RT 180,224,55808
MG a,196,v,164,a,12
RT 182,224,64000
MG0 a,200,v,160,a,16
ds_read_b128 a[88:91], v249 offset:34816
s_mov_b32 m0, 57344
MG a,200,v,164,a,20
buffer_load_dwordx4 v226, s[52:55], s72 offen offset:0 lds
MG0 a,204,v,160,a,24
ds_read_b128 a[92:95], v249 offset:35328
MG a,204,v,164,a,28
s_waitcnt lgkmcnt(8)
MG0 a,208,v,168,a,0
RT 160,224,39432
MG a,208,v,172,a,4
RT 162,224,47624
MG0 a,212,v,168,a,8
RT 164,224,55816
MG a,212,v,172,a,12
RT 166,224,64008
s_mov_b32 m0, 61440
MG0 a,216,v,168,a,16
buffer_load_dwordx4 v226, s[52:55], s73 offen offset:0 lds
MG a,216,v,172,a,20
MG0 a,220,v,168,a,24
s_waitcnt vmcnt(7) lgkmcnt(0)
MG a,220,v,172,a,28
s_barrier
MG0 a,224,v,176,a,0
RT 168,224,0
MG a,224,v,180,a,4
RT 170,224,8192
MG0 a,228,v,176,a,8
RT 172,224,16384
MG a,228,v,180,a,12
RT 174,224,24576
s_mov_b32 m0, 65536
MG0 a,232,v,176,a,16
buffer_load_dwordx4 v226, s[52:55], s74 offen offset:0 lds
MG a,232,v,180,a,20
MG0 a,236,v,176,a,24
MG a,236,v,180,a,28
MG0 a,240,v,160,a,0
RT 176,224,8
MG a,240,v,164,a,4
RT 178,224,8200
MG0 a,244,v,160,a,8
RT 180,224,16392
MG a,244,v,164,a,12
RT 182,224,24584
s_mov_b32 m0, 69632
MG0 a,248,v,160,a,16
buffer_load_dwordx4 v226, s[52:55], s75 offen offset:0 lds
v_add_u32 v226, 0x80, v226
MG a,248,v,164,a,20
MG0 a,252,v,160,a,24
MG a,252,v,164,a,28
s_waitcnt lgkmcnt(4)
MG v,0,v,168,a,48
RT 160,224,512
MG v,0,v,172,a,52
RT 162,224,8704
MG v,32,v,168,a,56
RT 164,224,16896
MG v,32,v,172,a,60
RT 166,224,25088
s_mov_b32 m0, 131072
MG v,64,v,168,a,64
buffer_load_dwordx4 v225, s[48:51], s76 offen offset:0 lds
MG v,64,v,172,a,68
MG v,96,v,168,a,72
MG v,96,v,172,a,76
s_mov_b32 m0, 132992
MG v,128,v,168,a,80
buffer_load_dwordx4 v225, s[48:51], s76 offen offset:128 lds
MG v,128,v,172,a,84
MG v,132,v,168,a,88
MG v,132,v,172,a,92
s_waitcnt lgkmcnt(4)
MG v,4,v,176,a,48
RT 168,224,520
MG v,4,v,180,a,52
RT 170,224,8712
MG v,36,v,176,a,56
RT 172,224,16904
MG v,36,v,180,a,60
RT 174,224,25096
s_mov_b32 m0, 134912
MG v,68,v,176,a,64
buffer_load_dwordx4 v225, s[48:51], s76 offen offset:256 lds
MG v,68,v,180,a,68
MG v,100,v,176,a,72
MG v,100,v,180,a,76
s_mov_b32 m0, 136832
MG v,136,v,176,a,80
buffer_load_dwordx4 v225, s[48:51], s76 offen offset:384 lds
s_add_u32 s76, s76, 262144
MG v,136,v,180,a,84
MG v,140,v,176,a,88
MG v,140,v,180,a,92
s_waitcnt lgkmcnt(4)
MG v,8,v,160,a,48
RT 176,224,2048
MG v,8,v,164,a,52
RT 178,224,10240
MG v,40,v,160,a,56
RT 180,224,18432
MG v,40,v,164,a,60
RT 182,224,26624
s_mov_b32 m0, 147456
MG v,72,v,160,a,64
buffer_load_dwordx4 v225, s[48:51], s76 offen offset:0 lds
MG v,72,v,164,a,68
MG v,104,v,160,a,72
MG v,104,v,164,a,76
s_mov_b32 m0, 149376
MG v,144,v,160,a,80
buffer_load_dwordx4 v225, s[48:51], s76 offen offset:128 lds
MG v,144,v,164,a,84
MG v,148,v,160,a,88
MG v,148,v,164,a,92
s_waitcnt lgkmcnt(4)
MG v,12,v,168,a,48
RT 160,224,2056
MG v,12,v,172,a,52
RT 162,224,10248
MG v,44,v,168,a,56
RT 164,224,18440
MG v,44,v,172,a,60
RT 166,224,26632
s_mov_b32 m0, 151296
MG v,76,v,168,a,64
buffer_load_dwordx4 v225, s[48:51], s76 offen offset:256 lds
MG v,76,v,172,a,68
MG v,108,v,168,a,72
MG v,108,v,172,a,76
s_mov_b32 m0, 153216
MG v,152,v,168,a,80
buffer_load_dwordx4 v225, s[48:51], s76 offen offset:384 lds
s_add_u32 s76, s76, 262144
MG v,152,v,172,a,84
MG v,156,v,168,a,88
MG v,156,v,172,a,92
s_waitcnt lgkmcnt(4)
MG v,16,v,176,a,48
RT 168,224,2560
MG v,16,v,180,a,52
RT 170,224,10752
MG v,48,v,176,a,56
RT 172,224,18944
MG v,48,v,180,a,60
RT 174,224,27136
s_mov_b32 m0, 0
MG v,80,v,176,a,64
buffer_load_dwordx4 v226, s[52:55], s67 offen offset:0 lds
MG v,80,v,180,a,68
MG v,112,v,176,a,72
MG v,112,v,180,a,76
s_waitcnt lgkmcnt(4)
MG v,20,v,160,a,48
RT 176,224,2568
MG v,20,v,164,a,52
RT 178,224,10760
MG v,52,v,160,a,56
RT 180,224,18952
MG v,52,v,164,a,60
RT 182,224,27144
s_mov_b32 m0, 4096
MG v,84,v,160,a,64
buffer_load_dwordx4 v226, s[52:55], s68 offen offset:0 lds
MG v,84,v,164,a,68
MG v,116,v,160,a,72
MG v,116,v,164,a,76
s_waitcnt lgkmcnt(4)
MG v,24,v,168,a,48
RT 160,224,4096
MG v,24,v,172,a,52
RT 162,224,12288
MG v,56,v,168,a,56
RT 164,224,20480
MG v,56,v,172,a,60
RT 166,224,28672
s_mov_b32 m0, 8192
MG v,88,v,168,a,64
buffer_load_dwordx4 v226, s[52:55], s69 offen offset:0 lds
MG v,88,v,172,a,68
MG v,120,v,168,a,72
s_waitcnt vmcnt(11) lgkmcnt(4)
MG v,120,v,172,a,76
s_barrier
MG v,28,v,176,a,48
RT 168,224,4104
MG v,28,v,180,a,52
RT 170,224,12296
MG v,60,v,176,a,56
RT 172,224,20488
MG v,60,v,180,a,60
RT 174,224,28680
MG v,92,v,176,a,64
ds_read_b128 a[0:3], v227 offset:0
s_mov_b32 m0, 12288
MG v,92,v,180,a,68
buffer_load_dwordx4 v226, s[52:55], s70 offen offset:0 lds
MG v,124,v,176,a,72
ds_read_b128 a[4:7], v227 offset:512
MG v,124,v,180,a,76
s_waitcnt lgkmcnt(6)
MG a,128,v,160,a,48
RT 176,224,4608
MG a,128,v,164,a,52
RT 178,224,12800
MG a,132,v,160,a,56
RT 180,224,20992
MG a,132,v,164,a,60
RT 182,224,29184
MG a,136,v,160,a,64
ds_read_b128 a[8:11], v227 offset:2048
s_mov_b32 m0, 16384
MG a,136,v,164,a,68
buffer_load_dwordx4 v226, s[52:55], s71 offen offset:0 lds
MG a,140,v,160,a,72
ds_read_b128 a[12:15], v227 offset:2560
MG a,140,v,164,a,76
s_waitcnt lgkmcnt(8)
MG a,144,v,168,a,48
RT 160,224,4616
MG a,144,v,172,a,52
RT 162,224,12808
MG a,148,v,168,a,56
RT 164,224,21000
MG a,148,v,172,a,60
RT 166,224,29192
MG a,152,v,168,a,64
ds_read_b128 a[16:19], v227 offset:4096
s_mov_b32 m0, 20480
MG a,152,v,172,a,68
buffer_load_dwordx4 v226, s[52:55], s72 offen offset:0 lds
MG a,156,v,168,a,72
ds_read_b128 a[20:23], v227 offset:4608
MG a,156,v,172,a,76
s_waitcnt lgkmcnt(8)
MG a,160,v,176,a,48
RT 168,224,6144
MG a,160,v,180,a,52
RT 170,224,14336
MG a,164,v,176,a,56
RT 172,224,22528
MG a,164,v,180,a,60
RT 174,224,30720
MG a,168,v,176,a,64
ds_read_b128 a[24:27], v227 offset:6144
s_mov_b32 m0, 24576
MG a,168,v,180,a,68
buffer_load_dwordx4 v226, s[52:55], s73 offen offset:0 lds
MG a,172,v,176,a,72
ds_read_b128 a[28:31], v227 offset:6656
MG a,172,v,180,a,76
s_waitcnt lgkmcnt(8)
MG a,176,v,160,a,48
RT 176,224,6152
MG a,176,v,164,a,52
RT 178,224,14344
MG a,180,v,160,a,56
RT 180,224,22536
MG a,180,v,164,a,60
RT 182,224,30728
MG a,184,v,160,a,64
ds_read_b128 a[32:35], v227 offset:32768
s_mov_b32 m0, 28672
MG a,184,v,164,a,68
buffer_load_dwordx4 v226, s[52:55], s74 offen offset:0 lds
MG a,188,v,160,a,72
ds_read_b128 a[36:39], v227 offset:33280
MG a,188,v,164,a,76
s_waitcnt lgkmcnt(8)
MG a,192,v,168,a,48
RT 160,224,6656
MG a,192,v,172,a,52
RT 162,224,14848
MG a,196,v,168,a,56
RT 164,224,23040
MG a,196,v,172,a,60
RT 166,224,31232
MG a,200,v,168,a,64
ds_read_b128 a[40:43], v227 offset:34816
s_mov_b32 m0, 32768
MG a,200,v,172,a,68
buffer_load_dwordx4 v226, s[52:55], s75 offen offset:0 lds
v_add_u32 v226, 0x80, v226
MG a,204,v,168,a,72
ds_read_b128 a[44:47], v227 offset:35328
MG a,204,v,172,a,76
s_waitcnt lgkmcnt(8)
MG a,208,v,176,a,48
RT 168,224,6664
MG a,208,v,180,a,52
RT 170,224,14856
MG a,212,v,176,a,56
RT 172,224,23048
MG a,212,v,180,a,60
RT 174,224,31240
MG a,216,v,176,a,64
MG a,216,v,180,a,68
MG a,220,v,176,a,72
s_waitcnt vmcnt(9) lgkmcnt(0)
MG a,220,v,180,a,76
s_barrier
MG a,224,v,160,a,48
RT 176,224,32768
MG a,224,v,164,a,52
RT 178,224,40960
MG a,228,v,160,a,56
RT 180,224,49152
MG a,228,v,164,a,60
RT 182,224,57344
s_mov_b32 m0, 98304
MG a,232,v,160,a,64
buffer_load_dwordx4 v225, s[48:51], s76 offen offset:0 lds
MG a,232,v,164,a,68
MG a,236,v,160,a,72
MG a,236,v,164,a,76
MG a,240,v,168,a,48
RT 160,224,32776
MG a,240,v,172,a,52
RT 162,224,40968
MG a,244,v,168,a,56
RT 164,224,49160
MG a,244,v,172,a,60
RT 166,224,57352
s_mov_b32 m0, 100224
MG a,248,v,168,a,64
buffer_load_dwordx4 v225, s[48:51], s76 offen offset:128 lds
MG a,248,v,172,a,68
MG a,252,v,168,a,72
MG a,252,v,172,a,76
s_waitcnt lgkmcnt(4)
MG v,0,v,176,a,0
RT 168,224,33280
MG v,0,v,180,a,4
RT 170,224,41472
MG v,32,v,176,a,8
RT 172,224,49664
MG v,32,v,180,a,12
RT 174,224,57856
s_mov_b32 m0, 102144
MG v,64,v,176,a,16
buffer_load_dwordx4 v225, s[48:51], s76 offen offset:256 lds
MG v,64,v,180,a,20
MG v,96,v,176,a,24
MG v,96,v,180,a,28
s_mov_b32 m0, 104064
MG v,128,v,176,a,32
buffer_load_dwordx4 v225, s[48:51], s76 offen offset:384 lds
s_add_u32 s76, s76, 262144
MG v,128,v,180,a,36
MG v,132,v,176,a,40
MG v,132,v,180,a,44
s_waitcnt lgkmcnt(4)
MG v,4,v,160,a,0
RT 176,224,33288
MG v,4,v,164,a,4
RT 178,224,41480
MG v,36,v,160,a,8
RT 180,224,49672
MG v,36,v,164,a,12
RT 182,224,57864
s_mov_b32 m0, 114688
MG v,68,v,160,a,16
buffer_load_dwordx4 v225, s[48:51], s76 offen offset:0 lds
MG v,68,v,164,a,20
MG v,100,v,160,a,24
MG v,100,v,164,a,28
s_mov_b32 m0, 116608
MG v,136,v,160,a,32
buffer_load_dwordx4 v225, s[48:51], s76 offen offset:128 lds
MG v,136,v,164,a,36
MG v,140,v,160,a,40
MG v,140,v,164,a,44
s_waitcnt lgkmcnt(4)
MG v,8,v,168,a,0
RT 160,224,34816
MG v,8,v,172,a,4
RT 162,224,43008
MG v,40,v,168,a,8
RT 164,224,51200
MG v,40,v,172,a,12
RT 166,224,59392
s_mov_b32 m0, 118528
MG v,72,v,168,a,16
buffer_load_dwordx4 v225, s[48:51], s76 offen offset:256 lds
MG v,72,v,172,a,20
MG v,104,v,168,a,24
MG v,104,v,172,a,28
s_mov_b32 m0, 120448
MG v,144,v,168,a,32
buffer_load_dwordx4 v225, s[48:51], s76 offen offset:384 lds
s_add_u32 s76, s76, 262144
MG v,144,v,172,a,36
MG v,148,v,168,a,40
MG v,148,v,172,a,44
s_waitcnt lgkmcnt(4)
MG v,12,v,176,a,0
RT 168,224,34824
MG v,12,v,180,a,4
RT 170,224,43016
MG v,44,v,176,a,8
RT 172,224,51208
MG v,44,v,180,a,12
RT 174,224,59400
s_mov_b32 m0, 36864
MG v,76,v,176,a,16
buffer_load_dwordx4 v226, s[52:55], s67 offen offset:0 lds
MG v,76,v,180,a,20
MG v,108,v,176,a,24
MG v,108,v,180,a,28
s_mov_b32 m0, 40960
MG v,152,v,176,a,32
buffer_load_dwordx4 v226, s[52:55], s68 offen offset:0 lds
MG v,152,v,180,a,36
MG v,156,v,176,a,40
MG v,156,v,180,a,44
s_waitcnt lgkmcnt(4)
MG v,16,v,160,a,0
RT 176,224,35328
MG v,16,v,164,a,4
RT 178,224,43520
MG v,48,v,160,a,8
RT 180,224,51712
MG v,48,v,164,a,12
RT 182,224,59904
s_mov_b32 m0, 45056
MG v,80,v,160,a,16
buffer_load_dwordx4 v226, s[52:55], s69 offen offset:0 lds
MG v,80,v,164,a,20
MG v,112,v,160,a,24
MG v,112,v,164,a,28
s_waitcnt lgkmcnt(4)
MG v,20,v,168,a,0
RT 160,224,35336
MG v,20,v,172,a,4
RT 162,224,43528
MG v,52,v,168,a,8
RT 164,224,51720
MG v,52,v,172,a,12
RT 166,224,59912
s_mov_b32 m0, 49152
MG v,84,v,168,a,16
buffer_load_dwordx4 v226, s[52:55], s70 offen offset:0 lds
MG v,84,v,172,a,20
MG v,116,v,168,a,24
MG v,116,v,172,a,28
s_waitcnt lgkmcnt(4)
MG v,24,v,176,a,0
RT 168,224,36864
MG v,24,v,180,a,4
RT 170,224,45056
MG v,56,v,176,a,8
RT 172,224,53248
MG v,56,v,180,a,12
RT 174,224,61440
s_mov_b32 m0, 53248
MG v,88,v,176,a,16
buffer_load_dwordx4 v226, s[52:55], s71 offen offset:0 lds
MG v,88,v,180,a,20
MG v,120,v,176,a,24
s_waitcnt vmcnt(13) lgkmcnt(4)
MG v,120,v,180,a,28
s_barrier
MG v,28,v,160,a,0
RT 176,224,36872
MG v,28,v,164,a,4
RT 178,224,45064
MG v,60,v,160,a,8
RT 180,224,53256
MG v,60,v,164,a,12
RT 182,224,61448
MG v,92,v,160,a,16
ds_read_b128 a[48:51], v249 offset:0
s_mov_b32 m0, 57344
MG v,92,v,164,a,20
buffer_load_dwordx4 v226, s[52:55], s72 offen offset:0 lds
MG v,124,v,160,a,24
ds_read_b128 a[52:55], v249 offset:512
MG v,124,v,164,a,28
s_waitcnt lgkmcnt(6)
MG a,128,v,168,a,0
RT 160,224,37376
MG a,128,v,172,a,4
RT 162,224,45568
MG a,132,v,168,a,8
RT 164,224,53760
MG a,132,v,172,a,12
RT 166,224,61952
MG a,136,v,168,a,16
ds_read_b128 a[56:59], v249 offset:2048
s_mov_b32 m0, 61440
MG a,136,v,172,a,20
buffer_load_dwordx4 v226, s[52:55], s73 offen offset:0 lds
MG a,140,v,168,a,24
ds_read_b128 a[60:63], v249 offset:2560
MG a,140,v,172,a,28
s_waitcnt lgkmcnt(8)
MG a,144,v,176,a,0
RT 168,224,37384
MG a,144,v,180,a,4
RT 170,224,45576
MG a,148,v,176,a,8
RT 172,224,53768
MG a,148,v,180,a,12
RT 174,224,61960
MG a,152,v,176,a,16
ds_read_b128 a[64:67], v249 offset:4096
s_mov_b32 m0, 65536
MG a,152,v,180,a,20
buffer_load_dwordx4 v226, s[52:55], s74 offen offset:0 lds
MG a,156,v,176,a,24
ds_read_b128 a[68:71], v249 offset:4608
MG a,156,v,180,a,28
s_waitcnt lgkmcnt(8)
MG a,160,v,160,a,0
RT 176,224,38912
MG a,160,v,164,a,4
RT 178,224,47104
MG a,164,v,160,a,8
RT 180,224,55296
MG a,164,v,164,a,12
RT 182,224,63488
MG a,168,v,160,a,16
ds_read_b128 a[72:75], v249 offset:6144
s_mov_b32 m0, 69632
MG a,168,v,164,a,20
buffer_load_dwordx4 v226, s[52:55], s75 offen offset:0 lds
v_add_u32 v226, 0x80, v226
MG a,172,v,160,a,24
ds_read_b128 a[76:79], v249 offset:6656
MG a,172,v,164,a,28
s_waitcnt lgkmcnt(8)
MG a,176,v,168,a,0
RT 160,224,38920
MG a,176,v,172,a,4
RT 162,224,47112
MG a,180,v,168,a,8
RT 164,224,55304
MG a,180,v,172,a,12
RT 166,224,63496
MG a,184,v,168,a,16
ds_read_b128 a[80:83], v249 offset:32768
MG a,184,v,172,a,20
MG a,188,v,168,a,24
ds_read_b128 a[84:87], v249 offset:33280
MG a,188,v,172,a,28
s_waitcnt lgkmcnt(8)
MG a,192,v,176,a,0
RT 168,224,39424
MG a,192,v,180,a,4
RT 170,224,47616
MG a,196,v,176,a,8
RT 172,224,55808
MG a,196,v,180,a,12
RT 174,224,64000
MG a,200,v,176,a,16
ds_read_b128 a[88:91], v249 offset:34816
MG a,200,v,180,a,20
MG a,204,v,176,a,24
ds_read_b128 a[92:95], v249 offset:35328
MG a,204,v,180,a,28
s_waitcnt lgkmcnt(8)
MG a,208,v,160,a,0
RT 176,224,39432
MG a,208,v,164,a,4
RT 178,224,47624
MG a,212,v,160,a,8
RT 180,224,55816
MG a,212,v,164,a,12
RT 182,224,64008
MG a,216,v,160,a,16
MG a,216,v,164,a,20
MG a,220,v,160,a,24
s_waitcnt vmcnt(9) lgkmcnt(0)
MG a,220,v,164,a,28
s_barrier
MG a,224,v,168,a,0
RT 160,224,0
MG a,224,v,172,a,4
RT 162,224,8192
MG a,228,v,168,a,8
RT 164,224,16384
MG a,228,v,172,a,12
RT 166,224,24576
s_mov_b32 m0, 131072
MG a,232,v,168,a,16
buffer_load_dwordx4 v225, s[48:51], s76 offen offset:0 lds
MG a,232,v,172,a,20
MG a,236,v,168,a,24
MG a,236,v,172,a,28
MG a,240,v,176,a,0
RT 168,224,8
MG a,240,v,180,a,4
RT 170,224,8200
MG a,244,v,176,a,8
RT 172,224,16392
MG a,244,v,180,a,12
RT 174,224,24584
s_mov_b32 m0, 132992
MG a,248,v,176,a,16
buffer_load_dwordx4 v225, s[48:51], s76 offen offset:128 lds
MG a,248,v,180,a,20
MG a,252,v,176,a,24
MG a,252,v,180,a,28
s_waitcnt lgkmcnt(4)
MG v,0,v,160,a,48
RT 176,224,512
MG v,0,v,164,a,52
RT 178,224,8704
MG v,32,v,160,a,56
RT 180,224,16896
MG v,32,v,164,a,60
RT 182,224,25088
s_mov_b32 m0, 134912
MG v,64,v,160,a,64
buffer_load_dwordx4 v225, s[48:51], s76 offen offset:256 lds
MG v,64,v,164,a,68
MG v,96,v,160,a,72
MG v,96,v,164,a,76
s_mov_b32 m0, 136832
MG v,128,v,160,a,80
buffer_load_dwordx4 v225, s[48:51], s76 offen offset:384 lds
s_add_u32 s76, s76, 262144
MG v,128,v,164,a,84
MG v,132,v,160,a,88
MG v,132,v,164,a,92
s_waitcnt lgkmcnt(4)
MG v,4,v,168,a,48
RT 160,224,520
MG v,4,v,172,a,52
RT 162,224,8712
MG v,36,v,168,a,56
RT 164,224,16904
MG v,36,v,172,a,60
RT 166,224,25096
s_mov_b32 m0, 147456
MG v,68,v,168,a,64
buffer_load_dwordx4 v225, s[48:51], s76 offen offset:0 lds
MG v,68,v,172,a,68
MG v,100,v,168,a,72
MG v,100,v,172,a,76
s_mov_b32 m0, 149376
MG v,136,v,168,a,80
buffer_load_dwordx4 v225, s[48:51], s76 offen offset:128 lds
MG v,136,v,172,a,84
MG v,140,v,168,a,88
MG v,140,v,172,a,92
s_waitcnt lgkmcnt(4)
MG v,8,v,176,a,48
RT 168,224,2048
MG v,8,v,180,a,52
RT 170,224,10240
MG v,40,v,176,a,56
RT 172,224,18432
MG v,40,v,180,a,60
RT 174,224,26624
s_mov_b32 m0, 151296
MG v,72,v,176,a,64
buffer_load_dwordx4 v225, s[48:51], s76 offen offset:256 lds
MG v,72,v,180,a,68
MG v,104,v,176,a,72
MG v,104,v,180,a,76
s_mov_b32 m0, 153216
MG v,144,v,176,a,80
buffer_load_dwordx4 v225, s[48:51], s76 offen offset:384 lds
s_add_u32 s76, s76, 262144
MG v,144,v,180,a,84
MG v,148,v,176,a,88
MG v,148,v,180,a,92
s_waitcnt lgkmcnt(4)
MG v,12,v,160,a,48
RT 176,224,2056
MG v,12,v,164,a,52
RT 178,224,10248
MG v,44,v,160,a,56
RT 180,224,18440
MG v,44,v,164,a,60
RT 182,224,26632
s_mov_b32 m0, 0
MG v,76,v,160,a,64
buffer_load_dwordx4 v226, s[52:55], s67 offen offset:0 lds
MG v,76,v,164,a,68
MG v,108,v,160,a,72
MG v,108,v,164,a,76
s_mov_b32 m0, 4096
MG v,152,v,160,a,80
buffer_load_dwordx4 v226, s[52:55], s68 offen offset:0 lds
MG v,152,v,164,a,84
MG v,156,v,160,a,88
MG v,156,v,164,a,92
s_waitcnt lgkmcnt(4)
MG v,16,v,168,a,48
RT 160,224,2560
MG v,16,v,172,a,52
RT 162,224,10752
MG v,48,v,168,a,56
RT 164,224,18944
MG v,48,v,172,a,60
RT 166,224,27136
s_mov_b32 m0, 8192
MG v,80,v,168,a,64
buffer_load_dwordx4 v226, s[52:55], s69 offen offset:0 lds
MG v,80,v,172,a,68
MG v,112,v,168,a,72
MG v,112,v,172,a,76
s_waitcnt lgkmcnt(4)
MG v,20,v,176,a,48
RT 168,224,2568
MG v,20,v,180,a,52
RT 170,224,10760
MG v,52,v,176,a,56
RT 172,224,18952
MG v,52,v,180,a,60
RT 174,224,27144
s_mov_b32 m0, 12288
MG v,84,v,176,a,64
buffer_load_dwordx4 v226, s[52:55], s70 offen offset:0 lds
MG v,84,v,180,a,68
MG v,116,v,176,a,72
MG v,116,v,180,a,76
s_waitcnt lgkmcnt(4)
MG v,24,v,160,a,48
RT 176,224,4096
MG v,24,v,164,a,52
RT 178,224,12288
MG v,56,v,160,a,56
RT 180,224,20480
MG v,56,v,164,a,60
RT 182,224,28672
s_mov_b32 m0, 16384
MG v,88,v,160,a,64
buffer_load_dwordx4 v226, s[52:55], s71 offen offset:0 lds
MG v,88,v,164,a,68
MG v,120,v,160,a,72
s_waitcnt vmcnt(13) lgkmcnt(4)
MG v,120,v,164,a,76
s_barrier
MG v,28,v,168,a,48
RT 160,224,4104
MG v,28,v,172,a,52
RT 162,224,12296
MG v,60,v,168,a,56
RT 164,224,20488
MG v,60,v,172,a,60
RT 166,224,28680
MG v,92,v,168,a,64
ds_read_b128 a[0:3], v227 offset:0
s_mov_b32 m0, 20480
MG v,92,v,172,a,68
buffer_load_dwordx4 v226, s[52:55], s72 offen offset:0 lds
MG v,124,v,168,a,72
ds_read_b128 a[4:7], v227 offset:512
MG v,124,v,172,a,76
s_waitcnt lgkmcnt(6)
MG a,128,v,176,a,48
RT 168,224,4608
MG a,128,v,180,a,52
RT 170,224,12800
MG a,132,v,176,a,56
RT 172,224,20992
MG a,132,v,180,a,60
RT 174,224,29184
MG a,136,v,176,a,64
ds_read_b128 a[8:11], v227 offset:2048
s_mov_b32 m0, 24576
MG a,136,v,180,a,68
buffer_load_dwordx4 v226, s[52:55], s73 offen offset:0 lds
MG a,140,v,176,a,72
ds_read_b128 a[12:15], v227 offset:2560
MG a,140,v,180,a,76
s_waitcnt lgkmcnt(8)
MG a,144,v,160,a,48
RT 176,224,4616
MG a,144,v,164,a,52
RT 178,224,12808
MG a,148,v,160,a,56
RT 180,224,21000
MG a,148,v,164,a,60
RT 182,224,29192
MG a,152,v,160,a,64
ds_read_b128 a[16:19], v227 offset:4096
s_mov_b32 m0, 28672
MG a,152,v,164,a,68
buffer_load_dwordx4 v226, s[52:55], s74 offen offset:0 lds
MG a,156,v,160,a,72
ds_read_b128 a[20:23], v227 offset:4608
MG a,156,v,164,a,76
s_waitcnt lgkmcnt(8)
MG a,160,v,168,a,48
RT 160,224,6144
MG a,160,v,172,a,52
RT 162,224,14336
MG a,164,v,168,a,56
RT 164,224,22528
MG a,164,v,172,a,60
RT 166,224,30720
MG a,168,v,168,a,64
ds_read_b128 a[24:27], v227 offset:6144
s_mov_b32 m0, 32768
MG a,168,v,172,a,68
buffer_load_dwordx4 v226, s[52:55], s75 offen offset:0 lds
v_add_u32 v226, 0x80, v226
MG a,172,v,168,a,72
ds_read_b128 a[28:31], v227 offset:6656
MG a,172,v,172,a,76
s_waitcnt lgkmcnt(8)
MG a,176,v,176,a,48
RT 168,224,6152
MG a,176,v,180,a,52
RT 170,224,14344
MG a,180,v,176,a,56
RT 172,224,22536
MG a,180,v,180,a,60
RT 174,224,30728
MG a,184,v,176,a,64
ds_read_b128 a[32:35], v227 offset:32768
MG a,184,v,180,a,68
MG a,188,v,176,a,72
ds_read_b128 a[36:39], v227 offset:33280
MG a,188,v,180,a,76
s_waitcnt lgkmcnt(8)
MG a,192,v,160,a,48
RT 176,224,6656
MG a,192,v,164,a,52
RT 178,224,14848
MG a,196,v,160,a,56
RT 180,224,23040
MG a,196,v,164,a,60
RT 182,224,31232
MG a,200,v,160,a,64
ds_read_b128 a[40:43], v227 offset:34816
MG a,200,v,164,a,68
MG a,204,v,160,a,72
ds_read_b128 a[44:47], v227 offset:35328
MG a,204,v,164,a,76
s_waitcnt lgkmcnt(8)
MG a,208,v,168,a,48
RT 160,224,6664
MG a,208,v,172,a,52
RT 162,224,14856
MG a,212,v,168,a,56
RT 164,224,23048
MG a,212,v,172,a,60
RT 166,224,31240
MG a,216,v,168,a,64
MG a,216,v,172,a,68
MG a,220,v,168,a,72
s_waitcnt vmcnt(9) lgkmcnt(0)
MG a,220,v,172,a,76
s_barrier
MG a,224,v,176,a,48
RT 168,224,32768
MG a,224,v,180,a,52
RT 170,224,40960
MG a,228,v,176,a,56
RT 172,224,49152
MG a,228,v,180,a,60
RT 174,224,57344
s_mov_b32 m0, 98304
MG a,232,v,176,a,64
buffer_load_dwordx4 v225, s[48:51], s76 offen offset:0 lds
MG a,232,v,180,a,68
MG a,236,v,176,a,72
MG a,236,v,180,a,76
MG a,240,v,160,a,48
RT 176,224,32776
MG a,240,v,164,a,52
RT 178,224,40968
MG a,244,v,160,a,56
RT 180,224,49160
MG a,244,v,164,a,60
RT 182,224,57352
s_mov_b32 m0, 100224
MG a,248,v,160,a,64
buffer_load_dwordx4 v225, s[48:51], s76 offen offset:128 lds
MG a,248,v,164,a,68
MG a,252,v,160,a,72
MG a,252,v,164,a,76
s_waitcnt lgkmcnt(4)
MG v,0,v,168,a,0
RT 160,224,33280
MG v,0,v,172,a,4
RT 162,224,41472
MG v,32,v,168,a,8
RT 164,224,49664
MG v,32,v,172,a,12
RT 166,224,57856
s_mov_b32 m0, 102144
MG v,64,v,168,a,16
buffer_load_dwordx4 v225, s[48:51], s76 offen offset:256 lds
MG v,64,v,172,a,20
MG v,96,v,168,a,24
MG v,96,v,172,a,28
s_mov_b32 m0, 104064
MG v,128,v,168,a,32
buffer_load_dwordx4 v225, s[48:51], s76 offen offset:384 lds
s_add_u32 s76, s76, 262144
MG v,128,v,172,a,36
MG v,132,v,168,a,40
MG v,132,v,172,a,44
s_waitcnt lgkmcnt(4)
MG v,4,v,176,a,0
RT 168,224,33288
MG v,4,v,180,a,4
RT 170,224,41480
MG v,36,v,176,a,8
RT 172,224,49672
MG v,36,v,180,a,12
RT 174,224,57864
s_mov_b32 m0, 114688
MG v,68,v,176,a,16
buffer_load_dwordx4 v225, s[48:51], s76 offen offset:0 lds
MG v,68,v,180,a,20
MG v,100,v,176,a,24
MG v,100,v,180,a,28
s_mov_b32 m0, 116608
MG v,136,v,176,a,32
buffer_load_dwordx4 v225, s[48:51], s76 offen offset:128 lds
MG v,136,v,180,a,36
MG v,140,v,176,a,40
MG v,140,v,180,a,44
s_waitcnt lgkmcnt(4)
MG v,8,v,160,a,0
RT 176,224,34816
MG v,8,v,164,a,4
RT 178,224,43008
MG v,40,v,160,a,8
RT 180,224,51200
MG v,40,v,164,a,12
RT 182,224,59392
s_mov_b32 m0, 118528
MG v,72,v,160,a,16
buffer_load_dwordx4 v225, s[48:51], s76 offen offset:256 lds
MG v,72,v,164,a,20
MG v,104,v,160,a,24
MG v,104,v,164,a,28
s_mov_b32 m0, 120448
MG v,144,v,160,a,32
buffer_load_dwordx4 v225, s[48:51], s76 offen offset:384 lds
s_add_u32 s76, s76, 262144
MG v,144,v,164,a,36
MG v,148,v,160,a,40
MG v,148,v,164,a,44
s_waitcnt lgkmcnt(4)
MG v,12,v,168,a,0
RT 160,224,34824
MG v,12,v,172,a,4
RT 162,224,43016
MG v,44,v,168,a,8
RT 164,224,51208
MG v,44,v,172,a,12
RT 166,224,59400
s_mov_b32 m0, 36864
MG v,76,v,168,a,16
buffer_load_dwordx4 v226, s[52:55], s67 offen offset:0 lds
MG v,76,v,172,a,20
MG v,108,v,168,a,24
MG v,108,v,172,a,28
s_mov_b32 m0, 40960
MG v,152,v,168,a,32
buffer_load_dwordx4 v226, s[52:55], s68 offen offset:0 lds
MG v,152,v,172,a,36
MG v,156,v,168,a,40
MG v,156,v,172,a,44
s_waitcnt lgkmcnt(4)
MG v,16,v,176,a,0
RT 168,224,35328
MG v,16,v,180,a,4
RT 170,224,43520
MG v,48,v,176,a,8
RT 172,224,51712
MG v,48,v,180,a,12
RT 174,224,59904
s_mov_b32 m0, 45056
MG v,80,v,176,a,16
buffer_load_dwordx4 v226, s[52:55], s69 offen offset:0 lds
MG v,80,v,180,a,20
MG v,112,v,176,a,24
MG v,112,v,180,a,28
s_waitcnt lgkmcnt(4)
MG v,20,v,160,a,0
RT 176,224,35336
MG v,20,v,164,a,4
RT 178,224,43528
MG v,52,v,160,a,8
RT 180,224,51720
MG v,52,v,164,a,12
RT 182,224,59912
s_mov_b32 m0, 49152
MG v,84,v,160,a,16
buffer_load_dwordx4 v226, s[52:55], s70 offen offset:0 lds
MG v,84,v,164,a,20
MG v,116,v,160,a,24
MG v,116,v,164,a,28
s_waitcnt lgkmcnt(4)
MG v,24,v,168,a,0
RT 160,224,36864
MG v,24,v,172,a,4
RT 162,224,45056
MG v,56,v,168,a,8
RT 164,224,53248
MG v,56,v,172,a,12
RT 166,224,61440
s_mov_b32 m0, 53248
MG v,88,v,168,a,16
buffer_load_dwordx4 v226, s[52:55], s71 offen offset:0 lds
MG v,88,v,172,a,20
MG v,120,v,168,a,24
s_waitcnt vmcnt(13) lgkmcnt(4)
MG v,120,v,172,a,28
s_barrier
MG v,28,v,176,a,0
RT 168,224,36872
MG v,28,v,180,a,4
RT 170,224,45064
MG v,60,v,176,a,8
RT 172,224,53256
MG v,60,v,180,a,12
RT 174,224,61448
MG v,92,v,176,a,16
ds_read_b128 a[48:51], v249 offset:0
s_mov_b32 m0, 57344
MG v,92,v,180,a,20
buffer_load_dwordx4 v226, s[52:55], s72 offen offset:0 lds
MG v,124,v,176,a,24
ds_read_b128 a[52:55], v249 offset:512
MG v,124,v,180,a,28
s_waitcnt lgkmcnt(6)
MG a,128,v,160,a,0
RT 176,224,37376
MG a,128,v,164,a,4
RT 178,224,45568
MG a,132,v,160,a,8
RT 180,224,53760
MG a,132,v,164,a,12
RT 182,224,61952
MG a,136,v,160,a,16
ds_read_b128 a[56:59], v249 offset:2048
s_mov_b32 m0, 61440
MG a,136,v,164,a,20
buffer_load_dwordx4 v226, s[52:55], s73 offen offset:0 lds
MG a,140,v,160,a,24
ds_read_b128 a[60:63], v249 offset:2560
MG a,140,v,164,a,28
s_waitcnt lgkmcnt(8)
MG a,144,v,168,a,0
RT 160,224,37384
MG a,144,v,172,a,4
RT 162,224,45576
MG a,148,v,168,a,8
RT 164,224,53768
MG a,148,v,172,a,12
RT 166,224,61960
MG a,152,v,168,a,16
ds_read_b128 a[64:67], v249 offset:4096
s_mov_b32 m0, 65536
MG a,152,v,172,a,20
buffer_load_dwordx4 v226, s[52:55], s74 offen offset:0 lds
MG a,156,v,168,a,24
ds_read_b128 a[68:71], v249 offset:4608
MG a,156,v,172,a,28
s_waitcnt lgkmcnt(8)
MG a,160,v,176,a,0
RT 168,224,38912
MG a,160,v,180,a,4
RT 170,224,47104
MG a,164,v,176,a,8
RT 172,224,55296
MG a,164,v,180,a,12
RT 174,224,63488
MG a,168,v,176,a,16
ds_read_b128 a[72:75], v249 offset:6144
s_mov_b32 m0, 69632
MG a,168,v,180,a,20
buffer_load_dwordx4 v226, s[52:55], s75 offen offset:0 lds
v_add_u32 v226, 0x80, v226
MG a,172,v,176,a,24
ds_read_b128 a[76:79], v249 offset:6656
MG a,172,v,180,a,28
s_waitcnt lgkmcnt(8)
MG a,176,v,160,a,0
RT 176,224,38920
MG a,176,v,164,a,4
RT 178,224,47112
MG a,180,v,160,a,8
RT 180,224,55304
MG a,180,v,164,a,12
RT 182,224,63496
MG a,184,v,160,a,16
ds_read_b128 a[80:83], v249 offset:32768
MG a,184,v,164,a,20
MG a,188,v,160,a,24
ds_read_b128 a[84:87], v249 offset:33280
MG a,188,v,164,a,28
s_waitcnt lgkmcnt(8)
MG a,192,v,168,a,0
RT 160,224,39424
MG a,192,v,172,a,4
RT 162,224,47616
MG a,196,v,168,a,8
RT 164,224,55808
MG a,196,v,172,a,12
RT 166,224,64000
MG a,200,v,168,a,16
ds_read_b128 a[88:91], v249 offset:34816
MG a,200,v,172,a,20
MG a,204,v,168,a,24
ds_read_b128 a[92:95], v249 offset:35328
MG a,204,v,172,a,28
s_waitcnt lgkmcnt(8)
MG a,208,v,176,a,0
RT 168,224,39432
MG a,208,v,180,a,4
RT 170,224,47624
MG a,212,v,176,a,8
RT 172,224,55816
MG a,212,v,180,a,12
RT 174,224,64008
MG a,216,v,176,a,16
MG a,216,v,180,a,20
MG a,220,v,176,a,24
s_waitcnt vmcnt(9) lgkmcnt(0)
MG a,220,v,180,a,28
s_barrier
MG a,224,v,160,a,0
RT 176,224,0
MG a,224,v,164,a,4
RT 178,224,8192
MG a,228,v,160,a,8
RT 180,224,16384
MG a,228,v,164,a,12
RT 182,224,24576
s_mov_b32 m0, 131072
MG a,232,v,160,a,16
buffer_load_dwordx4 v225, s[48:51], s76 offen offset:0 lds
MG a,232,v,164,a,20
MG a,236,v,160,a,24
MG a,236,v,164,a,28
MG a,240,v,168,a,0
RT 160,224,8
MG a,240,v,172,a,4
RT 162,224,8200
MG a,244,v,168,a,8
RT 164,224,16392
MG a,244,v,172,a,12
RT 166,224,24584
s_mov_b32 m0, 132992
MG a,248,v,168,a,16
buffer_load_dwordx4 v225, s[48:51], s76 offen offset:128 lds
MG a,248,v,172,a,20
MG a,252,v,168,a,24
MG a,252,v,172,a,28
s_waitcnt lgkmcnt(4)
MG v,0,v,176,a,48
RT 168,224,512
MG v,0,v,180,a,52
RT 170,224,8704
MG v,32,v,176,a,56
RT 172,224,16896
MG v,32,v,180,a,60
RT 174,224,25088
s_mov_b32 m0, 134912
MG v,64,v,176,a,64
buffer_load_dwordx4 v225, s[48:51], s76 offen offset:256 lds
MG v,64,v,180,a,68
MG v,96,v,176,a,72
MG v,96,v,180,a,76
s_mov_b32 m0, 136832
MG v,128,v,176,a,80
buffer_load_dwordx4 v225, s[48:51], s76 offen offset:384 lds
s_add_u32 s76, s76, 262144
MG v,128,v,180,a,84
MG v,132,v,176,a,88
MG v,132,v,180,a,92
s_waitcnt lgkmcnt(4)
MG v,4,v,160,a,48
RT 176,224,520
MG v,4,v,164,a,52
RT 178,224,8712
MG v,36,v,160,a,56
RT 180,224,16904
MG v,36,v,164,a,60
RT 182,224,25096
s_mov_b32 m0, 147456
MG v,68,v,160,a,64
buffer_load_dwordx4 v225, s[48:51], s76 offen offset:0 lds
MG v,68,v,164,a,68
MG v,100,v,160,a,72
MG v,100,v,164,a,76
s_mov_b32 m0, 149376
MG v,136,v,160,a,80
buffer_load_dwordx4 v225, s[48:51], s76 offen offset:128 lds
MG v,136,v,164,a,84
MG v,140,v,160,a,88
MG v,140,v,164,a,92
s_waitcnt lgkmcnt(4)
MG v,8,v,168,a,48
RT 160,224,2048
MG v,8,v,172,a,52
RT 162,224,10240
MG v,40,v,168,a,56
RT 164,224,18432
MG v,40,v,172,a,60
RT 166,224,26624
s_mov_b32 m0, 151296
MG v,72,v,168,a,64
buffer_load_dwordx4 v225, s[48:51], s76 offen offset:256 lds
MG v,72,v,172,a,68
MG v,104,v,168,a,72
MG v,104,v,172,a,76
s_mov_b32 m0, 153216
MG v,144,v,168,a,80
buffer_load_dwordx4 v225, s[48:51], s76 offen offset:384 lds
s_add_u32 s76, s76, 262144
MG v,144,v,172,a,84
MG v,148,v,168,a,88
MG v,148,v,172,a,92
s_waitcnt lgkmcnt(4)
MG v,12,v,176,a,48
RT 168,224,2056
MG v,12,v,180,a,52
RT 170,224,10248
MG v,44,v,176,a,56
RT 172,224,18440
MG v,44,v,180,a,60
RT 174,224,26632
s_mov_b32 m0, 0
MG v,76,v,176,a,64
buffer_load_dwordx4 v253, s[40:43], s60 offen offset:0 nt lds
MG v,76,v,180,a,68
MG v,108,v,176,a,72
MG v,108,v,180,a,76
s_mov_b32 m0, 1920
MG v,152,v,176,a,80
buffer_load_dwordx4 v253, s[40:43], s60 offen offset:128 nt lds
MG v,152,v,180,a,84
MG v,156,v,176,a,88
MG v,156,v,180,a,92
s_waitcnt lgkmcnt(4)
MG v,16,v,160,a,48
RT 176,224,2560
MG v,16,v,164,a,52
RT 178,224,10752
MG v,48,v,160,a,56
RT 180,224,18944
MG v,48,v,164,a,60
RT 182,224,27136
s_mov_b32 m0, 3840
MG v,80,v,160,a,64
buffer_load_dwordx4 v253, s[40:43], s60 offen offset:256 nt lds
MG v,80,v,164,a,68
MG v,112,v,160,a,72
MG v,112,v,164,a,76
s_waitcnt lgkmcnt(4)
MG v,20,v,168,a,48
RT 160,224,2568
MG v,20,v,172,a,52
RT 162,224,10760
MG v,52,v,168,a,56
RT 164,224,18952
MG v,52,v,172,a,60
RT 166,224,27144
s_mov_b32 m0, 5760
MG v,84,v,168,a,64
buffer_load_dwordx4 v253, s[40:43], s60 offen offset:384 nt lds
s_add_u32 s60, s60, 16384
MG v,84,v,172,a,68
MG v,116,v,168,a,72
MG v,116,v,172,a,76
s_waitcnt lgkmcnt(4)
MG v,24,v,176,a,48
RT 168,224,4096
MG v,24,v,180,a,52
RT 170,224,12288
MG v,56,v,176,a,56
RT 172,224,20480
MG v,56,v,180,a,60
RT 174,224,28672
s_mov_b32 m0, 16384
MG v,88,v,176,a,64
buffer_load_dwordx4 v253, s[40:43], s60 offen offset:0 nt lds
MG v,88,v,180,a,68
MG v,120,v,176,a,72
s_waitcnt vmcnt(13) lgkmcnt(4)
MG v,120,v,180,a,76
s_barrier
MG v,28,v,160,a,48
RT 176,224,4104
MG v,28,v,164,a,52
RT 178,224,12296
MG v,60,v,160,a,56
RT 180,224,20488
MG v,60,v,164,a,60
RT 182,224,28680
MG v,92,v,160,a,64
ds_read_b128 a[0:3], v227 offset:0
s_mov_b32 m0, 18304
MG v,92,v,164,a,68
buffer_load_dwordx4 v253, s[40:43], s60 offen offset:128 nt lds
MG v,124,v,160,a,72
ds_read_b128 a[4:7], v227 offset:512
MG v,124,v,164,a,76
s_waitcnt lgkmcnt(6)
MG a,128,v,168,a,48
RT 160,224,4608
MG a,128,v,172,a,52
RT 162,224,12800
MG a,132,v,168,a,56
RT 164,224,20992
MG a,132,v,172,a,60
RT 166,224,29184
MG a,136,v,168,a,64
ds_read_b128 a[8:11], v227 offset:2048
s_mov_b32 m0, 20224
MG a,136,v,172,a,68
buffer_load_dwordx4 v253, s[40:43], s60 offen offset:256 nt lds
MG a,140,v,168,a,72
ds_read_b128 a[12:15], v227 offset:2560
MG a,140,v,172,a,76
s_waitcnt lgkmcnt(8)
MG a,144,v,176,a,48
RT 168,224,4616
MG a,144,v,180,a,52
RT 170,224,12808
MG a,148,v,176,a,56
RT 172,224,21000
MG a,148,v,180,a,60
RT 174,224,29192
MG a,152,v,176,a,64
ds_read_b128 a[16:19], v227 offset:4096
s_mov_b32 m0, 22144
MG a,152,v,180,a,68
buffer_load_dwordx4 v253, s[40:43], s60 offen offset:384 nt lds
s_add_u32 s60, s60, 16384
MG a,156,v,176,a,72
ds_read_b128 a[20:23], v227 offset:4608
MG a,156,v,180,a,76
s_waitcnt lgkmcnt(8)
MG a,160,v,160,a,48
RT 176,224,6144
MG a,160,v,164,a,52
RT 178,224,14336
MG a,164,v,160,a,56
RT 180,224,22528
MG a,164,v,164,a,60
RT 182,224,30720
MG a,168,v,160,a,64
ds_read_b128 a[24:27], v227 offset:6144
MG a,168,v,164,a,68
MG a,172,v,160,a,72
ds_read_b128 a[28:31], v227 offset:6656
MG a,172,v,164,a,76
s_waitcnt lgkmcnt(8)
MG a,176,v,168,a,48
RT 160,224,6152
MG a,176,v,172,a,52
RT 162,224,14344
MG a,180,v,168,a,56
RT 164,224,22536
MG a,180,v,172,a,60
RT 166,224,30728
MG a,184,v,168,a,64
ds_read_b128 a[32:35], v227 offset:32768
MG a,184,v,172,a,68
MG a,188,v,168,a,72
ds_read_b128 a[36:39], v227 offset:33280
MG a,188,v,172,a,76
s_waitcnt lgkmcnt(8)
MG a,192,v,176,a,48
RT 168,224,6656
MG a,192,v,180,a,52
RT 170,224,14848
MG a,196,v,176,a,56
RT 172,224,23040
MG a,196,v,180,a,60
RT 174,224,31232
MG a,200,v,176,a,64
ds_read_b128 a[40:43], v227 offset:34816
MG a,200,v,180,a,68
MG a,204,v,176,a,72
ds_read_b128 a[44:47], v227 offset:35328
MG a,204,v,180,a,76
s_waitcnt lgkmcnt(8)
MG a,208,v,160,a,48
RT 176,224,6664
MG a,208,v,164,a,52
RT 178,224,14856
MG a,212,v,160,a,56
RT 180,224,23048
MG a,212,v,164,a,60
RT 182,224,31240
MG a,216,v,160,a,64
MG a,216,v,164,a,68
MG a,220,v,160,a,72
s_waitcnt vmcnt(8) lgkmcnt(0)
MG a,220,v,164,a,76
s_barrier
MG a,224,v,168,a,48
RT 160,224,32768
MG a,224,v,172,a,52
RT 162,224,40960
MG a,228,v,168,a,56
RT 164,224,49152
MG a,228,v,172,a,60
RT 166,224,57344
s_mov_b32 m0, 98304
MG a,232,v,168,a,64
buffer_load_dwordx4 v253, s[44:47], s61 offen offset:0 nt lds
MG a,232,v,172,a,68
MG a,236,v,168,a,72
MG a,236,v,172,a,76
MG a,240,v,176,a,48
RT 168,224,32776
MG a,240,v,180,a,52
RT 170,224,40968
MG a,244,v,176,a,56
RT 172,224,49160
MG a,244,v,180,a,60
RT 174,224,57352
s_mov_b32 m0, 100224
MG a,248,v,176,a,64
buffer_load_dwordx4 v253, s[44:47], s61 offen offset:128 nt lds
MG a,248,v,180,a,68
MG a,252,v,176,a,72
MG a,252,v,180,a,76
s_waitcnt lgkmcnt(4)
MG v,0,v,160,a,0
RT 176,224,33280
MG v,0,v,164,a,4
RT 178,224,41472
MG v,32,v,160,a,8
RT 180,224,49664
MG v,32,v,164,a,12
RT 182,224,57856
s_mov_b32 m0, 102144
MG v,64,v,160,a,16
buffer_load_dwordx4 v253, s[44:47], s61 offen offset:256 nt lds
MG v,64,v,164,a,20
MG v,96,v,160,a,24
MG v,96,v,164,a,28
MG v,128,v,160,a,32
MG v,128,v,164,a,36
MG v,132,v,160,a,40
MG v,132,v,164,a,44
s_waitcnt lgkmcnt(4)
MG v,4,v,168,a,0
RT 160,224,33288
MG v,4,v,172,a,4
RT 162,224,41480
MG v,36,v,168,a,8
RT 164,224,49672
MG v,36,v,172,a,12
RT 166,224,57864
s_mov_b32 m0, 104064
MG v,68,v,168,a,16
buffer_load_dwordx4 v253, s[44:47], s61 offen offset:384 nt lds
s_add_u32 s61, s61, 16384
MG v,68,v,172,a,20
MG v,100,v,168,a,24
MG v,100,v,172,a,28
MG v,136,v,168,a,32
MG v,136,v,172,a,36
MG v,140,v,168,a,40
MG v,140,v,172,a,44
s_waitcnt lgkmcnt(4)
MG v,8,v,176,a,0
RT 168,224,34816
MG v,8,v,180,a,4
RT 170,224,43008
MG v,40,v,176,a,8
RT 172,224,51200
MG v,40,v,180,a,12
RT 174,224,59392
s_mov_b32 m0, 32768
MG v,72,v,176,a,16
buffer_load_dwordx4 v253, s[40:43], s60 offen offset:0 nt lds
MG v,72,v,180,a,20
CV 128,128,129
MG v,104,v,176,a,24
CV 129,130,131
CV 130,136,137
MG v,104,v,180,a,28
CV 131,138,139
MG v,144,v,176,a,32
ds_write_b128 v252, v[128:131] offset:0
MG v,144,v,180,a,36
CV 132,132,133
CV 133,134,135
MG v,148,v,176,a,40
CV 134,140,141
MG v,148,v,180,a,44
s_waitcnt lgkmcnt(5)
CV 135,142,143
MG v,12,v,160,a,0
RT 176,224,34824
MG v,12,v,164,a,4
RT 178,224,43016
MG v,44,v,160,a,8
RT 180,224,51208
MG v,44,v,164,a,12
RT 182,224,59400
s_mov_b32 m0, 34688
MG v,76,v,160,a,16
buffer_load_dwordx4 v253, s[40:43], s60 offen offset:128 nt lds
MG v,76,v,164,a,20
ds_write_b128 v252, v[132:135] offset:8192
MG v,108,v,160,a,24
CV 0,0,1
CV 1,2,3
MG v,108,v,164,a,28
CV 2,4,5
CV 3,6,7
MG v,152,v,160,a,32
CV 32,32,33
MG v,152,v,164,a,36
CV 33,34,35
CV 34,36,37
MG v,156,v,160,a,40
CV 35,38,39
CV 64,64,65
MG v,156,v,164,a,44
s_waitcnt lgkmcnt(6)
CV 65,66,67
CV 66,68,69
MG v,16,v,168,a,0
RT 160,224,35328
CV 67,70,71
MG v,16,v,172,a,4
RT 162,224,43520
CV 96,96,97
MG v,48,v,168,a,8
RT 164,224,51712
CV 97,98,99
MG v,48,v,172,a,12
RT 166,224,59904
s_mov_b32 m0, 36608
MG v,80,v,168,a,16
buffer_load_dwordx4 v253, s[40:43], s60 offen offset:256 nt lds
MG v,80,v,172,a,20
CV 98,100,101
CV 99,102,103
MG v,112,v,168,a,24
CV 144,144,145
MG v,112,v,172,a,28
s_waitcnt lgkmcnt(5)
CV 145,146,147
CV 146,152,153
MG v,20,v,176,a,0
RT 168,224,35336
CV 147,154,155
MG v,20,v,180,a,4
RT 170,224,43528
MG v,52,v,176,a,8
RT 172,224,51720
MG v,52,v,180,a,12
RT 174,224,59912
s_mov_b32 m0, 38528
MG v,84,v,176,a,16
buffer_load_dwordx4 v253, s[40:43], s60 offen offset:384 nt lds
s_add_u32 s60, s60, 16384
MG v,84,v,180,a,20
ds_write_b128 v252, v[144:147] offset:1024
MG v,116,v,176,a,24
CV 148,148,149
CV 149,150,151
MG v,116,v,180,a,28
s_waitcnt lgkmcnt(5)
CV 150,156,157
CV 151,158,159
MG v,24,v,160,a,0
RT 176,224,36864
MG v,24,v,164,a,4
RT 178,224,45056
MG v,56,v,160,a,8
RT 180,224,53248
MG v,56,v,164,a,12
RT 182,224,61440
s_mov_b32 m0, 114688
MG v,88,v,160,a,16
buffer_load_dwordx4 v253, s[44:47], s61 offen offset:0 nt lds
MG v,88,v,164,a,20
MG v,120,v,160,a,24
ds_write_b128 v252, v[148:151] offset:9216
MG v,120,v,164,a,28
s_waitcnt lgkmcnt(6)
CV 4,8,9
CV 5,10,11
MG v,28,v,168,a,0
RT 160,224,36872
CV 6,12,13
MG v,28,v,172,a,4
RT 162,224,45064
CV 7,14,15
MG v,60,v,168,a,8
RT 164,224,53256
CV 36,40,41
MG v,60,v,172,a,12
RT 166,224,61448
s_mov_b32 m0, 116608
MG v,92,v,168,a,16
buffer_load_dwordx4 v253, s[44:47], s61 offen offset:128 nt lds
MG v,92,v,172,a,20
CV 37,42,43
CV 38,44,45
MG v,124,v,168,a,24
CV 39,46,47
CV 68,72,73
MG v,124,v,172,a,28
s_waitcnt lgkmcnt(5)
CV 69,74,75
MG a,128,v,176,a,0
RT 168,224,37376
CV 70,76,77
MG a,128,v,180,a,4
RT 170,224,45568
CV 71,78,79
MG a,132,v,176,a,8
RT 172,224,53760
CV 100,104,105
MG a,132,v,180,a,12
RT 174,224,61952
s_mov_b32 m0, 118528
MG a,136,v,176,a,16
buffer_load_dwordx4 v253, s[44:47], s61 offen offset:256 nt lds
MG a,136,v,180,a,20
CV 101,106,107
CV 102,108,109
MG a,140,v,176,a,24
CV 103,110,111
CV 8,16,17
MG a,140,v,180,a,28
s_waitcnt lgkmcnt(4)
CV 9,18,19
CV 10,20,21
MG a,144,v,160,a,0
RT 176,224,37384
CV 11,22,23
MG a,144,v,164,a,4
RT 178,224,45576
CV 40,48,49
MG a,148,v,160,a,8
RT 180,224,53768
CV 41,50,51
MG a,148,v,164,a,12
RT 182,224,61960
s_mov_b32 m0, 120448
MG a,152,v,160,a,16
buffer_load_dwordx4 v253, s[44:47], s61 offen offset:384 nt lds
s_add_u32 s61, s61, 16384
MG a,152,v,164,a,20
CV 42,52,53
CV 43,54,55
MG a,156,v,160,a,24
CV 72,80,81
MG a,156,v,164,a,28
s_waitcnt lgkmcnt(4)
CV 73,82,83
CV 74,84,85
MG a,160,v,168,a,0
RT 160,224,38912
CV 75,86,87
MG a,160,v,172,a,4
RT 162,224,47104
CV 104,112,113
MG a,164,v,168,a,8
RT 164,224,55296
CV 105,114,115
MG a,164,v,172,a,12
RT 166,224,63488
s_mov_b32 m0, 49152
MG a,168,v,168,a,16
buffer_load_dwordx4 v253, s[40:43], s60 offen offset:0 nt lds
MG a,168,v,172,a,20
CV 106,116,117
CV 107,118,119
MG a,172,v,168,a,24
CV 12,24,25
CV 13,26,27
MG a,172,v,172,a,28
s_waitcnt lgkmcnt(4)
CV 14,28,29
CV 15,30,31
MG a,176,v,176,a,0
RT 168,224,38920
CV 44,56,57
MG a,176,v,180,a,4
RT 170,224,47112
CV 45,58,59
MG a,180,v,176,a,8
RT 172,224,55304
CV 46,60,61
MG a,180,v,180,a,12
RT 174,224,63496
s_mov_b32 m0, 51072
MG a,184,v,176,a,16
buffer_load_dwordx4 v253, s[40:43], s60 offen offset:128 nt lds
MG a,184,v,180,a,20
CV 47,62,63
MG a,188,v,176,a,24
CV 76,88,89
CV 77,90,91
MG a,188,v,180,a,28
s_waitcnt lgkmcnt(4)
CV 78,92,93
CV 79,94,95
MG a,192,v,160,a,0
RT 176,224,39424
CV 108,120,121
MG a,192,v,164,a,4
RT 178,224,47616
CV 109,122,123
MG a,196,v,160,a,8
RT 180,224,55808
CV 110,124,125
MG a,196,v,164,a,12
RT 182,224,64000
s_mov_b32 m0, 52992
MG a,200,v,160,a,16
buffer_load_dwordx4 v253, s[40:43], s60 offen offset:256 nt lds
MG a,200,v,164,a,20
CV 111,126,127
AR 192,128
MG a,204,v,160,a,24
AR 193,129
AR 194,130
MG a,204,v,164,a,28
s_waitcnt lgkmcnt(4)
AR 195,131
MG a,208,v,168,a,0
RT 160,224,39432
AR 196,144
MG a,208,v,172,a,4
RT 162,224,47624
AR 197,145
MG a,212,v,168,a,8
RT 164,224,55816
AR 198,146
MG a,212,v,172,a,12
RT 166,224,64008
s_mov_b32 m0, 54912
MG a,216,v,168,a,16
buffer_load_dwordx4 v253, s[40:43], s60 offen offset:384 nt lds
s_add_u32 s60, s60, 16384
MG a,216,v,172,a,20
AR 199,147
CV 16,192,193
MG a,220,v,168,a,24
CV 17,194,195
CV 18,196,197
MG a,220,v,172,a,28
s_waitcnt lgkmcnt(6)
CV 19,198,199
AR 200,132
MG a,224,v,176,a,0
s_waitcnt lgkmcnt(4)
AR 201,133
AR 202,134
MG a,224,v,180,a,4
AR 203,135
MG a,228,v,176,a,8
AR 204,148
AR 205,149
MG a,228,v,180,a,12
AR 206,150
AR 207,151
MG a,232,v,176,a,16
CV 48,200,201
CV 49,202,203
MG a,232,v,180,a,20
CV 50,204,205
CV 51,206,207
MG a,236,v,176,a,24
AR 192,136
MG a,236,v,180,a,28
s_waitcnt lgkmcnt(2)
AR 193,137
AR 194,138
MG a,240,v,160,a,0
s_waitcnt lgkmcnt(0)
AR 195,139
AR 196,152
MG a,240,v,164,a,4
AR 197,153
AR 198,154
MG a,244,v,160,a,8
AR 199,155
CV 80,192,193
MG a,244,v,164,a,12
CV 81,194,195
MG a,248,v,160,a,16
CV 82,196,197
CV 83,198,199
MG a,248,v,164,a,20
AR 200,140
AR 201,141
MG a,252,v,160,a,24
AR 202,142
AR 203,143
MG a,252,v,164,a,28
s_waitcnt vmcnt(20)
AR 204,156
AR 205,157
s_barrier
RB 176,249,0
RB 180,249,512
RB 224,252,0
RB 208,249,2048
RB 212,249,2560
RB 228,252,1024
AR 206,158
AR 207,159
CV 112,200,201
CV 113,202,203
CV 114,204,205
CV 115,206,207
AR 192,160
AR 193,161
AR 194,162
AR 195,163
AR 196,176
AR 197,177
AR 198,178
AR 199,179
CV 20,192,193
CV 21,194,195
CV 22,196,197
CV 23,198,199
AR 200,164
AR 201,165
AR 202,166
AR 203,167
AR 204,180
AR 205,181
AR 206,182
AR 207,183
CV 52,200,201
CV 53,202,203
CV 54,204,205
CV 55,206,207
AR 192,168
AR 193,169
AR 194,170
AR 195,171
AR 196,184
AR 197,185
AR 198,186
AR 199,187
CV 84,192,193
CV 85,194,195
CV 86,196,197
CV 87,198,199
AR 200,172
AR 201,173
AR 202,174
AR 203,175
AR 204,188
AR 205,189
AR 206,190
AR 207,191
CV 116,200,201
CV 117,202,203
CV 118,204,205
CV 119,206,207
AR 192,192
AR 193,193
AR 194,194
AR 195,195
AR 196,208
AR 197,209
AR 198,210
AR 199,211
CV 24,192,193
CV 25,194,195
CV 26,196,197
CV 27,198,199
AR 200,196
AR 201,197
AR 202,198
AR 203,199
AR 204,212
AR 205,213
AR 206,214
AR 207,215
CV 56,200,201
CV 57,202,203
CV 58,204,205
CV 59,206,207
AR 192,200
AR 193,201
AR 194,202
AR 195,203
AR 196,216
AR 197,217
AR 198,218
AR 199,219
CV 88,192,193
CV 89,194,195
CV 90,196,197
CV 91,198,199
AR 200,204
AR 201,205
AR 202,206
AR 203,207
AR 204,220
AR 205,221
AR 206,222
AR 207,223
CV 120,200,201
CV 121,202,203
CV 122,204,205
s_waitcnt lgkmcnt(5)
MV0 160,176,0
RB 216,249,4096
CV 123,206,207
s_waitcnt lgkmcnt(5)
MV 160,180,4
RB 220,249,4608
AR 192,224
MV0 164,176,32
AR 193,225
AR 194,226
MV 164,180,36
AR 195,227
AR 196,240
MV0 168,176,64
AR 197,241
AR 198,242
MV 168,180,68
AR 199,243
CV 28,192,193
MV0 172,176,96
CV 29,194,195
CV 30,196,197
MV 172,180,100
s_waitcnt lgkmcnt(2)
CV 31,198,199
AR 200,228
MV0 232,176,224
RB 224,252,2048
AR 201,229
MV 232,180,228
AR 202,230
AR 203,231
MV 160,208,8
RB 176,249,6144
AR 204,244
MV 160,212,12
RB 180,249,6656
AR 205,245
MV 164,208,40
RB 228,252,3072
AR 206,246
MV 164,212,44
AR 207,247
CV 60,200,201
MV 168,208,72
CV 61,202,203
CV 62,204,205
MV 168,212,76
CV 63,206,207
AR 192,232
MV 172,208,104
AR 193,233
AR 194,234
MV 172,212,108
s_waitcnt lgkmcnt(0)
AR 195,235
AR 196,248
MV 232,208,224
RB 224,252,4096
AR 197,249
MV 232,212,228
AR 198,250
AR 199,251
MV 160,216,16
RB 208,249,8192
CV 92,192,193
MV 160,220,20
RB 212,249,8704
CV 93,194,195
MV 164,216,48
RB 228,252,5120
CV 94,196,197
MV 164,220,52
CV 95,198,199
AR 200,236
MV 168,216,80
AR 201,237
AR 202,238
MV 168,220,84
AR 203,239
AR 204,252
MV 172,216,112
AR 205,253
AR 206,254
MV 172,220,116
s_waitcnt lgkmcnt(0)
AR 207,255
CV 124,200,201
MV 232,216,224
RB 216,249,10240
CV 125,202,203
MV 232,220,228
CV 126,204,205
CV 127,206,207
MV 160,176,24
RB 220,249,10752
MV 160,180,28
RB 224,252,6144
MV 164,176,56
RB 228,252,7168
MV 164,180,60
MV 168,176,88
MV 168,180,92
MV 172,176,120
MV 172,180,124
s_waitcnt lgkmcnt(0)
MV 232,176,224
RB 224,249,12288
MV 232,180,228
MV0 176,208,0
RB 228,249,12800
MV 176,212,4
MV0 180,208,32
MV 180,212,36
MV0 184,208,64
v_max3_f32 v192, v160, v161, v162
v_max3_f32 v193, v164, v165, v166
MV 184,212,68
v_max3_f32 v194, v168, v169, v170
v_max3_f32 v195, v172, v173, v174
MV0 188,208,96
RB 208,249,14336
v_max3_f32 v196, v232, v233, v234
MV 188,212,100
v_max_f32 v192, v192, v163
v_max_f32 v193, v193, v167
MV 176,216,8
RB 212,249,14848
v_max_f32 v194, v194, v171
MV 176,220,12
v_max_f32 v195, v195, v175
v_max_f32 v196, v196, v235
MV 180,216,40
v_mov_b32 v197, v192
v_mov_b32 v198, v193
MV 180,220,44
v_mov_b32 v199, v194
v_mov_b32 v200, v195
MV 184,216,72
v_mov_b32 v201, v196
v_permlane16_swap_b32 v192, v197
MV 184,220,76
v_permlane16_swap_b32 v193, v198
v_permlane16_swap_b32 v194, v199
MV 188,216,104
v_permlane16_swap_b32 v195, v200
v_permlane16_swap_b32 v196, v201
MV 188,220,108
s_waitcnt lgkmcnt(3)
v_max_f32 v192, v192, v197
v_max_f32 v193, v193, v198
MV 176,224,16
s_waitcnt lgkmcnt(2)
v_max_f32 v194, v194, v199
v_max_f32 v195, v195, v200
MV 176,228,20
v_max_f32 v196, v196, v201
v_mov_b32 v197, v192
MV 180,224,48
v_mov_b32 v198, v193
v_mov_b32 v199, v194
MV 180,228,52
v_mov_b32 v200, v195
v_mov_b32 v201, v196
MV 184,224,80
v_permlane32_swap_b32 v192, v197
v_permlane32_swap_b32 v193, v198
MV 184,228,84
v_permlane32_swap_b32 v194, v199
v_permlane32_swap_b32 v195, v200
MV 188,224,112
v_permlane32_swap_b32 v196, v201
v_max_f32 v192, v192, v197
MV 188,228,116
s_waitcnt lgkmcnt(1)
v_max_f32 v193, v193, v198
v_max_f32 v194, v194, v199
MV 176,208,24
s_waitcnt lgkmcnt(0)
v_max_f32 v195, v195, v200
v_max_f32 v196, v196, v201
MV 176,212,28
v_mul_f32 v196, s66, v196
v_mul_f32 v236, s66, v192
MV 180,208,56
v_mul_f32 v237, s66, v193
v_mul_f32 v238, s66, v194
MV 180,212,60
v_mul_f32 v239, s66, v195
MV 184,208,88
ds_write_b32 v252, v196 offset:28672
MV 184,212,92
v_fma_f32 v160, v160, s63, v236
v_fma_f32 v161, v161, s63, v236
MV 188,208,120
v_fma_f32 v162, v162, s63, v236
v_fma_f32 v163, v163, s63, v236
MV 188,212,124
s_waitcnt vmcnt(12) lgkmcnt(0)
v_fma_f32 v164, v164, s63, v237
v_fma_f32 v165, v165, s63, v237
s_barrier
ds_read_b32 v246, v252 offset:28672
ds_read_b32 v248, v252 offset:28676
s_mov_b32 m0, 131072
s_nop 0
buffer_load_dwordx4 v253, s[44:47], s61 offen offset:0 nt lds
s_mov_b32 m0, 132992
s_nop 0
buffer_load_dwordx4 v253, s[44:47], s61 offen offset:128 nt lds
s_mov_b32 m0, 134912
s_nop 0
buffer_load_dwordx4 v253, s[44:47], s61 offen offset:256 nt lds
s_mov_b32 m0, 136832
s_nop 0
buffer_load_dwordx4 v253, s[44:47], s61 offen offset:384 nt lds
s_add_u32 s61, s61, 16384
s_mov_b32 s48, s24
s_and_b32 s49, s25, 0xffff
s_mov_b32 s50, 3670016
s_and_b32 s76, s26, 127
s_mul_i32 s76, s76, 7
v_fma_f32 v166, v166, s63, v237
v_fma_f32 v167, v167, s63, v237
v_fma_f32 v168, v168, s63, v238
v_fma_f32 v169, v169, s63, v238
v_fma_f32 v170, v170, s63, v238
v_fma_f32 v171, v171, s63, v238
v_fma_f32 v172, v172, s63, v239
v_fma_f32 v173, v173, s63, v239
v_fma_f32 v174, v174, s63, v239
v_fma_f32 v175, v175, s63, v239
v_exp_f32 v160, v160
v_exp_f32 v161, v161
v_exp_f32 v162, v162
v_exp_f32 v163, v163
v_exp_f32 v164, v164
v_exp_f32 v165, v165
v_exp_f32 v166, v166
v_exp_f32 v167, v167
v_exp_f32 v168, v168
v_exp_f32 v169, v169
v_exp_f32 v170, v170
v_exp_f32 v171, v171
v_exp_f32 v172, v172
v_exp_f32 v173, v173
v_exp_f32 v174, v174
v_exp_f32 v175, v175
s_waitcnt lgkmcnt(0)
v_min_f32 v240, v246, v248
ds_write_b32 v252, v240 offset:29696
v_mov_b32 v247, 0
v_fma_f32 v176, v176, s63, v236
v_fma_f32 v177, v177, s63, v236
v_fma_f32 v178, v178, s63, v236
v_fma_f32 v179, v179, s63, v236
v_fma_f32 v182, v182, s63, v237
v_fma_f32 v180, v180, s63, v237
v_fma_f32 v181, v181, s63, v237
v_fma_f32 v183, v183, s63, v237
v_fma_f32 v184, v184, s63, v238
v_fma_f32 v185, v185, s63, v238
v_fma_f32 v186, v186, s63, v238
v_fma_f32 v187, v187, s63, v238
v_fma_f32 v188, v188, s63, v239
v_fma_f32 v189, v189, s63, v239
v_fma_f32 v190, v190, s63, v239
v_fma_f32 v191, v191, s63, v239
v_max3_f32 v246, v176, v177, v178
v_max3_f32 v246, v246, v179, v182
v_max3_f32 v246, v246, v180, v181
v_max3_f32 v246, v246, v183, v184
v_max3_f32 v246, v246, v185, v186
v_max3_f32 v246, v246, v187, v188
v_max3_f32 v246, v246, v189, v190
v_max3_f32 v246, v246, v191, v247
)ASM"
R"ASM(
v_cmp_lt_f32 vcc, s64, v246
v_fma_f32 v232, v232, s63, v240
v_fma_f32 v233, v233, s63, v240
v_fma_f32 v234, v234, s63, v240
v_fma_f32 v235, v235, s63, v240
s_cbranch_vccnz Lst0_0
Lrt0_0:
v_add_f32 v241, v241, v160
v_add_f32 v241, v241, v161
v_add_f32 v241, v241, v162
v_add_f32 v241, v241, v163
v_add_f32 v242, v242, v164
v_add_f32 v242, v242, v165
v_add_f32 v242, v242, v166
v_add_f32 v242, v242, v167
v_add_f32 v243, v243, v168
v_add_f32 v243, v243, v169
v_exp_f32 v176, v176
v_exp_f32 v177, v177
v_add_f32 v241, v241, v176
v_add_f32 v241, v241, v177
v_exp_f32 v178, v178
v_exp_f32 v179, v179
v_exp_f32 v180, v180
v_add_f32 v241, v241, v178
v_add_f32 v241, v241, v179
v_exp_f32 v181, v181
v_exp_f32 v182, v182
v_exp_f32 v183, v183
CV 160,160,161
CV 161,162,163
CV 162,176,177
CV 163,178,179
v_add_f32 v242, v242, v180
v_add_f32 v242, v242, v181
v_add_f32 v242, v242, v182
v_add_f32 v242, v242, v183
CV 164,164,165
CV 165,166,167
CV 166,180,181
CV 167,182,183
v_add_f32 v243, v243, v170
v_exp_f32 v232, v232
s_nop 0
v_add_f32 v245, v245, v232
v_exp_f32 v233, v233
v_exp_f32 v234, v234
v_add_f32 v245, v245, v233
RB 176,249,16384
RB 180,249,16896
RB 224,252,0
RB 208,249,18432
RB 212,249,18944
RB 228,252,1024
v_add_f32 v245, v245, v234
CV 254,232,233
v_exp_f32 v235, v235
s_nop 0
v_add_f32 v245, v245, v235
CV 255,234,235
v_add_f32 v243, v243, v171
CV 168,168,169
ds_write_b64 v252, v[254:255] offset:16384
CV 169,170,171
v_exp_f32 v184, v184
s_mov_b32 s65, 0
s_barrier
s_waitcnt lgkmcnt(6)
MV0 192,176,0
RB 216,249,20480
v_add_f32 v244, v244, v172
s_waitcnt lgkmcnt(6)
MV 192,180,4
RB 220,249,20992
v_add_f32 v244, v244, v173
MV0 196,176,32
s_cmp_lg_u32 s65, 0
v_exp_f32 v185, v185
MV 196,180,36
s_cbranch_scc1 Lst0_1
Lrt0_1:
v_exp_f32 v186, v186
MV0 200,176,64
v_exp_f32 v187, v187
MV 200,180,68
v_exp_f32 v188, v188
MV0 204,176,96
s_mov_b32 m0, 0
v_exp_f32 v189, v189
MV 204,180,100
s_waitcnt lgkmcnt(3)
v_exp_f32 v190, v190
MV0 232,176,224
RB 224,252,2048
v_add_f32 v244, v244, v174
MV 232,180,228
v_exp_f32 v191, v191
MV 192,208,8
RB 176,249,22528
v_add_f32 v244, v244, v175
MV 192,212,12
RB 180,249,23040
v_add_f32 v243, v243, v184
MV 196,208,40
RB 228,252,3072
v_add_f32 v243, v243, v185
MV 196,212,44
buffer_load_dwordx4 v253, s[40:43], s60 offen offset:0 nt lds
MV 200,208,72
s_mov_b32 m0, 1920
v_add_f32 v243, v243, v186
v_add_f32 v243, v243, v187
MV 200,212,76
buffer_load_dwordx4 v253, s[40:43], s60 offen offset:128 nt lds
MV 204,208,104
v_add_f32 v244, v244, v188
v_add_f32 v244, v244, v189
MV 204,212,108
s_waitcnt lgkmcnt(0)
v_add_f32 v244, v244, v190
v_add_f32 v244, v244, v191
MV 232,208,224
RB 224,252,4096
CV 170,184,185
MV 232,212,228
CV 172,172,173
CV 173,174,175
MV 192,216,16
RB 208,249,24576
CV 171,186,187
MV 192,220,20
RB 212,249,25088
CV 174,188,189
MV 196,216,48
RB 228,252,5120
CV 175,190,191
MV 196,220,52
s_mov_b32 m0, 3840
MV 200,216,80
buffer_load_dwordx4 v253, s[40:43], s60 offen offset:256 nt lds
MV 200,220,84
MV 204,216,112
MV 204,220,116
s_waitcnt lgkmcnt(0)
MV 232,216,224
RB 216,249,26624
MV 232,220,228
MV 192,176,24
RB 220,249,27136
MV 192,180,28
RB 224,252,6144
MV 196,176,56
RB 228,252,7168
MV 196,180,60
s_mov_b32 m0, 5760
MV 200,176,88
buffer_load_dwordx4 v253, s[40:43], s60 offen offset:384 nt lds
s_add_u32 s60, s60, 16384
MV 200,180,92
v_fma_f32 v192, v192, s63, v236
v_fma_f32 v193, v193, s63, v236
MV 204,176,120
MV 204,180,124
s_waitcnt lgkmcnt(0)
v_fma_f32 v194, v194, s63, v236
v_fma_f32 v195, v195, s63, v236
MV 232,176,224
RB 224,249,28672
v_fma_f32 v196, v196, s63, v237
MV 232,180,228
v_fma_f32 v197, v197, s63, v237
v_fma_f32 v198, v198, s63, v237
MV0 176,208,0
RB 228,249,29184
v_fma_f32 v199, v199, s63, v237
MV 176,212,4
v_fma_f32 v200, v200, s63, v238
v_fma_f32 v201, v201, s63, v238
MV0 180,208,32
v_fma_f32 v202, v202, s63, v238
v_fma_f32 v203, v203, s63, v238
MV 180,212,36
v_fma_f32 v204, v204, s63, v239
MV0 184,208,64
v_fma_f32 v205, v205, s63, v239
v_fma_f32 v206, v206, s63, v239
MV 184,212,68
v_fma_f32 v207, v207, s63, v239
v_fma_f32 v232, v232, s63, v240
MV0 188,208,96
RB 208,249,30720
v_fma_f32 v233, v233, s63, v240
MV 188,212,100
v_fma_f32 v234, v234, s63, v240
v_fma_f32 v235, v235, s63, v240
MV 176,216,8
RB 212,249,31232
v_max3_f32 v246, v192, v193, v194
MV 176,220,12
v_max3_f32 v246, v246, v195, v196
v_max3_f32 v246, v246, v197, v198
MV 180,216,40
v_max3_f32 v246, v246, v199, v200
v_max3_f32 v246, v246, v201, v202
MV 180,220,44
v_max3_f32 v246, v246, v203, v204
MV 184,216,72
v_max3_f32 v246, v246, v205, v206
v_max3_f32 v246, v246, v207, v232
MV 184,220,76
v_max3_f32 v246, v246, v233, v234
v_max_f32 v246, v246, v235
MV 188,216,104
RT 216,250,2048
v_cmp_lt_f32 vcc, s64, v246
MV 188,220,108
s_waitcnt lgkmcnt(3)
MV 176,224,16
RT 218,250,10240
MV 176,228,20
RT 220,251,2048
MV 180,224,48
RT 222,251,10240
MV 180,228,52
s_cbranch_vccnz Lst0_2
Lrt0_2:
v_exp_f32 v192, v192
MV 184,224,80
v_exp_f32 v193, v193
MV 184,228,84
v_exp_f32 v194, v194
MV 188,224,112
RT 224,250,2560
v_add_f32 v241, v241, v192
MV 188,228,116
s_waitcnt lgkmcnt(5)
v_exp_f32 v195, v195
MV 176,208,24
RT 226,250,10752
v_add_f32 v241, v241, v193
MV 176,212,28
RT 228,251,2560
v_add_f32 v241, v241, v194
MV 180,208,56
RT 230,251,10752
v_add_f32 v241, v241, v195
MV 180,212,60
v_exp_f32 v196, v196
MV 184,208,88
v_exp_f32 v197, v197
MV 184,212,92
v_fma_f32 v176, v176, s63, v236
v_fma_f32 v177, v177, s63, v236
MV 188,208,120
s_waitcnt vmcnt(12) lgkmcnt(6)
v_fma_f32 v178, v178, s63, v236
v_fma_f32 v179, v179, s63, v236
MV 188,212,124
s_barrier
v_fma_f32 v182, v182, s63, v237
MA0 64,216,160
RT 208,250,4096
v_fma_f32 v180, v180, s63, v237
MA0 68,216,164
RT 210,250,12288
v_fma_f32 v181, v181, s63, v237
MA0 72,216,168
RT 212,251,4096
v_fma_f32 v183, v183, s63, v237
MA0 76,216,172
s_waitcnt lgkmcnt(7)
v_fma_f32 v184, v184, s63, v238
v_fma_f32 v185, v185, s63, v238
MA0 80,220,160
RT 214,251,12288
v_fma_f32 v186, v186, s63, v238
MA0 84,220,164
RT 216,250,0
v_fma_f32 v187, v187, s63, v238
MA0 88,220,168
RT 218,250,8192
v_fma_f32 v188, v188, s63, v239
MA0 92,220,172
s_waitcnt lgkmcnt(8)
v_fma_f32 v189, v189, s63, v239
v_fma_f32 v190, v190, s63, v239
MA0 96,224,160
ds_read_b32 v247, v252 offset:29704
v_fma_f32 v191, v191, s63, v239
MA0 100,224,164
v_max3_f32 v246, v176, v177, v178
v_max3_f32 v246, v246, v179, v182
MA0 104,224,168
RT 220,251,0
v_max3_f32 v246, v246, v180, v181
MA0 108,224,172
s_waitcnt lgkmcnt(8)
v_max3_f32 v246, v246, v183, v184
v_max3_f32 v246, v246, v185, v186
MA0 112,228,160
RT 222,251,8192
v_max3_f32 v246, v246, v187, v188
MA0 116,228,164
RB 224,252,16384
v_max3_f32 v246, v246, v189, v190
MA0 120,228,168
s_mov_b32 m0, 147456
v_exp_f32 v198, v198
MA0 124,228,172
s_waitcnt lgkmcnt(3)
v_exp_f32 v199, v199
MA0 128,208,160
RB 228,252,17408
v_max3_f32 v246, v246, v191, v247
MA0 132,208,164
buffer_load_dwordx4 v253, s[44:47], s61 offen offset:0 nt lds
MA0 136,208,168
v_cmp_lt_f32 vcc, s64, v246
v_add_f32 v242, v242, v196
MA0 140,208,172
v_add_f32 v242, v242, v197
v_add_f32 v242, v242, v198
MA0 144,212,160
RT 208,250,512
v_add_f32 v242, v242, v199
MA0 148,212,164
RT 210,250,8704
MA0 152,212,168
s_mov_b32 m0, 149376
v_exp_f32 v200, v200
MA0 156,212,172
v_exp_f32 v201, v201
MA0 0,216,160
RT 212,251,512
v_add_f32 v243, v243, v200
MA0 4,216,164
RT 214,251,8704
v_add_f32 v243, v243, v201
MA0 8,216,168
buffer_load_dwordx4 v253, s[44:47], s61 offen offset:128 nt lds
MA0 12,216,172
s_waitcnt lgkmcnt(4)
v_exp_f32 v202, v202
MV0 128,216,224
s_cbranch_vccnz Lst0_3
Lrt0_3:
v_exp_f32 v176, v176
MV0 132,216,228
v_exp_f32 v177, v177
MA0 16,220,160
RT 216,250,4608
v_add_f32 v241, v241, v176
MA0 20,220,164
RT 218,250,12800
v_add_f32 v241, v241, v177
MA0 24,220,168
s_mov_b32 m0, 151296
v_exp_f32 v178, v178
MA0 28,220,172
buffer_load_dwordx4 v253, s[44:47], s61 offen offset:256 nt lds
MV0 136,220,224
v_exp_f32 v179, v179
MV0 140,220,228
s_waitcnt lgkmcnt(4)
v_exp_f32 v180, v180
MA0 32,208,160
RT 220,251,4608
v_add_f32 v241, v241, v178
MA0 36,208,164
RT 222,251,12800
v_add_f32 v241, v241, v179
MA0 40,208,168
v_exp_f32 v181, v181
MA0 44,208,172
v_exp_f32 v182, v182
MV0 144,208,224
s_mov_b32 m0, 153216
v_exp_f32 v183, v183
MV0 148,208,228
s_waitcnt lgkmcnt(4)
CV 192,192,193
CV 193,194,195
MA0 48,212,160
RT 208,250,6144
CV 194,176,177
MA0 52,212,164
RT 210,250,14336
CV 195,178,179
MA0 56,212,168
buffer_load_dwordx4 v253, s[44:47], s61 offen offset:384 nt lds
s_add_u32 s61, s61, 16384
MA0 60,212,172
v_add_f32 v242, v242, v180
v_add_f32 v242, v242, v181
MV0 152,212,224
v_add_f32 v242, v242, v182
v_add_f32 v242, v242, v183
MV0 156,212,228
s_waitcnt lgkmcnt(4)
CV 196,196,197
MA0 160,216,160
RT 212,251,6144
CV 197,198,199
MA0 164,216,164
RT 214,251,14336
CV 198,180,181
MA0 168,216,168
CV 199,182,183
v_add_f32 v243, v243, v202
MA0 172,216,172
s_waitcnt lgkmcnt(4)
v_exp_f32 v232, v232
MA0 176,220,160
RT 216,250,6656
v_add_f32 v245, v245, v232
MA0 180,220,164
RT 218,250,14848
MA0 184,220,168
v_exp_f32 v233, v233
MA0 188,220,172
s_waitcnt lgkmcnt(4)
v_exp_f32 v234, v234
MA0 192,208,160
RT 220,251,6656
v_add_f32 v245, v245, v233
MA0 196,208,164
RB 176,249,32768
v_add_f32 v245, v245, v234
MA0 200,208,168
RB 180,249,33280
CV 254,232,233
MA0 204,208,172
s_waitcnt lgkmcnt(5)
v_exp_f32 v235, v235
MA0 208,212,160
RT 222,251,14848
v_add_f32 v245, v245, v235
MA0 212,212,164
RB 224,252,0
CV 255,234,235
MA0 216,212,168
RB 208,249,34816
MA0 220,212,172
s_waitcnt lgkmcnt(6)
v_exp_f32 v203, v203
MA0 224,216,160
RB 212,249,35328
v_add_f32 v243, v243, v203
MA0 228,216,164
RB 228,252,1024
CV 200,200,201
MA0 232,216,168
ds_write_b64 v252, v[254:255] offset:18432
CV 201,202,203
MA0 236,216,172
s_waitcnt lgkmcnt(5)
v_exp_f32 v204, v204
MA0 240,220,160
v_exp_f32 v205, v205
MA0 244,220,164
v_exp_f32 v206, v206
MA0 248,220,168
v_exp_f32 v207, v207
MA0 252,220,172
v_exp_f32 v184, v184
.p2align 6
Lloop0:
MV0 160,176,0
RB 216,249,36864
v_add_f32 v244, v244, v204
MV 160,180,4
RB 220,249,37376
v_add_f32 v244, v244, v205
MV0 164,176,32
s_cmp_lg_u32 s65, 0
v_exp_f32 v185, v185
MV 164,180,36
s_cbranch_scc1 Lst0_4
Lrt0_4:
v_exp_f32 v186, v186
MV0 168,176,64
s_lshl_b32 s78, s62, 20
v_exp_f32 v187, v187
MV 168,180,68
s_add_u32 s78, s78, Lpeel0-Lbase
v_exp_f32 v188, v188
MV0 172,176,96
s_mov_b32 m0, 16384
v_exp_f32 v189, v189
MV 172,180,100
s_waitcnt lgkmcnt(3)
v_exp_f32 v190, v190
MV0 232,176,224
RB 224,252,2048
v_add_f32 v244, v244, v206
MV 232,180,228
v_exp_f32 v191, v191
MV 160,208,8
RB 176,249,38912
v_add_f32 v244, v244, v207
MV 160,212,12
RB 180,249,39424
v_add_f32 v243, v243, v184
MV 164,208,40
RB 228,252,3072
v_add_f32 v243, v243, v185
MV 164,212,44
buffer_load_dwordx4 v253, s[40:43], s60 offen offset:0 nt lds
MV 168,208,72
s_mov_b32 m0, 18304
v_add_f32 v243, v243, v186
v_add_f32 v243, v243, v187
MV 168,212,76
buffer_load_dwordx4 v253, s[40:43], s60 offen offset:128 nt lds
MV 172,208,104
v_add_f32 v244, v244, v188
v_add_f32 v244, v244, v189
MV 172,212,108
s_waitcnt lgkmcnt(0)
v_add_f32 v244, v244, v190
v_add_f32 v244, v244, v191
MV 232,208,224
RB 224,252,4096
CV 202,184,185
MV 232,212,228
CV 204,204,205
CV 205,206,207
MV 160,216,16
RB 208,249,40960
CV 203,186,187
MV 160,220,20
RB 212,249,41472
CV 206,188,189
MV 164,216,48
RB 228,252,5120
CV 207,190,191
MV 164,220,52
s_mov_b32 m0, 20224
MV 168,216,80
buffer_load_dwordx4 v253, s[40:43], s60 offen offset:256 nt lds
MV 168,220,84
MV 172,216,112
MV 172,220,116
s_waitcnt lgkmcnt(0)
MV 232,216,224
RB 216,249,43008
v_subrev_u32 v248, 0x10000, v252
MV 232,220,228
MV 160,176,24
RB 220,249,43520
v_lshlrev_b32 v248, 3, v248
MV 160,180,28
RB 224,252,6144
MV 164,176,56
RB 228,252,7168
MV 164,180,60
s_mov_b32 m0, 22144
MV 168,176,88
buffer_load_dwordx4 v253, s[40:43], s60 offen offset:384 nt lds
s_add_u32 s60, s60, 16384
MV 168,180,92
s_mov_b32 m0, 97280
v_fma_f32 v160, v160, s63, v236
v_fma_f32 v161, v161, s63, v236
MV 172,176,120
buffer_load_dword v248, s[56:59], s78 offen lds
MV 172,180,124
s_waitcnt lgkmcnt(0)
v_fma_f32 v162, v162, s63, v236
v_fma_f32 v163, v163, s63, v236
MV 232,176,224
RB 224,249,45056
v_fma_f32 v164, v164, s63, v237
MV 232,180,228
v_fma_f32 v165, v165, s63, v237
v_fma_f32 v166, v166, s63, v237
MV0 176,208,0
RB 228,249,45568
v_fma_f32 v167, v167, s63, v237
MV 176,212,4
v_fma_f32 v168, v168, s63, v238
v_fma_f32 v169, v169, s63, v238
MV0 180,208,32
v_fma_f32 v170, v170, s63, v238
v_fma_f32 v171, v171, s63, v238
MV 180,212,36
v_fma_f32 v172, v172, s63, v239
MV0 184,208,64
v_fma_f32 v173, v173, s63, v239
v_fma_f32 v174, v174, s63, v239
MV 184,212,68
v_fma_f32 v175, v175, s63, v239
v_fma_f32 v232, v232, s63, v240
MV0 188,208,96
RB 208,249,47104
v_fma_f32 v233, v233, s63, v240
MV 188,212,100
v_fma_f32 v234, v234, s63, v240
v_fma_f32 v235, v235, s63, v240
MV 176,216,8
RB 212,249,47616
v_max3_f32 v246, v160, v161, v162
MV 176,220,12
v_max3_f32 v246, v246, v163, v164
v_max3_f32 v246, v246, v165, v166
MV 180,216,40
v_max3_f32 v246, v246, v167, v168
v_max3_f32 v246, v246, v169, v170
MV 180,220,44
v_max3_f32 v246, v246, v171, v172
MV 184,216,72
v_max3_f32 v246, v246, v173, v174
v_max3_f32 v246, v246, v175, v232
MV 184,220,76
v_max3_f32 v246, v246, v233, v234
v_max_f32 v246, v246, v235
MV 188,216,104
RT 216,250,18432
v_cmp_lt_f32 vcc, s64, v246
MV 188,220,108
s_waitcnt lgkmcnt(3)
MV 176,224,16
RT 218,250,26624
MV 176,228,20
RT 220,251,18432
MV 180,224,48
RT 222,251,26624
MV 180,228,52
s_cbranch_vccnz Lst0_5
Lrt0_5:
v_exp_f32 v160, v160
MV 184,224,80
v_exp_f32 v161, v161
MV 184,228,84
v_exp_f32 v162, v162
MV 188,224,112
RT 224,250,18944
v_add_f32 v241, v241, v160
MV 188,228,116
s_waitcnt lgkmcnt(5)
v_exp_f32 v163, v163
MV 176,208,24
RT 226,250,27136
v_add_f32 v241, v241, v161
MV 176,212,28
RT 228,251,18944
v_add_f32 v241, v241, v162
MV 180,208,56
RT 230,251,27136
v_add_f32 v241, v241, v163
MV 180,212,60
v_exp_f32 v164, v164
MV 184,208,88
v_exp_f32 v165, v165
MV 184,212,92
v_fma_f32 v176, v176, s63, v236
v_fma_f32 v177, v177, s63, v236
MV 188,208,120
s_waitcnt vmcnt(13) lgkmcnt(6)
v_fma_f32 v178, v178, s63, v236
v_fma_f32 v179, v179, s63, v236
MV 188,212,124
s_barrier
v_fma_f32 v182, v182, s63, v237
MA 64,216,192
RT 208,250,20480
v_fma_f32 v180, v180, s63, v237
MA 68,216,196
RT 210,250,28672
v_fma_f32 v181, v181, s63, v237
MA 72,216,200
RT 212,251,20480
v_fma_f32 v183, v183, s63, v237
MA 76,216,204
s_waitcnt lgkmcnt(7)
v_fma_f32 v184, v184, s63, v238
v_fma_f32 v185, v185, s63, v238
MA 80,220,192
RT 214,251,28672
v_fma_f32 v186, v186, s63, v238
MA 84,220,196
RT 216,250,16384
v_fma_f32 v187, v187, s63, v238
MA 88,220,200
RT 218,250,24576
v_fma_f32 v188, v188, s63, v239
MA 92,220,204
s_waitcnt lgkmcnt(8)
v_fma_f32 v189, v189, s63, v239
v_fma_f32 v190, v190, s63, v239
MA 96,224,192
ds_read_b32 v247, v252 offset:29704
v_fma_f32 v191, v191, s63, v239
MA 100,224,196
v_max3_f32 v246, v176, v177, v178
v_max3_f32 v246, v246, v179, v182
MA 104,224,200
RT 220,251,16384
v_max3_f32 v246, v246, v180, v181
MA 108,224,204
s_waitcnt lgkmcnt(8)
v_max3_f32 v246, v246, v183, v184
v_max3_f32 v246, v246, v185, v186
MA 112,228,192
RT 222,251,24576
v_max3_f32 v246, v246, v187, v188
MA 116,228,196
RB 224,252,18432
v_max3_f32 v246, v246, v189, v190
MA 120,228,200
s_mov_b32 m0, 98304
v_exp_f32 v166, v166
MA 124,228,204
s_waitcnt lgkmcnt(3)
v_exp_f32 v167, v167
MA 128,208,192
RB 228,252,19456
v_max3_f32 v246, v246, v191, v247
MA 132,208,196
buffer_load_dwordx4 v253, s[44:47], s61 offen offset:0 nt lds
MA 136,208,200
v_cmp_lt_f32 vcc, s64, v246
v_add_f32 v242, v242, v164
MA 140,208,204
v_add_f32 v242, v242, v165
v_add_f32 v242, v242, v166
MA 144,212,192
RT 208,250,16896
v_add_f32 v242, v242, v167
MA 148,212,196
RT 210,250,25088
MA 152,212,200
s_mov_b32 m0, 100224
v_exp_f32 v168, v168
MA 156,212,204
v_exp_f32 v169, v169
MA 0,216,192
RT 212,251,16896
v_add_f32 v243, v243, v168
MA 4,216,196
RT 214,251,25088
v_add_f32 v243, v243, v169
MA 8,216,200
buffer_load_dwordx4 v253, s[44:47], s61 offen offset:128 nt lds
MA 12,216,204
s_waitcnt lgkmcnt(4)
v_exp_f32 v170, v170
MV 128,216,224
s_cbranch_vccnz Lst0_6
Lrt0_6:
v_exp_f32 v176, v176
MV 132,216,228
v_exp_f32 v177, v177
MA 16,220,192
RT 216,250,20992
v_add_f32 v241, v241, v176
MA 20,220,196
RT 218,250,29184
v_add_f32 v241, v241, v177
MA 24,220,200
s_mov_b32 m0, 102144
v_exp_f32 v178, v178
MA 28,220,204
buffer_load_dwordx4 v253, s[44:47], s61 offen offset:256 nt lds
MV 136,220,224
v_exp_f32 v179, v179
MV 140,220,228
s_waitcnt lgkmcnt(4)
v_exp_f32 v180, v180
MA 32,208,192
RT 220,251,20992
v_add_f32 v241, v241, v178
MA 36,208,196
RT 222,251,29184
v_add_f32 v241, v241, v179
MA 40,208,200
v_exp_f32 v181, v181
MA 44,208,204
v_exp_f32 v182, v182
MV 144,208,224
s_mov_b32 m0, 104064
v_exp_f32 v183, v183
MV 148,208,228
s_waitcnt lgkmcnt(4)
CV 160,160,161
CV 161,162,163
MA 48,212,192
RT 208,250,22528
CV 162,176,177
MA 52,212,196
RT 210,250,30720
CV 163,178,179
MA 56,212,200
buffer_load_dwordx4 v253, s[44:47], s61 offen offset:384 nt lds
s_add_u32 s61, s61, 16384
MA 60,212,204
v_add_f32 v242, v242, v180
v_add_f32 v242, v242, v181
MV 152,212,224
v_add_f32 v242, v242, v182
v_add_f32 v242, v242, v183
MV 156,212,228
s_waitcnt lgkmcnt(4)
CV 164,164,165
MA 160,216,192
RT 212,251,22528
CV 165,166,167
MA 164,216,196
RT 214,251,30720
CV 166,180,181
MA 168,216,200
CV 167,182,183
v_add_f32 v243, v243, v170
MA 172,216,204
s_waitcnt lgkmcnt(4)
v_exp_f32 v232, v232
MA 176,220,192
RT 216,250,23040
v_add_f32 v245, v245, v232
MA 180,220,196
RT 218,250,31232
MA 184,220,200
v_exp_f32 v233, v233
MA 188,220,204
s_waitcnt lgkmcnt(4)
v_exp_f32 v234, v234
MA 192,208,192
RT 220,251,23040
v_add_f32 v245, v245, v233
MA 196,208,196
RB 176,249,49152
v_add_f32 v245, v245, v234
MA 200,208,200
RB 180,249,49664
CV 254,232,233
MA 204,208,204
s_waitcnt lgkmcnt(5)
v_exp_f32 v235, v235
MA 208,212,192
RT 222,251,31232
v_add_f32 v245, v245, v235
MA 212,212,196
RB 224,252,0
CV 255,234,235
MA 216,212,200
RB 208,249,51200
MA 220,212,204
s_waitcnt lgkmcnt(6)
v_exp_f32 v171, v171
MA 224,216,192
RB 212,249,51712
v_add_f32 v243, v243, v171
MA 228,216,196
RB 228,252,1024
CV 168,168,169
MA 232,216,200
ds_write_b64 v252, v[254:255] offset:16384
CV 169,170,171
MA 236,216,204
s_waitcnt lgkmcnt(5)
v_exp_f32 v172, v172
MA 240,220,192
v_exp_f32 v173, v173
MA 244,220,196
v_exp_f32 v174, v174
MA 248,220,200
v_exp_f32 v175, v175
MA 252,220,204
v_exp_f32 v184, v184
MV0 192,176,0
RB 216,249,53248
v_add_f32 v244, v244, v172
MV 192,180,4
RB 220,249,53760
v_add_f32 v244, v244, v173
MV0 196,176,32
s_cmp_lg_u32 s65, 0
v_exp_f32 v185, v185
MV 196,180,36
s_cbranch_scc1 Lst0_7
Lrt0_7:
v_exp_f32 v186, v186
MV0 200,176,64
s_lshl_b32 s78, s62, 20
v_exp_f32 v187, v187
MV 200,180,68
s_add_u32 s78, s78, Lpeel0-Lbase+8192
v_exp_f32 v188, v188
MV0 204,176,96
s_mov_b32 m0, 32768
v_exp_f32 v189, v189
MV 204,180,100
s_waitcnt lgkmcnt(3)
v_exp_f32 v190, v190
MV0 232,176,224
RB 224,252,2048
v_add_f32 v244, v244, v174
MV 232,180,228
v_exp_f32 v191, v191
MV 192,208,8
RB 176,249,55296
v_add_f32 v244, v244, v175
MV 192,212,12
RB 180,249,55808
v_add_f32 v243, v243, v184
MV 196,208,40
RB 228,252,3072
v_add_f32 v243, v243, v185
MV 196,212,44
buffer_load_dwordx4 v253, s[40:43], s60 offen offset:0 nt lds
MV 200,208,72
s_mov_b32 m0, 34688
v_add_f32 v243, v243, v186
v_add_f32 v243, v243, v187
MV 200,212,76
buffer_load_dwordx4 v253, s[40:43], s60 offen offset:128 nt lds
MV 204,208,104
v_add_f32 v244, v244, v188
v_add_f32 v244, v244, v189
MV 204,212,108
s_waitcnt lgkmcnt(0)
v_add_f32 v244, v244, v190
v_add_f32 v244, v244, v191
MV 232,208,224
RB 224,252,4096
CV 170,184,185
MV 232,212,228
CV 172,172,173
CV 173,174,175
MV 192,216,16
RB 208,249,57344
CV 171,186,187
MV 192,220,20
RB 212,249,57856
CV 174,188,189
MV 196,216,48
RB 228,252,5120
CV 175,190,191
MV 196,220,52
s_mov_b32 m0, 36608
MV 200,216,80
buffer_load_dwordx4 v253, s[40:43], s60 offen offset:256 nt lds
MV 200,220,84
MV 204,216,112
MV 204,220,116
s_waitcnt lgkmcnt(0)
MV 232,216,224
RB 216,249,59392
v_subrev_u32 v248, 0x10000, v252
MV 232,220,228
MV 192,176,24
RB 220,249,59904
v_lshlrev_b32 v248, 3, v248
MV 192,180,28
RB 224,252,6144
MV 196,176,56
RB 228,252,7168
MV 196,180,60
s_mov_b32 m0, 38528
MV 200,176,88
buffer_load_dwordx4 v253, s[40:43], s60 offen offset:384 nt lds
s_add_u32 s60, s60, 16384
MV 200,180,92
s_mov_b32 m0, 97280
v_fma_f32 v192, v192, s63, v236
v_fma_f32 v193, v193, s63, v236
MV 204,176,120
buffer_load_dword v248, s[56:59], s78 offen lds
MV 204,180,124
s_waitcnt lgkmcnt(0)
v_fma_f32 v194, v194, s63, v236
v_fma_f32 v195, v195, s63, v236
MV 232,176,224
RB 224,249,61440
v_fma_f32 v196, v196, s63, v237
MV 232,180,228
v_fma_f32 v197, v197, s63, v237
v_fma_f32 v198, v198, s63, v237
MV0 176,208,0
RB 228,249,61952
v_fma_f32 v199, v199, s63, v237
MV 176,212,4
v_fma_f32 v200, v200, s63, v238
v_fma_f32 v201, v201, s63, v238
MV0 180,208,32
v_fma_f32 v202, v202, s63, v238
v_fma_f32 v203, v203, s63, v238
MV 180,212,36
v_fma_f32 v204, v204, s63, v239
MV0 184,208,64
v_fma_f32 v205, v205, s63, v239
v_fma_f32 v206, v206, s63, v239
MV 184,212,68
v_fma_f32 v207, v207, s63, v239
v_fma_f32 v232, v232, s63, v240
MV0 188,208,96
RB 208,249,63488
v_fma_f32 v233, v233, s63, v240
MV 188,212,100
v_fma_f32 v234, v234, s63, v240
v_fma_f32 v235, v235, s63, v240
MV 176,216,8
RB 212,249,64000
v_max3_f32 v246, v192, v193, v194
MV 176,220,12
v_max3_f32 v246, v246, v195, v196
v_max3_f32 v246, v246, v197, v198
MV 180,216,40
v_max3_f32 v246, v246, v199, v200
v_max3_f32 v246, v246, v201, v202
MV 180,220,44
v_max3_f32 v246, v246, v203, v204
MV 184,216,72
v_max3_f32 v246, v246, v205, v206
v_max3_f32 v246, v246, v207, v232
MV 184,220,76
v_max3_f32 v246, v246, v233, v234
v_max_f32 v246, v246, v235
MV 188,216,104
RT 216,250,34816
v_cmp_lt_f32 vcc, s64, v246
MV 188,220,108
s_waitcnt lgkmcnt(3)
MV 176,224,16
RT 218,250,43008
MV 176,228,20
RT 220,251,34816
MV 180,224,48
RT 222,251,43008
MV 180,228,52
s_cbranch_vccnz Lst0_8
Lrt0_8:
v_exp_f32 v192, v192
MV 184,224,80
v_exp_f32 v193, v193
MV 184,228,84
v_exp_f32 v194, v194
MV 188,224,112
RT 224,250,35328
v_add_f32 v241, v241, v192
MV 188,228,116
s_waitcnt lgkmcnt(5)
v_exp_f32 v195, v195
MV 176,208,24
RT 226,250,43520
v_add_f32 v241, v241, v193
MV 176,212,28
RT 228,251,35328
v_add_f32 v241, v241, v194
MV 180,208,56
RT 230,251,43520
v_add_f32 v241, v241, v195
MV 180,212,60
v_exp_f32 v196, v196
MV 184,208,88
v_exp_f32 v197, v197
MV 184,212,92
v_fma_f32 v176, v176, s63, v236
v_fma_f32 v177, v177, s63, v236
MV 188,208,120
s_waitcnt vmcnt(14) lgkmcnt(6)
v_fma_f32 v178, v178, s63, v236
v_fma_f32 v179, v179, s63, v236
MV 188,212,124
s_barrier
v_fma_f32 v182, v182, s63, v237
MA 64,216,160
RT 208,250,36864
v_fma_f32 v180, v180, s63, v237
MA 68,216,164
RT 210,250,45056
v_fma_f32 v181, v181, s63, v237
MA 72,216,168
RT 212,251,36864
v_fma_f32 v183, v183, s63, v237
MA 76,216,172
s_waitcnt lgkmcnt(7)
v_fma_f32 v184, v184, s63, v238
v_fma_f32 v185, v185, s63, v238
MA 80,220,160
RT 214,251,45056
v_fma_f32 v186, v186, s63, v238
MA 84,220,164
RT 216,250,32768
v_fma_f32 v187, v187, s63, v238
MA 88,220,168
RT 218,250,40960
v_fma_f32 v188, v188, s63, v239
MA 92,220,172
s_waitcnt lgkmcnt(8)
v_fma_f32 v189, v189, s63, v239
v_fma_f32 v190, v190, s63, v239
MA 96,224,160
ds_read_b32 v247, v252 offset:29704
v_fma_f32 v191, v191, s63, v239
MA 100,224,164
v_max3_f32 v246, v176, v177, v178
v_max3_f32 v246, v246, v179, v182
MA 104,224,168
RT 220,251,32768
v_max3_f32 v246, v246, v180, v181
MA 108,224,172
s_waitcnt lgkmcnt(8)
v_max3_f32 v246, v246, v183, v184
v_max3_f32 v246, v246, v185, v186
MA 112,228,160
RT 222,251,40960
v_max3_f32 v246, v246, v187, v188
MA 116,228,164
RB 224,252,16384
v_max3_f32 v246, v246, v189, v190
MA 120,228,168
s_mov_b32 m0, 114688
v_exp_f32 v198, v198
MA 124,228,172
s_waitcnt lgkmcnt(3)
v_exp_f32 v199, v199
MA 128,208,160
RB 228,252,17408
v_max3_f32 v246, v246, v191, v247
MA 132,208,164
buffer_load_dwordx4 v253, s[44:47], s61 offen offset:0 nt lds
MA 136,208,168
v_cmp_lt_f32 vcc, s64, v246
v_add_f32 v242, v242, v196
MA 140,208,172
v_add_f32 v242, v242, v197
v_add_f32 v242, v242, v198
MA 144,212,160
RT 208,250,33280
v_add_f32 v242, v242, v199
MA 148,212,164
RT 210,250,41472
MA 152,212,168
s_mov_b32 m0, 116608
v_exp_f32 v200, v200
MA 156,212,172
v_exp_f32 v201, v201
MA 0,216,160
RT 212,251,33280
v_add_f32 v243, v243, v200
MA 4,216,164
RT 214,251,41472
v_add_f32 v243, v243, v201
MA 8,216,168
buffer_load_dwordx4 v253, s[44:47], s61 offen offset:128 nt lds
MA 12,216,172
s_waitcnt lgkmcnt(4)
v_exp_f32 v202, v202
MV 128,216,224
s_cbranch_vccnz Lst0_9
Lrt0_9:
v_exp_f32 v176, v176
MV 132,216,228
v_exp_f32 v177, v177
MA 16,220,160
RT 216,250,37376
v_add_f32 v241, v241, v176
MA 20,220,164
RT 218,250,45568
v_add_f32 v241, v241, v177
MA 24,220,168
s_mov_b32 m0, 118528
v_exp_f32 v178, v178
MA 28,220,172
buffer_load_dwordx4 v253, s[44:47], s61 offen offset:256 nt lds
MV 136,220,224
v_exp_f32 v179, v179
MV 140,220,228
s_waitcnt lgkmcnt(4)
v_exp_f32 v180, v180
MA 32,208,160
RT 220,251,37376
v_add_f32 v241, v241, v178
MA 36,208,164
RT 222,251,45568
v_add_f32 v241, v241, v179
MA 40,208,168
v_exp_f32 v181, v181
MA 44,208,172
v_exp_f32 v182, v182
MV 144,208,224
s_mov_b32 m0, 120448
v_exp_f32 v183, v183
MV 148,208,228
s_waitcnt lgkmcnt(4)
CV 192,192,193
CV 193,194,195
MA 48,212,160
RT 208,250,38912
CV 194,176,177
MA 52,212,164
RT 210,250,47104
CV 195,178,179
MA 56,212,168
buffer_load_dwordx4 v253, s[44:47], s61 offen offset:384 nt lds
s_add_u32 s61, s61, 16384
MA 60,212,172
v_add_f32 v242, v242, v180
v_add_f32 v242, v242, v181
MV 152,212,224
v_add_f32 v242, v242, v182
v_add_f32 v242, v242, v183
MV 156,212,228
s_waitcnt lgkmcnt(4)
CV 196,196,197
MA 160,216,160
RT 212,251,38912
CV 197,198,199
MA 164,216,164
RT 214,251,47104
CV 198,180,181
MA 168,216,168
CV 199,182,183
v_add_f32 v243, v243, v202
MA 172,216,172
s_waitcnt lgkmcnt(4)
v_exp_f32 v232, v232
MA 176,220,160
RT 216,250,39424
v_add_f32 v245, v245, v232
MA 180,220,164
RT 218,250,47616
MA 184,220,168
v_exp_f32 v233, v233
MA 188,220,172
s_waitcnt lgkmcnt(4)
v_exp_f32 v234, v234
MA 192,208,160
RT 220,251,39424
v_add_f32 v245, v245, v233
MA 196,208,164
RB 176,249,0
v_add_f32 v245, v245, v234
MA 200,208,168
RB 180,249,512
CV 254,232,233
MA 204,208,172
s_waitcnt lgkmcnt(5)
v_exp_f32 v235, v235
MA 208,212,160
RT 222,251,47616
v_add_f32 v245, v245, v235
MA 212,212,164
RB 224,252,0
CV 255,234,235
MA 216,212,168
RB 208,249,2048
MA 220,212,172
s_waitcnt lgkmcnt(6)
v_exp_f32 v203, v203
MA 224,216,160
RB 212,249,2560
v_add_f32 v243, v243, v203
MA 228,216,164
RB 228,252,1024
CV 200,200,201
MA 232,216,168
ds_write_b64 v252, v[254:255] offset:18432
CV 201,202,203
MA 236,216,172
s_waitcnt lgkmcnt(5)
v_exp_f32 v204, v204
MA 240,220,160
v_exp_f32 v205, v205
MA 244,220,164
v_exp_f32 v206, v206
MA 248,220,168
v_exp_f32 v207, v207
MA 252,220,172
v_exp_f32 v184, v184
MV0 160,176,0
RB 216,249,4096
v_add_f32 v244, v244, v204
MV 160,180,4
RB 220,249,4608
v_add_f32 v244, v244, v205
MV0 164,176,32
s_cmp_lg_u32 s65, 0
v_exp_f32 v185, v185
MV 164,180,36
s_cbranch_scc1 Lst0_10
Lrt0_10:
v_exp_f32 v186, v186
MV0 168,176,64
s_lshl_b32 s78, s62, 20
v_exp_f32 v187, v187
MV 168,180,68
s_add_u32 s78, s78, LT-Lbase+0
v_exp_f32 v188, v188
MV0 172,176,96
s_mov_b32 m0, 49152
v_exp_f32 v189, v189
MV 172,180,100
s_waitcnt lgkmcnt(3)
v_exp_f32 v190, v190
MV0 232,176,224
RB 224,252,2048
v_add_f32 v244, v244, v206
MV 232,180,228
v_exp_f32 v191, v191
MV 160,208,8
RB 176,249,6144
v_add_f32 v244, v244, v207
MV 160,212,12
RB 180,249,6656
v_add_f32 v243, v243, v184
MV 164,208,40
RB 228,252,3072
v_add_f32 v243, v243, v185
MV 164,212,44
buffer_load_dwordx4 v253, s[40:43], s60 offen offset:0 nt lds
MV 168,208,72
s_mov_b32 m0, 51072
v_add_f32 v243, v243, v186
v_add_f32 v243, v243, v187
MV 168,212,76
buffer_load_dwordx4 v253, s[40:43], s60 offen offset:128 nt lds
MV 172,208,104
v_add_f32 v244, v244, v188
v_add_f32 v244, v244, v189
MV 172,212,108
s_waitcnt lgkmcnt(0)
v_add_f32 v244, v244, v190
v_add_f32 v244, v244, v191
MV 232,208,224
RB 224,252,4096
CV 202,184,185
MV 232,212,228
CV 204,204,205
CV 205,206,207
MV 160,216,16
RB 208,249,8192
CV 203,186,187
MV 160,220,20
RB 212,249,8704
CV 206,188,189
MV 164,216,48
RB 228,252,5120
CV 207,190,191
MV 164,220,52
s_mov_b32 m0, 52992
MV 168,216,80
buffer_load_dwordx4 v253, s[40:43], s60 offen offset:256 nt lds
MV 168,220,84
MV 172,216,112
MV 172,220,116
s_waitcnt lgkmcnt(0)
MV 232,216,224
RB 216,249,10240
v_subrev_u32 v248, 0x10000, v252
MV 232,220,228
MV 160,176,24
RB 220,249,10752
v_lshlrev_b32 v248, 3, v248
MV 160,180,28
RB 224,252,6144
MV 164,176,56
RB 228,252,7168
MV 164,180,60
s_mov_b32 m0, 54912
MV 168,176,88
buffer_load_dwordx4 v253, s[40:43], s60 offen offset:384 nt lds
s_add_u32 s60, s60, 16384
MV 168,180,92
s_mov_b32 m0, 97280
v_fma_f32 v160, v160, s63, v236
v_fma_f32 v161, v161, s63, v236
MV 172,176,120
buffer_load_dword v248, s[56:59], s78 offen lds
MV 172,180,124
s_waitcnt lgkmcnt(0)
v_fma_f32 v162, v162, s63, v236
v_fma_f32 v163, v163, s63, v236
MV 232,176,224
RB 224,249,12288
v_fma_f32 v164, v164, s63, v237
MV 232,180,228
v_fma_f32 v165, v165, s63, v237
v_fma_f32 v166, v166, s63, v237
MV0 176,208,0
RB 228,249,12800
v_fma_f32 v167, v167, s63, v237
MV 176,212,4
v_fma_f32 v168, v168, s63, v238
v_fma_f32 v169, v169, s63, v238
MV0 180,208,32
v_fma_f32 v170, v170, s63, v238
v_fma_f32 v171, v171, s63, v238
MV 180,212,36
v_fma_f32 v172, v172, s63, v239
MV0 184,208,64
v_fma_f32 v173, v173, s63, v239
v_fma_f32 v174, v174, s63, v239
MV 184,212,68
v_fma_f32 v175, v175, s63, v239
v_fma_f32 v232, v232, s63, v240
MV0 188,208,96
RB 208,249,14336
v_fma_f32 v233, v233, s63, v240
MV 188,212,100
v_fma_f32 v234, v234, s63, v240
v_fma_f32 v235, v235, s63, v240
MV 176,216,8
RB 212,249,14848
v_max3_f32 v246, v160, v161, v162
MV 176,220,12
v_max3_f32 v246, v246, v163, v164
v_max3_f32 v246, v246, v165, v166
MV 180,216,40
v_max3_f32 v246, v246, v167, v168
v_max3_f32 v246, v246, v169, v170
MV 180,220,44
v_max3_f32 v246, v246, v171, v172
MV 184,216,72
v_max3_f32 v246, v246, v173, v174
v_max3_f32 v246, v246, v175, v232
MV 184,220,76
v_max3_f32 v246, v246, v233, v234
v_max_f32 v246, v246, v235
MV 188,216,104
RT 216,250,51200
v_cmp_lt_f32 vcc, s64, v246
MV 188,220,108
s_waitcnt lgkmcnt(3)
MV 176,224,16
RT 218,250,59392
MV 176,228,20
RT 220,251,51200
MV 180,224,48
RT 222,251,59392
MV 180,228,52
s_cbranch_vccnz Lst0_11
Lrt0_11:
v_exp_f32 v160, v160
MV 184,224,80
v_exp_f32 v161, v161
MV 184,228,84
v_exp_f32 v162, v162
MV 188,224,112
RT 224,250,51712
v_add_f32 v241, v241, v160
MV 188,228,116
s_waitcnt lgkmcnt(5)
v_exp_f32 v163, v163
MV 176,208,24
RT 226,250,59904
v_add_f32 v241, v241, v161
MV 176,212,28
RT 228,251,51712
v_add_f32 v241, v241, v162
MV 180,208,56
RT 230,251,59904
v_add_f32 v241, v241, v163
MV 180,212,60
v_exp_f32 v164, v164
MV 184,208,88
v_exp_f32 v165, v165
MV 184,212,92
v_fma_f32 v176, v176, s63, v236
v_fma_f32 v177, v177, s63, v236
MV 188,208,120
s_waitcnt vmcnt(14) lgkmcnt(6)
v_fma_f32 v178, v178, s63, v236
v_fma_f32 v179, v179, s63, v236
MV 188,212,124
s_barrier
v_fma_f32 v182, v182, s63, v237
MA 64,216,192
RT 208,250,53248
v_fma_f32 v180, v180, s63, v237
MA 68,216,196
RT 210,250,61440
v_fma_f32 v181, v181, s63, v237
MA 72,216,200
RT 212,251,53248
v_fma_f32 v183, v183, s63, v237
MA 76,216,204
s_waitcnt lgkmcnt(7)
v_fma_f32 v184, v184, s63, v238
v_fma_f32 v185, v185, s63, v238
MA 80,220,192
RT 214,251,61440
v_fma_f32 v186, v186, s63, v238
MA 84,220,196
RT 216,250,49152
v_fma_f32 v187, v187, s63, v238
MA 88,220,200
RT 218,250,57344
v_fma_f32 v188, v188, s63, v239
MA 92,220,204
s_waitcnt lgkmcnt(8)
v_fma_f32 v189, v189, s63, v239
v_fma_f32 v190, v190, s63, v239
MA 96,224,192
ds_read_b32 v247, v252 offset:29704
v_fma_f32 v191, v191, s63, v239
MA 100,224,196
v_max3_f32 v246, v176, v177, v178
v_max3_f32 v246, v246, v179, v182
MA 104,224,200
RT 220,251,49152
v_max3_f32 v246, v246, v180, v181
MA 108,224,204
s_waitcnt lgkmcnt(8)
v_max3_f32 v246, v246, v183, v184
v_max3_f32 v246, v246, v185, v186
MA 112,228,192
RT 222,251,57344
v_max3_f32 v246, v246, v187, v188
MA 116,228,196
RB 224,252,18432
v_max3_f32 v246, v246, v189, v190
MA 120,228,200
s_mov_b32 m0, 131072
v_exp_f32 v166, v166
MA 124,228,204
s_waitcnt lgkmcnt(3)
v_exp_f32 v167, v167
MA 128,208,192
RB 228,252,19456
v_max3_f32 v246, v246, v191, v247
MA 132,208,196
buffer_load_dwordx4 v253, s[44:47], s61 offen offset:0 nt lds
MA 136,208,200
v_cmp_lt_f32 vcc, s64, v246
v_add_f32 v242, v242, v164
MA 140,208,204
v_add_f32 v242, v242, v165
v_add_f32 v242, v242, v166
MA 144,212,192
RT 208,250,49664
v_add_f32 v242, v242, v167
MA 148,212,196
RT 210,250,57856
MA 152,212,200
s_mov_b32 m0, 132992
v_exp_f32 v168, v168
MA 156,212,204
v_exp_f32 v169, v169
MA 0,216,192
RT 212,251,49664
v_add_f32 v243, v243, v168
MA 4,216,196
RT 214,251,57856
v_add_f32 v243, v243, v169
MA 8,216,200
buffer_load_dwordx4 v253, s[44:47], s61 offen offset:128 nt lds
MA 12,216,204
s_waitcnt lgkmcnt(4)
v_exp_f32 v170, v170
MV 128,216,224
s_cbranch_vccnz Lst0_12
Lrt0_12:
v_exp_f32 v176, v176
MV 132,216,228
v_exp_f32 v177, v177
MA 16,220,192
RT 216,250,53760
v_add_f32 v241, v241, v176
MA 20,220,196
RT 218,250,61952
v_add_f32 v241, v241, v177
MA 24,220,200
s_mov_b32 m0, 134912
v_exp_f32 v178, v178
MA 28,220,204
buffer_load_dwordx4 v253, s[44:47], s61 offen offset:256 nt lds
MV 136,220,224
v_exp_f32 v179, v179
MV 140,220,228
s_waitcnt lgkmcnt(4)
v_exp_f32 v180, v180
MA 32,208,192
RT 220,251,53760
v_add_f32 v241, v241, v178
MA 36,208,196
RT 222,251,61952
v_add_f32 v241, v241, v179
MA 40,208,200
v_exp_f32 v181, v181
MA 44,208,204
v_exp_f32 v182, v182
MV 144,208,224
s_mov_b32 m0, 136832
v_exp_f32 v183, v183
MV 148,208,228
s_waitcnt lgkmcnt(4)
CV 160,160,161
CV 161,162,163
MA 48,212,192
RT 208,250,55296
CV 162,176,177
MA 52,212,196
RT 210,250,63488
CV 163,178,179
MA 56,212,200
buffer_load_dwordx4 v253, s[44:47], s61 offen offset:384 nt lds
s_add_u32 s61, s61, 16384
MA 60,212,204
v_add_f32 v242, v242, v180
v_add_f32 v242, v242, v181
MV 152,212,224
v_add_f32 v242, v242, v182
v_add_f32 v242, v242, v183
MV 156,212,228
s_waitcnt lgkmcnt(4)
CV 164,164,165
MA 160,216,192
RT 212,251,55296
CV 165,166,167
MA 164,216,196
RT 214,251,63488
CV 166,180,181
MA 168,216,200
CV 167,182,183
v_add_f32 v243, v243, v170
MA 172,216,204
s_waitcnt lgkmcnt(4)
v_exp_f32 v232, v232
MA 176,220,192
RT 216,250,55808
v_add_f32 v245, v245, v232
MA 180,220,196
RT 218,250,64000
MA 184,220,200
v_exp_f32 v233, v233
MA 188,220,204
s_waitcnt lgkmcnt(4)
v_exp_f32 v234, v234
MA 192,208,192
RT 220,251,55808
v_add_f32 v245, v245, v233
MA 196,208,196
RB 176,249,16384
v_add_f32 v245, v245, v234
MA 200,208,200
RB 180,249,16896
CV 254,232,233
MA 204,208,204
s_waitcnt lgkmcnt(5)
v_exp_f32 v235, v235
MA 208,212,192
RT 222,251,64000
v_add_f32 v245, v245, v235
MA 212,212,196
RB 224,252,0
CV 255,234,235
MA 216,212,200
RB 208,249,18432
MA 220,212,204
s_waitcnt lgkmcnt(6)
v_exp_f32 v171, v171
MA 224,216,192
RB 212,249,18944
v_add_f32 v243, v243, v171
MA 228,216,196
RB 228,252,1024
CV 168,168,169
MA 232,216,200
ds_write_b64 v252, v[254:255] offset:16384
CV 169,170,171
MA 236,216,204
s_waitcnt lgkmcnt(5)
v_exp_f32 v172, v172
MA 240,220,192
v_exp_f32 v173, v173
MA 244,220,196
v_exp_f32 v174, v174
MA 248,220,200
v_exp_f32 v175, v175
MA 252,220,204
v_exp_f32 v184, v184
MV0 192,176,0
RB 216,249,20480
v_add_f32 v244, v244, v172
MV 192,180,4
RB 220,249,20992
v_add_f32 v244, v244, v173
MV0 196,176,32
s_cmp_lg_u32 s65, 0
v_exp_f32 v185, v185
MV 196,180,36
s_cbranch_scc1 Lst0_13
Lrt0_13:
v_exp_f32 v186, v186
MV0 200,176,64
s_lshr_b32 s78, s76, 2
s_lshl_b32 s78, s78, 7
v_exp_f32 v187, v187
MV 200,180,68
s_and_b32 s67, s76, 3
s_add_u32 s78, s78, s67
v_exp_f32 v188, v188
MV0 204,176,96
s_mov_b32 m0, 0
v_exp_f32 v189, v189
MV 204,180,100
s_waitcnt lgkmcnt(3)
v_exp_f32 v190, v190
MV0 232,176,224
RB 224,252,2048
v_add_f32 v244, v244, v174
MV 232,180,228
v_exp_f32 v191, v191
MV 192,208,8
RB 176,249,22528
v_add_f32 v244, v244, v175
MV 192,212,12
RB 180,249,23040
v_add_f32 v243, v243, v184
MV 196,208,40
RB 228,252,3072
v_add_f32 v243, v243, v185
MV 196,212,44
buffer_load_dwordx4 v253, s[40:43], s60 offen offset:0 nt lds
MV 200,208,72
s_mov_b32 m0, 1920
v_add_f32 v243, v243, v186
v_add_f32 v243, v243, v187
MV 200,212,76
buffer_load_dwordx4 v253, s[40:43], s60 offen offset:128 nt lds
MV 204,208,104
s_lshl_b32 s78, s78, 7
s_add_u32 s76, s76, 1
v_add_f32 v244, v244, v188
v_add_f32 v244, v244, v189
MV 204,212,108
s_waitcnt lgkmcnt(0)
v_add_f32 v244, v244, v190
v_add_f32 v244, v244, v191
MV 232,208,224
RB 224,252,4096
CV 170,184,185
MV 232,212,228
CV 172,172,173
CV 173,174,175
MV 192,216,16
RB 208,249,24576
CV 171,186,187
MV 192,220,20
RB 212,249,25088
CV 174,188,189
MV 196,216,48
RB 228,252,5120
CV 175,190,191
MV 196,220,52
s_mov_b32 m0, 3840
MV 200,216,80
buffer_load_dwordx4 v253, s[40:43], s60 offen offset:256 nt lds
MV 200,220,84
MV 204,216,112
MV 204,220,116
s_waitcnt lgkmcnt(0)
MV 232,216,224
RB 216,249,26624
MV 232,220,228
MV 192,176,24
RB 220,249,27136
MV 192,180,28
RB 224,252,6144
MV 196,176,56
RB 228,252,7168
MV 196,180,60
s_mov_b32 m0, 5760
MV 200,176,88
buffer_load_dwordx4 v253, s[40:43], s60 offen offset:384 nt lds
s_add_u32 s60, s60, 16384
MV 200,180,92
s_mov_b32 m0, 97280
v_fma_f32 v192, v192, s63, v236
v_fma_f32 v193, v193, s63, v236
MV 204,176,120
buffer_load_dword v253, s[48:51], s78 offen lds
MV 204,180,124
s_waitcnt lgkmcnt(0)
v_fma_f32 v194, v194, s63, v236
v_fma_f32 v195, v195, s63, v236
MV 232,176,224
RB 224,249,28672
v_fma_f32 v196, v196, s63, v237
MV 232,180,228
v_fma_f32 v197, v197, s63, v237
v_fma_f32 v198, v198, s63, v237
MV0 176,208,0
RB 228,249,29184
v_fma_f32 v199, v199, s63, v237
MV 176,212,4
v_fma_f32 v200, v200, s63, v238
v_fma_f32 v201, v201, s63, v238
MV0 180,208,32
v_fma_f32 v202, v202, s63, v238
v_fma_f32 v203, v203, s63, v238
MV 180,212,36
v_fma_f32 v204, v204, s63, v239
MV0 184,208,64
v_fma_f32 v205, v205, s63, v239
v_fma_f32 v206, v206, s63, v239
MV 184,212,68
v_fma_f32 v207, v207, s63, v239
v_fma_f32 v232, v232, s63, v240
MV0 188,208,96
RB 208,249,30720
v_fma_f32 v233, v233, s63, v240
MV 188,212,100
v_fma_f32 v234, v234, s63, v240
v_fma_f32 v235, v235, s63, v240
MV 176,216,8
RB 212,249,31232
v_max3_f32 v246, v192, v193, v194
MV 176,220,12
v_max3_f32 v246, v246, v195, v196
v_max3_f32 v246, v246, v197, v198
MV 180,216,40
v_max3_f32 v246, v246, v199, v200
v_max3_f32 v246, v246, v201, v202
MV 180,220,44
v_max3_f32 v246, v246, v203, v204
MV 184,216,72
v_max3_f32 v246, v246, v205, v206
v_max3_f32 v246, v246, v207, v232
MV 184,220,76
v_max3_f32 v246, v246, v233, v234
v_max_f32 v246, v246, v235
MV 188,216,104
RT 216,250,2048
v_cmp_lt_f32 vcc, s64, v246
MV 188,220,108
s_waitcnt lgkmcnt(3)
MV 176,224,16
RT 218,250,10240
MV 176,228,20
RT 220,251,2048
MV 180,224,48
RT 222,251,10240
MV 180,228,52
s_cbranch_vccnz Lst0_14
Lrt0_14:
v_exp_f32 v192, v192
MV 184,224,80
v_exp_f32 v193, v193
MV 184,228,84
v_exp_f32 v194, v194
MV 188,224,112
RT 224,250,2560
v_add_f32 v241, v241, v192
MV 188,228,116
s_waitcnt lgkmcnt(5)
v_exp_f32 v195, v195
MV 176,208,24
RT 226,250,10752
v_add_f32 v241, v241, v193
MV 176,212,28
RT 228,251,2560
v_add_f32 v241, v241, v194
MV 180,208,56
RT 230,251,10752
v_add_f32 v241, v241, v195
MV 180,212,60
v_exp_f32 v196, v196
MV 184,208,88
v_exp_f32 v197, v197
MV 184,212,92
v_fma_f32 v176, v176, s63, v236
v_fma_f32 v177, v177, s63, v236
MV 188,208,120
s_waitcnt vmcnt(14) lgkmcnt(6)
v_fma_f32 v178, v178, s63, v236
v_fma_f32 v179, v179, s63, v236
MV 188,212,124
s_barrier
v_fma_f32 v182, v182, s63, v237
MA 64,216,160
RT 208,250,4096
v_fma_f32 v180, v180, s63, v237
MA 68,216,164
RT 210,250,12288
v_fma_f32 v181, v181, s63, v237
MA 72,216,168
RT 212,251,4096
v_fma_f32 v183, v183, s63, v237
MA 76,216,172
s_waitcnt lgkmcnt(7)
v_fma_f32 v184, v184, s63, v238
v_fma_f32 v185, v185, s63, v238
MA 80,220,160
RT 214,251,12288
v_fma_f32 v186, v186, s63, v238
MA 84,220,164
RT 216,250,0
v_fma_f32 v187, v187, s63, v238
MA 88,220,168
RT 218,250,8192
v_fma_f32 v188, v188, s63, v239
MA 92,220,172
s_waitcnt lgkmcnt(8)
v_fma_f32 v189, v189, s63, v239
v_fma_f32 v190, v190, s63, v239
MA 96,224,160
ds_read_b32 v247, v252 offset:29704
v_fma_f32 v191, v191, s63, v239
MA 100,224,164
v_max3_f32 v246, v176, v177, v178
v_max3_f32 v246, v246, v179, v182
MA 104,224,168
RT 220,251,0
v_max3_f32 v246, v246, v180, v181
MA 108,224,172
s_waitcnt lgkmcnt(8)
v_max3_f32 v246, v246, v183, v184
v_max3_f32 v246, v246, v185, v186
MA 112,228,160
RT 222,251,8192
v_max3_f32 v246, v246, v187, v188
MA 116,228,164
RB 224,252,16384
v_max3_f32 v246, v246, v189, v190
MA 120,228,168
s_mov_b32 m0, 147456
v_exp_f32 v198, v198
MA 124,228,172
s_waitcnt lgkmcnt(3)
v_exp_f32 v199, v199
MA 128,208,160
RB 228,252,17408
v_max3_f32 v246, v246, v191, v247
MA 132,208,164
buffer_load_dwordx4 v253, s[44:47], s61 offen offset:0 nt lds
MA 136,208,168
v_cmp_lt_f32 vcc, s64, v246
v_add_f32 v242, v242, v196
MA 140,208,172
v_add_f32 v242, v242, v197
v_add_f32 v242, v242, v198
MA 144,212,160
RT 208,250,512
v_add_f32 v242, v242, v199
MA 148,212,164
RT 210,250,8704
MA 152,212,168
s_mov_b32 m0, 149376
v_exp_f32 v200, v200
MA 156,212,172
v_exp_f32 v201, v201
MA 0,216,160
RT 212,251,512
v_add_f32 v243, v243, v200
MA 4,216,164
RT 214,251,8704
v_add_f32 v243, v243, v201
MA 8,216,168
buffer_load_dwordx4 v253, s[44:47], s61 offen offset:128 nt lds
MA 12,216,172
s_waitcnt lgkmcnt(4)
v_exp_f32 v202, v202
MV 128,216,224
s_cbranch_vccnz Lst0_15
Lrt0_15:
v_exp_f32 v176, v176
MV 132,216,228
v_exp_f32 v177, v177
MA 16,220,160
RT 216,250,4608
v_add_f32 v241, v241, v176
MA 20,220,164
RT 218,250,12800
v_add_f32 v241, v241, v177
MA 24,220,168
s_mov_b32 m0, 151296
v_exp_f32 v178, v178
MA 28,220,172
buffer_load_dwordx4 v253, s[44:47], s61 offen offset:256 nt lds
MV 136,220,224
v_exp_f32 v179, v179
MV 140,220,228
s_waitcnt lgkmcnt(4)
v_exp_f32 v180, v180
MA 32,208,160
RT 220,251,4608
v_add_f32 v241, v241, v178
MA 36,208,164
RT 222,251,12800
v_add_f32 v241, v241, v179
MA 40,208,168
v_exp_f32 v181, v181
MA 44,208,172
v_exp_f32 v182, v182
MV 144,208,224
s_mov_b32 m0, 153216
v_exp_f32 v183, v183
MV 148,208,228
s_waitcnt lgkmcnt(4)
CV 192,192,193
CV 193,194,195
MA 48,212,160
RT 208,250,6144
CV 194,176,177
MA 52,212,164
RT 210,250,14336
CV 195,178,179
MA 56,212,168
buffer_load_dwordx4 v253, s[44:47], s61 offen offset:384 nt lds
s_add_u32 s61, s61, 16384
MA 60,212,172
v_add_f32 v242, v242, v180
v_add_f32 v242, v242, v181
MV 152,212,224
v_add_f32 v242, v242, v182
v_add_f32 v242, v242, v183
MV 156,212,228
s_waitcnt lgkmcnt(4)
CV 196,196,197
MA 160,216,160
RT 212,251,6144
CV 197,198,199
MA 164,216,164
RT 214,251,14336
CV 198,180,181
MA 168,216,168
CV 199,182,183
v_add_f32 v243, v243, v202
MA 172,216,172
s_waitcnt lgkmcnt(4)
v_exp_f32 v232, v232
MA 176,220,160
RT 216,250,6656
v_add_f32 v245, v245, v232
MA 180,220,164
RT 218,250,14848
MA 184,220,168
v_exp_f32 v233, v233
MA 188,220,172
s_waitcnt lgkmcnt(4)
v_exp_f32 v234, v234
MA 192,208,160
RT 220,251,6656
v_add_f32 v245, v245, v233
MA 196,208,164
RB 176,249,32768
v_add_f32 v245, v245, v234
MA 200,208,168
RB 180,249,33280
CV 254,232,233
MA 204,208,172
s_waitcnt lgkmcnt(5)
v_exp_f32 v235, v235
MA 208,212,160
RT 222,251,14848
v_add_f32 v245, v245, v235
MA 212,212,164
RB 224,252,0
CV 255,234,235
MA 216,212,168
RB 208,249,34816
MA 220,212,172
s_waitcnt lgkmcnt(6)
v_exp_f32 v203, v203
MA 224,216,160
RB 212,249,35328
v_add_f32 v243, v243, v203
MA 228,216,164
RB 228,252,1024
CV 200,200,201
MA 232,216,168
ds_write_b64 v252, v[254:255] offset:18432
CV 201,202,203
MA 236,216,172
s_waitcnt lgkmcnt(5)
v_exp_f32 v204, v204
MA 240,220,160
v_exp_f32 v205, v205
MA 244,220,164
v_exp_f32 v206, v206
MA 248,220,168
v_exp_f32 v207, v207
MA 252,220,172
v_exp_f32 v184, v184
s_sub_u32 s62, s62, 1
s_cbranch_scc0 Lloop0
Lpeel0:
s_mov_b32 s48, s24
s_and_b32 s49, s25, 0xffff
s_mov_b32 s50, 3670016
s_and_b32 s77, s26, 15
s_mul_i32 s67, s77, 229376
s_xor_b32 s77, s77, 1
s_mul_i32 s69, s77, 229376
s_mov_b32 s68, 0
v_bfe_u32 v248, v252, 6, 3
v_add_u32 v248, 0, v248
v_mul_u32_u24 v248, 0x380, v248
v_and_b32 v253, 0x7f, v253
v_add_u32 v253, v253, v248
MV0 160,176,0
RB 216,249,36864
v_add_f32 v244, v244, v204
MV 160,180,4
RB 220,249,37376
v_add_f32 v244, v244, v205
MV0 164,176,32
s_cmp_lg_u32 s65, 0
v_exp_f32 v185, v185
MV 164,180,36
s_cbranch_scc1 Lst0_16
Lrt0_16:
v_exp_f32 v186, v186
MV0 168,176,64
v_exp_f32 v187, v187
MV 168,180,68
v_exp_f32 v188, v188
MV0 172,176,96
v_exp_f32 v189, v189
MV 172,180,100
s_waitcnt lgkmcnt(3)
v_exp_f32 v190, v190
MV0 232,176,224
RB 224,252,2048
v_add_f32 v244, v244, v206
MV 232,180,228
v_exp_f32 v191, v191
MV 160,208,8
RB 176,249,38912
v_add_f32 v244, v244, v207
MV 160,212,12
RB 180,249,39424
v_add_f32 v243, v243, v184
MV 164,208,40
RB 228,252,3072
v_add_f32 v243, v243, v185
MV 164,212,44
MV 168,208,72
v_add_f32 v243, v243, v186
v_add_f32 v243, v243, v187
MV 168,212,76
MV 172,208,104
v_add_f32 v244, v244, v188
v_add_f32 v244, v244, v189
MV 172,212,108
s_waitcnt lgkmcnt(0)
v_add_f32 v244, v244, v190
v_add_f32 v244, v244, v191
MV 232,208,224
RB 224,252,4096
CV 202,184,185
MV 232,212,228
CV 204,204,205
CV 205,206,207
MV 160,216,16
RB 208,249,40960
CV 203,186,187
MV 160,220,20
RB 212,249,41472
CV 206,188,189
MV 164,216,48
RB 228,252,5120
CV 207,190,191
MV 164,220,52
MV 168,216,80
MV 168,220,84
MV 172,216,112
MV 172,220,116
s_waitcnt lgkmcnt(0)
MV 232,216,224
RB 216,249,43008
MV 232,220,228
MV 160,176,24
RB 220,249,43520
MV 160,180,28
RB 224,252,6144
MV 164,176,56
RB 228,252,7168
MV 164,180,60
MV 168,176,88
MV 168,180,92
s_nop 1
v_fma_f32 v160, v160, s63, v236
v_fma_f32 v161, v161, s63, v236
MV 172,176,120
MV 172,180,124
s_waitcnt lgkmcnt(0)
v_fma_f32 v162, v162, s63, v236
v_fma_f32 v163, v163, s63, v236
MV 232,176,224
RB 224,249,45056
v_fma_f32 v164, v164, s63, v237
MV 232,180,228
v_fma_f32 v165, v165, s63, v237
v_fma_f32 v166, v166, s63, v237
MV0 176,208,0
RB 228,249,45568
v_fma_f32 v167, v167, s63, v237
MV 176,212,4
v_fma_f32 v168, v168, s63, v238
v_fma_f32 v169, v169, s63, v238
MV0 180,208,32
v_fma_f32 v170, v170, s63, v238
v_fma_f32 v171, v171, s63, v238
MV 180,212,36
v_fma_f32 v172, v172, s63, v239
MV0 184,208,64
v_fma_f32 v173, v173, s63, v239
v_fma_f32 v174, v174, s63, v239
MV 184,212,68
v_fma_f32 v175, v175, s63, v239
v_fma_f32 v232, v232, s63, v240
MV0 188,208,96
RB 208,249,47104
v_fma_f32 v233, v233, s63, v240
MV 188,212,100
v_fma_f32 v234, v234, s63, v240
v_fma_f32 v235, v235, s63, v240
MV 176,216,8
RB 212,249,47616
v_max3_f32 v246, v160, v161, v162
MV 176,220,12
v_max3_f32 v246, v246, v163, v164
v_max3_f32 v246, v246, v165, v166
MV 180,216,40
v_max3_f32 v246, v246, v167, v168
v_max3_f32 v246, v246, v169, v170
MV 180,220,44
v_max3_f32 v246, v246, v171, v172
MV 184,216,72
v_max3_f32 v246, v246, v173, v174
v_max3_f32 v246, v246, v175, v232
MV 184,220,76
v_max3_f32 v246, v246, v233, v234
v_max_f32 v246, v246, v235
MV 188,216,104
RT 216,250,18432
v_cmp_lt_f32 vcc, s64, v246
MV 188,220,108
s_waitcnt lgkmcnt(3)
MV 176,224,16
RT 218,250,26624
MV 176,228,20
RT 220,251,18432
MV 180,224,48
RT 222,251,26624
MV 180,228,52
s_cbranch_vccnz Lst0_17
Lrt0_17:
v_exp_f32 v160, v160
MV 184,224,80
v_exp_f32 v161, v161
MV 184,228,84
v_exp_f32 v162, v162
MV 188,224,112
RT 224,250,18944
v_add_f32 v241, v241, v160
MV 188,228,116
s_waitcnt lgkmcnt(5)
v_exp_f32 v163, v163
MV 176,208,24
RT 226,250,27136
v_add_f32 v241, v241, v161
MV 176,212,28
RT 228,251,18944
v_add_f32 v241, v241, v162
MV 180,208,56
RT 230,251,27136
v_add_f32 v241, v241, v163
MV 180,212,60
v_exp_f32 v164, v164
MV 184,208,88
v_exp_f32 v165, v165
MV 184,212,92
v_fma_f32 v176, v176, s63, v236
v_fma_f32 v177, v177, s63, v236
MV 188,208,120
s_waitcnt vmcnt(5) lgkmcnt(6)
v_fma_f32 v178, v178, s63, v236
v_fma_f32 v179, v179, s63, v236
MV 188,212,124
s_barrier
v_fma_f32 v182, v182, s63, v237
MA 64,216,192
RT 208,250,20480
v_fma_f32 v180, v180, s63, v237
MA 68,216,196
RT 210,250,28672
v_fma_f32 v181, v181, s63, v237
MA 72,216,200
RT 212,251,20480
v_fma_f32 v183, v183, s63, v237
MA 76,216,204
s_waitcnt lgkmcnt(7)
v_fma_f32 v184, v184, s63, v238
v_fma_f32 v185, v185, s63, v238
MA 80,220,192
RT 214,251,28672
v_fma_f32 v186, v186, s63, v238
MA 84,220,196
RT 216,250,16384
v_fma_f32 v187, v187, s63, v238
MA 88,220,200
RT 218,250,24576
v_fma_f32 v188, v188, s63, v239
MA 92,220,204
s_waitcnt lgkmcnt(8)
v_fma_f32 v189, v189, s63, v239
v_fma_f32 v190, v190, s63, v239
MA 96,224,192
ds_read_b32 v247, v252 offset:29704
v_fma_f32 v191, v191, s63, v239
MA 100,224,196
v_max3_f32 v246, v176, v177, v178
v_max3_f32 v246, v246, v179, v182
MA 104,224,200
RT 220,251,16384
v_max3_f32 v246, v246, v180, v181
MA 108,224,204
s_waitcnt lgkmcnt(8)
v_max3_f32 v246, v246, v183, v184
v_max3_f32 v246, v246, v185, v186
MA 112,228,192
RT 222,251,24576
v_max3_f32 v246, v246, v187, v188
MA 116,228,196
RB 224,252,18432
v_max3_f32 v246, v246, v189, v190
MA 120,228,200
s_mov_b32 m0, 0
v_exp_f32 v166, v166
MA 124,228,204
s_waitcnt lgkmcnt(3)
v_exp_f32 v167, v167
MA 128,208,192
RB 228,252,19456
v_max3_f32 v246, v246, v191, v247
MA 132,208,196
buffer_load_dwordx4 v253, s[48:51], s67 offen offset:0 lds
MA 136,208,200
v_cmp_lt_f32 vcc, s64, v246
v_add_f32 v242, v242, v164
MA 140,208,204
v_add_f32 v242, v242, v165
v_add_f32 v242, v242, v166
MA 144,212,192
RT 208,250,16896
v_add_f32 v242, v242, v167
MA 148,212,196
RT 210,250,25088
MA 152,212,200
s_mov_b32 m0, 3968
v_exp_f32 v168, v168
MA 156,212,204
v_exp_f32 v169, v169
MA 0,216,192
RT 212,251,16896
v_add_f32 v243, v243, v168
MA 4,216,196
RT 214,251,25088
v_add_f32 v243, v243, v169
MA 8,216,200
buffer_load_dwordx4 v253, s[48:51], s67 offen offset:128 lds
MA 12,216,204
s_waitcnt lgkmcnt(4)
v_exp_f32 v170, v170
MV 128,216,224
s_cbranch_vccnz Lst0_18
Lrt0_18:
v_exp_f32 v176, v176
MV 132,216,228
v_exp_f32 v177, v177
MA 16,220,192
RT 216,250,20992
v_add_f32 v241, v241, v176
MA 20,220,196
RT 218,250,29184
v_add_f32 v241, v241, v177
MA 24,220,200
s_mov_b32 m0, 7936
v_exp_f32 v178, v178
MA 28,220,204
buffer_load_dwordx4 v253, s[48:51], s67 offen offset:256 lds
MV 136,220,224
v_exp_f32 v179, v179
MV 140,220,228
s_waitcnt lgkmcnt(4)
v_exp_f32 v180, v180
MA 32,208,192
RT 220,251,20992
v_add_f32 v241, v241, v178
MA 36,208,196
RT 222,251,29184
v_add_f32 v241, v241, v179
MA 40,208,200
v_exp_f32 v181, v181
MA 44,208,204
v_exp_f32 v182, v182
MV 144,208,224
s_mov_b32 m0, 11904
v_exp_f32 v183, v183
MV 148,208,228
s_waitcnt lgkmcnt(4)
CV 160,160,161
CV 161,162,163
MA 48,212,192
RT 208,250,22528
CV 162,176,177
MA 52,212,196
RT 210,250,30720
CV 163,178,179
MA 56,212,200
buffer_load_dwordx4 v253, s[48:51], s67 offen offset:384 lds
MA 60,212,204
v_add_f32 v242, v242, v180
v_add_f32 v242, v242, v181
MV 152,212,224
v_add_f32 v242, v242, v182
v_add_f32 v242, v242, v183
MV 156,212,228
s_waitcnt lgkmcnt(4)
CV 164,164,165
MA 160,216,192
RT 212,251,22528
CV 165,166,167
MA 164,216,196
RT 214,251,30720
CV 166,180,181
MA 168,216,200
CV 167,182,183
v_add_f32 v243, v243, v170
MA 172,216,204
s_waitcnt lgkmcnt(4)
v_exp_f32 v232, v232
MA 176,220,192
RT 216,250,23040
v_add_f32 v245, v245, v232
MA 180,220,196
RT 218,250,31232
MA 184,220,200
v_exp_f32 v233, v233
MA 188,220,204
s_waitcnt lgkmcnt(4)
v_exp_f32 v234, v234
MA 192,208,192
RT 220,251,23040
v_add_f32 v245, v245, v233
MA 196,208,196
RB 176,249,49152
v_add_f32 v245, v245, v234
MA 200,208,200
RB 180,249,49664
CV 254,232,233
MA 204,208,204
s_waitcnt lgkmcnt(5)
v_exp_f32 v235, v235
MA 208,212,192
RT 222,251,31232
v_add_f32 v245, v245, v235
MA 212,212,196
RB 224,252,0
CV 255,234,235
MA 216,212,200
RB 208,249,51200
MA 220,212,204
s_waitcnt lgkmcnt(6)
v_exp_f32 v171, v171
MA 224,216,192
RB 212,249,51712
v_add_f32 v243, v243, v171
MA 228,216,196
RB 228,252,1024
CV 168,168,169
MA 232,216,200
ds_write_b64 v252, v[254:255] offset:16384
CV 169,170,171
MA 236,216,204
s_waitcnt lgkmcnt(5)
v_exp_f32 v172, v172
MA 240,220,192
v_exp_f32 v173, v173
MA 244,220,196
v_exp_f32 v174, v174
MA 248,220,200
v_exp_f32 v175, v175
MA 252,220,204
v_exp_f32 v184, v184
MV0 192,176,0
RB 216,249,53248
v_add_f32 v244, v244, v172
MV 192,180,4
RB 220,249,53760
v_add_f32 v244, v244, v173
MV0 196,176,32
s_cmp_lg_u32 s65, 0
v_exp_f32 v185, v185
MV 196,180,36
s_cbranch_scc1 Lst0_19
Lrt0_19:
v_exp_f32 v186, v186
MV0 200,176,64
v_exp_f32 v187, v187
MV 200,180,68
v_exp_f32 v188, v188
MV0 204,176,96
s_mov_b32 m0, 15872
v_exp_f32 v189, v189
MV 204,180,100
s_waitcnt lgkmcnt(3)
v_exp_f32 v190, v190
MV0 232,176,224
RB 224,252,2048
v_add_f32 v244, v244, v174
MV 232,180,228
v_exp_f32 v191, v191
MV 192,208,8
RB 176,249,55296
v_add_f32 v244, v244, v175
MV 192,212,12
RB 180,249,55808
v_add_f32 v243, v243, v184
MV 196,208,40
RB 228,252,3072
v_add_f32 v243, v243, v185
MV 196,212,44
buffer_load_dwordx4 v253, s[48:51], s67 offen offset:512 lds
MV 200,208,72
s_mov_b32 m0, 19840
v_add_f32 v243, v243, v186
v_add_f32 v243, v243, v187
MV 200,212,76
buffer_load_dwordx4 v253, s[48:51], s67 offen offset:640 lds
MV 204,208,104
v_add_f32 v244, v244, v188
v_add_f32 v244, v244, v189
MV 204,212,108
s_waitcnt lgkmcnt(0)
v_add_f32 v244, v244, v190
v_add_f32 v244, v244, v191
MV 232,208,224
RB 224,252,4096
CV 170,184,185
MV 232,212,228
CV 172,172,173
CV 173,174,175
MV 192,216,16
RB 208,249,57344
CV 171,186,187
MV 192,220,20
RB 212,249,57856
CV 174,188,189
MV 196,216,48
RB 228,252,5120
CV 175,190,191
MV 196,220,52
s_mov_b32 m0, 23808
MV 200,216,80
buffer_load_dwordx4 v253, s[48:51], s67 offen offset:768 lds
s_add_u32 s67, s67, 28672
MV 200,220,84
MV 204,216,112
MV 204,220,116
s_waitcnt lgkmcnt(0)
MV 232,216,224
RB 216,249,59392
MV 232,220,228
MV 192,176,24
RB 220,249,59904
MV 192,180,28
RB 224,252,6144
MV 196,176,56
RB 228,252,7168
MV 196,180,60
s_mov_b32 m0, 28672
MV 200,176,88
buffer_load_dwordx4 v253, s[48:51], s67 offen offset:0 lds
MV 200,180,92
v_fma_f32 v192, v192, s63, v236
v_fma_f32 v193, v193, s63, v236
MV 204,176,120
MV 204,180,124
s_waitcnt lgkmcnt(0)
v_fma_f32 v194, v194, s63, v236
v_fma_f32 v195, v195, s63, v236
MV 232,176,224
RB 224,249,61440
v_fma_f32 v196, v196, s63, v237
MV 232,180,228
v_fma_f32 v197, v197, s63, v237
v_fma_f32 v198, v198, s63, v237
MV0 176,208,0
RB 228,249,61952
v_fma_f32 v199, v199, s63, v237
MV 176,212,4
v_fma_f32 v200, v200, s63, v238
v_fma_f32 v201, v201, s63, v238
MV0 180,208,32
v_fma_f32 v202, v202, s63, v238
v_fma_f32 v203, v203, s63, v238
MV 180,212,36
v_fma_f32 v204, v204, s63, v239
MV0 184,208,64
v_fma_f32 v205, v205, s63, v239
v_fma_f32 v206, v206, s63, v239
MV 184,212,68
v_fma_f32 v207, v207, s63, v239
v_fma_f32 v232, v232, s63, v240
MV0 188,208,96
RB 208,249,63488
v_fma_f32 v233, v233, s63, v240
MV 188,212,100
v_fma_f32 v234, v234, s63, v240
v_fma_f32 v235, v235, s63, v240
MV 176,216,8
RB 212,249,64000
v_max3_f32 v246, v192, v193, v194
MV 176,220,12
v_max3_f32 v246, v246, v195, v196
v_max3_f32 v246, v246, v197, v198
MV 180,216,40
v_max3_f32 v246, v246, v199, v200
v_max3_f32 v246, v246, v201, v202
MV 180,220,44
v_max3_f32 v246, v246, v203, v204
MV 184,216,72
v_max3_f32 v246, v246, v205, v206
v_max3_f32 v246, v246, v207, v232
MV 184,220,76
v_max3_f32 v246, v246, v233, v234
v_max_f32 v246, v246, v235
MV 188,216,104
RT 216,250,34816
v_cmp_lt_f32 vcc, s64, v246
MV 188,220,108
s_waitcnt lgkmcnt(3)
MV 176,224,16
RT 218,250,43008
MV 176,228,20
RT 220,251,34816
MV 180,224,48
RT 222,251,43008
MV 180,228,52
s_cbranch_vccnz Lst0_20
Lrt0_20:
v_exp_f32 v192, v192
MV 184,224,80
v_exp_f32 v193, v193
MV 184,228,84
v_exp_f32 v194, v194
MV 188,224,112
RT 224,250,35328
v_add_f32 v241, v241, v192
MV 188,228,116
s_waitcnt lgkmcnt(5)
v_exp_f32 v195, v195
MV 176,208,24
RT 226,250,43520
v_add_f32 v241, v241, v193
MV 176,212,28
RT 228,251,35328
v_add_f32 v241, v241, v194
MV 180,208,56
RT 230,251,43520
v_add_f32 v241, v241, v195
MV 180,212,60
v_exp_f32 v196, v196
MV 184,208,88
v_exp_f32 v197, v197
MV 184,212,92
v_fma_f32 v176, v176, s63, v236
v_fma_f32 v177, v177, s63, v236
MV 188,208,120
s_waitcnt vmcnt(8) lgkmcnt(6)
v_fma_f32 v178, v178, s63, v236
v_fma_f32 v179, v179, s63, v236
MV 188,212,124
s_barrier
v_fma_f32 v182, v182, s63, v237
MA 64,216,160
RT 208,250,36864
v_fma_f32 v180, v180, s63, v237
MA 68,216,164
RT 210,250,45056
v_fma_f32 v181, v181, s63, v237
MA 72,216,168
RT 212,251,36864
v_fma_f32 v183, v183, s63, v237
MA 76,216,172
s_waitcnt lgkmcnt(7)
v_fma_f32 v184, v184, s63, v238
v_fma_f32 v185, v185, s63, v238
MA 80,220,160
RT 214,251,45056
v_fma_f32 v186, v186, s63, v238
MA 84,220,164
RT 216,250,32768
v_fma_f32 v187, v187, s63, v238
MA 88,220,168
RT 218,250,40960
v_fma_f32 v188, v188, s63, v239
MA 92,220,172
s_waitcnt lgkmcnt(8)
v_fma_f32 v189, v189, s63, v239
v_fma_f32 v190, v190, s63, v239
MA 96,224,160
ds_read_b32 v247, v252 offset:29704
v_fma_f32 v191, v191, s63, v239
MA 100,224,164
v_max3_f32 v246, v176, v177, v178
v_max3_f32 v246, v246, v179, v182
MA 104,224,168
RT 220,251,32768
v_max3_f32 v246, v246, v180, v181
MA 108,224,172
s_waitcnt lgkmcnt(8)
v_max3_f32 v246, v246, v183, v184
v_max3_f32 v246, v246, v185, v186
MA 112,228,160
RT 222,251,40960
v_max3_f32 v246, v246, v187, v188
MA 116,228,164
RB 224,252,16384
v_max3_f32 v246, v246, v189, v190
MA 120,228,168
s_mov_b32 m0, 32640
v_exp_f32 v198, v198
MA 124,228,172
s_waitcnt lgkmcnt(3)
v_exp_f32 v199, v199
MA 128,208,160
RB 228,252,17408
v_max3_f32 v246, v246, v191, v247
MA 132,208,164
buffer_load_dwordx4 v253, s[48:51], s67 offen offset:128 lds
MA 136,208,168
v_cmp_lt_f32 vcc, s64, v246
v_add_f32 v242, v242, v196
MA 140,208,172
v_add_f32 v242, v242, v197
v_add_f32 v242, v242, v198
MA 144,212,160
RT 208,250,33280
v_add_f32 v242, v242, v199
MA 148,212,164
RT 210,250,41472
MA 152,212,168
s_mov_b32 m0, 36608
v_exp_f32 v200, v200
MA 156,212,172
v_exp_f32 v201, v201
MA 0,216,160
RT 212,251,33280
v_add_f32 v243, v243, v200
MA 4,216,164
RT 214,251,41472
v_add_f32 v243, v243, v201
MA 8,216,168
buffer_load_dwordx4 v253, s[48:51], s67 offen offset:256 lds
MA 12,216,172
s_waitcnt lgkmcnt(4)
v_exp_f32 v202, v202
MV 128,216,224
s_cbranch_vccnz Lst0_21
Lrt0_21:
v_exp_f32 v176, v176
MV 132,216,228
v_exp_f32 v177, v177
MA 16,220,160
RT 216,250,37376
v_add_f32 v241, v241, v176
MA 20,220,164
RT 218,250,45568
v_add_f32 v241, v241, v177
MA 24,220,168
s_mov_b32 m0, 40576
v_exp_f32 v178, v178
MA 28,220,172
buffer_load_dwordx4 v253, s[48:51], s67 offen offset:384 lds
MV 136,220,224
v_exp_f32 v179, v179
MV 140,220,228
s_waitcnt lgkmcnt(4)
v_exp_f32 v180, v180
MA 32,208,160
RT 220,251,37376
v_add_f32 v241, v241, v178
MA 36,208,164
RT 222,251,45568
v_add_f32 v241, v241, v179
MA 40,208,168
v_exp_f32 v181, v181
MA 44,208,172
v_exp_f32 v182, v182
MV 144,208,224
s_mov_b32 m0, 44544
v_exp_f32 v183, v183
MV 148,208,228
s_waitcnt lgkmcnt(4)
CV 192,192,193
CV 193,194,195
MA 48,212,160
RT 208,250,38912
CV 194,176,177
MA 52,212,164
RT 210,250,47104
CV 195,178,179
MA 56,212,168
buffer_load_dwordx4 v253, s[48:51], s67 offen offset:512 lds
MA 60,212,172
v_add_f32 v242, v242, v180
v_add_f32 v242, v242, v181
MV 152,212,224
v_add_f32 v242, v242, v182
v_add_f32 v242, v242, v183
MV 156,212,228
s_waitcnt lgkmcnt(4)
CV 196,196,197
MA 160,216,160
RT 212,251,38912
CV 197,198,199
MA 164,216,164
RT 214,251,47104
CV 198,180,181
MA 168,216,168
CV 199,182,183
v_add_f32 v243, v243, v202
MA 172,216,172
s_waitcnt lgkmcnt(4)
v_exp_f32 v232, v232
MA 176,220,160
RT 216,250,39424
v_add_f32 v245, v245, v232
MA 180,220,164
RT 218,250,47616
MA 184,220,168
v_exp_f32 v233, v233
MA 188,220,172
s_waitcnt lgkmcnt(4)
v_exp_f32 v234, v234
MA 192,208,160
RT 220,251,39424
v_add_f32 v245, v245, v233
MA 196,208,164
RB 176,249,0
v_add_f32 v245, v245, v234
MA 200,208,168
RB 180,249,512
CV 254,232,233
MA 204,208,172
s_waitcnt lgkmcnt(5)
v_exp_f32 v235, v235
MA 208,212,160
RT 222,251,47616
v_add_f32 v245, v245, v235
MA 212,212,164
RB 224,252,0
CV 255,234,235
MA 216,212,168
RB 208,249,2048
MA 220,212,172
s_waitcnt lgkmcnt(6)
v_exp_f32 v203, v203
MA 224,216,160
RB 212,249,2560
v_add_f32 v243, v243, v203
MA 228,216,164
RB 228,252,1024
CV 200,200,201
MA 232,216,168
ds_write_b64 v252, v[254:255] offset:18432
CV 201,202,203
MA 236,216,172
s_waitcnt lgkmcnt(5)
v_exp_f32 v204, v204
MA 240,220,160
v_exp_f32 v205, v205
MA 244,220,164
v_exp_f32 v206, v206
MA 248,220,168
v_exp_f32 v207, v207
MA 252,220,172
s_waitcnt lgkmcnt(1)
v_exp_f32 v184, v184
s_mov_b32 m0, 48512
s_nop 0
buffer_load_dwordx4 v253, s[48:51], s67 offen offset:640 lds
s_mov_b32 m0, 52480
s_nop 0
buffer_load_dwordx4 v253, s[48:51], s67 offen offset:768 lds
)ASM"
R"ASM(
s_add_u32 s67, s67, 28672
v_add_f32 v244, v244, v204
v_add_f32 v244, v244, v205
s_cmp_lg_u32 s65, 0
v_exp_f32 v185, v185
s_cbranch_scc1 Lst0_22
Lrt0_22:
v_exp_f32 v186, v186
v_exp_f32 v187, v187
v_exp_f32 v188, v188
v_exp_f32 v189, v189
v_exp_f32 v190, v190
v_add_f32 v244, v244, v206
v_exp_f32 v191, v191
v_add_f32 v244, v244, v207
v_add_f32 v243, v243, v184
v_add_f32 v243, v243, v185
v_add_f32 v243, v243, v186
v_add_f32 v243, v243, v187
v_add_f32 v244, v244, v188
v_add_f32 v244, v244, v189
v_add_f32 v244, v244, v190
v_add_f32 v244, v244, v191
CV 202,184,185
CV 204,204,205
CV 205,206,207
CV 203,186,187
CV 206,188,189
CV 207,190,191
RT 216,250,51200
RT 218,250,59392
RT 220,251,51200
RT 222,251,59392
RT 224,250,51712
RT 226,250,59904
RT 228,251,51712
RT 230,251,59904
v_mov_b32 v176, v241
v_mov_b32 v177, v242
v_mov_b32 v178, v243
v_mov_b32 v179, v244
v_mov_b32 v180, v245
v_permlane16_swap_b32 v241, v176
v_permlane16_swap_b32 v242, v177
v_permlane16_swap_b32 v243, v178
v_permlane16_swap_b32 v244, v179
v_permlane16_swap_b32 v245, v180
v_add_f32 v241, v241, v176
v_add_f32 v242, v242, v177
v_add_f32 v243, v243, v178
v_add_f32 v244, v244, v179
v_add_f32 v245, v245, v180
v_mov_b32 v176, v241
v_mov_b32 v177, v242
v_mov_b32 v178, v243
v_mov_b32 v179, v244
v_mov_b32 v180, v245
v_permlane32_swap_b32 v241, v176
v_permlane32_swap_b32 v242, v177
v_permlane32_swap_b32 v243, v178
v_permlane32_swap_b32 v244, v179
v_permlane32_swap_b32 v245, v180
v_add_f32 v241, v241, v176
v_add_f32 v242, v242, v177
v_add_f32 v243, v243, v178
v_add_f32 v244, v244, v179
v_add_f32 v245, v245, v180
ds_write_b32 v252, v245 offset:30720
v_rcp_f32 v241, v241
v_rcp_f32 v242, v242
v_rcp_f32 v243, v243
v_rcp_f32 v244, v244
s_waitcnt lgkmcnt(0)
s_barrier
RB 236,252,30720
MA 64,216,192
RT 208,250,53248
MA 68,216,196
RT 210,250,61440
MA 72,216,200
RT 212,251,53248
MA 76,216,204
MA 80,220,192
RT 214,251,61440
MA 84,220,196
RT 216,250,49152
MA 88,220,200
RT 218,250,57344
MA 92,220,204
MA 96,224,192
MA 100,224,196
MA 104,224,200
RT 220,251,49152
MA 108,224,204
MA 112,228,192
RT 222,251,57344
MA 116,228,196
RB 224,252,18432
MA 120,228,200
s_waitcnt lgkmcnt(9)
v_add_f32 v236, v236, v237
MA 124,228,204
s_waitcnt lgkmcnt(7)
v_add_f32 v237, v238, v239
MA 128,208,192
RB 228,252,19456
MA 132,208,196
v_rcp_f32 v236, v236
MA 136,208,200
v_rcp_f32 v237, v237
MA 140,208,204
s_waitcnt lgkmcnt(6)
MA 144,212,192
RT 208,250,49664
MA 148,212,196
RT 210,250,57856
MA 152,212,200
MA 156,212,204
s_waitcnt lgkmcnt(6)
MA 0,216,192
RT 212,251,49664
MA 4,216,196
RT 214,251,57856
MA 8,216,200
MA 12,216,204
s_waitcnt lgkmcnt(5)
MV 128,216,224
s_waitcnt lgkmcnt(4)
MV 132,216,228
MA 16,220,192
RT 216,250,53760
MA 20,220,196
RT 218,250,61952
MA 24,220,200
MA 28,220,204
MV 136,220,224
MV 140,220,228
s_waitcnt lgkmcnt(4)
MA 32,208,192
RT 220,251,53760
MA 36,208,196
RT 222,251,61952
MA 40,208,200
MA 44,208,204
MV 144,208,224
MV 148,208,228
s_waitcnt lgkmcnt(4)
MA 48,212,192
RT 208,250,55296
MA 52,212,196
RT 210,250,63488
MA 56,212,200
MA 60,212,204
MV 152,212,224
MV 156,212,228
s_waitcnt lgkmcnt(4)
MA 160,216,192
RT 212,251,55296
MA 164,216,196
RT 214,251,63488
MA 168,216,200
MA 172,216,204
s_waitcnt lgkmcnt(4)
MA 176,220,192
RT 216,250,55808
MA 180,220,196
RT 218,250,64000
MA 184,220,200
MA 188,220,204
s_waitcnt lgkmcnt(4)
MA 192,208,192
RT 220,251,55808
MA 196,208,196
MA 200,208,200
MA 204,208,204
s_waitcnt lgkmcnt(3)
MA 208,212,192
RT 222,251,64000
MA 212,212,196
MA 216,212,200
MA 220,212,204
s_waitcnt lgkmcnt(2)
MA 224,216,192
MA 228,216,196
MA 232,216,200
MA 236,216,204
s_waitcnt lgkmcnt(0)
MA 240,220,192
MA 244,220,196
MA 248,220,200
MA 252,220,204
s_waitcnt vmcnt(0)
s_waitcnt lgkmcnt(0)
s_barrier
s_getpc_b64 s[80:81]
Ljt0:
s_add_u32 s80, s80, LT-Ljt0
s_addc_u32 s81, s81, 0
s_setpc_b64 s[80:81]
Lst0_0:
s_call_b64 s[80:81], LR2f_0
s_branch Lrt0_0
Lst0_1:
s_call_b64 s[80:81], LH_0
s_branch Lrt0_1
Lst0_2:
s_call_b64 s[80:81], LR1_0_1
s_branch Lrt0_2
Lst0_3:
s_call_b64 s[80:81], LR2_0_1
s_branch Lrt0_3
Lst0_4:
s_call_b64 s[80:81], LH_0
s_branch Lrt0_4
Lst0_5:
s_call_b64 s[80:81], LR1_0_0
s_branch Lrt0_5
Lst0_6:
s_call_b64 s[80:81], LR2_0_0
s_branch Lrt0_6
Lst0_7:
s_call_b64 s[80:81], LH_0
s_branch Lrt0_7
Lst0_8:
s_call_b64 s[80:81], LR1_0_1
s_branch Lrt0_8
Lst0_9:
s_call_b64 s[80:81], LR2_0_1
s_branch Lrt0_9
Lst0_10:
s_call_b64 s[80:81], LH_0
s_branch Lrt0_10
Lst0_11:
s_call_b64 s[80:81], LR1_0_0
s_branch Lrt0_11
Lst0_12:
s_call_b64 s[80:81], LR2_0_0
s_branch Lrt0_12
Lst0_13:
s_call_b64 s[80:81], LH_0
s_branch Lrt0_13
Lst0_14:
s_call_b64 s[80:81], LR1_0_1
s_branch Lrt0_14
Lst0_15:
s_call_b64 s[80:81], LR2_0_1
s_branch Lrt0_15
Lst0_16:
s_call_b64 s[80:81], LH_0
s_branch Lrt0_16
Lst0_17:
s_call_b64 s[80:81], LR1_0_0
s_branch Lrt0_17
Lst0_18:
s_call_b64 s[80:81], LR2_0_0
s_branch Lrt0_18
Lst0_19:
s_call_b64 s[80:81], LH_0
s_branch Lrt0_19
Lst0_20:
s_call_b64 s[80:81], LR1_0_1
s_branch Lrt0_20
Lst0_21:
s_call_b64 s[80:81], LR2_0_1
s_branch Lrt0_21
Lst0_22:
s_call_b64 s[80:81], LH_0
s_branch Lrt0_22
LR1_0_0:
s_nop 8
s_waitcnt lgkmcnt(0)
s_bitcmp1_b32 s65, 0
s_cbranch_scc1 Lsv0_10
ds_write_b128 v252, v[236:239] offset:20480
s_or_b32 s65, s65, 1
s_waitcnt lgkmcnt(0)
Lsv0_10:
v_max3_f32 v246, v160, v161, v162
v_max_f32 v246, v246, v163
v_mov_b32 v248, v246
s_nop 1
v_permlane16_swap_b32 v246, v248
v_max_f32 v246, v246, v248
v_mov_b32 v248, v246
s_nop 1
v_permlane32_swap_b32 v246, v248
v_max_f32 v246, v246, v248
v_max_f32 v246, 0, v246
v_sub_f32 v236, v236, v246
v_sub_f32 v160, v160, v246
v_sub_f32 v161, v161, v246
v_sub_f32 v162, v162, v246
v_sub_f32 v163, v163, v246
v_exp_f32 v248, -v246
s_nop 0
v_mul_f32 v241, v241, v248
v_max3_f32 v246, v164, v165, v166
v_max_f32 v246, v246, v167
v_mov_b32 v248, v246
s_nop 1
v_permlane16_swap_b32 v246, v248
v_max_f32 v246, v246, v248
v_mov_b32 v248, v246
s_nop 1
v_permlane32_swap_b32 v246, v248
v_max_f32 v246, v246, v248
v_max_f32 v246, 0, v246
v_sub_f32 v237, v237, v246
v_sub_f32 v164, v164, v246
v_sub_f32 v165, v165, v246
v_sub_f32 v166, v166, v246
v_sub_f32 v167, v167, v246
v_exp_f32 v248, -v246
s_nop 0
v_mul_f32 v242, v242, v248
v_max3_f32 v246, v168, v169, v170
v_max_f32 v246, v246, v171
v_mov_b32 v248, v246
s_nop 1
v_permlane16_swap_b32 v246, v248
v_max_f32 v246, v246, v248
v_mov_b32 v248, v246
s_nop 1
v_permlane32_swap_b32 v246, v248
v_max_f32 v246, v246, v248
v_max_f32 v246, 0, v246
v_sub_f32 v238, v238, v246
v_sub_f32 v168, v168, v246
v_sub_f32 v169, v169, v246
v_sub_f32 v170, v170, v246
v_sub_f32 v171, v171, v246
v_exp_f32 v248, -v246
s_nop 0
v_mul_f32 v243, v243, v248
v_max3_f32 v246, v172, v173, v174
v_max_f32 v246, v246, v175
v_mov_b32 v248, v246
s_nop 1
v_permlane16_swap_b32 v246, v248
v_max_f32 v246, v246, v248
v_mov_b32 v248, v246
s_nop 1
v_permlane32_swap_b32 v246, v248
v_max_f32 v246, v246, v248
v_max_f32 v246, 0, v246
v_sub_f32 v239, v239, v246
v_sub_f32 v172, v172, v246
v_sub_f32 v173, v173, v246
v_sub_f32 v174, v174, v246
v_sub_f32 v175, v175, v246
v_exp_f32 v248, -v246
s_nop 0
v_mul_f32 v244, v244, v248
v_max3_f32 v246, v232, v233, v234
v_max_f32 v246, v246, v235
v_mov_b32 v248, v246
s_nop 1
v_permlane16_swap_b32 v246, v248
v_max_f32 v246, v246, v248
v_mov_b32 v248, v246
s_nop 1
v_permlane32_swap_b32 v246, v248
v_max_f32 v246, v246, v248
v_max_f32 v246, 0, v246
v_sub_f32 v246, v240, v246
ds_write_b32 v252, v246 offset:28672
v_mov_b32 v248, 0x7149f2ca
ds_write_b32 v252, v248 offset:29704
s_waitcnt lgkmcnt(0)
s_nop 2
s_setpc_b64 s[80:81]
LR2_0_0:
s_nop 8
s_waitcnt lgkmcnt(0)
v_readfirstlane_b32 s77, v247
s_bitcmp1_b32 s65, 0
s_cbranch_scc1 Lsv0_200
ds_write_b128 v252, v[236:239] offset:20480
s_or_b32 s65, s65, 1
s_waitcnt lgkmcnt(0)
Lsv0_200:
v_max3_f32 v246, v176, v177, v178
v_max_f32 v246, v246, v179
v_mov_b32 v248, v246
s_nop 1
v_permlane16_swap_b32 v246, v248
v_max_f32 v246, v246, v248
v_mov_b32 v248, v246
s_nop 1
v_permlane32_swap_b32 v246, v248
v_max_f32 v246, v246, v248
v_max_f32 v246, 0, v246
v_sub_f32 v236, v236, v246
v_sub_f32 v176, v176, v246
v_sub_f32 v177, v177, v246
v_sub_f32 v178, v178, v246
v_sub_f32 v179, v179, v246
v_exp_f32 v248, -v246
s_nop 0
v_mul_f32 v241, v241, v248
v_mul_f32 v160, v160, v248
v_mul_f32 v161, v161, v248
v_mul_f32 v162, v162, v248
v_mul_f32 v163, v163, v248
v_max3_f32 v246, v180, v181, v182
v_max_f32 v246, v246, v183
v_mov_b32 v248, v246
s_nop 1
v_permlane16_swap_b32 v246, v248
v_max_f32 v246, v246, v248
v_mov_b32 v248, v246
s_nop 1
v_permlane32_swap_b32 v246, v248
v_max_f32 v246, v246, v248
v_max_f32 v246, 0, v246
v_sub_f32 v237, v237, v246
v_sub_f32 v180, v180, v246
v_sub_f32 v181, v181, v246
v_sub_f32 v182, v182, v246
v_sub_f32 v183, v183, v246
v_exp_f32 v248, -v246
s_nop 0
v_mul_f32 v242, v242, v248
v_mul_f32 v164, v164, v248
v_mul_f32 v165, v165, v248
v_mul_f32 v166, v166, v248
v_mul_f32 v167, v167, v248
v_max3_f32 v246, v184, v185, v186
v_max_f32 v246, v246, v187
v_mov_b32 v248, v246
s_nop 1
v_permlane16_swap_b32 v246, v248
v_max_f32 v246, v246, v248
v_mov_b32 v248, v246
s_nop 1
v_permlane32_swap_b32 v246, v248
v_max_f32 v246, v246, v248
v_max_f32 v246, 0, v246
v_sub_f32 v238, v238, v246
v_sub_f32 v184, v184, v246
v_sub_f32 v185, v185, v246
v_sub_f32 v186, v186, v246
v_sub_f32 v187, v187, v246
v_exp_f32 v248, -v246
s_nop 0
v_mul_f32 v243, v243, v248
v_mul_f32 v168, v168, v248
v_mul_f32 v169, v169, v248
v_mul_f32 v170, v170, v248
v_sub_f32 v171, v171, v246
v_max3_f32 v246, v188, v189, v190
v_max_f32 v246, v246, v191
v_mov_b32 v248, v246
s_nop 1
v_permlane16_swap_b32 v246, v248
v_max_f32 v246, v246, v248
v_mov_b32 v248, v246
s_nop 1
v_permlane32_swap_b32 v246, v248
v_max_f32 v246, v246, v248
v_max_f32 v246, 0, v246
v_sub_f32 v239, v239, v246
v_sub_f32 v188, v188, v246
v_sub_f32 v189, v189, v246
v_sub_f32 v190, v190, v246
v_sub_f32 v191, v191, v246
v_exp_f32 v248, -v246
s_nop 0
v_mul_f32 v244, v244, v248
v_sub_f32 v172, v172, v246
v_sub_f32 v173, v173, v246
v_sub_f32 v174, v174, v246
v_sub_f32 v175, v175, v246
s_cmp_eq_u32 s77, 0
s_cbranch_scc1 LR2s_0_0
ds_read_b32 v246, v252 offset:29696
ds_read_b32 v247, v252 offset:28672
ds_read_b32 v248, v252 offset:28676
s_waitcnt lgkmcnt(0)
v_min3_f32 v254, v246, v247, v248
v_sub_f32 v246, v254, v246
v_exp_f32 v246, v246
s_nop 0
ds_write_b32 v252, v246 offset:21504
s_waitcnt lgkmcnt(0)
ds_read_b32 v246, v252 offset:29700
ds_read_b32 v247, v252 offset:28680
ds_read_b32 v248, v252 offset:28684
s_waitcnt lgkmcnt(0)
v_min3_f32 v255, v246, v247, v248
v_sub_f32 v246, v255, v246
v_exp_f32 v246, v246
s_nop 0
ds_write_b32 v252, v246 offset:21508
s_waitcnt lgkmcnt(0)
v_sub_f32 v246, v240, v254
v_mov_b32 v240, v254
v_sub_f32 v232, v232, v246
v_sub_f32 v233, v233, v246
v_sub_f32 v234, v234, v246
v_sub_f32 v235, v235, v246
v_exp_f32 v248, -v246
s_nop 0
v_mul_f32 v245, v245, v248
s_or_b32 s65, s65, 2
s_waitcnt lgkmcnt(0)
s_barrier
ds_write_b64 v252, v[254:255] offset:29696
v_mov_b32 v248, 0
ds_write_b32 v252, v248 offset:29704
s_waitcnt lgkmcnt(0)
s_barrier
LR2s_0_0:
s_waitcnt lgkmcnt(0)
s_nop 2
s_setpc_b64 s[80:81]
LR2f_0:
s_nop 8
s_waitcnt lgkmcnt(0)
v_readfirstlane_b32 s77, v247
s_bitcmp1_b32 s65, 0
s_cbranch_scc1 Lsv0_201
ds_write_b128 v252, v[236:239] offset:20480
s_or_b32 s65, s65, 1
s_waitcnt lgkmcnt(0)
Lsv0_201:
v_max3_f32 v246, v176, v177, v178
v_max_f32 v246, v246, v179
v_mov_b32 v248, v246
s_nop 1
v_permlane16_swap_b32 v246, v248
v_max_f32 v246, v246, v248
v_mov_b32 v248, v246
s_nop 1
v_permlane32_swap_b32 v246, v248
v_max_f32 v246, v246, v248
v_max_f32 v246, 0, v246
v_sub_f32 v236, v236, v246
v_sub_f32 v176, v176, v246
v_sub_f32 v177, v177, v246
v_sub_f32 v178, v178, v246
v_sub_f32 v179, v179, v246
v_exp_f32 v248, -v246
s_nop 0
v_mul_f32 v241, v241, v248
v_mul_f32 v160, v160, v248
v_mul_f32 v161, v161, v248
v_mul_f32 v162, v162, v248
v_mul_f32 v163, v163, v248
v_max3_f32 v246, v180, v181, v182
v_max_f32 v246, v246, v183
v_mov_b32 v248, v246
s_nop 1
v_permlane16_swap_b32 v246, v248
v_max_f32 v246, v246, v248
v_mov_b32 v248, v246
s_nop 1
v_permlane32_swap_b32 v246, v248
v_max_f32 v246, v246, v248
v_max_f32 v246, 0, v246
v_sub_f32 v237, v237, v246
v_sub_f32 v180, v180, v246
v_sub_f32 v181, v181, v246
v_sub_f32 v182, v182, v246
v_sub_f32 v183, v183, v246
v_exp_f32 v248, -v246
s_nop 0
v_mul_f32 v242, v242, v248
v_mul_f32 v164, v164, v248
v_mul_f32 v165, v165, v248
v_mul_f32 v166, v166, v248
v_mul_f32 v167, v167, v248
v_max3_f32 v246, v184, v185, v186
v_max_f32 v246, v246, v187
v_mov_b32 v248, v246
s_nop 1
v_permlane16_swap_b32 v246, v248
v_max_f32 v246, v246, v248
v_mov_b32 v248, v246
s_nop 1
v_permlane32_swap_b32 v246, v248
v_max_f32 v246, v246, v248
v_max_f32 v246, 0, v246
v_sub_f32 v238, v238, v246
v_sub_f32 v184, v184, v246
v_sub_f32 v185, v185, v246
v_sub_f32 v186, v186, v246
v_sub_f32 v187, v187, v246
v_exp_f32 v248, -v246
s_nop 0
v_mul_f32 v243, v243, v248
v_mul_f32 v168, v168, v248
v_mul_f32 v169, v169, v248
v_mul_f32 v170, v170, v248
v_mul_f32 v171, v171, v248
v_max3_f32 v246, v188, v189, v190
v_max_f32 v246, v246, v191
v_mov_b32 v248, v246
s_nop 1
v_permlane16_swap_b32 v246, v248
v_max_f32 v246, v246, v248
v_mov_b32 v248, v246
s_nop 1
v_permlane32_swap_b32 v246, v248
v_max_f32 v246, v246, v248
v_max_f32 v246, 0, v246
v_sub_f32 v239, v239, v246
v_sub_f32 v188, v188, v246
v_sub_f32 v189, v189, v246
v_sub_f32 v190, v190, v246
v_sub_f32 v191, v191, v246
v_exp_f32 v248, -v246
s_nop 0
v_mul_f32 v244, v244, v248
v_mul_f32 v172, v172, v248
v_mul_f32 v173, v173, v248
v_mul_f32 v174, v174, v248
v_mul_f32 v175, v175, v248
s_waitcnt lgkmcnt(0)
s_nop 2
s_setpc_b64 s[80:81]
LR1_0_1:
s_nop 8
s_waitcnt lgkmcnt(0)
s_bitcmp1_b32 s65, 0
s_cbranch_scc1 Lsv0_11
ds_write_b128 v252, v[236:239] offset:20480
s_or_b32 s65, s65, 1
s_waitcnt lgkmcnt(0)
Lsv0_11:
v_max3_f32 v246, v192, v193, v194
v_max_f32 v246, v246, v195
v_mov_b32 v248, v246
s_nop 1
v_permlane16_swap_b32 v246, v248
v_max_f32 v246, v246, v248
v_mov_b32 v248, v246
s_nop 1
v_permlane32_swap_b32 v246, v248
v_max_f32 v246, v246, v248
v_max_f32 v246, 0, v246
v_sub_f32 v236, v236, v246
v_sub_f32 v192, v192, v246
v_sub_f32 v193, v193, v246
v_sub_f32 v194, v194, v246
v_sub_f32 v195, v195, v246
v_exp_f32 v248, -v246
s_nop 0
v_mul_f32 v241, v241, v248
v_max3_f32 v246, v196, v197, v198
v_max_f32 v246, v246, v199
v_mov_b32 v248, v246
s_nop 1
v_permlane16_swap_b32 v246, v248
v_max_f32 v246, v246, v248
v_mov_b32 v248, v246
s_nop 1
v_permlane32_swap_b32 v246, v248
v_max_f32 v246, v246, v248
v_max_f32 v246, 0, v246
v_sub_f32 v237, v237, v246
v_sub_f32 v196, v196, v246
v_sub_f32 v197, v197, v246
v_sub_f32 v198, v198, v246
v_sub_f32 v199, v199, v246
v_exp_f32 v248, -v246
s_nop 0
v_mul_f32 v242, v242, v248
v_max3_f32 v246, v200, v201, v202
v_max_f32 v246, v246, v203
v_mov_b32 v248, v246
s_nop 1
v_permlane16_swap_b32 v246, v248
v_max_f32 v246, v246, v248
v_mov_b32 v248, v246
s_nop 1
v_permlane32_swap_b32 v246, v248
v_max_f32 v246, v246, v248
v_max_f32 v246, 0, v246
v_sub_f32 v238, v238, v246
v_sub_f32 v200, v200, v246
v_sub_f32 v201, v201, v246
v_sub_f32 v202, v202, v246
v_sub_f32 v203, v203, v246
v_exp_f32 v248, -v246
s_nop 0
v_mul_f32 v243, v243, v248
v_max3_f32 v246, v204, v205, v206
v_max_f32 v246, v246, v207
v_mov_b32 v248, v246
s_nop 1
v_permlane16_swap_b32 v246, v248
v_max_f32 v246, v246, v248
v_mov_b32 v248, v246
s_nop 1
v_permlane32_swap_b32 v246, v248
v_max_f32 v246, v246, v248
v_max_f32 v246, 0, v246
v_sub_f32 v239, v239, v246
v_sub_f32 v204, v204, v246
v_sub_f32 v205, v205, v246
v_sub_f32 v206, v206, v246
v_sub_f32 v207, v207, v246
v_exp_f32 v248, -v246
s_nop 0
v_mul_f32 v244, v244, v248
v_max3_f32 v246, v232, v233, v234
v_max_f32 v246, v246, v235
v_mov_b32 v248, v246
s_nop 1
v_permlane16_swap_b32 v246, v248
v_max_f32 v246, v246, v248
v_mov_b32 v248, v246
s_nop 1
v_permlane32_swap_b32 v246, v248
v_max_f32 v246, v246, v248
v_max_f32 v246, 0, v246
v_sub_f32 v246, v240, v246
ds_write_b32 v252, v246 offset:28672
v_mov_b32 v248, 0x7149f2ca
ds_write_b32 v252, v248 offset:29704
s_waitcnt lgkmcnt(0)
s_nop 2
s_setpc_b64 s[80:81]
LR2_0_1:
s_nop 8
s_waitcnt lgkmcnt(0)
v_readfirstlane_b32 s77, v247
s_bitcmp1_b32 s65, 0
s_cbranch_scc1 Lsv0_210
ds_write_b128 v252, v[236:239] offset:20480
s_or_b32 s65, s65, 1
s_waitcnt lgkmcnt(0)
Lsv0_210:
v_max3_f32 v246, v176, v177, v178
v_max_f32 v246, v246, v179
v_mov_b32 v248, v246
s_nop 1
v_permlane16_swap_b32 v246, v248
v_max_f32 v246, v246, v248
v_mov_b32 v248, v246
s_nop 1
v_permlane32_swap_b32 v246, v248
v_max_f32 v246, v246, v248
v_max_f32 v246, 0, v246
v_sub_f32 v236, v236, v246
v_sub_f32 v176, v176, v246
v_sub_f32 v177, v177, v246
v_sub_f32 v178, v178, v246
v_sub_f32 v179, v179, v246
v_exp_f32 v248, -v246
s_nop 0
v_mul_f32 v241, v241, v248
v_mul_f32 v192, v192, v248
v_mul_f32 v193, v193, v248
v_mul_f32 v194, v194, v248
v_mul_f32 v195, v195, v248
v_max3_f32 v246, v180, v181, v182
v_max_f32 v246, v246, v183
v_mov_b32 v248, v246
s_nop 1
v_permlane16_swap_b32 v246, v248
v_max_f32 v246, v246, v248
v_mov_b32 v248, v246
s_nop 1
v_permlane32_swap_b32 v246, v248
v_max_f32 v246, v246, v248
v_max_f32 v246, 0, v246
v_sub_f32 v237, v237, v246
v_sub_f32 v180, v180, v246
v_sub_f32 v181, v181, v246
v_sub_f32 v182, v182, v246
v_sub_f32 v183, v183, v246
v_exp_f32 v248, -v246
s_nop 0
v_mul_f32 v242, v242, v248
v_mul_f32 v196, v196, v248
v_mul_f32 v197, v197, v248
v_mul_f32 v198, v198, v248
v_mul_f32 v199, v199, v248
v_max3_f32 v246, v184, v185, v186
v_max_f32 v246, v246, v187
v_mov_b32 v248, v246
s_nop 1
v_permlane16_swap_b32 v246, v248
v_max_f32 v246, v246, v248
v_mov_b32 v248, v246
s_nop 1
v_permlane32_swap_b32 v246, v248
v_max_f32 v246, v246, v248
v_max_f32 v246, 0, v246
v_sub_f32 v238, v238, v246
v_sub_f32 v184, v184, v246
v_sub_f32 v185, v185, v246
v_sub_f32 v186, v186, v246
v_sub_f32 v187, v187, v246
v_exp_f32 v248, -v246
s_nop 0
v_mul_f32 v243, v243, v248
v_mul_f32 v200, v200, v248
v_mul_f32 v201, v201, v248
v_mul_f32 v202, v202, v248
v_sub_f32 v203, v203, v246
v_max3_f32 v246, v188, v189, v190
v_max_f32 v246, v246, v191
v_mov_b32 v248, v246
s_nop 1
v_permlane16_swap_b32 v246, v248
v_max_f32 v246, v246, v248
v_mov_b32 v248, v246
s_nop 1
v_permlane32_swap_b32 v246, v248
v_max_f32 v246, v246, v248
v_max_f32 v246, 0, v246
v_sub_f32 v239, v239, v246
v_sub_f32 v188, v188, v246
v_sub_f32 v189, v189, v246
v_sub_f32 v190, v190, v246
v_sub_f32 v191, v191, v246
v_exp_f32 v248, -v246
s_nop 0
v_mul_f32 v244, v244, v248
v_sub_f32 v204, v204, v246
v_sub_f32 v205, v205, v246
v_sub_f32 v206, v206, v246
v_sub_f32 v207, v207, v246
s_cmp_eq_u32 s77, 0
s_cbranch_scc1 LR2s_0_1
ds_read_b32 v246, v252 offset:29696
ds_read_b32 v247, v252 offset:28672
ds_read_b32 v248, v252 offset:28676
s_waitcnt lgkmcnt(0)
v_min3_f32 v254, v246, v247, v248
v_sub_f32 v246, v254, v246
v_exp_f32 v246, v246
s_nop 0
ds_write_b32 v252, v246 offset:21504
s_waitcnt lgkmcnt(0)
ds_read_b32 v246, v252 offset:29700
ds_read_b32 v247, v252 offset:28680
ds_read_b32 v248, v252 offset:28684
s_waitcnt lgkmcnt(0)
v_min3_f32 v255, v246, v247, v248
v_sub_f32 v246, v255, v246
v_exp_f32 v246, v246
s_nop 0
ds_write_b32 v252, v246 offset:21508
s_waitcnt lgkmcnt(0)
v_sub_f32 v246, v240, v254
v_mov_b32 v240, v254
v_sub_f32 v232, v232, v246
v_sub_f32 v233, v233, v246
v_sub_f32 v234, v234, v246
v_sub_f32 v235, v235, v246
v_exp_f32 v248, -v246
s_nop 0
v_mul_f32 v245, v245, v248
s_or_b32 s65, s65, 2
s_waitcnt lgkmcnt(0)
s_barrier
ds_write_b64 v252, v[254:255] offset:29696
v_mov_b32 v248, 0
ds_write_b32 v252, v248 offset:29704
s_waitcnt lgkmcnt(0)
s_barrier
LR2s_0_1:
s_waitcnt lgkmcnt(0)
s_nop 2
s_setpc_b64 s[80:81]
LH_0:
s_nop 15
s_waitcnt lgkmcnt(0)
s_bitcmp1_b32 s65, 0
s_cbranch_scc0 LH2_0
ds_read_b32 v246, v252 offset:20480
s_waitcnt lgkmcnt(0)
v_sub_f32 v246, v236, v246
v_exp_f32 v246, v246
s_nop 1
.set ci_, 0
.rept 16
v_accvgpr_read_b32 v247, a[ci_+0]
v_accvgpr_read_b32 v248, a[ci_+1]
v_accvgpr_read_b32 v254, a[ci_+2]
v_accvgpr_read_b32 v255, a[ci_+3]
v_mul_f32 v247, v247, v246
v_mul_f32 v248, v248, v246
v_mul_f32 v254, v254, v246
v_mul_f32 v255, v255, v246
v_accvgpr_write_b32 a[ci_+0], v247
v_accvgpr_write_b32 a[ci_+1], v248
v_accvgpr_write_b32 a[ci_+2], v254
v_accvgpr_write_b32 a[ci_+3], v255
.set ci_, ci_+16
.endr
ds_read_b32 v246, v252 offset:20484
s_waitcnt lgkmcnt(0)
v_sub_f32 v246, v237, v246
v_exp_f32 v246, v246
s_nop 1
.set ci_, 4
.rept 16
v_accvgpr_read_b32 v247, a[ci_+0]
v_accvgpr_read_b32 v248, a[ci_+1]
v_accvgpr_read_b32 v254, a[ci_+2]
v_accvgpr_read_b32 v255, a[ci_+3]
v_mul_f32 v247, v247, v246
v_mul_f32 v248, v248, v246
v_mul_f32 v254, v254, v246
v_mul_f32 v255, v255, v246
v_accvgpr_write_b32 a[ci_+0], v247
v_accvgpr_write_b32 a[ci_+1], v248
v_accvgpr_write_b32 a[ci_+2], v254
v_accvgpr_write_b32 a[ci_+3], v255
.set ci_, ci_+16
.endr
ds_read_b32 v246, v252 offset:20488
s_waitcnt lgkmcnt(0)
v_sub_f32 v246, v238, v246
v_exp_f32 v246, v246
s_nop 1
.set ci_, 8
.rept 16
v_accvgpr_read_b32 v247, a[ci_+0]
v_accvgpr_read_b32 v248, a[ci_+1]
v_accvgpr_read_b32 v254, a[ci_+2]
v_accvgpr_read_b32 v255, a[ci_+3]
v_mul_f32 v247, v247, v246
v_mul_f32 v248, v248, v246
v_mul_f32 v254, v254, v246
v_mul_f32 v255, v255, v246
v_accvgpr_write_b32 a[ci_+0], v247
v_accvgpr_write_b32 a[ci_+1], v248
v_accvgpr_write_b32 a[ci_+2], v254
v_accvgpr_write_b32 a[ci_+3], v255
.set ci_, ci_+16
.endr
ds_read_b32 v246, v252 offset:20492
s_waitcnt lgkmcnt(0)
v_sub_f32 v246, v239, v246
v_exp_f32 v246, v246
s_nop 1
.set ci_, 12
.rept 16
v_accvgpr_read_b32 v247, a[ci_+0]
v_accvgpr_read_b32 v248, a[ci_+1]
v_accvgpr_read_b32 v254, a[ci_+2]
v_accvgpr_read_b32 v255, a[ci_+3]
v_mul_f32 v247, v247, v246
v_mul_f32 v248, v248, v246
v_mul_f32 v254, v254, v246
v_mul_f32 v255, v255, v246
v_accvgpr_write_b32 a[ci_+0], v247
v_accvgpr_write_b32 a[ci_+1], v248
v_accvgpr_write_b32 a[ci_+2], v254
v_accvgpr_write_b32 a[ci_+3], v255
.set ci_, ci_+16
.endr
LH2_0:
s_bitcmp1_b32 s65, 1
s_cbranch_scc0 LH3_0
ds_read_b32 v246, v252 offset:21504
s_waitcnt lgkmcnt(0)
v_mul_f32 v128, v128, v246
v_mul_f32 v129, v129, v246
v_mul_f32 v130, v130, v246
v_mul_f32 v131, v131, v246
v_mul_f32 v136, v136, v246
v_mul_f32 v137, v137, v246
v_mul_f32 v138, v138, v246
v_mul_f32 v139, v139, v246
v_mul_f32 v144, v144, v246
v_mul_f32 v145, v145, v246
v_mul_f32 v146, v146, v246
v_mul_f32 v147, v147, v246
v_mul_f32 v152, v152, v246
v_mul_f32 v153, v153, v246
v_mul_f32 v154, v154, v246
v_mul_f32 v155, v155, v246
ds_read_b32 v246, v252 offset:21508
s_waitcnt lgkmcnt(0)
v_mul_f32 v132, v132, v246
v_mul_f32 v133, v133, v246
v_mul_f32 v134, v134, v246
v_mul_f32 v135, v135, v246
v_mul_f32 v140, v140, v246
v_mul_f32 v141, v141, v246
v_mul_f32 v142, v142, v246
v_mul_f32 v143, v143, v246
v_mul_f32 v148, v148, v246
v_mul_f32 v149, v149, v246
v_mul_f32 v150, v150, v246
v_mul_f32 v151, v151, v246
v_mul_f32 v156, v156, v246
v_mul_f32 v157, v157, v246
v_mul_f32 v158, v158, v246
v_mul_f32 v159, v159, v246
LH3_0:
s_mov_b32 s65, 0
s_waitcnt lgkmcnt(0)
s_nop 2
s_setpc_b64 s[80:81]
LW1:
v_mbcnt_lo_u32_b32 v228, -1, 0
v_mbcnt_hi_u32_b32 v228, -1, v228
v_bfe_u32 v231, v228, 2, 3
v_lshrrev_b32 v232, 2, v231
v_sub_u32 v234, 2, v232
v_and_b32 v234, 3, v234
v_and_b32 v233, 3, v228
v_xor_b32 v233, v233, v234
v_lshlrev_b32 v233, 4, v233
v_lshrrev_b32 v234, 5, v228
v_lshl_add_u32 v233, v234, 6, v233
v_and_b32 v234, 3, v231
v_lshl_add_u32 v234, v232, 3, v234
v_add_u32 v234, 16, v234
v_lshl_add_u32 v225, v234, 13, v233
v_add_u32 v234, 8, v231
v_lshl_add_u32 v253, v234, 9, v233
v_add_u32 v234, 8, v231
v_mul_u32_u24 v234, 0x380, v234
v_add_u32 v226, v234, v233
s_and_b32 s77, s26, 1
s_mul_i32 s80, s77, 129024
s_sub_u32 s81, 129024, s80
s_add_u32 s67, s80, 0
s_add_u32 s68, s81, 0
s_add_u32 s69, s80, 28672
s_add_u32 s70, s81, 28672
s_add_u32 s71, s80, 57344
s_add_u32 s72, s81, 57344
s_add_u32 s73, s80, 86016
s_add_u32 s74, s81, 86016
s_add_u32 s75, s80, 114688
s_mov_b32 s76, 0
s_mov_b32 s62, 6
s_mov_b32 s63, 0x3db8aa3b
s_mov_b32 s66, 0xbdb8aa3b
s_mov_b32 s64, 0x41800000
s_mov_b32 s65, 1
s_mov_b32 s50, 3662336
s_mov_b32 s51, 0x20000
s_mov_b32 s54, 258048
s_mov_b32 s55, 0x20000
s_mov_b32 s42, 524288
s_mov_b32 s43, 0x20000
s_mov_b32 s46, 524288
s_mov_b32 s47, 0x20000
s_mov_b32 s58, Lcodeend-Lbase
s_mov_b32 s59, 0x20000
v_lshlrev_b32 v246, 7, v228
buffer_load_dword v247, v246, s[56:59], s78 offen
s_waitcnt lgkmcnt(0)
s_add_u32 s52, s16, s60
s_addc_u32 s53, s17, 0
s_and_b32 s53, s53, 0xffff
s_add_u32 s48, s22, s61
s_addc_u32 s49, s23, 0
s_and_b32 s49, s49, 0xffff
s_mov_b32 m0, 37888
s_nop 0
buffer_load_dwordx4 v226, s[52:55], s67 offen offset:0 lds
s_mov_b32 m0, 41984
s_nop 0
buffer_load_dwordx4 v226, s[52:55], s68 offen offset:0 lds
s_mov_b32 m0, 46080
s_nop 0
buffer_load_dwordx4 v226, s[52:55], s69 offen offset:0 lds
s_mov_b32 m0, 50176
s_nop 0
buffer_load_dwordx4 v226, s[52:55], s70 offen offset:0 lds
s_mov_b32 m0, 54272
s_nop 0
buffer_load_dwordx4 v226, s[52:55], s71 offen offset:0 lds
s_mov_b32 m0, 58368
s_nop 0
buffer_load_dwordx4 v226, s[52:55], s72 offen offset:0 lds
s_mov_b32 m0, 62464
s_nop 0
buffer_load_dwordx4 v226, s[52:55], s73 offen offset:0 lds
s_mov_b32 m0, 66560
s_nop 0
buffer_load_dwordx4 v226, s[52:55], s74 offen offset:0 lds
s_mov_b32 m0, 70656
s_nop 0
buffer_load_dwordx4 v226, s[52:55], s75 offen offset:0 lds
v_add_u32 v226, 0x80, v226
s_mov_b32 m0, 132096
s_nop 0
buffer_load_dwordx4 v225, s[48:51], s76 offen offset:0 lds
s_mov_b32 m0, 134016
s_nop 0
buffer_load_dwordx4 v225, s[48:51], s76 offen offset:128 lds
s_mov_b32 m0, 135936
s_nop 0
buffer_load_dwordx4 v225, s[48:51], s76 offen offset:256 lds
s_mov_b32 m0, 137856
s_nop 0
buffer_load_dwordx4 v225, s[48:51], s76 offen offset:384 lds
s_add_u32 s76, s76, 262144
s_mov_b32 m0, 148480
s_nop 0
buffer_load_dwordx4 v225, s[48:51], s76 offen offset:0 lds
s_mov_b32 m0, 150400
s_nop 0
buffer_load_dwordx4 v225, s[48:51], s76 offen offset:128 lds
s_mov_b32 m0, 152320
s_nop 0
buffer_load_dwordx4 v225, s[48:51], s76 offen offset:256 lds
s_mov_b32 m0, 154240
s_nop 0
buffer_load_dwordx4 v225, s[48:51], s76 offen offset:384 lds
s_add_u32 s76, s76, 262144
s_and_b32 s60, s26, 127
s_lshl_b32 s60, s60, 19
s_add_u32 s40, s18, s60
s_addc_u32 s41, s19, 0
s_and_b32 s41, s41, 0xffff
s_add_u32 s44, s20, s60
s_addc_u32 s45, s21, 0
s_and_b32 s45, s45, 0xffff
s_mov_b32 s60, 0
s_mov_b32 s61, 0
s_lshl_b32 s77, s26, 9
s_and_b32 s77, s77, 0xffffe000
s_add_u32 s77, s77, 100663296
s_add_u32 s38, s28, s77
s_addc_u32 s39, s29, 0
s_and_b32 s77, s26, 15
s_lshl_b32 s36, s77, 8
s_add_u32 s36, s36, 256
s_add_u32 s36, s36, s38
s_addc_u32 s37, s39, 0
s_lshl_b32 s77, s77, 2
s_add_u32 s38, s38, s77
s_addc_u32 s39, s39, 0
s_mov_b32 m0, 1024
s_nop 0
buffer_load_dwordx4 v226, s[52:55], s67 offen offset:0 lds
s_mov_b32 m0, 5120
s_nop 0
buffer_load_dwordx4 v226, s[52:55], s68 offen offset:0 lds
s_mov_b32 m0, 9216
s_nop 0
buffer_load_dwordx4 v226, s[52:55], s69 offen offset:0 lds
s_mov_b32 m0, 13312
s_nop 0
buffer_load_dwordx4 v226, s[52:55], s70 offen offset:0 lds
s_mov_b32 m0, 17408
s_nop 0
buffer_load_dwordx4 v226, s[52:55], s71 offen offset:0 lds
s_mov_b32 m0, 21504
s_nop 0
buffer_load_dwordx4 v226, s[52:55], s72 offen offset:0 lds
s_mov_b32 m0, 25600
s_nop 0
buffer_load_dwordx4 v226, s[52:55], s73 offen offset:0 lds
s_mov_b32 m0, 29696
s_nop 0
buffer_load_dwordx4 v226, s[52:55], s74 offen offset:0 lds
s_mov_b32 m0, 33792
s_nop 0
buffer_load_dwordx4 v226, s[52:55], s75 offen offset:0 lds
v_add_u32 v226, 0x80, v226
v_lshrrev_b32 v229, 4, v228
v_and_b32 v230, 15, v228
v_lshlrev_b32 v231, 4, v228
v_add_u32 v252, 0x10000, v231
v_lshrrev_b32 v231, 2, v230
v_sub_u32 v234, 4, v231
v_and_b32 v234, 3, v234
v_xor_b32 v234, v234, v229
v_lshlrev_b32 v234, 4, v234
v_and_b32 v231, 7, v230
v_lshl_add_u32 v234, v231, 6, v234
v_lshrrev_b32 v231, 3, v230
v_lshl_add_u32 v249, v231, 10, v234
v_add_u32 v227, 0x9000, v249
v_lshrrev_b32 v231, 2, v230
v_and_b32 v232, 1, v229
v_lshl_add_u32 v231, v232, 2, v231
v_lshlrev_b32 v231, 6, v231
v_lshrrev_b32 v232, 1, v229
v_lshl_add_u32 v231, v232, 10, v231
v_add_u32 v231, 0x18000, v231
v_sub_u32 v234, 4, v229
v_and_b32 v234, 3, v234
v_and_b32 v232, 3, v228
v_xor_b32 v233, v232, v234
v_lshl_add_u32 v224, v233, 4, v231
v_lshrrev_b32 v233, 1, v232
v_xor_b32 v233, v233, v234
v_lshl_add_u32 v233, v233, 4, v231
v_and_b32 v232, 1, v232
v_lshl_add_u32 v250, v232, 3, v233
v_xor_b32 v251, 32, v250
v_mov_b32 v241, 0
v_mov_b32 v242, 0
v_mov_b32 v243, 0
v_mov_b32 v244, 0
v_mov_b32 v245, 0
s_waitcnt vmcnt(9)
s_barrier
RT 160,224,32768
RT 162,224,40960
RT 164,224,49152
RT 166,224,57344
ds_read_b128 a[0:3], v227 offset:8192
ds_read_b128 a[4:7], v227 offset:8704
ds_read_b128 a[8:11], v227 offset:10240
ds_read_b128 a[12:15], v227 offset:10752
ds_read_b128 a[16:19], v227 offset:12288
ds_read_b128 a[20:23], v227 offset:12800
ds_read_b128 a[24:27], v227 offset:14336
ds_read_b128 a[28:31], v227 offset:14848
ds_read_b128 a[32:35], v227 offset:32768
ds_read_b128 a[36:39], v227 offset:33280
ds_read_b128 a[40:43], v227 offset:34816
ds_read_b128 a[44:47], v227 offset:35328
RT 168,224,32776
RT 170,224,40968
RT 172,224,49160
RT 174,224,57352
s_waitcnt lgkmcnt(15)
MG0 v,0,v,160,a,0
RT 176,224,33280
s_waitcnt lgkmcnt(15)
MG v,0,v,164,a,4
RT 178,224,41472
s_waitcnt lgkmcnt(15)
MG0 v,32,v,160,a,8
RT 180,224,49664
s_waitcnt lgkmcnt(15)
MG v,32,v,164,a,12
RT 182,224,57856
s_mov_b32 m0, 99328
s_waitcnt lgkmcnt(15)
MG0 v,64,v,160,a,16
buffer_load_dwordx4 v225, s[48:51], s76 offen offset:0 lds
s_waitcnt lgkmcnt(14)
MG v,64,v,164,a,20
s_waitcnt lgkmcnt(13)
MG0 v,96,v,160,a,24
s_waitcnt lgkmcnt(12)
MG v,96,v,164,a,28
s_waitcnt lgkmcnt(4)
MG0 v,4,v,168,a,0
RT 160,224,33288
MG v,4,v,172,a,4
RT 162,224,41480
MG0 v,36,v,168,a,8
RT 164,224,49672
MG v,36,v,172,a,12
RT 166,224,57864
s_mov_b32 m0, 101248
MG0 v,68,v,168,a,16
buffer_load_dwordx4 v225, s[48:51], s76 offen offset:128 lds
MG v,68,v,172,a,20
MG0 v,100,v,168,a,24
MG v,100,v,172,a,28
s_waitcnt lgkmcnt(4)
MG0 v,8,v,176,a,0
RT 168,224,34816
MG v,8,v,180,a,4
RT 170,224,43008
MG0 v,40,v,176,a,8
RT 172,224,51200
MG v,40,v,180,a,12
RT 174,224,59392
s_mov_b32 m0, 103168
MG0 v,72,v,176,a,16
buffer_load_dwordx4 v225, s[48:51], s76 offen offset:256 lds
MG v,72,v,180,a,20
MG0 v,104,v,176,a,24
MG v,104,v,180,a,28
s_waitcnt lgkmcnt(4)
MG0 v,12,v,160,a,0
RT 176,224,34824
MG v,12,v,164,a,4
RT 178,224,43016
MG0 v,44,v,160,a,8
RT 180,224,51208
MG v,44,v,164,a,12
RT 182,224,59400
s_mov_b32 m0, 105088
MG0 v,76,v,160,a,16
buffer_load_dwordx4 v225, s[48:51], s76 offen offset:384 lds
s_add_u32 s76, s76, 262144
MG v,76,v,164,a,20
MG0 v,108,v,160,a,24
MG v,108,v,164,a,28
s_waitcnt lgkmcnt(4)
MG0 v,16,v,168,a,0
RT 160,224,35328
MG v,16,v,172,a,4
RT 162,224,43520
MG0 v,48,v,168,a,8
RT 164,224,51712
MG v,48,v,172,a,12
RT 166,224,59904
s_mov_b32 m0, 115712
MG0 v,80,v,168,a,16
buffer_load_dwordx4 v225, s[48:51], s76 offen offset:0 lds
MG v,80,v,172,a,20
MG0 v,112,v,168,a,24
MG v,112,v,172,a,28
s_mov_b32 m0, 117632
MG0 v,128,v,168,a,32
buffer_load_dwordx4 v225, s[48:51], s76 offen offset:128 lds
MG v,128,v,172,a,36
MG0 v,132,v,168,a,40
MG v,132,v,172,a,44
s_waitcnt lgkmcnt(4)
MG0 v,20,v,176,a,0
RT 168,224,35336
MG v,20,v,180,a,4
RT 170,224,43528
MG0 v,52,v,176,a,8
RT 172,224,51720
MG v,52,v,180,a,12
RT 174,224,59912
s_mov_b32 m0, 119552
MG0 v,84,v,176,a,16
buffer_load_dwordx4 v225, s[48:51], s76 offen offset:256 lds
MG v,84,v,180,a,20
MG0 v,116,v,176,a,24
MG v,116,v,180,a,28
s_mov_b32 m0, 121472
MG0 v,136,v,176,a,32
buffer_load_dwordx4 v225, s[48:51], s76 offen offset:384 lds
s_add_u32 s76, s76, 262144
MG v,136,v,180,a,36
MG0 v,140,v,176,a,40
MG v,140,v,180,a,44
s_waitcnt lgkmcnt(4)
MG0 v,24,v,160,a,0
RT 176,224,36864
MG v,24,v,164,a,4
RT 178,224,45056
MG0 v,56,v,160,a,8
RT 180,224,53248
MG v,56,v,164,a,12
RT 182,224,61440
MG0 v,88,v,160,a,16
MG v,88,v,164,a,20
MG0 v,120,v,160,a,24
MG v,120,v,164,a,28
MG0 v,144,v,160,a,32
MG v,144,v,164,a,36
MG0 v,148,v,160,a,40
s_waitcnt vmcnt(8) lgkmcnt(4)
MG v,148,v,164,a,44
s_barrier
MG0 v,28,v,168,a,0
RT 160,224,36872
MG v,28,v,172,a,4
RT 162,224,45064
MG0 v,60,v,168,a,8
RT 164,224,53256
MG v,60,v,172,a,12
RT 166,224,61448
MG0 v,92,v,168,a,16
ds_read_b128 a[48:51], v249 offset:8192
s_mov_b32 m0, 37888
MG v,92,v,172,a,20
buffer_load_dwordx4 v226, s[52:55], s67 offen offset:0 lds
MG0 v,124,v,168,a,24
ds_read_b128 a[52:55], v249 offset:8704
MG v,124,v,172,a,28
MG0 v,152,v,168,a,32
ds_read_b128 a[56:59], v249 offset:10240
s_mov_b32 m0, 41984
MG v,152,v,172,a,36
buffer_load_dwordx4 v226, s[52:55], s68 offen offset:0 lds
MG0 v,156,v,168,a,40
ds_read_b128 a[60:63], v249 offset:10752
MG v,156,v,172,a,44
s_waitcnt lgkmcnt(8)
MG0 a,128,v,176,a,0
RT 168,224,37376
MG a,128,v,180,a,4
RT 170,224,45568
MG0 a,132,v,176,a,8
RT 172,224,53760
MG a,132,v,180,a,12
RT 174,224,61952
MG0 a,136,v,176,a,16
ds_read_b128 a[64:67], v249 offset:12288
s_mov_b32 m0, 46080
MG a,136,v,180,a,20
buffer_load_dwordx4 v226, s[52:55], s69 offen offset:0 lds
MG0 a,140,v,176,a,24
ds_read_b128 a[68:71], v249 offset:12800
MG a,140,v,180,a,28
s_waitcnt lgkmcnt(10)
MG0 a,144,v,160,a,0
RT 176,224,37384
MG a,144,v,164,a,4
RT 178,224,45576
MG0 a,148,v,160,a,8
RT 180,224,53768
MG a,148,v,164,a,12
RT 182,224,61960
MG0 a,152,v,160,a,16
ds_read_b128 a[72:75], v249 offset:14336
s_mov_b32 m0, 50176
MG a,152,v,164,a,20
buffer_load_dwordx4 v226, s[52:55], s70 offen offset:0 lds
MG0 a,156,v,160,a,24
ds_read_b128 a[76:79], v249 offset:14848
MG a,156,v,164,a,28
s_waitcnt lgkmcnt(8)
MG0 a,160,v,168,a,0
RT 160,224,38912
MG a,160,v,172,a,4
RT 162,224,47104
MG0 a,164,v,168,a,8
RT 164,224,55296
MG a,164,v,172,a,12
RT 166,224,63488
MG0 a,168,v,168,a,16
ds_read_b128 a[80:83], v249 offset:32768
s_mov_b32 m0, 54272
MG a,168,v,172,a,20
buffer_load_dwordx4 v226, s[52:55], s71 offen offset:0 lds
MG0 a,172,v,168,a,24
ds_read_b128 a[84:87], v249 offset:33280
MG a,172,v,172,a,28
s_waitcnt lgkmcnt(8)
MG0 a,176,v,176,a,0
RT 168,224,38920
MG a,176,v,180,a,4
RT 170,224,47112
MG0 a,180,v,176,a,8
RT 172,224,55304
MG a,180,v,180,a,12
RT 174,224,63496
MG0 a,184,v,176,a,16
ds_read_b128 a[88:91], v249 offset:34816
s_mov_b32 m0, 58368
MG a,184,v,180,a,20
buffer_load_dwordx4 v226, s[52:55], s72 offen offset:0 lds
MG0 a,188,v,176,a,24
ds_read_b128 a[92:95], v249 offset:35328
MG a,188,v,180,a,28
s_waitcnt lgkmcnt(8)
MG0 a,192,v,160,a,0
RT 176,224,39424
MG a,192,v,164,a,4
RT 178,224,47616
MG0 a,196,v,160,a,8
RT 180,224,55808
MG a,196,v,164,a,12
RT 182,224,64000
s_mov_b32 m0, 62464
MG0 a,200,v,160,a,16
buffer_load_dwordx4 v226, s[52:55], s73 offen offset:0 lds
MG a,200,v,164,a,20
MG0 a,204,v,160,a,24
MG a,204,v,164,a,28
s_waitcnt lgkmcnt(6)
MG0 a,208,v,168,a,0
RT 160,224,39432
MG a,208,v,172,a,4
RT 162,224,47624
MG0 a,212,v,168,a,8
RT 164,224,55816
MG a,212,v,172,a,12
RT 166,224,64008
s_mov_b32 m0, 66560
MG0 a,216,v,168,a,16
buffer_load_dwordx4 v226, s[52:55], s74 offen offset:0 lds
MG a,216,v,172,a,20
MG0 a,220,v,168,a,24
s_waitcnt vmcnt(8) lgkmcnt(0)
MG a,220,v,172,a,28
s_barrier
MG0 a,224,v,176,a,0
RT 168,224,0
MG a,224,v,180,a,4
RT 170,224,8192
MG0 a,228,v,176,a,8
RT 172,224,16384
MG a,228,v,180,a,12
RT 174,224,24576
s_mov_b32 m0, 70656
MG0 a,232,v,176,a,16
buffer_load_dwordx4 v226, s[52:55], s75 offen offset:0 lds
v_add_u32 v226, 0x80, v226
MG a,232,v,180,a,20
MG0 a,236,v,176,a,24
MG a,236,v,180,a,28
MG0 a,240,v,160,a,0
RT 176,224,8
MG a,240,v,164,a,4
RT 178,224,8200
MG0 a,244,v,160,a,8
RT 180,224,16392
MG a,244,v,164,a,12
RT 182,224,24584
s_mov_b32 m0, 132096
MG0 a,248,v,160,a,16
buffer_load_dwordx4 v225, s[48:51], s76 offen offset:0 lds
MG a,248,v,164,a,20
MG0 a,252,v,160,a,24
MG a,252,v,164,a,28
s_waitcnt lgkmcnt(4)
MG v,0,v,168,a,48
RT 160,224,512
MG v,0,v,172,a,52
RT 162,224,8704
MG v,32,v,168,a,56
RT 164,224,16896
MG v,32,v,172,a,60
RT 166,224,25088
s_mov_b32 m0, 134016
MG v,64,v,168,a,64
buffer_load_dwordx4 v225, s[48:51], s76 offen offset:128 lds
MG v,64,v,172,a,68
MG v,96,v,168,a,72
MG v,96,v,172,a,76
s_waitcnt lgkmcnt(4)
MG v,4,v,176,a,48
RT 168,224,520
MG v,4,v,180,a,52
RT 170,224,8712
MG v,36,v,176,a,56
RT 172,224,16904
MG v,36,v,180,a,60
RT 174,224,25096
s_mov_b32 m0, 135936
MG v,68,v,176,a,64
buffer_load_dwordx4 v225, s[48:51], s76 offen offset:256 lds
MG v,68,v,180,a,68
MG v,100,v,176,a,72
MG v,100,v,180,a,76
s_waitcnt lgkmcnt(4)
MG v,8,v,160,a,48
RT 176,224,2048
MG v,8,v,164,a,52
RT 178,224,10240
MG v,40,v,160,a,56
RT 180,224,18432
MG v,40,v,164,a,60
RT 182,224,26624
s_mov_b32 m0, 137856
MG v,72,v,160,a,64
buffer_load_dwordx4 v225, s[48:51], s76 offen offset:384 lds
s_add_u32 s76, s76, 262144
MG v,72,v,164,a,68
MG v,104,v,160,a,72
MG v,104,v,164,a,76
s_waitcnt lgkmcnt(4)
MG v,12,v,168,a,48
RT 160,224,2056
MG v,12,v,172,a,52
RT 162,224,10248
MG v,44,v,168,a,56
RT 164,224,18440
MG v,44,v,172,a,60
RT 166,224,26632
s_mov_b32 m0, 148480
MG v,76,v,168,a,64
buffer_load_dwordx4 v225, s[48:51], s76 offen offset:0 lds
MG v,76,v,172,a,68
MG v,108,v,168,a,72
MG v,108,v,172,a,76
s_waitcnt lgkmcnt(4)
MG v,16,v,176,a,48
RT 168,224,2560
MG v,16,v,180,a,52
RT 170,224,10752
MG v,48,v,176,a,56
RT 172,224,18944
MG v,48,v,180,a,60
RT 174,224,27136
s_mov_b32 m0, 150400
MG v,80,v,176,a,64
buffer_load_dwordx4 v225, s[48:51], s76 offen offset:128 lds
MG v,80,v,180,a,68
MG v,112,v,176,a,72
MG v,112,v,180,a,76
s_mov_b32 m0, 152320
MG v,128,v,176,a,80
buffer_load_dwordx4 v225, s[48:51], s76 offen offset:256 lds
MG v,128,v,180,a,84
MG v,132,v,176,a,88
MG v,132,v,180,a,92
s_waitcnt lgkmcnt(4)
MG v,20,v,160,a,48
RT 176,224,2568
MG v,20,v,164,a,52
RT 178,224,10760
MG v,52,v,160,a,56
RT 180,224,18952
MG v,52,v,164,a,60
RT 182,224,27144
s_mov_b32 m0, 154240
MG v,84,v,160,a,64
buffer_load_dwordx4 v225, s[48:51], s76 offen offset:384 lds
s_add_u32 s76, s76, 262144
MG v,84,v,164,a,68
MG v,116,v,160,a,72
MG v,116,v,164,a,76
s_mov_b32 m0, 1024
MG v,136,v,160,a,80
buffer_load_dwordx4 v226, s[52:55], s67 offen offset:0 lds
MG v,136,v,164,a,84
MG v,140,v,160,a,88
MG v,140,v,164,a,92
s_waitcnt lgkmcnt(4)
MG v,24,v,168,a,48
RT 160,224,4096
MG v,24,v,172,a,52
RT 162,224,12288
MG v,56,v,168,a,56
RT 164,224,20480
MG v,56,v,172,a,60
RT 166,224,28672
s_mov_b32 m0, 5120
MG v,88,v,168,a,64
buffer_load_dwordx4 v226, s[52:55], s68 offen offset:0 lds
MG v,88,v,172,a,68
MG v,120,v,168,a,72
MG v,120,v,172,a,76
s_mov_b32 m0, 9216
MG v,144,v,168,a,80
buffer_load_dwordx4 v226, s[52:55], s69 offen offset:0 lds
MG v,144,v,172,a,84
MG v,148,v,168,a,88
s_waitcnt vmcnt(11) lgkmcnt(4)
MG v,148,v,172,a,92
s_barrier
MG v,28,v,176,a,48
RT 168,224,4104
MG v,28,v,180,a,52
RT 170,224,12296
MG v,60,v,176,a,56
RT 172,224,20488
MG v,60,v,180,a,60
RT 174,224,28680
MG v,92,v,176,a,64
ds_read_b128 a[0:3], v227 offset:8192
s_mov_b32 m0, 13312
MG v,92,v,180,a,68
buffer_load_dwordx4 v226, s[52:55], s70 offen offset:0 lds
MG v,124,v,176,a,72
ds_read_b128 a[4:7], v227 offset:8704
MG v,124,v,180,a,76
MG v,152,v,176,a,80
ds_read_b128 a[8:11], v227 offset:10240
s_mov_b32 m0, 17408
MG v,152,v,180,a,84
buffer_load_dwordx4 v226, s[52:55], s71 offen offset:0 lds
MG v,156,v,176,a,88
ds_read_b128 a[12:15], v227 offset:10752
MG v,156,v,180,a,92
s_waitcnt lgkmcnt(8)
MG a,128,v,160,a,48
RT 176,224,4608
MG a,128,v,164,a,52
RT 178,224,12800
MG a,132,v,160,a,56
RT 180,224,20992
MG a,132,v,164,a,60
RT 182,224,29184
MG a,136,v,160,a,64
ds_read_b128 a[16:19], v227 offset:12288
s_mov_b32 m0, 21504
MG a,136,v,164,a,68
buffer_load_dwordx4 v226, s[52:55], s72 offen offset:0 lds
MG a,140,v,160,a,72
ds_read_b128 a[20:23], v227 offset:12800
MG a,140,v,164,a,76
s_waitcnt lgkmcnt(10)
MG a,144,v,168,a,48
RT 160,224,4616
MG a,144,v,172,a,52
RT 162,224,12808
MG a,148,v,168,a,56
RT 164,224,21000
MG a,148,v,172,a,60
RT 166,224,29192
MG a,152,v,168,a,64
ds_read_b128 a[24:27], v227 offset:14336
s_mov_b32 m0, 25600
MG a,152,v,172,a,68
buffer_load_dwordx4 v226, s[52:55], s73 offen offset:0 lds
MG a,156,v,168,a,72
ds_read_b128 a[28:31], v227 offset:14848
MG a,156,v,172,a,76
s_waitcnt lgkmcnt(8)
MG a,160,v,176,a,48
RT 168,224,6144
MG a,160,v,180,a,52
RT 170,224,14336
MG a,164,v,176,a,56
RT 172,224,22528
MG a,164,v,180,a,60
RT 174,224,30720
MG a,168,v,176,a,64
ds_read_b128 a[32:35], v227 offset:32768
s_mov_b32 m0, 29696
MG a,168,v,180,a,68
buffer_load_dwordx4 v226, s[52:55], s74 offen offset:0 lds
MG a,172,v,176,a,72
ds_read_b128 a[36:39], v227 offset:33280
MG a,172,v,180,a,76
s_waitcnt lgkmcnt(8)
MG a,176,v,160,a,48
RT 176,224,6152
MG a,176,v,164,a,52
RT 178,224,14344
MG a,180,v,160,a,56
RT 180,224,22536
MG a,180,v,164,a,60
RT 182,224,30728
MG a,184,v,160,a,64
ds_read_b128 a[40:43], v227 offset:34816
s_mov_b32 m0, 33792
MG a,184,v,164,a,68
buffer_load_dwordx4 v226, s[52:55], s75 offen offset:0 lds
v_add_u32 v226, 0x80, v226
MG a,188,v,160,a,72
ds_read_b128 a[44:47], v227 offset:35328
MG a,188,v,164,a,76
s_waitcnt lgkmcnt(8)
MG a,192,v,168,a,48
RT 160,224,6656
MG a,192,v,172,a,52
RT 162,224,14848
MG a,196,v,168,a,56
RT 164,224,23040
MG a,196,v,172,a,60
RT 166,224,31232
MG a,200,v,168,a,64
MG a,200,v,172,a,68
MG a,204,v,168,a,72
MG a,204,v,172,a,76
s_waitcnt lgkmcnt(6)
MG a,208,v,176,a,48
RT 168,224,6664
MG a,208,v,180,a,52
RT 170,224,14856
MG a,212,v,176,a,56
RT 172,224,23048
MG a,212,v,180,a,60
RT 174,224,31240
MG a,216,v,176,a,64
MG a,216,v,180,a,68
MG a,220,v,176,a,72
s_waitcnt vmcnt(9) lgkmcnt(0)
MG a,220,v,180,a,76
s_barrier
MG a,224,v,160,a,48
RT 176,224,32768
MG a,224,v,164,a,52
RT 178,224,40960
MG a,228,v,160,a,56
RT 180,224,49152
MG a,228,v,164,a,60
RT 182,224,57344
s_mov_b32 m0, 99328
MG a,232,v,160,a,64
buffer_load_dwordx4 v225, s[48:51], s76 offen offset:0 lds
MG a,232,v,164,a,68
MG a,236,v,160,a,72
MG a,236,v,164,a,76
MG a,240,v,168,a,48
RT 160,224,32776
MG a,240,v,172,a,52
RT 162,224,40968
MG a,244,v,168,a,56
RT 164,224,49160
MG a,244,v,172,a,60
RT 166,224,57352
s_mov_b32 m0, 101248
MG a,248,v,168,a,64
buffer_load_dwordx4 v225, s[48:51], s76 offen offset:128 lds
MG a,248,v,172,a,68
MG a,252,v,168,a,72
MG a,252,v,172,a,76
s_waitcnt lgkmcnt(4)
MG v,0,v,176,a,0
RT 168,224,33280
MG v,0,v,180,a,4
RT 170,224,41472
MG v,32,v,176,a,8
RT 172,224,49664
MG v,32,v,180,a,12
RT 174,224,57856
s_mov_b32 m0, 103168
MG v,64,v,176,a,16
buffer_load_dwordx4 v225, s[48:51], s76 offen offset:256 lds
MG v,64,v,180,a,20
MG v,96,v,176,a,24
MG v,96,v,180,a,28
s_waitcnt lgkmcnt(4)
MG v,4,v,160,a,0
RT 176,224,33288
MG v,4,v,164,a,4
RT 178,224,41480
MG v,36,v,160,a,8
RT 180,224,49672
MG v,36,v,164,a,12
RT 182,224,57864
s_mov_b32 m0, 105088
MG v,68,v,160,a,16
buffer_load_dwordx4 v225, s[48:51], s76 offen offset:384 lds
s_add_u32 s76, s76, 262144
MG v,68,v,164,a,20
MG v,100,v,160,a,24
MG v,100,v,164,a,28
s_waitcnt lgkmcnt(4)
MG v,8,v,168,a,0
RT 160,224,34816
MG v,8,v,172,a,4
RT 162,224,43008
MG v,40,v,168,a,8
RT 164,224,51200
MG v,40,v,172,a,12
RT 166,224,59392
s_mov_b32 m0, 115712
MG v,72,v,168,a,16
buffer_load_dwordx4 v225, s[48:51], s76 offen offset:0 lds
MG v,72,v,172,a,20
MG v,104,v,168,a,24
MG v,104,v,172,a,28
s_waitcnt lgkmcnt(4)
MG v,12,v,176,a,0
RT 168,224,34824
MG v,12,v,180,a,4
RT 170,224,43016
MG v,44,v,176,a,8
RT 172,224,51208
MG v,44,v,180,a,12
RT 174,224,59400
s_mov_b32 m0, 117632
MG v,76,v,176,a,16
buffer_load_dwordx4 v225, s[48:51], s76 offen offset:128 lds
MG v,76,v,180,a,20
MG v,108,v,176,a,24
MG v,108,v,180,a,28
s_waitcnt lgkmcnt(4)
MG v,16,v,160,a,0
RT 176,224,35328
MG v,16,v,164,a,4
RT 178,224,43520
MG v,48,v,160,a,8
RT 180,224,51712
MG v,48,v,164,a,12
RT 182,224,59904
s_mov_b32 m0, 119552
MG v,80,v,160,a,16
buffer_load_dwordx4 v225, s[48:51], s76 offen offset:256 lds
MG v,80,v,164,a,20
MG v,112,v,160,a,24
MG v,112,v,164,a,28
s_mov_b32 m0, 121472
MG v,128,v,160,a,32
buffer_load_dwordx4 v225, s[48:51], s76 offen offset:384 lds
s_add_u32 s76, s76, 262144
MG v,128,v,164,a,36
MG v,132,v,160,a,40
MG v,132,v,164,a,44
s_waitcnt lgkmcnt(4)
MG v,20,v,168,a,0
RT 160,224,35336
MG v,20,v,172,a,4
RT 162,224,43528
MG v,52,v,168,a,8
RT 164,224,51720
MG v,52,v,172,a,12
RT 166,224,59912
s_mov_b32 m0, 37888
MG v,84,v,168,a,16
buffer_load_dwordx4 v226, s[52:55], s67 offen offset:0 lds
MG v,84,v,172,a,20
MG v,116,v,168,a,24
MG v,116,v,172,a,28
s_mov_b32 m0, 41984
MG v,136,v,168,a,32
buffer_load_dwordx4 v226, s[52:55], s68 offen offset:0 lds
MG v,136,v,172,a,36
MG v,140,v,168,a,40
MG v,140,v,172,a,44
s_waitcnt lgkmcnt(4)
MG v,24,v,176,a,0
RT 168,224,36864
MG v,24,v,180,a,4
RT 170,224,45056
MG v,56,v,176,a,8
RT 172,224,53248
MG v,56,v,180,a,12
RT 174,224,61440
s_mov_b32 m0, 46080
MG v,88,v,176,a,16
buffer_load_dwordx4 v226, s[52:55], s69 offen offset:0 lds
MG v,88,v,180,a,20
MG v,120,v,176,a,24
MG v,120,v,180,a,28
s_mov_b32 m0, 50176
MG v,144,v,176,a,32
buffer_load_dwordx4 v226, s[52:55], s70 offen offset:0 lds
MG v,144,v,180,a,36
MG v,148,v,176,a,40
s_waitcnt vmcnt(12) lgkmcnt(4)
MG v,148,v,180,a,44
s_barrier
MG v,28,v,160,a,0
RT 176,224,36872
MG v,28,v,164,a,4
RT 178,224,45064
MG v,60,v,160,a,8
RT 180,224,53256
MG v,60,v,164,a,12
RT 182,224,61448
MG v,92,v,160,a,16
ds_read_b128 a[48:51], v249 offset:8192
s_mov_b32 m0, 54272
MG v,92,v,164,a,20
buffer_load_dwordx4 v226, s[52:55], s71 offen offset:0 lds
MG v,124,v,160,a,24
ds_read_b128 a[52:55], v249 offset:8704
MG v,124,v,164,a,28
MG v,152,v,160,a,32
ds_read_b128 a[56:59], v249 offset:10240
s_mov_b32 m0, 58368
MG v,152,v,164,a,36
buffer_load_dwordx4 v226, s[52:55], s72 offen offset:0 lds
MG v,156,v,160,a,40
ds_read_b128 a[60:63], v249 offset:10752
MG v,156,v,164,a,44
s_waitcnt lgkmcnt(8)
MG a,128,v,168,a,0
RT 160,224,37376
MG a,128,v,172,a,4
RT 162,224,45568
MG a,132,v,168,a,8
RT 164,224,53760
MG a,132,v,172,a,12
RT 166,224,61952
MG a,136,v,168,a,16
ds_read_b128 a[64:67], v249 offset:12288
s_mov_b32 m0, 62464
MG a,136,v,172,a,20
buffer_load_dwordx4 v226, s[52:55], s73 offen offset:0 lds
MG a,140,v,168,a,24
ds_read_b128 a[68:71], v249 offset:12800
MG a,140,v,172,a,28
s_waitcnt lgkmcnt(10)
MG a,144,v,176,a,0
RT 168,224,37384
MG a,144,v,180,a,4
RT 170,224,45576
MG a,148,v,176,a,8
RT 172,224,53768
MG a,148,v,180,a,12
RT 174,224,61960
MG a,152,v,176,a,16
ds_read_b128 a[72:75], v249 offset:14336
s_mov_b32 m0, 66560
MG a,152,v,180,a,20
buffer_load_dwordx4 v226, s[52:55], s74 offen offset:0 lds
MG a,156,v,176,a,24
ds_read_b128 a[76:79], v249 offset:14848
MG a,156,v,180,a,28
s_waitcnt lgkmcnt(8)
MG a,160,v,160,a,0
RT 176,224,38912
MG a,160,v,164,a,4
RT 178,224,47104
MG a,164,v,160,a,8
RT 180,224,55296
MG a,164,v,164,a,12
RT 182,224,63488
MG a,168,v,160,a,16
ds_read_b128 a[80:83], v249 offset:32768
s_mov_b32 m0, 70656
MG a,168,v,164,a,20
buffer_load_dwordx4 v226, s[52:55], s75 offen offset:0 lds
v_add_u32 v226, 0x80, v226
MG a,172,v,160,a,24
ds_read_b128 a[84:87], v249 offset:33280
MG a,172,v,164,a,28
s_waitcnt lgkmcnt(8)
MG a,176,v,168,a,0
RT 160,224,38920
MG a,176,v,172,a,4
RT 162,224,47112
MG a,180,v,168,a,8
RT 164,224,55304
MG a,180,v,172,a,12
RT 166,224,63496
MG a,184,v,168,a,16
ds_read_b128 a[88:91], v249 offset:34816
MG a,184,v,172,a,20
MG a,188,v,168,a,24
ds_read_b128 a[92:95], v249 offset:35328
MG a,188,v,172,a,28
s_waitcnt lgkmcnt(8)
MG a,192,v,176,a,0
RT 168,224,39424
MG a,192,v,180,a,4
RT 170,224,47616
MG a,196,v,176,a,8
RT 172,224,55808
MG a,196,v,180,a,12
RT 174,224,64000
MG a,200,v,176,a,16
MG a,200,v,180,a,20
MG a,204,v,176,a,24
MG a,204,v,180,a,28
s_waitcnt lgkmcnt(6)
MG a,208,v,160,a,0
RT 176,224,39432
MG a,208,v,164,a,4
RT 178,224,47624
MG a,212,v,160,a,8
RT 180,224,55816
MG a,212,v,164,a,12
RT 182,224,64008
MG a,216,v,160,a,16
MG a,216,v,164,a,20
MG a,220,v,160,a,24
s_waitcnt vmcnt(9) lgkmcnt(0)
MG a,220,v,164,a,28
s_barrier
MG a,224,v,168,a,0
RT 160,224,0
MG a,224,v,172,a,4
RT 162,224,8192
MG a,228,v,168,a,8
RT 164,224,16384
MG a,228,v,172,a,12
RT 166,224,24576
s_mov_b32 m0, 132096
MG a,232,v,168,a,16
buffer_load_dwordx4 v225, s[48:51], s76 offen offset:0 lds
MG a,232,v,172,a,20
MG a,236,v,168,a,24
MG a,236,v,172,a,28
MG a,240,v,176,a,0
RT 168,224,8
MG a,240,v,180,a,4
RT 170,224,8200
MG a,244,v,176,a,8
RT 172,224,16392
MG a,244,v,180,a,12
RT 174,224,24584
s_mov_b32 m0, 134016
MG a,248,v,176,a,16
buffer_load_dwordx4 v225, s[48:51], s76 offen offset:128 lds
MG a,248,v,180,a,20
MG a,252,v,176,a,24
MG a,252,v,180,a,28
s_waitcnt lgkmcnt(4)
MG v,0,v,160,a,48
RT 176,224,512
MG v,0,v,164,a,52
RT 178,224,8704
MG v,32,v,160,a,56
RT 180,224,16896
MG v,32,v,164,a,60
RT 182,224,25088
s_mov_b32 m0, 135936
MG v,64,v,160,a,64
buffer_load_dwordx4 v225, s[48:51], s76 offen offset:256 lds
MG v,64,v,164,a,68
MG v,96,v,160,a,72
MG v,96,v,164,a,76
s_waitcnt lgkmcnt(4)
MG v,4,v,168,a,48
RT 160,224,520
MG v,4,v,172,a,52
RT 162,224,8712
MG v,36,v,168,a,56
RT 164,224,16904
MG v,36,v,172,a,60
RT 166,224,25096
s_mov_b32 m0, 137856
MG v,68,v,168,a,64
buffer_load_dwordx4 v225, s[48:51], s76 offen offset:384 lds
s_add_u32 s76, s76, 262144
MG v,68,v,172,a,68
MG v,100,v,168,a,72
MG v,100,v,172,a,76
s_waitcnt lgkmcnt(4)
MG v,8,v,176,a,48
RT 168,224,2048
MG v,8,v,180,a,52
RT 170,224,10240
MG v,40,v,176,a,56
RT 172,224,18432
MG v,40,v,180,a,60
RT 174,224,26624
s_mov_b32 m0, 148480
MG v,72,v,176,a,64
buffer_load_dwordx4 v225, s[48:51], s76 offen offset:0 lds
MG v,72,v,180,a,68
MG v,104,v,176,a,72
MG v,104,v,180,a,76
s_waitcnt lgkmcnt(4)
MG v,12,v,160,a,48
RT 176,224,2056
MG v,12,v,164,a,52
RT 178,224,10248
MG v,44,v,160,a,56
RT 180,224,18440
MG v,44,v,164,a,60
RT 182,224,26632
s_mov_b32 m0, 150400
MG v,76,v,160,a,64
buffer_load_dwordx4 v225, s[48:51], s76 offen offset:128 lds
MG v,76,v,164,a,68
MG v,108,v,160,a,72
MG v,108,v,164,a,76
s_waitcnt lgkmcnt(4)
MG v,16,v,168,a,48
RT 160,224,2560
MG v,16,v,172,a,52
RT 162,224,10752
MG v,48,v,168,a,56
RT 164,224,18944
MG v,48,v,172,a,60
RT 166,224,27136
s_mov_b32 m0, 152320
MG v,80,v,168,a,64
buffer_load_dwordx4 v225, s[48:51], s76 offen offset:256 lds
MG v,80,v,172,a,68
MG v,112,v,168,a,72
MG v,112,v,172,a,76
s_mov_b32 m0, 154240
MG v,128,v,168,a,80
buffer_load_dwordx4 v225, s[48:51], s76 offen offset:384 lds
s_add_u32 s76, s76, 262144
MG v,128,v,172,a,84
MG v,132,v,168,a,88
MG v,132,v,172,a,92
s_waitcnt lgkmcnt(4)
MG v,20,v,176,a,48
RT 168,224,2568
MG v,20,v,180,a,52
RT 170,224,10760
MG v,52,v,176,a,56
RT 172,224,18952
MG v,52,v,180,a,60
RT 174,224,27144
s_mov_b32 m0, 1024
MG v,84,v,176,a,64
buffer_load_dwordx4 v226, s[52:55], s67 offen offset:0 lds
MG v,84,v,180,a,68
MG v,116,v,176,a,72
MG v,116,v,180,a,76
s_mov_b32 m0, 5120
MG v,136,v,176,a,80
buffer_load_dwordx4 v226, s[52:55], s68 offen offset:0 lds
MG v,136,v,180,a,84
MG v,140,v,176,a,88
MG v,140,v,180,a,92
s_waitcnt lgkmcnt(4)
MG v,24,v,160,a,48
RT 176,224,4096
MG v,24,v,164,a,52
RT 178,224,12288
MG v,56,v,160,a,56
RT 180,224,20480
MG v,56,v,164,a,60
RT 182,224,28672
s_mov_b32 m0, 9216
MG v,88,v,160,a,64
buffer_load_dwordx4 v226, s[52:55], s69 offen offset:0 lds
MG v,88,v,164,a,68
MG v,120,v,160,a,72
MG v,120,v,164,a,76
s_mov_b32 m0, 13312
MG v,144,v,160,a,80
buffer_load_dwordx4 v226, s[52:55], s70 offen offset:0 lds
MG v,144,v,164,a,84
MG v,148,v,160,a,88
s_waitcnt vmcnt(12) lgkmcnt(4)
MG v,148,v,164,a,92
s_barrier
MG v,28,v,168,a,48
RT 160,224,4104
MG v,28,v,172,a,52
RT 162,224,12296
MG v,60,v,168,a,56
RT 164,224,20488
MG v,60,v,172,a,60
RT 166,224,28680
MG v,92,v,168,a,64
ds_read_b128 a[0:3], v227 offset:8192
s_mov_b32 m0, 17408
MG v,92,v,172,a,68
buffer_load_dwordx4 v226, s[52:55], s71 offen offset:0 lds
MG v,124,v,168,a,72
ds_read_b128 a[4:7], v227 offset:8704
MG v,124,v,172,a,76
MG v,152,v,168,a,80
ds_read_b128 a[8:11], v227 offset:10240
s_mov_b32 m0, 21504
MG v,152,v,172,a,84
buffer_load_dwordx4 v226, s[52:55], s72 offen offset:0 lds
MG v,156,v,168,a,88
ds_read_b128 a[12:15], v227 offset:10752
MG v,156,v,172,a,92
s_waitcnt lgkmcnt(8)
MG a,128,v,176,a,48
RT 168,224,4608
MG a,128,v,180,a,52
RT 170,224,12800
MG a,132,v,176,a,56
RT 172,224,20992
MG a,132,v,180,a,60
RT 174,224,29184
MG a,136,v,176,a,64
ds_read_b128 a[16:19], v227 offset:12288
s_mov_b32 m0, 25600
MG a,136,v,180,a,68
buffer_load_dwordx4 v226, s[52:55], s73 offen offset:0 lds
MG a,140,v,176,a,72
ds_read_b128 a[20:23], v227 offset:12800
MG a,140,v,180,a,76
s_waitcnt lgkmcnt(10)
MG a,144,v,160,a,48
RT 176,224,4616
MG a,144,v,164,a,52
RT 178,224,12808
MG a,148,v,160,a,56
RT 180,224,21000
MG a,148,v,164,a,60
RT 182,224,29192
MG a,152,v,160,a,64
ds_read_b128 a[24:27], v227 offset:14336
s_mov_b32 m0, 29696
MG a,152,v,164,a,68
buffer_load_dwordx4 v226, s[52:55], s74 offen offset:0 lds
MG a,156,v,160,a,72
ds_read_b128 a[28:31], v227 offset:14848
MG a,156,v,164,a,76
s_waitcnt lgkmcnt(8)
MG a,160,v,168,a,48
RT 160,224,6144
MG a,160,v,172,a,52
RT 162,224,14336
MG a,164,v,168,a,56
RT 164,224,22528
MG a,164,v,172,a,60
RT 166,224,30720
MG a,168,v,168,a,64
ds_read_b128 a[32:35], v227 offset:32768
s_mov_b32 m0, 33792
MG a,168,v,172,a,68
buffer_load_dwordx4 v226, s[52:55], s75 offen offset:0 lds
v_add_u32 v226, 0x80, v226
MG a,172,v,168,a,72
ds_read_b128 a[36:39], v227 offset:33280
MG a,172,v,172,a,76
s_waitcnt lgkmcnt(8)
MG a,176,v,176,a,48
RT 168,224,6152
MG a,176,v,180,a,52
RT 170,224,14344
MG a,180,v,176,a,56
RT 172,224,22536
MG a,180,v,180,a,60
RT 174,224,30728
MG a,184,v,176,a,64
ds_read_b128 a[40:43], v227 offset:34816
MG a,184,v,180,a,68
MG a,188,v,176,a,72
ds_read_b128 a[44:47], v227 offset:35328
MG a,188,v,180,a,76
s_waitcnt lgkmcnt(8)
MG a,192,v,160,a,48
RT 176,224,6656
MG a,192,v,164,a,52
RT 178,224,14848
MG a,196,v,160,a,56
RT 180,224,23040
MG a,196,v,164,a,60
RT 182,224,31232
MG a,200,v,160,a,64
MG a,200,v,164,a,68
MG a,204,v,160,a,72
MG a,204,v,164,a,76
s_waitcnt lgkmcnt(6)
MG a,208,v,168,a,48
RT 160,224,6664
MG a,208,v,172,a,52
RT 162,224,14856
MG a,212,v,168,a,56
RT 164,224,23048
MG a,212,v,172,a,60
RT 166,224,31240
MG a,216,v,168,a,64
MG a,216,v,172,a,68
MG a,220,v,168,a,72
s_waitcnt vmcnt(9) lgkmcnt(0)
MG a,220,v,172,a,76
s_barrier
MG a,224,v,176,a,48
RT 168,224,32768
MG a,224,v,180,a,52
RT 170,224,40960
MG a,228,v,176,a,56
RT 172,224,49152
MG a,228,v,180,a,60
RT 174,224,57344
s_mov_b32 m0, 99328
MG a,232,v,176,a,64
buffer_load_dwordx4 v225, s[48:51], s76 offen offset:0 lds
MG a,232,v,180,a,68
MG a,236,v,176,a,72
MG a,236,v,180,a,76
MG a,240,v,160,a,48
RT 176,224,32776
MG a,240,v,164,a,52
RT 178,224,40968
MG a,244,v,160,a,56
RT 180,224,49160
MG a,244,v,164,a,60
RT 182,224,57352
s_mov_b32 m0, 101248
MG a,248,v,160,a,64
buffer_load_dwordx4 v225, s[48:51], s76 offen offset:128 lds
MG a,248,v,164,a,68
MG a,252,v,160,a,72
MG a,252,v,164,a,76
s_waitcnt lgkmcnt(4)
MG v,0,v,168,a,0
RT 160,224,33280
MG v,0,v,172,a,4
RT 162,224,41472
MG v,32,v,168,a,8
RT 164,224,49664
MG v,32,v,172,a,12
RT 166,224,57856
s_mov_b32 m0, 103168
MG v,64,v,168,a,16
buffer_load_dwordx4 v225, s[48:51], s76 offen offset:256 lds
MG v,64,v,172,a,20
MG v,96,v,168,a,24
MG v,96,v,172,a,28
s_waitcnt lgkmcnt(4)
MG v,4,v,176,a,0
RT 168,224,33288
MG v,4,v,180,a,4
RT 170,224,41480
MG v,36,v,176,a,8
RT 172,224,49672
MG v,36,v,180,a,12
RT 174,224,57864
s_mov_b32 m0, 105088
MG v,68,v,176,a,16
buffer_load_dwordx4 v225, s[48:51], s76 offen offset:384 lds
s_add_u32 s76, s76, 262144
MG v,68,v,180,a,20
MG v,100,v,176,a,24
MG v,100,v,180,a,28
s_waitcnt lgkmcnt(4)
MG v,8,v,160,a,0
RT 176,224,34816
MG v,8,v,164,a,4
RT 178,224,43008
MG v,40,v,160,a,8
RT 180,224,51200
MG v,40,v,164,a,12
RT 182,224,59392
s_mov_b32 m0, 115712
MG v,72,v,160,a,16
buffer_load_dwordx4 v225, s[48:51], s76 offen offset:0 lds
MG v,72,v,164,a,20
MG v,104,v,160,a,24
MG v,104,v,164,a,28
s_waitcnt lgkmcnt(4)
MG v,12,v,168,a,0
RT 160,224,34824
MG v,12,v,172,a,4
RT 162,224,43016
MG v,44,v,168,a,8
RT 164,224,51208
MG v,44,v,172,a,12
RT 166,224,59400
s_mov_b32 m0, 117632
MG v,76,v,168,a,16
buffer_load_dwordx4 v225, s[48:51], s76 offen offset:128 lds
MG v,76,v,172,a,20
MG v,108,v,168,a,24
MG v,108,v,172,a,28
s_waitcnt lgkmcnt(4)
MG v,16,v,176,a,0
RT 168,224,35328
MG v,16,v,180,a,4
RT 170,224,43520
MG v,48,v,176,a,8
RT 172,224,51712
MG v,48,v,180,a,12
RT 174,224,59904
s_mov_b32 m0, 119552
MG v,80,v,176,a,16
buffer_load_dwordx4 v225, s[48:51], s76 offen offset:256 lds
MG v,80,v,180,a,20
MG v,112,v,176,a,24
MG v,112,v,180,a,28
s_mov_b32 m0, 121472
MG v,128,v,176,a,32
buffer_load_dwordx4 v225, s[48:51], s76 offen offset:384 lds
s_add_u32 s76, s76, 262144
MG v,128,v,180,a,36
MG v,132,v,176,a,40
MG v,132,v,180,a,44
s_waitcnt lgkmcnt(4)
MG v,20,v,160,a,0
RT 176,224,35336
MG v,20,v,164,a,4
RT 178,224,43528
MG v,52,v,160,a,8
RT 180,224,51720
MG v,52,v,164,a,12
RT 182,224,59912
s_mov_b32 m0, 37888
MG v,84,v,160,a,16
buffer_load_dwordx4 v226, s[52:55], s67 offen offset:0 lds
MG v,84,v,164,a,20
MG v,116,v,160,a,24
MG v,116,v,164,a,28
s_mov_b32 m0, 41984
MG v,136,v,160,a,32
buffer_load_dwordx4 v226, s[52:55], s68 offen offset:0 lds
MG v,136,v,164,a,36
MG v,140,v,160,a,40
MG v,140,v,164,a,44
s_waitcnt lgkmcnt(4)
MG v,24,v,168,a,0
RT 160,224,36864
MG v,24,v,172,a,4
RT 162,224,45056
MG v,56,v,168,a,8
RT 164,224,53248
MG v,56,v,172,a,12
RT 166,224,61440
s_mov_b32 m0, 46080
MG v,88,v,168,a,16
buffer_load_dwordx4 v226, s[52:55], s69 offen offset:0 lds
MG v,88,v,172,a,20
MG v,120,v,168,a,24
MG v,120,v,172,a,28
s_mov_b32 m0, 50176
MG v,144,v,168,a,32
buffer_load_dwordx4 v226, s[52:55], s70 offen offset:0 lds
MG v,144,v,172,a,36
MG v,148,v,168,a,40
s_waitcnt vmcnt(12) lgkmcnt(4)
MG v,148,v,172,a,44
s_barrier
MG v,28,v,176,a,0
RT 168,224,36872
MG v,28,v,180,a,4
RT 170,224,45064
MG v,60,v,176,a,8
RT 172,224,53256
MG v,60,v,180,a,12
RT 174,224,61448
MG v,92,v,176,a,16
ds_read_b128 a[48:51], v249 offset:8192
s_mov_b32 m0, 54272
MG v,92,v,180,a,20
buffer_load_dwordx4 v226, s[52:55], s71 offen offset:0 lds
MG v,124,v,176,a,24
ds_read_b128 a[52:55], v249 offset:8704
MG v,124,v,180,a,28
MG v,152,v,176,a,32
ds_read_b128 a[56:59], v249 offset:10240
s_mov_b32 m0, 58368
MG v,152,v,180,a,36
buffer_load_dwordx4 v226, s[52:55], s72 offen offset:0 lds
MG v,156,v,176,a,40
ds_read_b128 a[60:63], v249 offset:10752
MG v,156,v,180,a,44
s_waitcnt lgkmcnt(8)
MG a,128,v,160,a,0
RT 176,224,37376
MG a,128,v,164,a,4
RT 178,224,45568
MG a,132,v,160,a,8
RT 180,224,53760
MG a,132,v,164,a,12
RT 182,224,61952
MG a,136,v,160,a,16
ds_read_b128 a[64:67], v249 offset:12288
s_mov_b32 m0, 62464
MG a,136,v,164,a,20
buffer_load_dwordx4 v226, s[52:55], s73 offen offset:0 lds
MG a,140,v,160,a,24
ds_read_b128 a[68:71], v249 offset:12800
MG a,140,v,164,a,28
s_waitcnt lgkmcnt(10)
MG a,144,v,168,a,0
RT 160,224,37384
MG a,144,v,172,a,4
RT 162,224,45576
MG a,148,v,168,a,8
RT 164,224,53768
MG a,148,v,172,a,12
RT 166,224,61960
MG a,152,v,168,a,16
ds_read_b128 a[72:75], v249 offset:14336
s_mov_b32 m0, 66560
MG a,152,v,172,a,20
buffer_load_dwordx4 v226, s[52:55], s74 offen offset:0 lds
MG a,156,v,168,a,24
ds_read_b128 a[76:79], v249 offset:14848
MG a,156,v,172,a,28
s_waitcnt lgkmcnt(8)
MG a,160,v,176,a,0
RT 168,224,38912
MG a,160,v,180,a,4
RT 170,224,47104
MG a,164,v,176,a,8
RT 172,224,55296
MG a,164,v,180,a,12
RT 174,224,63488
MG a,168,v,176,a,16
ds_read_b128 a[80:83], v249 offset:32768
s_mov_b32 m0, 70656
MG a,168,v,180,a,20
buffer_load_dwordx4 v226, s[52:55], s75 offen offset:0 lds
v_add_u32 v226, 0x80, v226
MG a,172,v,176,a,24
ds_read_b128 a[84:87], v249 offset:33280
MG a,172,v,180,a,28
s_waitcnt lgkmcnt(8)
MG a,176,v,160,a,0
RT 176,224,38920
MG a,176,v,164,a,4
RT 178,224,47112
MG a,180,v,160,a,8
RT 180,224,55304
MG a,180,v,164,a,12
RT 182,224,63496
MG a,184,v,160,a,16
ds_read_b128 a[88:91], v249 offset:34816
MG a,184,v,164,a,20
MG a,188,v,160,a,24
ds_read_b128 a[92:95], v249 offset:35328
MG a,188,v,164,a,28
s_waitcnt lgkmcnt(8)
MG a,192,v,168,a,0
RT 160,224,39424
MG a,192,v,172,a,4
RT 162,224,47616
MG a,196,v,168,a,8
RT 164,224,55808
MG a,196,v,172,a,12
RT 166,224,64000
MG a,200,v,168,a,16
MG a,200,v,172,a,20
MG a,204,v,168,a,24
MG a,204,v,172,a,28
s_waitcnt lgkmcnt(6)
MG a,208,v,176,a,0
RT 168,224,39432
MG a,208,v,180,a,4
RT 170,224,47624
MG a,212,v,176,a,8
RT 172,224,55816
MG a,212,v,180,a,12
RT 174,224,64008
MG a,216,v,176,a,16
MG a,216,v,180,a,20
MG a,220,v,176,a,24
s_waitcnt vmcnt(9) lgkmcnt(0)
MG a,220,v,180,a,28
s_barrier
MG a,224,v,160,a,0
RT 176,224,0
MG a,224,v,164,a,4
RT 178,224,8192
MG a,228,v,160,a,8
RT 180,224,16384
MG a,228,v,164,a,12
RT 182,224,24576
s_mov_b32 m0, 132096
MG a,232,v,160,a,16
buffer_load_dwordx4 v225, s[48:51], s76 offen offset:0 lds
MG a,232,v,164,a,20
MG a,236,v,160,a,24
MG a,236,v,164,a,28
MG a,240,v,168,a,0
RT 160,224,8
MG a,240,v,172,a,4
RT 162,224,8200
MG a,244,v,168,a,8
RT 164,224,16392
MG a,244,v,172,a,12
RT 166,224,24584
s_mov_b32 m0, 134016
MG a,248,v,168,a,16
buffer_load_dwordx4 v225, s[48:51], s76 offen offset:128 lds
MG a,248,v,172,a,20
MG a,252,v,168,a,24
MG a,252,v,172,a,28
)ASM"
R"ASM(
s_waitcnt lgkmcnt(4)
MG v,0,v,176,a,48
RT 168,224,512
MG v,0,v,180,a,52
RT 170,224,8704
MG v,32,v,176,a,56
RT 172,224,16896
MG v,32,v,180,a,60
RT 174,224,25088
s_mov_b32 m0, 135936
MG v,64,v,176,a,64
buffer_load_dwordx4 v225, s[48:51], s76 offen offset:256 lds
MG v,64,v,180,a,68
MG v,96,v,176,a,72
MG v,96,v,180,a,76
s_waitcnt lgkmcnt(4)
MG v,4,v,160,a,48
RT 176,224,520
MG v,4,v,164,a,52
RT 178,224,8712
MG v,36,v,160,a,56
RT 180,224,16904
MG v,36,v,164,a,60
RT 182,224,25096
s_mov_b32 m0, 137856
MG v,68,v,160,a,64
buffer_load_dwordx4 v225, s[48:51], s76 offen offset:384 lds
s_add_u32 s76, s76, 262144
MG v,68,v,164,a,68
MG v,100,v,160,a,72
MG v,100,v,164,a,76
s_waitcnt lgkmcnt(4)
MG v,8,v,168,a,48
RT 160,224,2048
MG v,8,v,172,a,52
RT 162,224,10240
MG v,40,v,168,a,56
RT 164,224,18432
MG v,40,v,172,a,60
RT 166,224,26624
s_mov_b32 m0, 148480
MG v,72,v,168,a,64
buffer_load_dwordx4 v225, s[48:51], s76 offen offset:0 lds
MG v,72,v,172,a,68
MG v,104,v,168,a,72
MG v,104,v,172,a,76
s_waitcnt lgkmcnt(4)
MG v,12,v,176,a,48
RT 168,224,2056
MG v,12,v,180,a,52
RT 170,224,10248
MG v,44,v,176,a,56
RT 172,224,18440
MG v,44,v,180,a,60
RT 174,224,26632
s_mov_b32 m0, 150400
MG v,76,v,176,a,64
buffer_load_dwordx4 v225, s[48:51], s76 offen offset:128 lds
MG v,76,v,180,a,68
MG v,108,v,176,a,72
MG v,108,v,180,a,76
s_waitcnt lgkmcnt(4)
MG v,16,v,160,a,48
RT 176,224,2560
MG v,16,v,164,a,52
RT 178,224,10752
MG v,48,v,160,a,56
RT 180,224,18944
MG v,48,v,164,a,60
RT 182,224,27136
s_mov_b32 m0, 152320
MG v,80,v,160,a,64
buffer_load_dwordx4 v225, s[48:51], s76 offen offset:256 lds
MG v,80,v,164,a,68
MG v,112,v,160,a,72
MG v,112,v,164,a,76
s_mov_b32 m0, 154240
MG v,128,v,160,a,80
buffer_load_dwordx4 v225, s[48:51], s76 offen offset:384 lds
s_add_u32 s76, s76, 262144
MG v,128,v,164,a,84
MG v,132,v,160,a,88
MG v,132,v,164,a,92
s_waitcnt lgkmcnt(4)
MG v,20,v,168,a,48
RT 160,224,2568
MG v,20,v,172,a,52
RT 162,224,10760
MG v,52,v,168,a,56
RT 164,224,18952
MG v,52,v,172,a,60
RT 166,224,27144
s_mov_b32 m0, 1024
MG v,84,v,168,a,64
buffer_load_dwordx4 v253, s[40:43], s60 offen offset:0 nt lds
MG v,84,v,172,a,68
MG v,116,v,168,a,72
MG v,116,v,172,a,76
s_mov_b32 m0, 2944
MG v,136,v,168,a,80
buffer_load_dwordx4 v253, s[40:43], s60 offen offset:128 nt lds
MG v,136,v,172,a,84
MG v,140,v,168,a,88
MG v,140,v,172,a,92
s_waitcnt lgkmcnt(4)
MG v,24,v,176,a,48
RT 168,224,4096
MG v,24,v,180,a,52
RT 170,224,12288
MG v,56,v,176,a,56
RT 172,224,20480
MG v,56,v,180,a,60
RT 174,224,28672
s_mov_b32 m0, 4864
MG v,88,v,176,a,64
buffer_load_dwordx4 v253, s[40:43], s60 offen offset:256 nt lds
MG v,88,v,180,a,68
MG v,120,v,176,a,72
MG v,120,v,180,a,76
s_mov_b32 m0, 6784
MG v,144,v,176,a,80
buffer_load_dwordx4 v253, s[40:43], s60 offen offset:384 nt lds
s_add_u32 s60, s60, 16384
MG v,144,v,180,a,84
MG v,148,v,176,a,88
s_waitcnt vmcnt(12) lgkmcnt(4)
MG v,148,v,180,a,92
s_barrier
MG v,28,v,160,a,48
RT 176,224,4104
MG v,28,v,164,a,52
RT 178,224,12296
MG v,60,v,160,a,56
RT 180,224,20488
MG v,60,v,164,a,60
RT 182,224,28680
MG v,92,v,160,a,64
ds_read_b128 a[0:3], v227 offset:8192
s_mov_b32 m0, 17408
MG v,92,v,164,a,68
buffer_load_dwordx4 v253, s[40:43], s60 offen offset:0 nt lds
MG v,124,v,160,a,72
ds_read_b128 a[4:7], v227 offset:8704
MG v,124,v,164,a,76
MG v,152,v,160,a,80
ds_read_b128 a[8:11], v227 offset:10240
s_mov_b32 m0, 19328
MG v,152,v,164,a,84
buffer_load_dwordx4 v253, s[40:43], s60 offen offset:128 nt lds
MG v,156,v,160,a,88
ds_read_b128 a[12:15], v227 offset:10752
MG v,156,v,164,a,92
s_waitcnt lgkmcnt(8)
MG a,128,v,168,a,48
RT 160,224,4608
MG a,128,v,172,a,52
RT 162,224,12800
MG a,132,v,168,a,56
RT 164,224,20992
MG a,132,v,172,a,60
RT 166,224,29184
MG a,136,v,168,a,64
ds_read_b128 a[16:19], v227 offset:12288
s_mov_b32 m0, 21248
MG a,136,v,172,a,68
buffer_load_dwordx4 v253, s[40:43], s60 offen offset:256 nt lds
MG a,140,v,168,a,72
ds_read_b128 a[20:23], v227 offset:12800
MG a,140,v,172,a,76
s_waitcnt lgkmcnt(10)
MG a,144,v,176,a,48
RT 168,224,4616
MG a,144,v,180,a,52
RT 170,224,12808
MG a,148,v,176,a,56
RT 172,224,21000
MG a,148,v,180,a,60
RT 174,224,29192
MG a,152,v,176,a,64
ds_read_b128 a[24:27], v227 offset:14336
s_mov_b32 m0, 23168
MG a,152,v,180,a,68
buffer_load_dwordx4 v253, s[40:43], s60 offen offset:384 nt lds
s_add_u32 s60, s60, 16384
MG a,156,v,176,a,72
ds_read_b128 a[28:31], v227 offset:14848
MG a,156,v,180,a,76
s_waitcnt lgkmcnt(8)
MG a,160,v,160,a,48
RT 176,224,6144
MG a,160,v,164,a,52
RT 178,224,14336
MG a,164,v,160,a,56
RT 180,224,22528
MG a,164,v,164,a,60
RT 182,224,30720
MG a,168,v,160,a,64
ds_read_b128 a[32:35], v227 offset:32768
MG a,168,v,164,a,68
MG a,172,v,160,a,72
ds_read_b128 a[36:39], v227 offset:33280
MG a,172,v,164,a,76
s_waitcnt lgkmcnt(8)
MG a,176,v,168,a,48
RT 160,224,6152
MG a,176,v,172,a,52
RT 162,224,14344
MG a,180,v,168,a,56
RT 164,224,22536
MG a,180,v,172,a,60
RT 166,224,30728
MG a,184,v,168,a,64
ds_read_b128 a[40:43], v227 offset:34816
MG a,184,v,172,a,68
MG a,188,v,168,a,72
ds_read_b128 a[44:47], v227 offset:35328
MG a,188,v,172,a,76
s_waitcnt lgkmcnt(8)
MG a,192,v,176,a,48
RT 168,224,6656
MG a,192,v,180,a,52
RT 170,224,14848
MG a,196,v,176,a,56
RT 172,224,23040
MG a,196,v,180,a,60
RT 174,224,31232
MG a,200,v,176,a,64
MG a,200,v,180,a,68
MG a,204,v,176,a,72
MG a,204,v,180,a,76
s_waitcnt lgkmcnt(6)
MG a,208,v,160,a,48
RT 176,224,6664
MG a,208,v,164,a,52
RT 178,224,14856
MG a,212,v,160,a,56
RT 180,224,23048
MG a,212,v,164,a,60
RT 182,224,31240
MG a,216,v,160,a,64
MG a,216,v,164,a,68
MG a,220,v,160,a,72
s_waitcnt vmcnt(8) lgkmcnt(0)
MG a,220,v,164,a,76
s_barrier
MG a,224,v,168,a,48
RT 160,224,32768
MG a,224,v,172,a,52
RT 162,224,40960
MG a,228,v,168,a,56
RT 164,224,49152
MG a,228,v,172,a,60
RT 166,224,57344
s_mov_b32 m0, 99328
MG a,232,v,168,a,64
buffer_load_dwordx4 v253, s[44:47], s61 offen offset:0 nt lds
MG a,232,v,172,a,68
MG a,236,v,168,a,72
MG a,236,v,172,a,76
MG a,240,v,176,a,48
RT 168,224,32776
MG a,240,v,180,a,52
RT 170,224,40968
MG a,244,v,176,a,56
RT 172,224,49160
MG a,244,v,180,a,60
RT 174,224,57352
s_mov_b32 m0, 101248
MG a,248,v,176,a,64
buffer_load_dwordx4 v253, s[44:47], s61 offen offset:128 nt lds
MG a,248,v,180,a,68
MG a,252,v,176,a,72
MG a,252,v,180,a,76
s_waitcnt lgkmcnt(4)
MG v,0,v,160,a,0
RT 176,224,33280
MG v,0,v,164,a,4
RT 178,224,41472
MG v,32,v,160,a,8
RT 180,224,49664
MG v,32,v,164,a,12
RT 182,224,57856
s_mov_b32 m0, 103168
MG v,64,v,160,a,16
buffer_load_dwordx4 v253, s[44:47], s61 offen offset:256 nt lds
MG v,64,v,164,a,20
MG v,96,v,160,a,24
MG v,96,v,164,a,28
s_waitcnt lgkmcnt(4)
MG v,4,v,168,a,0
RT 160,224,33288
MG v,4,v,172,a,4
RT 162,224,41480
MG v,36,v,168,a,8
RT 164,224,49672
MG v,36,v,172,a,12
RT 166,224,57864
s_mov_b32 m0, 105088
MG v,68,v,168,a,16
buffer_load_dwordx4 v253, s[44:47], s61 offen offset:384 nt lds
s_add_u32 s61, s61, 16384
MG v,68,v,172,a,20
MG v,100,v,168,a,24
MG v,100,v,172,a,28
s_waitcnt lgkmcnt(4)
MG v,8,v,176,a,0
RT 168,224,34816
MG v,8,v,180,a,4
RT 170,224,43008
MG v,40,v,176,a,8
RT 172,224,51200
MG v,40,v,180,a,12
RT 174,224,59392
s_mov_b32 m0, 33792
MG v,72,v,176,a,16
buffer_load_dwordx4 v253, s[40:43], s60 offen offset:0 nt lds
MG v,72,v,180,a,20
CV 0,0,1
CV 1,2,3
MG v,104,v,176,a,24
CV 2,4,5
CV 3,6,7
MG v,104,v,180,a,28
s_waitcnt lgkmcnt(4)
CV 32,32,33
CV 33,34,35
MG v,12,v,160,a,0
RT 176,224,34824
CV 34,36,37
MG v,12,v,164,a,4
RT 178,224,43016
CV 35,38,39
MG v,44,v,160,a,8
RT 180,224,51208
CV 64,64,65
MG v,44,v,164,a,12
RT 182,224,59400
s_mov_b32 m0, 35712
MG v,76,v,160,a,16
buffer_load_dwordx4 v253, s[40:43], s60 offen offset:128 nt lds
MG v,76,v,164,a,20
CV 65,66,67
MG v,108,v,160,a,24
CV 66,68,69
CV 67,70,71
MG v,108,v,164,a,28
s_waitcnt lgkmcnt(4)
CV 96,96,97
CV 97,98,99
MG v,16,v,168,a,0
RT 160,224,35328
CV 98,100,101
MG v,16,v,172,a,4
RT 162,224,43520
CV 99,102,103
MG v,48,v,168,a,8
RT 164,224,51712
MG v,48,v,172,a,12
RT 166,224,59904
s_mov_b32 m0, 37632
MG v,80,v,168,a,16
buffer_load_dwordx4 v253, s[40:43], s60 offen offset:256 nt lds
MG v,80,v,172,a,20
CV 4,8,9
CV 5,10,11
MG v,112,v,168,a,24
CV 6,12,13
CV 7,14,15
MG v,112,v,172,a,28
CV 36,40,41
MG v,128,v,168,a,32
CV 37,42,43
CV 38,44,45
MG v,128,v,172,a,36
CV 39,46,47
CV 68,72,73
MG v,132,v,168,a,40
CV 69,74,75
CV 70,76,77
MG v,132,v,172,a,44
s_waitcnt lgkmcnt(4)
CV 71,78,79
CV 100,104,105
MG v,20,v,176,a,0
RT 168,224,35336
CV 101,106,107
MG v,20,v,180,a,4
RT 170,224,43528
CV 102,108,109
MG v,52,v,176,a,8
RT 172,224,51720
CV 103,110,111
MG v,52,v,180,a,12
RT 174,224,59912
s_mov_b32 m0, 39552
MG v,84,v,176,a,16
buffer_load_dwordx4 v253, s[40:43], s60 offen offset:384 nt lds
s_add_u32 s60, s60, 16384
MG v,84,v,180,a,20
MG v,116,v,176,a,24
MG v,116,v,180,a,28
MG v,136,v,176,a,32
MG v,136,v,180,a,36
MG v,140,v,176,a,40
MG v,140,v,180,a,44
s_waitcnt lgkmcnt(4)
MG v,24,v,160,a,0
RT 176,224,36864
MG v,24,v,164,a,4
RT 178,224,45056
MG v,56,v,160,a,8
RT 180,224,53248
MG v,56,v,164,a,12
RT 182,224,61440
s_mov_b32 m0, 115712
MG v,88,v,160,a,16
buffer_load_dwordx4 v253, s[44:47], s61 offen offset:0 nt lds
MG v,88,v,164,a,20
CV 128,128,129
CV 129,130,131
MG v,120,v,160,a,24
CV 130,136,137
CV 131,138,139
MG v,120,v,164,a,28
ds_write_b128 v252, v[128:131] offset:2048
MG v,144,v,160,a,32
CV 132,132,133
MG v,144,v,164,a,36
CV 133,134,135
CV 134,140,141
MG v,148,v,160,a,40
CV 135,142,143
MG v,148,v,164,a,44
s_waitcnt lgkmcnt(5)
MG v,28,v,168,a,0
RT 160,224,36872
MG v,28,v,172,a,4
RT 162,224,45064
MG v,60,v,168,a,8
RT 164,224,53256
MG v,60,v,172,a,12
RT 166,224,61448
s_mov_b32 m0, 117632
MG v,92,v,168,a,16
buffer_load_dwordx4 v253, s[44:47], s61 offen offset:128 nt lds
MG v,92,v,172,a,20
ds_write_b128 v252, v[132:135] offset:10240
MG v,124,v,168,a,24
CV 8,16,17
MG v,124,v,172,a,28
CV 9,18,19
CV 10,20,21
MG v,152,v,168,a,32
CV 11,22,23
CV 40,48,49
MG v,152,v,172,a,36
CV 41,50,51
CV 42,52,53
MG v,156,v,168,a,40
CV 43,54,55
CV 72,80,81
MG v,156,v,172,a,44
s_waitcnt lgkmcnt(6)
CV 73,82,83
MG a,128,v,176,a,0
RT 168,224,37376
CV 74,84,85
MG a,128,v,180,a,4
RT 170,224,45568
CV 75,86,87
MG a,132,v,176,a,8
RT 172,224,53760
CV 104,112,113
MG a,132,v,180,a,12
RT 174,224,61952
s_mov_b32 m0, 119552
MG a,136,v,176,a,16
buffer_load_dwordx4 v253, s[44:47], s61 offen offset:256 nt lds
MG a,136,v,180,a,20
CV 105,114,115
CV 106,116,117
MG a,140,v,176,a,24
CV 107,118,119
CV 144,144,145
MG a,140,v,180,a,28
s_waitcnt lgkmcnt(5)
CV 145,146,147
CV 146,152,153
MG a,144,v,160,a,0
RT 176,224,37384
CV 147,154,155
MG a,144,v,164,a,4
RT 178,224,45576
MG a,148,v,160,a,8
RT 180,224,53768
MG a,148,v,164,a,12
RT 182,224,61960
s_mov_b32 m0, 121472
MG a,152,v,160,a,16
buffer_load_dwordx4 v253, s[44:47], s61 offen offset:384 nt lds
s_add_u32 s61, s61, 16384
MG a,152,v,164,a,20
ds_write_b128 v252, v[144:147] offset:3072
MG a,156,v,160,a,24
CV 148,148,149
MG a,156,v,164,a,28
s_waitcnt lgkmcnt(5)
CV 149,150,151
CV 150,156,157
MG a,160,v,168,a,0
RT 160,224,38912
CV 151,158,159
MG a,160,v,172,a,4
RT 162,224,47104
MG a,164,v,168,a,8
RT 164,224,55296
MG a,164,v,172,a,12
RT 166,224,63488
s_mov_b32 m0, 50176
MG a,168,v,168,a,16
buffer_load_dwordx4 v253, s[40:43], s60 offen offset:0 nt lds
MG a,168,v,172,a,20
ds_write_b128 v252, v[148:151] offset:11264
MG a,172,v,168,a,24
CV 12,24,25
CV 13,26,27
MG a,172,v,172,a,28
s_waitcnt lgkmcnt(6)
CV 14,28,29
CV 15,30,31
MG a,176,v,176,a,0
RT 168,224,38920
CV 44,56,57
MG a,176,v,180,a,4
RT 170,224,47112
CV 45,58,59
MG a,180,v,176,a,8
RT 172,224,55304
CV 46,60,61
MG a,180,v,180,a,12
RT 174,224,63496
s_mov_b32 m0, 52096
MG a,184,v,176,a,16
buffer_load_dwordx4 v253, s[40:43], s60 offen offset:128 nt lds
MG a,184,v,180,a,20
CV 47,62,63
MG a,188,v,176,a,24
CV 76,88,89
CV 77,90,91
MG a,188,v,180,a,28
s_waitcnt lgkmcnt(5)
CV 78,92,93
CV 79,94,95
MG a,192,v,160,a,0
RT 176,224,39424
CV 108,120,121
MG a,192,v,164,a,4
RT 178,224,47616
CV 109,122,123
MG a,196,v,160,a,8
RT 180,224,55808
CV 110,124,125
MG a,196,v,164,a,12
RT 182,224,64000
s_mov_b32 m0, 54016
MG a,200,v,160,a,16
buffer_load_dwordx4 v253, s[40:43], s60 offen offset:256 nt lds
MG a,200,v,164,a,20
CV 111,126,127
AR 192,128
MG a,204,v,160,a,24
AR 193,129
AR 194,130
MG a,204,v,164,a,28
s_waitcnt lgkmcnt(4)
AR 195,131
MG a,208,v,168,a,0
RT 160,224,39432
AR 196,144
MG a,208,v,172,a,4
RT 162,224,47624
AR 197,145
MG a,212,v,168,a,8
RT 164,224,55816
AR 198,146
MG a,212,v,172,a,12
RT 166,224,64008
s_mov_b32 m0, 55936
MG a,216,v,168,a,16
buffer_load_dwordx4 v253, s[40:43], s60 offen offset:384 nt lds
s_add_u32 s60, s60, 16384
MG a,216,v,172,a,20
AR 199,147
CV 16,192,193
MG a,220,v,168,a,24
CV 17,194,195
CV 18,196,197
MG a,220,v,172,a,28
s_waitcnt lgkmcnt(6)
CV 19,198,199
AR 200,132
MG a,224,v,176,a,0
s_waitcnt lgkmcnt(4)
AR 201,133
AR 202,134
MG a,224,v,180,a,4
AR 203,135
MG a,228,v,176,a,8
AR 204,148
AR 205,149
MG a,228,v,180,a,12
AR 206,150
AR 207,151
MG a,232,v,176,a,16
CV 48,200,201
CV 49,202,203
MG a,232,v,180,a,20
CV 50,204,205
CV 51,206,207
MG a,236,v,176,a,24
AR 192,136
MG a,236,v,180,a,28
s_waitcnt lgkmcnt(2)
AR 193,137
AR 194,138
MG a,240,v,160,a,0
s_waitcnt lgkmcnt(0)
AR 195,139
AR 196,152
MG a,240,v,164,a,4
AR 197,153
AR 198,154
MG a,244,v,160,a,8
AR 199,155
CV 80,192,193
MG a,244,v,164,a,12
CV 81,194,195
MG a,248,v,160,a,16
CV 82,196,197
CV 83,198,199
MG a,248,v,164,a,20
AR 200,140
AR 201,141
MG a,252,v,160,a,24
AR 202,142
AR 203,143
MG a,252,v,164,a,28
s_waitcnt vmcnt(20)
AR 204,156
AR 205,157
s_barrier
RB 176,249,8192
RB 180,249,8704
RB 224,252,0
RB 208,249,10240
RB 212,249,10752
RB 228,252,1024
AR 206,158
AR 207,159
CV 112,200,201
CV 113,202,203
CV 114,204,205
CV 115,206,207
AR 192,160
AR 193,161
AR 194,162
AR 195,163
AR 196,176
AR 197,177
AR 198,178
AR 199,179
CV 20,192,193
CV 21,194,195
CV 22,196,197
CV 23,198,199
AR 200,164
AR 201,165
AR 202,166
AR 203,167
AR 204,180
AR 205,181
AR 206,182
AR 207,183
CV 52,200,201
CV 53,202,203
CV 54,204,205
CV 55,206,207
AR 192,168
AR 193,169
AR 194,170
AR 195,171
AR 196,184
AR 197,185
AR 198,186
AR 199,187
CV 84,192,193
CV 85,194,195
CV 86,196,197
CV 87,198,199
AR 200,172
AR 201,173
AR 202,174
AR 203,175
AR 204,188
AR 205,189
AR 206,190
AR 207,191
CV 116,200,201
CV 117,202,203
CV 118,204,205
CV 119,206,207
AR 192,192
AR 193,193
AR 194,194
AR 195,195
AR 196,208
AR 197,209
AR 198,210
AR 199,211
CV 24,192,193
CV 25,194,195
CV 26,196,197
CV 27,198,199
AR 200,196
AR 201,197
AR 202,198
AR 203,199
AR 204,212
AR 205,213
AR 206,214
AR 207,215
CV 56,200,201
CV 57,202,203
CV 58,204,205
CV 59,206,207
AR 192,200
AR 193,201
AR 194,202
AR 195,203
AR 196,216
AR 197,217
AR 198,218
AR 199,219
CV 88,192,193
CV 89,194,195
CV 90,196,197
CV 91,198,199
AR 200,204
AR 201,205
AR 202,206
AR 203,207
AR 204,220
AR 205,221
AR 206,222
AR 207,223
CV 120,200,201
CV 121,202,203
CV 122,204,205
s_waitcnt lgkmcnt(5)
MV0 160,176,0
RB 216,249,12288
CV 123,206,207
s_waitcnt lgkmcnt(5)
MV 160,180,4
RB 220,249,12800
AR 192,224
MV0 164,176,32
AR 193,225
AR 194,226
MV 164,180,36
AR 195,227
AR 196,240
MV0 168,176,64
AR 197,241
AR 198,242
MV 168,180,68
AR 199,243
CV 28,192,193
MV0 172,176,96
CV 29,194,195
CV 30,196,197
MV 172,180,100
s_waitcnt lgkmcnt(2)
CV 31,198,199
AR 200,228
MV0 232,176,224
RB 224,252,2048
AR 201,229
MV 232,180,228
AR 202,230
AR 203,231
MV 160,208,8
RB 176,249,14336
AR 204,244
MV 160,212,12
RB 180,249,14848
AR 205,245
MV 164,208,40
RB 228,252,3072
AR 206,246
MV 164,212,44
AR 207,247
CV 60,200,201
MV 168,208,72
CV 61,202,203
CV 62,204,205
MV 168,212,76
CV 63,206,207
AR 192,232
MV 172,208,104
AR 193,233
AR 194,234
MV 172,212,108
s_waitcnt lgkmcnt(0)
AR 195,235
AR 196,248
MV 232,208,224
RB 224,252,4096
AR 197,249
MV 232,212,228
AR 198,250
AR 199,251
MV 160,216,16
RB 208,249,0
CV 92,192,193
MV 160,220,20
RB 212,249,512
CV 93,194,195
MV 164,216,48
RB 228,252,5120
CV 94,196,197
MV 164,220,52
CV 95,198,199
AR 200,236
MV 168,216,80
AR 201,237
AR 202,238
MV 168,220,84
AR 203,239
AR 204,252
MV 172,216,112
AR 205,253
AR 206,254
MV 172,220,116
s_waitcnt lgkmcnt(0)
AR 207,255
CV 124,200,201
MV 232,216,224
RB 216,249,2048
CV 125,202,203
MV 232,220,228
CV 126,204,205
CV 127,206,207
MV 160,176,24
RB 220,249,2560
MV 160,180,28
RB 224,252,6144
MV 164,176,56
RB 228,252,7168
MV 164,180,60
MV 168,176,88
MV 168,180,92
MV 172,176,120
MV 172,180,124
s_waitcnt lgkmcnt(0)
MV 232,176,224
RB 224,249,4096
MV 232,180,228
MV0 176,208,0
RB 228,249,4608
MV 176,212,4
MV0 180,208,32
MV 180,212,36
MV0 184,208,64
v_max3_f32 v192, v160, v161, v162
v_max3_f32 v193, v164, v165, v166
MV 184,212,68
v_max3_f32 v194, v168, v169, v170
v_max3_f32 v195, v172, v173, v174
MV0 188,208,96
RB 208,249,6144
v_max3_f32 v196, v232, v233, v234
MV 188,212,100
v_max_f32 v192, v192, v163
v_max_f32 v193, v193, v167
MV 176,216,8
RB 212,249,6656
v_max_f32 v194, v194, v171
MV 176,220,12
v_max_f32 v195, v195, v175
v_max_f32 v196, v196, v235
MV 180,216,40
v_mov_b32 v197, v192
v_mov_b32 v198, v193
MV 180,220,44
v_mov_b32 v199, v194
v_mov_b32 v200, v195
MV 184,216,72
v_mov_b32 v201, v196
v_permlane16_swap_b32 v192, v197
MV 184,220,76
v_permlane16_swap_b32 v193, v198
v_permlane16_swap_b32 v194, v199
MV 188,216,104
v_permlane16_swap_b32 v195, v200
v_permlane16_swap_b32 v196, v201
MV 188,220,108
s_waitcnt lgkmcnt(3)
v_max_f32 v192, v192, v197
v_max_f32 v193, v193, v198
MV 176,224,16
s_waitcnt lgkmcnt(2)
v_max_f32 v194, v194, v199
v_max_f32 v195, v195, v200
MV 176,228,20
v_max_f32 v196, v196, v201
v_mov_b32 v197, v192
MV 180,224,48
v_mov_b32 v198, v193
v_mov_b32 v199, v194
MV 180,228,52
v_mov_b32 v200, v195
v_mov_b32 v201, v196
MV 184,224,80
v_permlane32_swap_b32 v192, v197
v_permlane32_swap_b32 v193, v198
MV 184,228,84
v_permlane32_swap_b32 v194, v199
v_permlane32_swap_b32 v195, v200
MV 188,224,112
v_permlane32_swap_b32 v196, v201
v_max_f32 v192, v192, v197
MV 188,228,116
s_waitcnt lgkmcnt(1)
v_max_f32 v193, v193, v198
v_max_f32 v194, v194, v199
MV 176,208,24
s_waitcnt lgkmcnt(0)
v_max_f32 v195, v195, v200
v_max_f32 v196, v196, v201
MV 176,212,28
v_mul_f32 v196, s66, v196
v_mul_f32 v236, s66, v192
MV 180,208,56
v_mul_f32 v237, s66, v193
v_mul_f32 v238, s66, v194
MV 180,212,60
v_mul_f32 v239, s66, v195
MV 184,208,88
ds_write_b32 v252, v196 offset:28676
MV 184,212,92
v_fma_f32 v160, v160, s63, v236
v_fma_f32 v161, v161, s63, v236
MV 188,208,120
v_fma_f32 v162, v162, s63, v236
v_fma_f32 v163, v163, s63, v236
MV 188,212,124
s_waitcnt vmcnt(12) lgkmcnt(0)
v_fma_f32 v164, v164, s63, v237
v_fma_f32 v165, v165, s63, v237
s_barrier
ds_read_b32 v246, v252 offset:28672
ds_read_b32 v248, v252 offset:28676
s_mov_b32 m0, 132096
s_nop 0
buffer_load_dwordx4 v253, s[44:47], s61 offen offset:0 nt lds
s_mov_b32 m0, 134016
s_nop 0
buffer_load_dwordx4 v253, s[44:47], s61 offen offset:128 nt lds
s_mov_b32 m0, 135936
s_nop 0
buffer_load_dwordx4 v253, s[44:47], s61 offen offset:256 nt lds
s_mov_b32 m0, 137856
s_nop 0
buffer_load_dwordx4 v253, s[44:47], s61 offen offset:384 nt lds
s_add_u32 s61, s61, 16384
s_mov_b32 s48, s24
s_and_b32 s49, s25, 0xffff
s_mov_b32 s50, 3670016
s_and_b32 s76, s26, 127
s_mul_i32 s76, s76, 7
v_fma_f32 v166, v166, s63, v237
v_fma_f32 v167, v167, s63, v237
v_fma_f32 v168, v168, s63, v238
v_fma_f32 v169, v169, s63, v238
v_fma_f32 v170, v170, s63, v238
v_fma_f32 v171, v171, s63, v238
v_fma_f32 v172, v172, s63, v239
v_fma_f32 v173, v173, s63, v239
v_fma_f32 v174, v174, s63, v239
v_fma_f32 v175, v175, s63, v239
v_exp_f32 v160, v160
v_exp_f32 v161, v161
v_exp_f32 v162, v162
v_exp_f32 v163, v163
v_exp_f32 v164, v164
v_exp_f32 v165, v165
v_exp_f32 v166, v166
v_exp_f32 v167, v167
v_exp_f32 v168, v168
v_exp_f32 v169, v169
v_exp_f32 v170, v170
v_exp_f32 v171, v171
v_exp_f32 v172, v172
v_exp_f32 v173, v173
v_exp_f32 v174, v174
v_exp_f32 v175, v175
s_waitcnt lgkmcnt(0)
v_min_f32 v240, v246, v248
v_mov_b32 v247, 0
v_fma_f32 v176, v176, s63, v236
v_fma_f32 v177, v177, s63, v236
v_fma_f32 v178, v178, s63, v236
v_fma_f32 v179, v179, s63, v236
v_fma_f32 v182, v182, s63, v237
v_fma_f32 v180, v180, s63, v237
v_fma_f32 v181, v181, s63, v237
v_fma_f32 v183, v183, s63, v237
v_fma_f32 v184, v184, s63, v238
v_fma_f32 v185, v185, s63, v238
v_fma_f32 v186, v186, s63, v238
v_fma_f32 v187, v187, s63, v238
v_fma_f32 v188, v188, s63, v239
v_fma_f32 v189, v189, s63, v239
v_fma_f32 v190, v190, s63, v239
v_fma_f32 v191, v191, s63, v239
v_max3_f32 v246, v176, v177, v178
v_max3_f32 v246, v246, v179, v182
v_max3_f32 v246, v246, v180, v181
v_max3_f32 v246, v246, v183, v184
v_max3_f32 v246, v246, v185, v186
v_max3_f32 v246, v246, v187, v188
v_max3_f32 v246, v246, v189, v190
v_max3_f32 v246, v246, v191, v247
v_cmp_lt_f32 vcc, s64, v246
v_fma_f32 v232, v232, s63, v240
v_fma_f32 v233, v233, s63, v240
v_fma_f32 v234, v234, s63, v240
v_fma_f32 v235, v235, s63, v240
s_cbranch_vccnz Lst1_0
Lrt1_0:
v_add_f32 v241, v241, v160
v_add_f32 v241, v241, v161
v_add_f32 v241, v241, v162
v_add_f32 v241, v241, v163
v_add_f32 v242, v242, v164
v_add_f32 v242, v242, v165
v_add_f32 v242, v242, v166
v_add_f32 v242, v242, v167
v_add_f32 v243, v243, v168
v_add_f32 v243, v243, v169
v_exp_f32 v176, v176
v_exp_f32 v177, v177
v_add_f32 v241, v241, v176
v_add_f32 v241, v241, v177
v_exp_f32 v178, v178
v_exp_f32 v179, v179
v_exp_f32 v180, v180
v_add_f32 v241, v241, v178
v_add_f32 v241, v241, v179
v_exp_f32 v181, v181
v_exp_f32 v182, v182
v_exp_f32 v183, v183
CV 163,162,163
CV 162,160,161
CV 160,176,177
CV 161,178,179
v_add_f32 v242, v242, v180
v_add_f32 v242, v242, v181
v_add_f32 v242, v242, v182
v_add_f32 v242, v242, v183
CV 167,166,167
CV 166,164,165
CV 164,180,181
CV 165,182,183
v_add_f32 v243, v243, v170
v_exp_f32 v232, v232
s_nop 0
v_add_f32 v245, v245, v232
v_exp_f32 v233, v233
v_exp_f32 v234, v234
v_add_f32 v245, v245, v233
RB 176,249,24576
RB 180,249,25088
RB 224,252,0
RB 208,249,26624
RB 212,249,27136
RB 228,252,1024
v_add_f32 v245, v245, v234
CV 254,232,233
v_exp_f32 v235, v235
s_nop 0
v_add_f32 v245, v245, v235
CV 255,234,235
v_add_f32 v243, v243, v171
CV 171,170,171
ds_write_b64 v252, v[254:255] offset:16392
CV 170,168,169
v_exp_f32 v184, v184
s_mov_b32 s65, 0
s_barrier
s_waitcnt lgkmcnt(6)
MV0 192,176,0
RB 216,249,28672
v_add_f32 v244, v244, v172
s_waitcnt lgkmcnt(6)
MV 192,180,4
RB 220,249,29184
v_add_f32 v244, v244, v173
MV0 196,176,32
s_cmp_lg_u32 s65, 0
v_exp_f32 v185, v185
MV 196,180,36
s_cbranch_scc1 Lst1_1
Lrt1_1:
v_exp_f32 v186, v186
MV0 200,176,64
v_exp_f32 v187, v187
MV 200,180,68
v_exp_f32 v188, v188
MV0 204,176,96
s_mov_b32 m0, 1024
v_exp_f32 v189, v189
MV 204,180,100
s_waitcnt lgkmcnt(3)
v_exp_f32 v190, v190
MV0 232,176,224
RB 224,252,2048
v_add_f32 v244, v244, v174
MV 232,180,228
v_exp_f32 v191, v191
MV 192,208,8
RB 176,249,30720
v_add_f32 v244, v244, v175
MV 192,212,12
RB 180,249,31232
v_add_f32 v243, v243, v184
MV 196,208,40
RB 228,252,3072
v_add_f32 v243, v243, v185
MV 196,212,44
buffer_load_dwordx4 v253, s[40:43], s60 offen offset:0 nt lds
MV 200,208,72
s_mov_b32 m0, 2944
v_add_f32 v243, v243, v186
v_add_f32 v243, v243, v187
MV 200,212,76
buffer_load_dwordx4 v253, s[40:43], s60 offen offset:128 nt lds
MV 204,208,104
v_add_f32 v244, v244, v188
v_add_f32 v244, v244, v189
MV 204,212,108
s_waitcnt lgkmcnt(0)
v_add_f32 v244, v244, v190
v_add_f32 v244, v244, v191
MV 232,208,224
RB 224,252,4096
CV 168,184,185
MV 232,212,228
CV 175,174,175
CV 174,172,173
MV 192,216,16
RB 208,249,16384
CV 169,186,187
MV 192,220,20
RB 212,249,16896
CV 172,188,189
MV 196,216,48
RB 228,252,5120
CV 173,190,191
MV 196,220,52
s_mov_b32 m0, 4864
MV 200,216,80
buffer_load_dwordx4 v253, s[40:43], s60 offen offset:256 nt lds
MV 200,220,84
MV 204,216,112
MV 204,220,116
s_waitcnt lgkmcnt(0)
MV 232,216,224
RB 216,249,18432
MV 232,220,228
MV 192,176,24
RB 220,249,18944
MV 192,180,28
RB 224,252,6144
MV 196,176,56
RB 228,252,7168
MV 196,180,60
s_mov_b32 m0, 6784
MV 200,176,88
buffer_load_dwordx4 v253, s[40:43], s60 offen offset:384 nt lds
s_add_u32 s60, s60, 16384
MV 200,180,92
v_fma_f32 v192, v192, s63, v236
v_fma_f32 v193, v193, s63, v236
MV 204,176,120
MV 204,180,124
s_waitcnt lgkmcnt(0)
v_fma_f32 v194, v194, s63, v236
v_fma_f32 v195, v195, s63, v236
MV 232,176,224
RB 224,249,20480
v_fma_f32 v196, v196, s63, v237
MV 232,180,228
v_fma_f32 v197, v197, s63, v237
v_fma_f32 v198, v198, s63, v237
MV0 176,208,0
RB 228,249,20992
v_fma_f32 v199, v199, s63, v237
MV 176,212,4
v_fma_f32 v200, v200, s63, v238
v_fma_f32 v201, v201, s63, v238
MV0 180,208,32
v_fma_f32 v202, v202, s63, v238
v_fma_f32 v203, v203, s63, v238
MV 180,212,36
v_fma_f32 v204, v204, s63, v239
MV0 184,208,64
v_fma_f32 v205, v205, s63, v239
v_fma_f32 v206, v206, s63, v239
MV 184,212,68
v_fma_f32 v207, v207, s63, v239
v_fma_f32 v232, v232, s63, v240
MV0 188,208,96
RB 208,249,22528
v_fma_f32 v233, v233, s63, v240
MV 188,212,100
v_fma_f32 v234, v234, s63, v240
v_fma_f32 v235, v235, s63, v240
MV 176,216,8
RB 212,249,23040
v_max3_f32 v246, v192, v193, v194
MV 176,220,12
v_max3_f32 v246, v246, v195, v196
v_max3_f32 v246, v246, v197, v198
MV 180,216,40
v_max3_f32 v246, v246, v199, v200
v_max3_f32 v246, v246, v201, v202
MV 180,220,44
v_max3_f32 v246, v246, v203, v204
MV 184,216,72
v_max3_f32 v246, v246, v205, v206
v_max3_f32 v246, v246, v207, v232
MV 184,220,76
v_max3_f32 v246, v246, v233, v234
v_max_f32 v246, v246, v235
MV 188,216,104
RT 216,250,0
v_cmp_lt_f32 vcc, s64, v246
MV 188,220,108
s_waitcnt lgkmcnt(3)
MV 176,224,16
RT 218,250,8192
MV 176,228,20
RT 220,251,0
MV 180,224,48
RT 222,251,8192
MV 180,228,52
s_cbranch_vccnz Lst1_2
Lrt1_2:
v_exp_f32 v192, v192
MV 184,224,80
v_exp_f32 v193, v193
MV 184,228,84
v_exp_f32 v194, v194
MV 188,224,112
RT 224,250,512
v_add_f32 v241, v241, v192
MV 188,228,116
s_waitcnt lgkmcnt(5)
v_exp_f32 v195, v195
MV 176,208,24
RT 226,250,8704
v_add_f32 v241, v241, v193
MV 176,212,28
RT 228,251,512
v_add_f32 v241, v241, v194
MV 180,208,56
RT 230,251,8704
v_add_f32 v241, v241, v195
MV 180,212,60
v_exp_f32 v196, v196
MV 184,208,88
v_exp_f32 v197, v197
MV 184,212,92
v_fma_f32 v176, v176, s63, v236
v_fma_f32 v177, v177, s63, v236
MV 188,208,120
s_waitcnt vmcnt(12) lgkmcnt(6)
v_fma_f32 v178, v178, s63, v236
v_fma_f32 v179, v179, s63, v236
MV 188,212,124
s_barrier
v_fma_f32 v182, v182, s63, v237
MA0 0,216,160
RT 208,250,4096
v_fma_f32 v180, v180, s63, v237
MA0 4,216,164
RT 210,250,12288
v_fma_f32 v181, v181, s63, v237
MA0 8,216,168
RT 212,251,4096
v_fma_f32 v183, v183, s63, v237
MA0 12,216,172
s_waitcnt lgkmcnt(7)
v_fma_f32 v184, v184, s63, v238
v_fma_f32 v185, v185, s63, v238
MA0 16,220,160
RT 214,251,12288
v_fma_f32 v186, v186, s63, v238
MA0 20,220,164
RT 216,250,2048
v_fma_f32 v187, v187, s63, v238
MA0 24,220,168
RT 218,250,10240
v_fma_f32 v188, v188, s63, v239
MA0 28,220,172
s_waitcnt lgkmcnt(8)
v_fma_f32 v189, v189, s63, v239
v_fma_f32 v190, v190, s63, v239
MA0 32,224,160
ds_read_b32 v247, v252 offset:29704
v_fma_f32 v191, v191, s63, v239
MA0 36,224,164
v_max3_f32 v246, v176, v177, v178
v_max3_f32 v246, v246, v179, v182
MA0 40,224,168
RT 220,251,2048
v_max3_f32 v246, v246, v180, v181
MA0 44,224,172
s_waitcnt lgkmcnt(8)
v_max3_f32 v246, v246, v183, v184
v_max3_f32 v246, v246, v185, v186
MA0 48,228,160
RT 222,251,10240
v_max3_f32 v246, v246, v187, v188
MA0 52,228,164
RB 224,252,16384
v_max3_f32 v246, v246, v189, v190
MA0 56,228,168
s_mov_b32 m0, 148480
v_exp_f32 v198, v198
MA0 60,228,172
s_waitcnt lgkmcnt(3)
v_exp_f32 v199, v199
MA0 128,208,160
RB 228,252,17408
v_max3_f32 v246, v246, v191, v247
MA0 132,208,164
buffer_load_dwordx4 v253, s[44:47], s61 offen offset:0 nt lds
MA0 136,208,168
v_cmp_lt_f32 vcc, s64, v246
v_add_f32 v242, v242, v196
MA0 140,208,172
v_add_f32 v242, v242, v197
v_add_f32 v242, v242, v198
MA0 144,212,160
RT 208,250,2560
v_add_f32 v242, v242, v199
MA0 148,212,164
RT 210,250,10752
MA0 152,212,168
s_mov_b32 m0, 150400
v_exp_f32 v200, v200
MA0 156,212,172
v_exp_f32 v201, v201
MA0 64,216,160
RT 212,251,2560
v_add_f32 v243, v243, v200
MA0 68,216,164
RT 214,251,10752
v_add_f32 v243, v243, v201
MA0 72,216,168
buffer_load_dwordx4 v253, s[44:47], s61 offen offset:128 nt lds
MA0 76,216,172
s_waitcnt lgkmcnt(4)
v_exp_f32 v202, v202
MV0 128,216,224
s_cbranch_vccnz Lst1_3
Lrt1_3:
v_exp_f32 v176, v176
MV0 132,216,228
v_exp_f32 v177, v177
MA0 80,220,160
RT 216,250,4608
v_add_f32 v241, v241, v176
MA0 84,220,164
RT 218,250,12800
v_add_f32 v241, v241, v177
MA0 88,220,168
s_mov_b32 m0, 152320
v_exp_f32 v178, v178
MA0 92,220,172
buffer_load_dwordx4 v253, s[44:47], s61 offen offset:256 nt lds
MV0 136,220,224
v_exp_f32 v179, v179
MV0 140,220,228
s_waitcnt lgkmcnt(4)
v_exp_f32 v180, v180
MA0 96,208,160
RT 220,251,4608
v_add_f32 v241, v241, v178
MA0 100,208,164
RT 222,251,12800
v_add_f32 v241, v241, v179
MA0 104,208,168
v_exp_f32 v181, v181
MA0 108,208,172
v_exp_f32 v182, v182
MV0 144,208,224
s_mov_b32 m0, 154240
v_exp_f32 v183, v183
MV0 148,208,228
s_waitcnt lgkmcnt(4)
CV 195,194,195
CV 194,192,193
MA0 112,212,160
RT 208,250,6144
CV 192,176,177
MA0 116,212,164
RT 210,250,14336
CV 193,178,179
MA0 120,212,168
buffer_load_dwordx4 v253, s[44:47], s61 offen offset:384 nt lds
s_add_u32 s61, s61, 16384
MA0 124,212,172
v_add_f32 v242, v242, v180
v_add_f32 v242, v242, v181
MV0 152,212,224
v_add_f32 v242, v242, v182
v_add_f32 v242, v242, v183
MV0 156,212,228
s_waitcnt lgkmcnt(4)
CV 199,198,199
MA0 160,216,160
RT 212,251,6144
CV 198,196,197
MA0 164,216,164
RT 214,251,14336
CV 196,180,181
MA0 168,216,168
CV 197,182,183
v_add_f32 v243, v243, v202
MA0 172,216,172
s_waitcnt lgkmcnt(4)
v_exp_f32 v232, v232
MA0 176,220,160
RT 216,250,6656
v_add_f32 v245, v245, v232
MA0 180,220,164
RT 218,250,14848
MA0 184,220,168
v_exp_f32 v233, v233
MA0 188,220,172
s_waitcnt lgkmcnt(4)
v_exp_f32 v234, v234
MA0 192,208,160
RT 220,251,6656
v_add_f32 v245, v245, v233
MA0 196,208,164
RB 176,249,40960
v_add_f32 v245, v245, v234
MA0 200,208,168
RB 180,249,41472
CV 254,232,233
MA0 204,208,172
s_waitcnt lgkmcnt(5)
v_exp_f32 v235, v235
MA0 208,212,160
RT 222,251,14848
v_add_f32 v245, v245, v235
MA0 212,212,164
RB 224,252,0
CV 255,234,235
MA0 216,212,168
RB 208,249,43008
MA0 220,212,172
s_waitcnt lgkmcnt(6)
v_exp_f32 v203, v203
MA0 224,216,160
RB 212,249,43520
v_add_f32 v243, v243, v203
MA0 228,216,164
RB 228,252,1024
CV 203,202,203
MA0 232,216,168
ds_write_b64 v252, v[254:255] offset:18440
CV 202,200,201
MA0 236,216,172
s_waitcnt lgkmcnt(5)
v_exp_f32 v204, v204
MA0 240,220,160
v_exp_f32 v205, v205
MA0 244,220,164
v_exp_f32 v206, v206
MA0 248,220,168
v_exp_f32 v207, v207
MA0 252,220,172
v_exp_f32 v184, v184
.p2align 6
Lloop1:
MV0 160,176,0
RB 216,249,45056
v_add_f32 v244, v244, v204
MV 160,180,4
RB 220,249,45568
v_add_f32 v244, v244, v205
MV0 164,176,32
s_cmp_lg_u32 s65, 0
v_exp_f32 v185, v185
MV 164,180,36
s_cbranch_scc1 Lst1_4
Lrt1_4:
v_exp_f32 v186, v186
MV0 168,176,64
s_lshl_b32 s78, s62, 20
v_exp_f32 v187, v187
MV 168,180,68
s_add_u32 s78, s78, Lpeel1-Lbase
v_exp_f32 v188, v188
MV0 172,176,96
s_mov_b32 m0, 17408
v_exp_f32 v189, v189
MV 172,180,100
s_waitcnt lgkmcnt(3)
v_exp_f32 v190, v190
MV0 232,176,224
RB 224,252,2048
v_add_f32 v244, v244, v206
MV 232,180,228
v_exp_f32 v191, v191
MV 160,208,8
RB 176,249,47104
v_add_f32 v244, v244, v207
MV 160,212,12
RB 180,249,47616
v_add_f32 v243, v243, v184
MV 164,208,40
RB 228,252,3072
v_add_f32 v243, v243, v185
MV 164,212,44
buffer_load_dwordx4 v253, s[40:43], s60 offen offset:0 nt lds
MV 168,208,72
s_mov_b32 m0, 19328
v_add_f32 v243, v243, v186
v_add_f32 v243, v243, v187
MV 168,212,76
buffer_load_dwordx4 v253, s[40:43], s60 offen offset:128 nt lds
MV 172,208,104
v_add_f32 v244, v244, v188
v_add_f32 v244, v244, v189
MV 172,212,108
s_waitcnt lgkmcnt(0)
v_add_f32 v244, v244, v190
v_add_f32 v244, v244, v191
MV 232,208,224
RB 224,252,4096
CV 200,184,185
MV 232,212,228
CV 207,206,207
CV 206,204,205
MV 160,216,16
RB 208,249,32768
CV 201,186,187
MV 160,220,20
RB 212,249,33280
CV 204,188,189
MV 164,216,48
RB 228,252,5120
CV 205,190,191
MV 164,220,52
s_mov_b32 m0, 21248
MV 168,216,80
buffer_load_dwordx4 v253, s[40:43], s60 offen offset:256 nt lds
MV 168,220,84
MV 172,216,112
MV 172,220,116
s_waitcnt lgkmcnt(0)
MV 232,216,224
RB 216,249,34816
v_subrev_u32 v248, 0x10000, v252
MV 232,220,228
MV 160,176,24
RB 220,249,35328
v_lshlrev_b32 v248, 3, v248
MV 160,180,28
RB 224,252,6144
MV 164,176,56
RB 228,252,7168
MV 164,180,60
s_mov_b32 m0, 23168
MV 168,176,88
buffer_load_dwordx4 v253, s[40:43], s60 offen offset:384 nt lds
s_add_u32 s60, s60, 16384
MV 168,180,92
s_mov_b32 m0, 97536
v_fma_f32 v160, v160, s63, v236
v_fma_f32 v161, v161, s63, v236
MV 172,176,120
buffer_load_dword v248, s[56:59], s78 offen lds
MV 172,180,124
s_waitcnt lgkmcnt(0)
v_fma_f32 v162, v162, s63, v236
v_fma_f32 v163, v163, s63, v236
MV 232,176,224
RB 224,249,36864
v_fma_f32 v164, v164, s63, v237
MV 232,180,228
v_fma_f32 v165, v165, s63, v237
v_fma_f32 v166, v166, s63, v237
MV0 176,208,0
RB 228,249,37376
v_fma_f32 v167, v167, s63, v237
MV 176,212,4
v_fma_f32 v168, v168, s63, v238
v_fma_f32 v169, v169, s63, v238
MV0 180,208,32
v_fma_f32 v170, v170, s63, v238
v_fma_f32 v171, v171, s63, v238
MV 180,212,36
v_fma_f32 v172, v172, s63, v239
MV0 184,208,64
v_fma_f32 v173, v173, s63, v239
v_fma_f32 v174, v174, s63, v239
MV 184,212,68
v_fma_f32 v175, v175, s63, v239
v_fma_f32 v232, v232, s63, v240
MV0 188,208,96
RB 208,249,38912
v_fma_f32 v233, v233, s63, v240
MV 188,212,100
v_fma_f32 v234, v234, s63, v240
v_fma_f32 v235, v235, s63, v240
MV 176,216,8
RB 212,249,39424
v_max3_f32 v246, v160, v161, v162
MV 176,220,12
v_max3_f32 v246, v246, v163, v164
v_max3_f32 v246, v246, v165, v166
MV 180,216,40
v_max3_f32 v246, v246, v167, v168
v_max3_f32 v246, v246, v169, v170
MV 180,220,44
v_max3_f32 v246, v246, v171, v172
MV 184,216,72
v_max3_f32 v246, v246, v173, v174
v_max3_f32 v246, v246, v175, v232
MV 184,220,76
v_max3_f32 v246, v246, v233, v234
v_max_f32 v246, v246, v235
MV 188,216,104
RT 216,250,16384
v_cmp_lt_f32 vcc, s64, v246
MV 188,220,108
s_waitcnt lgkmcnt(3)
MV 176,224,16
RT 218,250,24576
MV 176,228,20
RT 220,251,16384
MV 180,224,48
RT 222,251,24576
MV 180,228,52
s_cbranch_vccnz Lst1_5
Lrt1_5:
v_exp_f32 v160, v160
MV 184,224,80
v_exp_f32 v161, v161
MV 184,228,84
v_exp_f32 v162, v162
MV 188,224,112
RT 224,250,16896
v_add_f32 v241, v241, v160
MV 188,228,116
s_waitcnt lgkmcnt(5)
v_exp_f32 v163, v163
MV 176,208,24
RT 226,250,25088
v_add_f32 v241, v241, v161
MV 176,212,28
RT 228,251,16896
v_add_f32 v241, v241, v162
MV 180,208,56
RT 230,251,25088
v_add_f32 v241, v241, v163
MV 180,212,60
v_exp_f32 v164, v164
MV 184,208,88
v_exp_f32 v165, v165
MV 184,212,92
v_fma_f32 v176, v176, s63, v236
v_fma_f32 v177, v177, s63, v236
MV 188,208,120
s_waitcnt vmcnt(13) lgkmcnt(6)
v_fma_f32 v178, v178, s63, v236
v_fma_f32 v179, v179, s63, v236
MV 188,212,124
s_barrier
v_fma_f32 v182, v182, s63, v237
MA 0,216,192
RT 208,250,20480
v_fma_f32 v180, v180, s63, v237
MA 4,216,196
RT 210,250,28672
v_fma_f32 v181, v181, s63, v237
MA 8,216,200
RT 212,251,20480
v_fma_f32 v183, v183, s63, v237
MA 12,216,204
s_waitcnt lgkmcnt(7)
v_fma_f32 v184, v184, s63, v238
v_fma_f32 v185, v185, s63, v238
MA 16,220,192
RT 214,251,28672
v_fma_f32 v186, v186, s63, v238
MA 20,220,196
RT 216,250,18432
v_fma_f32 v187, v187, s63, v238
MA 24,220,200
RT 218,250,26624
v_fma_f32 v188, v188, s63, v239
MA 28,220,204
s_waitcnt lgkmcnt(8)
v_fma_f32 v189, v189, s63, v239
v_fma_f32 v190, v190, s63, v239
MA 32,224,192
ds_read_b32 v247, v252 offset:29704
v_fma_f32 v191, v191, s63, v239
MA 36,224,196
v_max3_f32 v246, v176, v177, v178
v_max3_f32 v246, v246, v179, v182
MA 40,224,200
RT 220,251,18432
v_max3_f32 v246, v246, v180, v181
MA 44,224,204
s_waitcnt lgkmcnt(8)
v_max3_f32 v246, v246, v183, v184
v_max3_f32 v246, v246, v185, v186
MA 48,228,192
RT 222,251,26624
v_max3_f32 v246, v246, v187, v188
MA 52,228,196
RB 224,252,18432
v_max3_f32 v246, v246, v189, v190
MA 56,228,200
s_mov_b32 m0, 99328
v_exp_f32 v166, v166
MA 60,228,204
s_waitcnt lgkmcnt(3)
v_exp_f32 v167, v167
MA 128,208,192
RB 228,252,19456
v_max3_f32 v246, v246, v191, v247
MA 132,208,196
buffer_load_dwordx4 v253, s[44:47], s61 offen offset:0 nt lds
MA 136,208,200
v_cmp_lt_f32 vcc, s64, v246
v_add_f32 v242, v242, v164
MA 140,208,204
v_add_f32 v242, v242, v165
v_add_f32 v242, v242, v166
MA 144,212,192
RT 208,250,18944
v_add_f32 v242, v242, v167
MA 148,212,196
RT 210,250,27136
MA 152,212,200
s_mov_b32 m0, 101248
v_exp_f32 v168, v168
MA 156,212,204
v_exp_f32 v169, v169
MA 64,216,192
RT 212,251,18944
v_add_f32 v243, v243, v168
MA 68,216,196
RT 214,251,27136
v_add_f32 v243, v243, v169
MA 72,216,200
buffer_load_dwordx4 v253, s[44:47], s61 offen offset:128 nt lds
MA 76,216,204
s_waitcnt lgkmcnt(4)
v_exp_f32 v170, v170
MV 128,216,224
s_cbranch_vccnz Lst1_6
Lrt1_6:
v_exp_f32 v176, v176
MV 132,216,228
v_exp_f32 v177, v177
MA 80,220,192
RT 216,250,20992
v_add_f32 v241, v241, v176
MA 84,220,196
RT 218,250,29184
v_add_f32 v241, v241, v177
MA 88,220,200
s_mov_b32 m0, 103168
v_exp_f32 v178, v178
MA 92,220,204
buffer_load_dwordx4 v253, s[44:47], s61 offen offset:256 nt lds
MV 136,220,224
v_exp_f32 v179, v179
MV 140,220,228
s_waitcnt lgkmcnt(4)
v_exp_f32 v180, v180
MA 96,208,192
RT 220,251,20992
v_add_f32 v241, v241, v178
MA 100,208,196
RT 222,251,29184
v_add_f32 v241, v241, v179
MA 104,208,200
v_exp_f32 v181, v181
MA 108,208,204
v_exp_f32 v182, v182
MV 144,208,224
s_mov_b32 m0, 105088
v_exp_f32 v183, v183
MV 148,208,228
s_waitcnt lgkmcnt(4)
CV 163,162,163
CV 162,160,161
MA 112,212,192
RT 208,250,22528
CV 160,176,177
MA 116,212,196
RT 210,250,30720
CV 161,178,179
MA 120,212,200
buffer_load_dwordx4 v253, s[44:47], s61 offen offset:384 nt lds
s_add_u32 s61, s61, 16384
MA 124,212,204
v_add_f32 v242, v242, v180
v_add_f32 v242, v242, v181
MV 152,212,224
v_add_f32 v242, v242, v182
v_add_f32 v242, v242, v183
MV 156,212,228
s_waitcnt lgkmcnt(4)
CV 167,166,167
MA 160,216,192
RT 212,251,22528
CV 166,164,165
MA 164,216,196
RT 214,251,30720
CV 164,180,181
MA 168,216,200
CV 165,182,183
v_add_f32 v243, v243, v170
MA 172,216,204
s_waitcnt lgkmcnt(4)
v_exp_f32 v232, v232
MA 176,220,192
RT 216,250,23040
v_add_f32 v245, v245, v232
MA 180,220,196
RT 218,250,31232
MA 184,220,200
v_exp_f32 v233, v233
MA 188,220,204
s_waitcnt lgkmcnt(4)
v_exp_f32 v234, v234
MA 192,208,192
RT 220,251,23040
v_add_f32 v245, v245, v233
MA 196,208,196
RB 176,249,57344
v_add_f32 v245, v245, v234
MA 200,208,200
RB 180,249,57856
CV 254,232,233
MA 204,208,204
s_waitcnt lgkmcnt(5)
v_exp_f32 v235, v235
MA 208,212,192
RT 222,251,31232
v_add_f32 v245, v245, v235
MA 212,212,196
RB 224,252,0
CV 255,234,235
MA 216,212,200
RB 208,249,59392
MA 220,212,204
s_waitcnt lgkmcnt(6)
v_exp_f32 v171, v171
MA 224,216,192
RB 212,249,59904
v_add_f32 v243, v243, v171
MA 228,216,196
RB 228,252,1024
CV 171,170,171
MA 232,216,200
ds_write_b64 v252, v[254:255] offset:16392
CV 170,168,169
MA 236,216,204
s_waitcnt lgkmcnt(5)
v_exp_f32 v172, v172
MA 240,220,192
v_exp_f32 v173, v173
MA 244,220,196
v_exp_f32 v174, v174
MA 248,220,200
v_exp_f32 v175, v175
MA 252,220,204
v_exp_f32 v184, v184
MV0 192,176,0
RB 216,249,61440
v_add_f32 v244, v244, v172
MV 192,180,4
RB 220,249,61952
v_add_f32 v244, v244, v173
MV0 196,176,32
s_cmp_lg_u32 s65, 0
v_exp_f32 v185, v185
MV 196,180,36
s_cbranch_scc1 Lst1_7
Lrt1_7:
v_exp_f32 v186, v186
MV0 200,176,64
s_lshl_b32 s78, s62, 20
v_exp_f32 v187, v187
MV 200,180,68
s_add_u32 s78, s78, Lpeel1-Lbase+8192
v_exp_f32 v188, v188
MV0 204,176,96
s_mov_b32 m0, 33792
v_exp_f32 v189, v189
MV 204,180,100
s_waitcnt lgkmcnt(3)
v_exp_f32 v190, v190
MV0 232,176,224
RB 224,252,2048
v_add_f32 v244, v244, v174
MV 232,180,228
v_exp_f32 v191, v191
MV 192,208,8
RB 176,249,63488
v_add_f32 v244, v244, v175
MV 192,212,12
RB 180,249,64000
v_add_f32 v243, v243, v184
MV 196,208,40
RB 228,252,3072
v_add_f32 v243, v243, v185
MV 196,212,44
buffer_load_dwordx4 v253, s[40:43], s60 offen offset:0 nt lds
MV 200,208,72
s_mov_b32 m0, 35712
v_add_f32 v243, v243, v186
v_add_f32 v243, v243, v187
MV 200,212,76
buffer_load_dwordx4 v253, s[40:43], s60 offen offset:128 nt lds
MV 204,208,104
v_add_f32 v244, v244, v188
v_add_f32 v244, v244, v189
MV 204,212,108
s_waitcnt lgkmcnt(0)
v_add_f32 v244, v244, v190
v_add_f32 v244, v244, v191
MV 232,208,224
RB 224,252,4096
CV 168,184,185
MV 232,212,228
CV 175,174,175
CV 174,172,173
MV 192,216,16
RB 208,249,49152
CV 169,186,187
MV 192,220,20
RB 212,249,49664
CV 172,188,189
MV 196,216,48
RB 228,252,5120
CV 173,190,191
MV 196,220,52
s_mov_b32 m0, 37632
MV 200,216,80
buffer_load_dwordx4 v253, s[40:43], s60 offen offset:256 nt lds
MV 200,220,84
MV 204,216,112
MV 204,220,116
s_waitcnt lgkmcnt(0)
MV 232,216,224
RB 216,249,51200
v_subrev_u32 v248, 0x10000, v252
MV 232,220,228
MV 192,176,24
RB 220,249,51712
v_lshlrev_b32 v248, 3, v248
MV 192,180,28
RB 224,252,6144
MV 196,176,56
RB 228,252,7168
MV 196,180,60
s_mov_b32 m0, 39552
MV 200,176,88
buffer_load_dwordx4 v253, s[40:43], s60 offen offset:384 nt lds
s_add_u32 s60, s60, 16384
MV 200,180,92
s_mov_b32 m0, 97536
v_fma_f32 v192, v192, s63, v236
v_fma_f32 v193, v193, s63, v236
MV 204,176,120
buffer_load_dword v248, s[56:59], s78 offen lds
MV 204,180,124
s_waitcnt lgkmcnt(0)
v_fma_f32 v194, v194, s63, v236
v_fma_f32 v195, v195, s63, v236
MV 232,176,224
RB 224,249,53248
v_fma_f32 v196, v196, s63, v237
MV 232,180,228
v_fma_f32 v197, v197, s63, v237
v_fma_f32 v198, v198, s63, v237
MV0 176,208,0
RB 228,249,53760
v_fma_f32 v199, v199, s63, v237
MV 176,212,4
v_fma_f32 v200, v200, s63, v238
v_fma_f32 v201, v201, s63, v238
MV0 180,208,32
v_fma_f32 v202, v202, s63, v238
v_fma_f32 v203, v203, s63, v238
MV 180,212,36
v_fma_f32 v204, v204, s63, v239
MV0 184,208,64
v_fma_f32 v205, v205, s63, v239
v_fma_f32 v206, v206, s63, v239
MV 184,212,68
v_fma_f32 v207, v207, s63, v239
v_fma_f32 v232, v232, s63, v240
MV0 188,208,96
RB 208,249,55296
v_fma_f32 v233, v233, s63, v240
MV 188,212,100
v_fma_f32 v234, v234, s63, v240
v_fma_f32 v235, v235, s63, v240
MV 176,216,8
RB 212,249,55808
v_max3_f32 v246, v192, v193, v194
MV 176,220,12
v_max3_f32 v246, v246, v195, v196
v_max3_f32 v246, v246, v197, v198
MV 180,216,40
v_max3_f32 v246, v246, v199, v200
v_max3_f32 v246, v246, v201, v202
MV 180,220,44
v_max3_f32 v246, v246, v203, v204
MV 184,216,72
v_max3_f32 v246, v246, v205, v206
v_max3_f32 v246, v246, v207, v232
MV 184,220,76
v_max3_f32 v246, v246, v233, v234
v_max_f32 v246, v246, v235
MV 188,216,104
RT 216,250,32768
v_cmp_lt_f32 vcc, s64, v246
MV 188,220,108
s_waitcnt lgkmcnt(3)
MV 176,224,16
RT 218,250,40960
MV 176,228,20
RT 220,251,32768
MV 180,224,48
RT 222,251,40960
MV 180,228,52
s_cbranch_vccnz Lst1_8
Lrt1_8:
v_exp_f32 v192, v192
MV 184,224,80
v_exp_f32 v193, v193
MV 184,228,84
v_exp_f32 v194, v194
MV 188,224,112
RT 224,250,33280
v_add_f32 v241, v241, v192
MV 188,228,116
s_waitcnt lgkmcnt(5)
v_exp_f32 v195, v195
MV 176,208,24
RT 226,250,41472
v_add_f32 v241, v241, v193
MV 176,212,28
RT 228,251,33280
v_add_f32 v241, v241, v194
MV 180,208,56
RT 230,251,41472
v_add_f32 v241, v241, v195
MV 180,212,60
v_exp_f32 v196, v196
MV 184,208,88
v_exp_f32 v197, v197
MV 184,212,92
v_fma_f32 v176, v176, s63, v236
v_fma_f32 v177, v177, s63, v236
MV 188,208,120
s_waitcnt vmcnt(14) lgkmcnt(6)
v_fma_f32 v178, v178, s63, v236
v_fma_f32 v179, v179, s63, v236
MV 188,212,124
s_barrier
v_fma_f32 v182, v182, s63, v237
MA 0,216,160
RT 208,250,36864
v_fma_f32 v180, v180, s63, v237
MA 4,216,164
RT 210,250,45056
v_fma_f32 v181, v181, s63, v237
MA 8,216,168
RT 212,251,36864
v_fma_f32 v183, v183, s63, v237
MA 12,216,172
s_waitcnt lgkmcnt(7)
v_fma_f32 v184, v184, s63, v238
v_fma_f32 v185, v185, s63, v238
MA 16,220,160
RT 214,251,45056
v_fma_f32 v186, v186, s63, v238
MA 20,220,164
RT 216,250,34816
v_fma_f32 v187, v187, s63, v238
MA 24,220,168
RT 218,250,43008
v_fma_f32 v188, v188, s63, v239
MA 28,220,172
s_waitcnt lgkmcnt(8)
v_fma_f32 v189, v189, s63, v239
v_fma_f32 v190, v190, s63, v239
MA 32,224,160
ds_read_b32 v247, v252 offset:29704
v_fma_f32 v191, v191, s63, v239
MA 36,224,164
v_max3_f32 v246, v176, v177, v178
v_max3_f32 v246, v246, v179, v182
MA 40,224,168
RT 220,251,34816
v_max3_f32 v246, v246, v180, v181
MA 44,224,172
s_waitcnt lgkmcnt(8)
v_max3_f32 v246, v246, v183, v184
v_max3_f32 v246, v246, v185, v186
MA 48,228,160
RT 222,251,43008
v_max3_f32 v246, v246, v187, v188
MA 52,228,164
RB 224,252,16384
v_max3_f32 v246, v246, v189, v190
MA 56,228,168
s_mov_b32 m0, 115712
v_exp_f32 v198, v198
MA 60,228,172
s_waitcnt lgkmcnt(3)
v_exp_f32 v199, v199
MA 128,208,160
RB 228,252,17408
v_max3_f32 v246, v246, v191, v247
MA 132,208,164
buffer_load_dwordx4 v253, s[44:47], s61 offen offset:0 nt lds
MA 136,208,168
v_cmp_lt_f32 vcc, s64, v246
v_add_f32 v242, v242, v196
MA 140,208,172
v_add_f32 v242, v242, v197
v_add_f32 v242, v242, v198
MA 144,212,160
RT 208,250,35328
v_add_f32 v242, v242, v199
MA 148,212,164
RT 210,250,43520
MA 152,212,168
s_mov_b32 m0, 117632
v_exp_f32 v200, v200
MA 156,212,172
v_exp_f32 v201, v201
MA 64,216,160
RT 212,251,35328
v_add_f32 v243, v243, v200
MA 68,216,164
RT 214,251,43520
v_add_f32 v243, v243, v201
MA 72,216,168
buffer_load_dwordx4 v253, s[44:47], s61 offen offset:128 nt lds
MA 76,216,172
s_waitcnt lgkmcnt(4)
v_exp_f32 v202, v202
MV 128,216,224
s_cbranch_vccnz Lst1_9
Lrt1_9:
v_exp_f32 v176, v176
MV 132,216,228
v_exp_f32 v177, v177
MA 80,220,160
RT 216,250,37376
v_add_f32 v241, v241, v176
MA 84,220,164
RT 218,250,45568
v_add_f32 v241, v241, v177
MA 88,220,168
s_mov_b32 m0, 119552
v_exp_f32 v178, v178
MA 92,220,172
buffer_load_dwordx4 v253, s[44:47], s61 offen offset:256 nt lds
MV 136,220,224
v_exp_f32 v179, v179
MV 140,220,228
s_waitcnt lgkmcnt(4)
v_exp_f32 v180, v180
MA 96,208,160
RT 220,251,37376
v_add_f32 v241, v241, v178
MA 100,208,164
RT 222,251,45568
v_add_f32 v241, v241, v179
MA 104,208,168
v_exp_f32 v181, v181
MA 108,208,172
v_exp_f32 v182, v182
MV 144,208,224
s_mov_b32 m0, 121472
v_exp_f32 v183, v183
MV 148,208,228
s_waitcnt lgkmcnt(4)
CV 195,194,195
CV 194,192,193
MA 112,212,160
RT 208,250,38912
CV 192,176,177
MA 116,212,164
RT 210,250,47104
CV 193,178,179
MA 120,212,168
buffer_load_dwordx4 v253, s[44:47], s61 offen offset:384 nt lds
s_add_u32 s61, s61, 16384
MA 124,212,172
v_add_f32 v242, v242, v180
v_add_f32 v242, v242, v181
MV 152,212,224
v_add_f32 v242, v242, v182
v_add_f32 v242, v242, v183
MV 156,212,228
s_waitcnt lgkmcnt(4)
CV 199,198,199
MA 160,216,160
RT 212,251,38912
CV 198,196,197
MA 164,216,164
RT 214,251,47104
CV 196,180,181
MA 168,216,168
CV 197,182,183
v_add_f32 v243, v243, v202
MA 172,216,172
s_waitcnt lgkmcnt(4)
v_exp_f32 v232, v232
MA 176,220,160
RT 216,250,39424
v_add_f32 v245, v245, v232
MA 180,220,164
RT 218,250,47616
MA 184,220,168
v_exp_f32 v233, v233
MA 188,220,172
s_waitcnt lgkmcnt(4)
v_exp_f32 v234, v234
MA 192,208,160
RT 220,251,39424
v_add_f32 v245, v245, v233
MA 196,208,164
RB 176,249,8192
v_add_f32 v245, v245, v234
MA 200,208,168
RB 180,249,8704
CV 254,232,233
MA 204,208,172
s_waitcnt lgkmcnt(5)
v_exp_f32 v235, v235
MA 208,212,160
RT 222,251,47616
v_add_f32 v245, v245, v235
MA 212,212,164
RB 224,252,0
CV 255,234,235
MA 216,212,168
RB 208,249,10240
MA 220,212,172
s_waitcnt lgkmcnt(6)
v_exp_f32 v203, v203
MA 224,216,160
RB 212,249,10752
v_add_f32 v243, v243, v203
MA 228,216,164
RB 228,252,1024
CV 203,202,203
MA 232,216,168
ds_write_b64 v252, v[254:255] offset:18440
CV 202,200,201
MA 236,216,172
s_waitcnt lgkmcnt(5)
v_exp_f32 v204, v204
MA 240,220,160
v_exp_f32 v205, v205
MA 244,220,164
v_exp_f32 v206, v206
MA 248,220,168
v_exp_f32 v207, v207
MA 252,220,172
v_exp_f32 v184, v184
MV0 160,176,0
RB 216,249,12288
v_add_f32 v244, v244, v204
MV 160,180,4
RB 220,249,12800
v_add_f32 v244, v244, v205
MV0 164,176,32
s_cmp_lg_u32 s65, 0
v_exp_f32 v185, v185
MV 164,180,36
s_cbranch_scc1 Lst1_10
Lrt1_10:
v_exp_f32 v186, v186
MV0 168,176,64
s_lshl_b32 s78, s62, 20
v_exp_f32 v187, v187
MV 168,180,68
s_add_u32 s78, s78, LT-Lbase+8192
v_exp_f32 v188, v188
MV0 172,176,96
s_mov_b32 m0, 50176
v_exp_f32 v189, v189
MV 172,180,100
s_waitcnt lgkmcnt(3)
v_exp_f32 v190, v190
MV0 232,176,224
RB 224,252,2048
v_add_f32 v244, v244, v206
MV 232,180,228
v_exp_f32 v191, v191
MV 160,208,8
RB 176,249,14336
v_add_f32 v244, v244, v207
MV 160,212,12
RB 180,249,14848
v_add_f32 v243, v243, v184
MV 164,208,40
RB 228,252,3072
v_add_f32 v243, v243, v185
MV 164,212,44
buffer_load_dwordx4 v253, s[40:43], s60 offen offset:0 nt lds
MV 168,208,72
s_mov_b32 m0, 52096
v_add_f32 v243, v243, v186
v_add_f32 v243, v243, v187
MV 168,212,76
buffer_load_dwordx4 v253, s[40:43], s60 offen offset:128 nt lds
MV 172,208,104
v_add_f32 v244, v244, v188
v_add_f32 v244, v244, v189
MV 172,212,108
s_waitcnt lgkmcnt(0)
v_add_f32 v244, v244, v190
v_add_f32 v244, v244, v191
MV 232,208,224
RB 224,252,4096
CV 200,184,185
MV 232,212,228
CV 207,206,207
CV 206,204,205
MV 160,216,16
RB 208,249,0
CV 201,186,187
MV 160,220,20
RB 212,249,512
CV 204,188,189
MV 164,216,48
RB 228,252,5120
CV 205,190,191
MV 164,220,52
s_mov_b32 m0, 54016
MV 168,216,80
buffer_load_dwordx4 v253, s[40:43], s60 offen offset:256 nt lds
MV 168,220,84
MV 172,216,112
MV 172,220,116
s_waitcnt lgkmcnt(0)
MV 232,216,224
RB 216,249,2048
v_subrev_u32 v248, 0x10000, v252
MV 232,220,228
MV 160,176,24
RB 220,249,2560
v_lshlrev_b32 v248, 3, v248
MV 160,180,28
RB 224,252,6144
MV 164,176,56
RB 228,252,7168
MV 164,180,60
s_mov_b32 m0, 55936
MV 168,176,88
buffer_load_dwordx4 v253, s[40:43], s60 offen offset:384 nt lds
s_add_u32 s60, s60, 16384
MV 168,180,92
s_mov_b32 m0, 97536
v_fma_f32 v160, v160, s63, v236
v_fma_f32 v161, v161, s63, v236
MV 172,176,120
buffer_load_dword v248, s[56:59], s78 offen lds
MV 172,180,124
s_waitcnt lgkmcnt(0)
v_fma_f32 v162, v162, s63, v236
v_fma_f32 v163, v163, s63, v236
MV 232,176,224
RB 224,249,4096
v_fma_f32 v164, v164, s63, v237
MV 232,180,228
v_fma_f32 v165, v165, s63, v237
v_fma_f32 v166, v166, s63, v237
MV0 176,208,0
RB 228,249,4608
v_fma_f32 v167, v167, s63, v237
MV 176,212,4
v_fma_f32 v168, v168, s63, v238
v_fma_f32 v169, v169, s63, v238
MV0 180,208,32
v_fma_f32 v170, v170, s63, v238
v_fma_f32 v171, v171, s63, v238
MV 180,212,36
v_fma_f32 v172, v172, s63, v239
MV0 184,208,64
v_fma_f32 v173, v173, s63, v239
v_fma_f32 v174, v174, s63, v239
MV 184,212,68
v_fma_f32 v175, v175, s63, v239
v_fma_f32 v232, v232, s63, v240
MV0 188,208,96
RB 208,249,6144
v_fma_f32 v233, v233, s63, v240
MV 188,212,100
v_fma_f32 v234, v234, s63, v240
v_fma_f32 v235, v235, s63, v240
MV 176,216,8
RB 212,249,6656
v_max3_f32 v246, v160, v161, v162
MV 176,220,12
v_max3_f32 v246, v246, v163, v164
v_max3_f32 v246, v246, v165, v166
MV 180,216,40
v_max3_f32 v246, v246, v167, v168
v_max3_f32 v246, v246, v169, v170
MV 180,220,44
v_max3_f32 v246, v246, v171, v172
MV 184,216,72
v_max3_f32 v246, v246, v173, v174
v_max3_f32 v246, v246, v175, v232
MV 184,220,76
v_max3_f32 v246, v246, v233, v234
v_max_f32 v246, v246, v235
MV 188,216,104
RT 216,250,49152
v_cmp_lt_f32 vcc, s64, v246
MV 188,220,108
s_waitcnt lgkmcnt(3)
MV 176,224,16
RT 218,250,57344
MV 176,228,20
RT 220,251,49152
MV 180,224,48
RT 222,251,57344
MV 180,228,52
s_cbranch_vccnz Lst1_11
Lrt1_11:
v_exp_f32 v160, v160
MV 184,224,80
v_exp_f32 v161, v161
MV 184,228,84
v_exp_f32 v162, v162
MV 188,224,112
RT 224,250,49664
v_add_f32 v241, v241, v160
MV 188,228,116
s_waitcnt lgkmcnt(5)
v_exp_f32 v163, v163
MV 176,208,24
RT 226,250,57856
v_add_f32 v241, v241, v161
MV 176,212,28
RT 228,251,49664
v_add_f32 v241, v241, v162
MV 180,208,56
RT 230,251,57856
v_add_f32 v241, v241, v163
MV 180,212,60
v_exp_f32 v164, v164
MV 184,208,88
v_exp_f32 v165, v165
MV 184,212,92
v_fma_f32 v176, v176, s63, v236
v_fma_f32 v177, v177, s63, v236
MV 188,208,120
s_waitcnt vmcnt(14) lgkmcnt(6)
v_fma_f32 v178, v178, s63, v236
v_fma_f32 v179, v179, s63, v236
MV 188,212,124
s_barrier
v_fma_f32 v182, v182, s63, v237
MA 0,216,192
RT 208,250,53248
v_fma_f32 v180, v180, s63, v237
MA 4,216,196
RT 210,250,61440
v_fma_f32 v181, v181, s63, v237
MA 8,216,200
RT 212,251,53248
v_fma_f32 v183, v183, s63, v237
MA 12,216,204
s_waitcnt lgkmcnt(7)
v_fma_f32 v184, v184, s63, v238
v_fma_f32 v185, v185, s63, v238
MA 16,220,192
RT 214,251,61440
v_fma_f32 v186, v186, s63, v238
MA 20,220,196
RT 216,250,51200
v_fma_f32 v187, v187, s63, v238
MA 24,220,200
RT 218,250,59392
v_fma_f32 v188, v188, s63, v239
MA 28,220,204
s_waitcnt lgkmcnt(8)
v_fma_f32 v189, v189, s63, v239
v_fma_f32 v190, v190, s63, v239
MA 32,224,192
ds_read_b32 v247, v252 offset:29704
v_fma_f32 v191, v191, s63, v239
MA 36,224,196
v_max3_f32 v246, v176, v177, v178
v_max3_f32 v246, v246, v179, v182
MA 40,224,200
RT 220,251,51200
v_max3_f32 v246, v246, v180, v181
MA 44,224,204
s_waitcnt lgkmcnt(8)
v_max3_f32 v246, v246, v183, v184
v_max3_f32 v246, v246, v185, v186
MA 48,228,192
RT 222,251,59392
v_max3_f32 v246, v246, v187, v188
MA 52,228,196
RB 224,252,18432
v_max3_f32 v246, v246, v189, v190
MA 56,228,200
s_mov_b32 m0, 132096
v_exp_f32 v166, v166
MA 60,228,204
s_waitcnt lgkmcnt(3)
v_exp_f32 v167, v167
MA 128,208,192
RB 228,252,19456
v_max3_f32 v246, v246, v191, v247
MA 132,208,196
buffer_load_dwordx4 v253, s[44:47], s61 offen offset:0 nt lds
MA 136,208,200
v_cmp_lt_f32 vcc, s64, v246
v_add_f32 v242, v242, v164
MA 140,208,204
v_add_f32 v242, v242, v165
v_add_f32 v242, v242, v166
MA 144,212,192
RT 208,250,51712
v_add_f32 v242, v242, v167
MA 148,212,196
RT 210,250,59904
MA 152,212,200
s_mov_b32 m0, 134016
v_exp_f32 v168, v168
MA 156,212,204
v_exp_f32 v169, v169
MA 64,216,192
RT 212,251,51712
v_add_f32 v243, v243, v168
MA 68,216,196
RT 214,251,59904
v_add_f32 v243, v243, v169
MA 72,216,200
buffer_load_dwordx4 v253, s[44:47], s61 offen offset:128 nt lds
MA 76,216,204
s_waitcnt lgkmcnt(4)
v_exp_f32 v170, v170
MV 128,216,224
s_cbranch_vccnz Lst1_12
Lrt1_12:
v_exp_f32 v176, v176
MV 132,216,228
v_exp_f32 v177, v177
MA 80,220,192
RT 216,250,53760
v_add_f32 v241, v241, v176
MA 84,220,196
RT 218,250,61952
v_add_f32 v241, v241, v177
MA 88,220,200
s_mov_b32 m0, 135936
v_exp_f32 v178, v178
MA 92,220,204
buffer_load_dwordx4 v253, s[44:47], s61 offen offset:256 nt lds
MV 136,220,224
v_exp_f32 v179, v179
MV 140,220,228
s_waitcnt lgkmcnt(4)
v_exp_f32 v180, v180
MA 96,208,192
RT 220,251,53760
v_add_f32 v241, v241, v178
MA 100,208,196
RT 222,251,61952
v_add_f32 v241, v241, v179
MA 104,208,200
v_exp_f32 v181, v181
MA 108,208,204
v_exp_f32 v182, v182
MV 144,208,224
s_mov_b32 m0, 137856
v_exp_f32 v183, v183
MV 148,208,228
s_waitcnt lgkmcnt(4)
CV 163,162,163
CV 162,160,161
MA 112,212,192
RT 208,250,55296
CV 160,176,177
MA 116,212,196
RT 210,250,63488
CV 161,178,179
MA 120,212,200
buffer_load_dwordx4 v253, s[44:47], s61 offen offset:384 nt lds
s_add_u32 s61, s61, 16384
MA 124,212,204
v_add_f32 v242, v242, v180
v_add_f32 v242, v242, v181
MV 152,212,224
v_add_f32 v242, v242, v182
v_add_f32 v242, v242, v183
MV 156,212,228
s_waitcnt lgkmcnt(4)
CV 167,166,167
MA 160,216,192
RT 212,251,55296
CV 166,164,165
MA 164,216,196
RT 214,251,63488
CV 164,180,181
MA 168,216,200
CV 165,182,183
v_add_f32 v243, v243, v170
MA 172,216,204
s_waitcnt lgkmcnt(4)
v_exp_f32 v232, v232
MA 176,220,192
RT 216,250,55808
v_add_f32 v245, v245, v232
MA 180,220,196
RT 218,250,64000
MA 184,220,200
v_exp_f32 v233, v233
MA 188,220,204
s_waitcnt lgkmcnt(4)
v_exp_f32 v234, v234
MA 192,208,192
RT 220,251,55808
v_add_f32 v245, v245, v233
MA 196,208,196
RB 176,249,24576
v_add_f32 v245, v245, v234
MA 200,208,200
RB 180,249,25088
CV 254,232,233
MA 204,208,204
s_waitcnt lgkmcnt(5)
v_exp_f32 v235, v235
MA 208,212,192
RT 222,251,64000
v_add_f32 v245, v245, v235
MA 212,212,196
RB 224,252,0
CV 255,234,235
MA 216,212,200
RB 208,249,26624
MA 220,212,204
s_waitcnt lgkmcnt(6)
v_exp_f32 v171, v171
MA 224,216,192
RB 212,249,27136
v_add_f32 v243, v243, v171
MA 228,216,196
RB 228,252,1024
CV 171,170,171
MA 232,216,200
ds_write_b64 v252, v[254:255] offset:16392
CV 170,168,169
MA 236,216,204
s_waitcnt lgkmcnt(5)
v_exp_f32 v172, v172
MA 240,220,192
v_exp_f32 v173, v173
MA 244,220,196
v_exp_f32 v174, v174
MA 248,220,200
v_exp_f32 v175, v175
MA 252,220,204
v_exp_f32 v184, v184
MV0 192,176,0
RB 216,249,28672
v_add_f32 v244, v244, v172
MV 192,180,4
RB 220,249,29184
v_add_f32 v244, v244, v173
MV0 196,176,32
s_cmp_lg_u32 s65, 0
v_exp_f32 v185, v185
MV 196,180,36
s_cbranch_scc1 Lst1_13
Lrt1_13:
v_exp_f32 v186, v186
MV0 200,176,64
s_lshr_b32 s78, s76, 2
s_lshl_b32 s78, s78, 7
v_exp_f32 v187, v187
MV 200,180,68
s_and_b32 s67, s76, 3
s_add_u32 s78, s78, s67
v_exp_f32 v188, v188
MV0 204,176,96
s_mov_b32 m0, 1024
v_exp_f32 v189, v189
MV 204,180,100
s_waitcnt lgkmcnt(3)
v_exp_f32 v190, v190
MV0 232,176,224
RB 224,252,2048
v_add_f32 v244, v244, v174
MV 232,180,228
v_exp_f32 v191, v191
MV 192,208,8
RB 176,249,30720
v_add_f32 v244, v244, v175
MV 192,212,12
RB 180,249,31232
v_add_f32 v243, v243, v184
MV 196,208,40
RB 228,252,3072
v_add_f32 v243, v243, v185
MV 196,212,44
buffer_load_dwordx4 v253, s[40:43], s60 offen offset:0 nt lds
MV 200,208,72
s_mov_b32 m0, 2944
v_add_f32 v243, v243, v186
v_add_f32 v243, v243, v187
MV 200,212,76
buffer_load_dwordx4 v253, s[40:43], s60 offen offset:128 nt lds
MV 204,208,104
s_lshl_b32 s78, s78, 7
s_add_u32 s76, s76, 1
v_add_f32 v244, v244, v188
v_add_f32 v244, v244, v189
MV 204,212,108
s_waitcnt lgkmcnt(0)
v_add_f32 v244, v244, v190
v_add_f32 v244, v244, v191
MV 232,208,224
RB 224,252,4096
CV 168,184,185
MV 232,212,228
CV 175,174,175
CV 174,172,173
MV 192,216,16
RB 208,249,16384
CV 169,186,187
MV 192,220,20
RB 212,249,16896
CV 172,188,189
MV 196,216,48
RB 228,252,5120
CV 173,190,191
MV 196,220,52
s_mov_b32 m0, 4864
MV 200,216,80
buffer_load_dwordx4 v253, s[40:43], s60 offen offset:256 nt lds
MV 200,220,84
MV 204,216,112
MV 204,220,116
s_waitcnt lgkmcnt(0)
MV 232,216,224
RB 216,249,18432
MV 232,220,228
MV 192,176,24
RB 220,249,18944
MV 192,180,28
RB 224,252,6144
MV 196,176,56
RB 228,252,7168
MV 196,180,60
s_mov_b32 m0, 6784
MV 200,176,88
buffer_load_dwordx4 v253, s[40:43], s60 offen offset:384 nt lds
s_add_u32 s60, s60, 16384
MV 200,180,92
s_mov_b32 m0, 97536
v_fma_f32 v192, v192, s63, v236
v_fma_f32 v193, v193, s63, v236
MV 204,176,120
buffer_load_dword v253, s[48:51], s78 offen lds
MV 204,180,124
s_waitcnt lgkmcnt(0)
v_fma_f32 v194, v194, s63, v236
v_fma_f32 v195, v195, s63, v236
MV 232,176,224
RB 224,249,20480
v_fma_f32 v196, v196, s63, v237
MV 232,180,228
v_fma_f32 v197, v197, s63, v237
v_fma_f32 v198, v198, s63, v237
MV0 176,208,0
RB 228,249,20992
v_fma_f32 v199, v199, s63, v237
MV 176,212,4
v_fma_f32 v200, v200, s63, v238
v_fma_f32 v201, v201, s63, v238
MV0 180,208,32
v_fma_f32 v202, v202, s63, v238
v_fma_f32 v203, v203, s63, v238
MV 180,212,36
v_fma_f32 v204, v204, s63, v239
MV0 184,208,64
v_fma_f32 v205, v205, s63, v239
v_fma_f32 v206, v206, s63, v239
MV 184,212,68
v_fma_f32 v207, v207, s63, v239
v_fma_f32 v232, v232, s63, v240
MV0 188,208,96
RB 208,249,22528
v_fma_f32 v233, v233, s63, v240
MV 188,212,100
v_fma_f32 v234, v234, s63, v240
v_fma_f32 v235, v235, s63, v240
MV 176,216,8
RB 212,249,23040
v_max3_f32 v246, v192, v193, v194
MV 176,220,12
v_max3_f32 v246, v246, v195, v196
v_max3_f32 v246, v246, v197, v198
MV 180,216,40
v_max3_f32 v246, v246, v199, v200
v_max3_f32 v246, v246, v201, v202
MV 180,220,44
v_max3_f32 v246, v246, v203, v204
MV 184,216,72
v_max3_f32 v246, v246, v205, v206
v_max3_f32 v246, v246, v207, v232
MV 184,220,76
v_max3_f32 v246, v246, v233, v234
v_max_f32 v246, v246, v235
MV 188,216,104
RT 216,250,0
v_cmp_lt_f32 vcc, s64, v246
MV 188,220,108
s_waitcnt lgkmcnt(3)
MV 176,224,16
RT 218,250,8192
MV 176,228,20
RT 220,251,0
MV 180,224,48
RT 222,251,8192
MV 180,228,52
s_cbranch_vccnz Lst1_14
Lrt1_14:
v_exp_f32 v192, v192
MV 184,224,80
v_exp_f32 v193, v193
MV 184,228,84
v_exp_f32 v194, v194
MV 188,224,112
RT 224,250,512
v_add_f32 v241, v241, v192
MV 188,228,116
s_waitcnt lgkmcnt(5)
v_exp_f32 v195, v195
MV 176,208,24
RT 226,250,8704
v_add_f32 v241, v241, v193
MV 176,212,28
RT 228,251,512
v_add_f32 v241, v241, v194
MV 180,208,56
RT 230,251,8704
v_add_f32 v241, v241, v195
MV 180,212,60
v_exp_f32 v196, v196
MV 184,208,88
v_exp_f32 v197, v197
MV 184,212,92
v_fma_f32 v176, v176, s63, v236
v_fma_f32 v177, v177, s63, v236
MV 188,208,120
s_waitcnt vmcnt(14) lgkmcnt(6)
v_fma_f32 v178, v178, s63, v236
v_fma_f32 v179, v179, s63, v236
MV 188,212,124
s_barrier
v_fma_f32 v182, v182, s63, v237
MA 0,216,160
RT 208,250,4096
v_fma_f32 v180, v180, s63, v237
MA 4,216,164
RT 210,250,12288
v_fma_f32 v181, v181, s63, v237
MA 8,216,168
RT 212,251,4096
v_fma_f32 v183, v183, s63, v237
MA 12,216,172
s_waitcnt lgkmcnt(7)
v_fma_f32 v184, v184, s63, v238
v_fma_f32 v185, v185, s63, v238
MA 16,220,160
RT 214,251,12288
v_fma_f32 v186, v186, s63, v238
MA 20,220,164
RT 216,250,2048
v_fma_f32 v187, v187, s63, v238
MA 24,220,168
)ASM"
R"ASM(
RT 218,250,10240
v_fma_f32 v188, v188, s63, v239
MA 28,220,172
s_waitcnt lgkmcnt(8)
v_fma_f32 v189, v189, s63, v239
v_fma_f32 v190, v190, s63, v239
MA 32,224,160
ds_read_b32 v247, v252 offset:29704
v_fma_f32 v191, v191, s63, v239
MA 36,224,164
v_max3_f32 v246, v176, v177, v178
v_max3_f32 v246, v246, v179, v182
MA 40,224,168
RT 220,251,2048
v_max3_f32 v246, v246, v180, v181
MA 44,224,172
s_waitcnt lgkmcnt(8)
v_max3_f32 v246, v246, v183, v184
v_max3_f32 v246, v246, v185, v186
MA 48,228,160
RT 222,251,10240
v_max3_f32 v246, v246, v187, v188
MA 52,228,164
RB 224,252,16384
v_max3_f32 v246, v246, v189, v190
MA 56,228,168
s_mov_b32 m0, 148480
v_exp_f32 v198, v198
MA 60,228,172
s_waitcnt lgkmcnt(3)
v_exp_f32 v199, v199
MA 128,208,160
RB 228,252,17408
v_max3_f32 v246, v246, v191, v247
MA 132,208,164
buffer_load_dwordx4 v253, s[44:47], s61 offen offset:0 nt lds
MA 136,208,168
v_cmp_lt_f32 vcc, s64, v246
v_add_f32 v242, v242, v196
MA 140,208,172
v_add_f32 v242, v242, v197
v_add_f32 v242, v242, v198
MA 144,212,160
RT 208,250,2560
v_add_f32 v242, v242, v199
MA 148,212,164
RT 210,250,10752
MA 152,212,168
s_mov_b32 m0, 150400
v_exp_f32 v200, v200
MA 156,212,172
v_exp_f32 v201, v201
MA 64,216,160
RT 212,251,2560
v_add_f32 v243, v243, v200
MA 68,216,164
RT 214,251,10752
v_add_f32 v243, v243, v201
MA 72,216,168
buffer_load_dwordx4 v253, s[44:47], s61 offen offset:128 nt lds
MA 76,216,172
s_waitcnt lgkmcnt(4)
v_exp_f32 v202, v202
MV 128,216,224
s_cbranch_vccnz Lst1_15
Lrt1_15:
v_exp_f32 v176, v176
MV 132,216,228
v_exp_f32 v177, v177
MA 80,220,160
RT 216,250,4608
v_add_f32 v241, v241, v176
MA 84,220,164
RT 218,250,12800
v_add_f32 v241, v241, v177
MA 88,220,168
s_mov_b32 m0, 152320
v_exp_f32 v178, v178
MA 92,220,172
buffer_load_dwordx4 v253, s[44:47], s61 offen offset:256 nt lds
MV 136,220,224
v_exp_f32 v179, v179
MV 140,220,228
s_waitcnt lgkmcnt(4)
v_exp_f32 v180, v180
MA 96,208,160
RT 220,251,4608
v_add_f32 v241, v241, v178
MA 100,208,164
RT 222,251,12800
v_add_f32 v241, v241, v179
MA 104,208,168
v_exp_f32 v181, v181
MA 108,208,172
v_exp_f32 v182, v182
MV 144,208,224
s_mov_b32 m0, 154240
v_exp_f32 v183, v183
MV 148,208,228
s_waitcnt lgkmcnt(4)
CV 195,194,195
CV 194,192,193
MA 112,212,160
RT 208,250,6144
CV 192,176,177
MA 116,212,164
RT 210,250,14336
CV 193,178,179
MA 120,212,168
buffer_load_dwordx4 v253, s[44:47], s61 offen offset:384 nt lds
s_add_u32 s61, s61, 16384
MA 124,212,172
v_add_f32 v242, v242, v180
v_add_f32 v242, v242, v181
MV 152,212,224
v_add_f32 v242, v242, v182
v_add_f32 v242, v242, v183
MV 156,212,228
s_waitcnt lgkmcnt(4)
CV 199,198,199
MA 160,216,160
RT 212,251,6144
CV 198,196,197
MA 164,216,164
RT 214,251,14336
CV 196,180,181
MA 168,216,168
CV 197,182,183
v_add_f32 v243, v243, v202
MA 172,216,172
s_waitcnt lgkmcnt(4)
v_exp_f32 v232, v232
MA 176,220,160
RT 216,250,6656
v_add_f32 v245, v245, v232
MA 180,220,164
RT 218,250,14848
MA 184,220,168
v_exp_f32 v233, v233
MA 188,220,172
s_waitcnt lgkmcnt(4)
v_exp_f32 v234, v234
MA 192,208,160
RT 220,251,6656
v_add_f32 v245, v245, v233
MA 196,208,164
RB 176,249,40960
v_add_f32 v245, v245, v234
MA 200,208,168
RB 180,249,41472
CV 254,232,233
MA 204,208,172
s_waitcnt lgkmcnt(5)
v_exp_f32 v235, v235
MA 208,212,160
RT 222,251,14848
v_add_f32 v245, v245, v235
MA 212,212,164
RB 224,252,0
CV 255,234,235
MA 216,212,168
RB 208,249,43008
MA 220,212,172
s_waitcnt lgkmcnt(6)
v_exp_f32 v203, v203
MA 224,216,160
RB 212,249,43520
v_add_f32 v243, v243, v203
MA 228,216,164
RB 228,252,1024
CV 203,202,203
MA 232,216,168
ds_write_b64 v252, v[254:255] offset:18440
CV 202,200,201
MA 236,216,172
s_waitcnt lgkmcnt(5)
v_exp_f32 v204, v204
MA 240,220,160
v_exp_f32 v205, v205
MA 244,220,164
v_exp_f32 v206, v206
MA 248,220,168
v_exp_f32 v207, v207
MA 252,220,172
v_exp_f32 v184, v184
s_sub_u32 s62, s62, 1
s_cbranch_scc0 Lloop1
Lpeel1:
s_mov_b32 s48, s24
s_and_b32 s49, s25, 0xffff
s_mov_b32 s50, 3670016
s_and_b32 s77, s26, 15
s_mul_i32 s67, s77, 229376
s_xor_b32 s77, s77, 1
s_mul_i32 s69, s77, 229376
s_mov_b32 s68, 1024
v_bfe_u32 v248, v252, 6, 3
v_add_u32 v248, 8, v248
v_mul_u32_u24 v248, 0x380, v248
v_and_b32 v253, 0x7f, v253
v_add_u32 v253, v253, v248
MV0 160,176,0
RB 216,249,45056
v_add_f32 v244, v244, v204
MV 160,180,4
RB 220,249,45568
v_add_f32 v244, v244, v205
MV0 164,176,32
s_cmp_lg_u32 s65, 0
v_exp_f32 v185, v185
MV 164,180,36
s_cbranch_scc1 Lst1_16
Lrt1_16:
v_exp_f32 v186, v186
MV0 168,176,64
v_exp_f32 v187, v187
MV 168,180,68
v_exp_f32 v188, v188
MV0 172,176,96
v_exp_f32 v189, v189
MV 172,180,100
s_waitcnt lgkmcnt(3)
v_exp_f32 v190, v190
MV0 232,176,224
RB 224,252,2048
v_add_f32 v244, v244, v206
MV 232,180,228
v_exp_f32 v191, v191
MV 160,208,8
RB 176,249,47104
v_add_f32 v244, v244, v207
MV 160,212,12
RB 180,249,47616
v_add_f32 v243, v243, v184
MV 164,208,40
RB 228,252,3072
v_add_f32 v243, v243, v185
MV 164,212,44
MV 168,208,72
v_add_f32 v243, v243, v186
v_add_f32 v243, v243, v187
MV 168,212,76
MV 172,208,104
v_add_f32 v244, v244, v188
v_add_f32 v244, v244, v189
MV 172,212,108
s_waitcnt lgkmcnt(0)
v_add_f32 v244, v244, v190
v_add_f32 v244, v244, v191
MV 232,208,224
RB 224,252,4096
CV 200,184,185
MV 232,212,228
CV 207,206,207
CV 206,204,205
MV 160,216,16
RB 208,249,32768
CV 201,186,187
MV 160,220,20
RB 212,249,33280
CV 204,188,189
MV 164,216,48
RB 228,252,5120
CV 205,190,191
MV 164,220,52
MV 168,216,80
MV 168,220,84
MV 172,216,112
MV 172,220,116
s_waitcnt lgkmcnt(0)
MV 232,216,224
RB 216,249,34816
MV 232,220,228
MV 160,176,24
RB 220,249,35328
MV 160,180,28
RB 224,252,6144
MV 164,176,56
RB 228,252,7168
MV 164,180,60
MV 168,176,88
MV 168,180,92
s_nop 1
v_fma_f32 v160, v160, s63, v236
v_fma_f32 v161, v161, s63, v236
MV 172,176,120
MV 172,180,124
s_waitcnt lgkmcnt(0)
v_fma_f32 v162, v162, s63, v236
v_fma_f32 v163, v163, s63, v236
MV 232,176,224
RB 224,249,36864
v_fma_f32 v164, v164, s63, v237
MV 232,180,228
v_fma_f32 v165, v165, s63, v237
v_fma_f32 v166, v166, s63, v237
MV0 176,208,0
RB 228,249,37376
v_fma_f32 v167, v167, s63, v237
MV 176,212,4
v_fma_f32 v168, v168, s63, v238
v_fma_f32 v169, v169, s63, v238
MV0 180,208,32
v_fma_f32 v170, v170, s63, v238
v_fma_f32 v171, v171, s63, v238
MV 180,212,36
v_fma_f32 v172, v172, s63, v239
MV0 184,208,64
v_fma_f32 v173, v173, s63, v239
v_fma_f32 v174, v174, s63, v239
MV 184,212,68
v_fma_f32 v175, v175, s63, v239
v_fma_f32 v232, v232, s63, v240
MV0 188,208,96
RB 208,249,38912
v_fma_f32 v233, v233, s63, v240
MV 188,212,100
v_fma_f32 v234, v234, s63, v240
v_fma_f32 v235, v235, s63, v240
MV 176,216,8
RB 212,249,39424
v_max3_f32 v246, v160, v161, v162
MV 176,220,12
v_max3_f32 v246, v246, v163, v164
v_max3_f32 v246, v246, v165, v166
MV 180,216,40
v_max3_f32 v246, v246, v167, v168
v_max3_f32 v246, v246, v169, v170
MV 180,220,44
v_max3_f32 v246, v246, v171, v172
MV 184,216,72
v_max3_f32 v246, v246, v173, v174
v_max3_f32 v246, v246, v175, v232
MV 184,220,76
v_max3_f32 v246, v246, v233, v234
v_max_f32 v246, v246, v235
MV 188,216,104
RT 216,250,16384
v_cmp_lt_f32 vcc, s64, v246
MV 188,220,108
s_waitcnt lgkmcnt(3)
MV 176,224,16
RT 218,250,24576
MV 176,228,20
RT 220,251,16384
MV 180,224,48
RT 222,251,24576
MV 180,228,52
s_cbranch_vccnz Lst1_17
Lrt1_17:
v_exp_f32 v160, v160
MV 184,224,80
v_exp_f32 v161, v161
MV 184,228,84
v_exp_f32 v162, v162
MV 188,224,112
RT 224,250,16896
v_add_f32 v241, v241, v160
MV 188,228,116
s_waitcnt lgkmcnt(5)
v_exp_f32 v163, v163
MV 176,208,24
RT 226,250,25088
v_add_f32 v241, v241, v161
MV 176,212,28
RT 228,251,16896
v_add_f32 v241, v241, v162
MV 180,208,56
RT 230,251,25088
v_add_f32 v241, v241, v163
MV 180,212,60
v_exp_f32 v164, v164
MV 184,208,88
v_exp_f32 v165, v165
MV 184,212,92
v_fma_f32 v176, v176, s63, v236
v_fma_f32 v177, v177, s63, v236
MV 188,208,120
s_waitcnt vmcnt(5) lgkmcnt(6)
v_fma_f32 v178, v178, s63, v236
v_fma_f32 v179, v179, s63, v236
MV 188,212,124
s_barrier
v_fma_f32 v182, v182, s63, v237
MA 0,216,192
RT 208,250,20480
v_fma_f32 v180, v180, s63, v237
MA 4,216,196
RT 210,250,28672
v_fma_f32 v181, v181, s63, v237
MA 8,216,200
RT 212,251,20480
v_fma_f32 v183, v183, s63, v237
MA 12,216,204
s_waitcnt lgkmcnt(7)
v_fma_f32 v184, v184, s63, v238
v_fma_f32 v185, v185, s63, v238
MA 16,220,192
RT 214,251,28672
v_fma_f32 v186, v186, s63, v238
MA 20,220,196
RT 216,250,18432
v_fma_f32 v187, v187, s63, v238
MA 24,220,200
RT 218,250,26624
v_fma_f32 v188, v188, s63, v239
MA 28,220,204
s_waitcnt lgkmcnt(8)
v_fma_f32 v189, v189, s63, v239
v_fma_f32 v190, v190, s63, v239
MA 32,224,192
ds_read_b32 v247, v252 offset:29704
v_fma_f32 v191, v191, s63, v239
MA 36,224,196
v_max3_f32 v246, v176, v177, v178
v_max3_f32 v246, v246, v179, v182
MA 40,224,200
RT 220,251,18432
v_max3_f32 v246, v246, v180, v181
MA 44,224,204
s_waitcnt lgkmcnt(8)
v_max3_f32 v246, v246, v183, v184
v_max3_f32 v246, v246, v185, v186
MA 48,228,192
RT 222,251,26624
v_max3_f32 v246, v246, v187, v188
MA 52,228,196
RB 224,252,18432
v_max3_f32 v246, v246, v189, v190
MA 56,228,200
s_mov_b32 m0, 1024
v_exp_f32 v166, v166
MA 60,228,204
s_waitcnt lgkmcnt(3)
v_exp_f32 v167, v167
MA 128,208,192
RB 228,252,19456
v_max3_f32 v246, v246, v191, v247
MA 132,208,196
buffer_load_dwordx4 v253, s[48:51], s67 offen offset:0 lds
MA 136,208,200
v_cmp_lt_f32 vcc, s64, v246
v_add_f32 v242, v242, v164
MA 140,208,204
v_add_f32 v242, v242, v165
v_add_f32 v242, v242, v166
MA 144,212,192
RT 208,250,18944
v_add_f32 v242, v242, v167
MA 148,212,196
RT 210,250,27136
MA 152,212,200
s_mov_b32 m0, 4992
v_exp_f32 v168, v168
MA 156,212,204
v_exp_f32 v169, v169
MA 64,216,192
RT 212,251,18944
v_add_f32 v243, v243, v168
MA 68,216,196
RT 214,251,27136
v_add_f32 v243, v243, v169
MA 72,216,200
buffer_load_dwordx4 v253, s[48:51], s67 offen offset:128 lds
MA 76,216,204
s_waitcnt lgkmcnt(4)
v_exp_f32 v170, v170
MV 128,216,224
s_cbranch_vccnz Lst1_18
Lrt1_18:
v_exp_f32 v176, v176
MV 132,216,228
v_exp_f32 v177, v177
MA 80,220,192
RT 216,250,20992
v_add_f32 v241, v241, v176
MA 84,220,196
RT 218,250,29184
v_add_f32 v241, v241, v177
MA 88,220,200
s_mov_b32 m0, 8960
v_exp_f32 v178, v178
MA 92,220,204
buffer_load_dwordx4 v253, s[48:51], s67 offen offset:256 lds
MV 136,220,224
v_exp_f32 v179, v179
MV 140,220,228
s_waitcnt lgkmcnt(4)
v_exp_f32 v180, v180
MA 96,208,192
RT 220,251,20992
v_add_f32 v241, v241, v178
MA 100,208,196
RT 222,251,29184
v_add_f32 v241, v241, v179
MA 104,208,200
v_exp_f32 v181, v181
MA 108,208,204
v_exp_f32 v182, v182
MV 144,208,224
s_mov_b32 m0, 12928
v_exp_f32 v183, v183
MV 148,208,228
s_waitcnt lgkmcnt(4)
CV 163,162,163
CV 162,160,161
MA 112,212,192
RT 208,250,22528
CV 160,176,177
MA 116,212,196
RT 210,250,30720
CV 161,178,179
MA 120,212,200
buffer_load_dwordx4 v253, s[48:51], s67 offen offset:384 lds
MA 124,212,204
v_add_f32 v242, v242, v180
v_add_f32 v242, v242, v181
MV 152,212,224
v_add_f32 v242, v242, v182
v_add_f32 v242, v242, v183
MV 156,212,228
s_waitcnt lgkmcnt(4)
CV 167,166,167
MA 160,216,192
RT 212,251,22528
CV 166,164,165
MA 164,216,196
RT 214,251,30720
CV 164,180,181
MA 168,216,200
CV 165,182,183
v_add_f32 v243, v243, v170
MA 172,216,204
s_waitcnt lgkmcnt(4)
v_exp_f32 v232, v232
MA 176,220,192
RT 216,250,23040
v_add_f32 v245, v245, v232
MA 180,220,196
RT 218,250,31232
MA 184,220,200
v_exp_f32 v233, v233
MA 188,220,204
s_waitcnt lgkmcnt(4)
v_exp_f32 v234, v234
MA 192,208,192
RT 220,251,23040
v_add_f32 v245, v245, v233
MA 196,208,196
RB 176,249,57344
v_add_f32 v245, v245, v234
MA 200,208,200
RB 180,249,57856
CV 254,232,233
MA 204,208,204
s_waitcnt lgkmcnt(5)
v_exp_f32 v235, v235
MA 208,212,192
RT 222,251,31232
v_add_f32 v245, v245, v235
MA 212,212,196
RB 224,252,0
CV 255,234,235
MA 216,212,200
RB 208,249,59392
MA 220,212,204
s_waitcnt lgkmcnt(6)
v_exp_f32 v171, v171
MA 224,216,192
RB 212,249,59904
v_add_f32 v243, v243, v171
MA 228,216,196
RB 228,252,1024
CV 171,170,171
MA 232,216,200
ds_write_b64 v252, v[254:255] offset:16392
CV 170,168,169
MA 236,216,204
s_waitcnt lgkmcnt(5)
v_exp_f32 v172, v172
MA 240,220,192
v_exp_f32 v173, v173
MA 244,220,196
v_exp_f32 v174, v174
MA 248,220,200
v_exp_f32 v175, v175
MA 252,220,204
v_exp_f32 v184, v184
MV0 192,176,0
RB 216,249,61440
v_add_f32 v244, v244, v172
MV 192,180,4
RB 220,249,61952
v_add_f32 v244, v244, v173
MV0 196,176,32
s_cmp_lg_u32 s65, 0
v_exp_f32 v185, v185
MV 196,180,36
s_cbranch_scc1 Lst1_19
Lrt1_19:
v_exp_f32 v186, v186
MV0 200,176,64
v_exp_f32 v187, v187
MV 200,180,68
v_exp_f32 v188, v188
MV0 204,176,96
s_mov_b32 m0, 16896
v_exp_f32 v189, v189
MV 204,180,100
s_waitcnt lgkmcnt(3)
v_exp_f32 v190, v190
MV0 232,176,224
RB 224,252,2048
v_add_f32 v244, v244, v174
MV 232,180,228
v_exp_f32 v191, v191
MV 192,208,8
RB 176,249,63488
v_add_f32 v244, v244, v175
MV 192,212,12
RB 180,249,64000
v_add_f32 v243, v243, v184
MV 196,208,40
RB 228,252,3072
v_add_f32 v243, v243, v185
MV 196,212,44
buffer_load_dwordx4 v253, s[48:51], s67 offen offset:512 lds
MV 200,208,72
s_mov_b32 m0, 20864
v_add_f32 v243, v243, v186
v_add_f32 v243, v243, v187
MV 200,212,76
buffer_load_dwordx4 v253, s[48:51], s67 offen offset:640 lds
MV 204,208,104
v_add_f32 v244, v244, v188
v_add_f32 v244, v244, v189
MV 204,212,108
s_waitcnt lgkmcnt(0)
v_add_f32 v244, v244, v190
v_add_f32 v244, v244, v191
MV 232,208,224
RB 224,252,4096
CV 168,184,185
MV 232,212,228
CV 175,174,175
CV 174,172,173
MV 192,216,16
RB 208,249,49152
CV 169,186,187
MV 192,220,20
RB 212,249,49664
CV 172,188,189
MV 196,216,48
RB 228,252,5120
CV 173,190,191
MV 196,220,52
s_mov_b32 m0, 24832
MV 200,216,80
buffer_load_dwordx4 v253, s[48:51], s67 offen offset:768 lds
s_add_u32 s67, s67, 28672
MV 200,220,84
MV 204,216,112
MV 204,220,116
s_waitcnt lgkmcnt(0)
MV 232,216,224
RB 216,249,51200
MV 232,220,228
MV 192,176,24
RB 220,249,51712
MV 192,180,28
RB 224,252,6144
MV 196,176,56
RB 228,252,7168
MV 196,180,60
s_mov_b32 m0, 29696
MV 200,176,88
buffer_load_dwordx4 v253, s[48:51], s67 offen offset:0 lds
MV 200,180,92
v_fma_f32 v192, v192, s63, v236
v_fma_f32 v193, v193, s63, v236
MV 204,176,120
MV 204,180,124
s_waitcnt lgkmcnt(0)
v_fma_f32 v194, v194, s63, v236
v_fma_f32 v195, v195, s63, v236
MV 232,176,224
RB 224,249,53248
v_fma_f32 v196, v196, s63, v237
MV 232,180,228
v_fma_f32 v197, v197, s63, v237
v_fma_f32 v198, v198, s63, v237
MV0 176,208,0
RB 228,249,53760
v_fma_f32 v199, v199, s63, v237
MV 176,212,4
v_fma_f32 v200, v200, s63, v238
v_fma_f32 v201, v201, s63, v238
MV0 180,208,32
v_fma_f32 v202, v202, s63, v238
v_fma_f32 v203, v203, s63, v238
MV 180,212,36
v_fma_f32 v204, v204, s63, v239
MV0 184,208,64
v_fma_f32 v205, v205, s63, v239
v_fma_f32 v206, v206, s63, v239
MV 184,212,68
v_fma_f32 v207, v207, s63, v239
v_fma_f32 v232, v232, s63, v240
MV0 188,208,96
RB 208,249,55296
v_fma_f32 v233, v233, s63, v240
MV 188,212,100
v_fma_f32 v234, v234, s63, v240
v_fma_f32 v235, v235, s63, v240
MV 176,216,8
RB 212,249,55808
v_max3_f32 v246, v192, v193, v194
MV 176,220,12
v_max3_f32 v246, v246, v195, v196
v_max3_f32 v246, v246, v197, v198
MV 180,216,40
v_max3_f32 v246, v246, v199, v200
v_max3_f32 v246, v246, v201, v202
MV 180,220,44
v_max3_f32 v246, v246, v203, v204
MV 184,216,72
v_max3_f32 v246, v246, v205, v206
v_max3_f32 v246, v246, v207, v232
MV 184,220,76
v_max3_f32 v246, v246, v233, v234
v_max_f32 v246, v246, v235
MV 188,216,104
RT 216,250,32768
v_cmp_lt_f32 vcc, s64, v246
MV 188,220,108
s_waitcnt lgkmcnt(3)
MV 176,224,16
RT 218,250,40960
MV 176,228,20
RT 220,251,32768
MV 180,224,48
RT 222,251,40960
MV 180,228,52
s_cbranch_vccnz Lst1_20
Lrt1_20:
v_exp_f32 v192, v192
MV 184,224,80
v_exp_f32 v193, v193
MV 184,228,84
v_exp_f32 v194, v194
MV 188,224,112
RT 224,250,33280
v_add_f32 v241, v241, v192
MV 188,228,116
s_waitcnt lgkmcnt(5)
v_exp_f32 v195, v195
MV 176,208,24
RT 226,250,41472
v_add_f32 v241, v241, v193
MV 176,212,28
RT 228,251,33280
v_add_f32 v241, v241, v194
MV 180,208,56
RT 230,251,41472
v_add_f32 v241, v241, v195
MV 180,212,60
v_exp_f32 v196, v196
MV 184,208,88
v_exp_f32 v197, v197
MV 184,212,92
v_fma_f32 v176, v176, s63, v236
v_fma_f32 v177, v177, s63, v236
MV 188,208,120
s_waitcnt vmcnt(8) lgkmcnt(6)
v_fma_f32 v178, v178, s63, v236
v_fma_f32 v179, v179, s63, v236
MV 188,212,124
s_barrier
v_fma_f32 v182, v182, s63, v237
MA 0,216,160
RT 208,250,36864
v_fma_f32 v180, v180, s63, v237
MA 4,216,164
RT 210,250,45056
v_fma_f32 v181, v181, s63, v237
MA 8,216,168
RT 212,251,36864
v_fma_f32 v183, v183, s63, v237
MA 12,216,172
s_waitcnt lgkmcnt(7)
v_fma_f32 v184, v184, s63, v238
v_fma_f32 v185, v185, s63, v238
MA 16,220,160
RT 214,251,45056
v_fma_f32 v186, v186, s63, v238
MA 20,220,164
RT 216,250,34816
v_fma_f32 v187, v187, s63, v238
MA 24,220,168
RT 218,250,43008
v_fma_f32 v188, v188, s63, v239
MA 28,220,172
s_waitcnt lgkmcnt(8)
v_fma_f32 v189, v189, s63, v239
v_fma_f32 v190, v190, s63, v239
MA 32,224,160
ds_read_b32 v247, v252 offset:29704
v_fma_f32 v191, v191, s63, v239
MA 36,224,164
v_max3_f32 v246, v176, v177, v178
v_max3_f32 v246, v246, v179, v182
MA 40,224,168
RT 220,251,34816
v_max3_f32 v246, v246, v180, v181
MA 44,224,172
s_waitcnt lgkmcnt(8)
v_max3_f32 v246, v246, v183, v184
v_max3_f32 v246, v246, v185, v186
MA 48,228,160
RT 222,251,43008
v_max3_f32 v246, v246, v187, v188
MA 52,228,164
RB 224,252,16384
v_max3_f32 v246, v246, v189, v190
MA 56,228,168
s_mov_b32 m0, 33664
v_exp_f32 v198, v198
MA 60,228,172
s_waitcnt lgkmcnt(3)
v_exp_f32 v199, v199
MA 128,208,160
RB 228,252,17408
v_max3_f32 v246, v246, v191, v247
MA 132,208,164
buffer_load_dwordx4 v253, s[48:51], s67 offen offset:128 lds
MA 136,208,168
v_cmp_lt_f32 vcc, s64, v246
v_add_f32 v242, v242, v196
MA 140,208,172
v_add_f32 v242, v242, v197
v_add_f32 v242, v242, v198
MA 144,212,160
RT 208,250,35328
v_add_f32 v242, v242, v199
MA 148,212,164
RT 210,250,43520
MA 152,212,168
s_mov_b32 m0, 37632
v_exp_f32 v200, v200
MA 156,212,172
v_exp_f32 v201, v201
MA 64,216,160
RT 212,251,35328
v_add_f32 v243, v243, v200
MA 68,216,164
RT 214,251,43520
v_add_f32 v243, v243, v201
MA 72,216,168
buffer_load_dwordx4 v253, s[48:51], s67 offen offset:256 lds
MA 76,216,172
s_waitcnt lgkmcnt(4)
v_exp_f32 v202, v202
MV 128,216,224
s_cbranch_vccnz Lst1_21
Lrt1_21:
v_exp_f32 v176, v176
MV 132,216,228
v_exp_f32 v177, v177
MA 80,220,160
RT 216,250,37376
v_add_f32 v241, v241, v176
MA 84,220,164
RT 218,250,45568
v_add_f32 v241, v241, v177
MA 88,220,168
s_mov_b32 m0, 41600
v_exp_f32 v178, v178
MA 92,220,172
buffer_load_dwordx4 v253, s[48:51], s67 offen offset:384 lds
MV 136,220,224
v_exp_f32 v179, v179
MV 140,220,228
s_waitcnt lgkmcnt(4)
v_exp_f32 v180, v180
MA 96,208,160
RT 220,251,37376
v_add_f32 v241, v241, v178
MA 100,208,164
RT 222,251,45568
v_add_f32 v241, v241, v179
MA 104,208,168
v_exp_f32 v181, v181
MA 108,208,172
v_exp_f32 v182, v182
MV 144,208,224
s_mov_b32 m0, 45568
v_exp_f32 v183, v183
MV 148,208,228
s_waitcnt lgkmcnt(4)
CV 195,194,195
CV 194,192,193
MA 112,212,160
RT 208,250,38912
CV 192,176,177
MA 116,212,164
RT 210,250,47104
CV 193,178,179
MA 120,212,168
buffer_load_dwordx4 v253, s[48:51], s67 offen offset:512 lds
MA 124,212,172
v_add_f32 v242, v242, v180
v_add_f32 v242, v242, v181
MV 152,212,224
v_add_f32 v242, v242, v182
v_add_f32 v242, v242, v183
MV 156,212,228
s_waitcnt lgkmcnt(4)
CV 199,198,199
MA 160,216,160
RT 212,251,38912
CV 198,196,197
MA 164,216,164
RT 214,251,47104
CV 196,180,181
MA 168,216,168
CV 197,182,183
v_add_f32 v243, v243, v202
MA 172,216,172
s_waitcnt lgkmcnt(4)
v_exp_f32 v232, v232
MA 176,220,160
RT 216,250,39424
v_add_f32 v245, v245, v232
MA 180,220,164
RT 218,250,47616
MA 184,220,168
v_exp_f32 v233, v233
MA 188,220,172
s_waitcnt lgkmcnt(4)
v_exp_f32 v234, v234
MA 192,208,160
RT 220,251,39424
v_add_f32 v245, v245, v233
MA 196,208,164
RB 176,249,8192
v_add_f32 v245, v245, v234
MA 200,208,168
RB 180,249,8704
CV 254,232,233
MA 204,208,172
s_waitcnt lgkmcnt(5)
v_exp_f32 v235, v235
MA 208,212,160
RT 222,251,47616
v_add_f32 v245, v245, v235
MA 212,212,164
RB 224,252,0
CV 255,234,235
MA 216,212,168
RB 208,249,10240
MA 220,212,172
s_waitcnt lgkmcnt(6)
v_exp_f32 v203, v203
MA 224,216,160
RB 212,249,10752
v_add_f32 v243, v243, v203
MA 228,216,164
RB 228,252,1024
CV 203,202,203
MA 232,216,168
ds_write_b64 v252, v[254:255] offset:18440
CV 202,200,201
MA 236,216,172
s_waitcnt lgkmcnt(5)
v_exp_f32 v204, v204
MA 240,220,160
v_exp_f32 v205, v205
MA 244,220,164
v_exp_f32 v206, v206
MA 248,220,168
v_exp_f32 v207, v207
MA 252,220,172
s_waitcnt lgkmcnt(1)
v_exp_f32 v184, v184
s_mov_b32 m0, 49536
s_nop 0
buffer_load_dwordx4 v253, s[48:51], s67 offen offset:640 lds
s_mov_b32 m0, 53504
s_nop 0
buffer_load_dwordx4 v253, s[48:51], s67 offen offset:768 lds
s_add_u32 s67, s67, 28672
v_add_f32 v244, v244, v204
v_add_f32 v244, v244, v205
s_cmp_lg_u32 s65, 0
v_exp_f32 v185, v185
s_cbranch_scc1 Lst1_22
Lrt1_22:
v_exp_f32 v186, v186
v_exp_f32 v187, v187
v_exp_f32 v188, v188
v_exp_f32 v189, v189
v_exp_f32 v190, v190
v_add_f32 v244, v244, v206
v_exp_f32 v191, v191
v_add_f32 v244, v244, v207
v_add_f32 v243, v243, v184
v_add_f32 v243, v243, v185
v_add_f32 v243, v243, v186
v_add_f32 v243, v243, v187
v_add_f32 v244, v244, v188
v_add_f32 v244, v244, v189
v_add_f32 v244, v244, v190
v_add_f32 v244, v244, v191
CV 200,184,185
CV 207,206,207
CV 206,204,205
CV 201,186,187
CV 204,188,189
CV 205,190,191
RT 216,250,49152
RT 218,250,57344
RT 220,251,49152
RT 222,251,57344
RT 224,250,49664
RT 226,250,57856
RT 228,251,49664
RT 230,251,57856
v_mov_b32 v176, v241
v_mov_b32 v177, v242
v_mov_b32 v178, v243
v_mov_b32 v179, v244
v_mov_b32 v180, v245
v_permlane16_swap_b32 v241, v176
v_permlane16_swap_b32 v242, v177
v_permlane16_swap_b32 v243, v178
v_permlane16_swap_b32 v244, v179
v_permlane16_swap_b32 v245, v180
v_add_f32 v241, v241, v176
v_add_f32 v242, v242, v177
v_add_f32 v243, v243, v178
v_add_f32 v244, v244, v179
v_add_f32 v245, v245, v180
v_mov_b32 v176, v241
v_mov_b32 v177, v242
v_mov_b32 v178, v243
v_mov_b32 v179, v244
v_mov_b32 v180, v245
v_permlane32_swap_b32 v241, v176
v_permlane32_swap_b32 v242, v177
v_permlane32_swap_b32 v243, v178
v_permlane32_swap_b32 v244, v179
v_permlane32_swap_b32 v245, v180
v_add_f32 v241, v241, v176
v_add_f32 v242, v242, v177
v_add_f32 v243, v243, v178
v_add_f32 v244, v244, v179
v_add_f32 v245, v245, v180
ds_write_b32 v252, v245 offset:30724
v_rcp_f32 v241, v241
v_rcp_f32 v242, v242
v_rcp_f32 v243, v243
v_rcp_f32 v244, v244
s_waitcnt lgkmcnt(0)
s_barrier
RB 236,252,30720
MA 0,216,192
RT 208,250,53248
MA 4,216,196
RT 210,250,61440
MA 8,216,200
RT 212,251,53248
MA 12,216,204
MA 16,220,192
RT 214,251,61440
MA 20,220,196
RT 216,250,51200
MA 24,220,200
RT 218,250,59392
MA 28,220,204
MA 32,224,192
MA 36,224,196
MA 40,224,200
RT 220,251,51200
MA 44,224,204
MA 48,228,192
RT 222,251,59392
MA 52,228,196
RB 224,252,18432
MA 56,228,200
s_waitcnt lgkmcnt(9)
v_add_f32 v236, v236, v237
MA 60,228,204
s_waitcnt lgkmcnt(7)
v_add_f32 v237, v238, v239
MA 128,208,192
RB 228,252,19456
MA 132,208,196
v_rcp_f32 v236, v236
MA 136,208,200
v_rcp_f32 v237, v237
MA 140,208,204
s_waitcnt lgkmcnt(6)
MA 144,212,192
RT 208,250,51712
MA 148,212,196
RT 210,250,59904
MA 152,212,200
MA 156,212,204
s_waitcnt lgkmcnt(6)
MA 64,216,192
RT 212,251,51712
MA 68,216,196
RT 214,251,59904
MA 72,216,200
MA 76,216,204
s_waitcnt lgkmcnt(5)
MV 128,216,224
s_waitcnt lgkmcnt(4)
MV 132,216,228
MA 80,220,192
RT 216,250,53760
MA 84,220,196
RT 218,250,61952
MA 88,220,200
MA 92,220,204
MV 136,220,224
MV 140,220,228
s_waitcnt lgkmcnt(4)
MA 96,208,192
RT 220,251,53760
MA 100,208,196
RT 222,251,61952
MA 104,208,200
MA 108,208,204
MV 144,208,224
MV 148,208,228
s_waitcnt lgkmcnt(4)
MA 112,212,192
RT 208,250,55296
MA 116,212,196
RT 210,250,63488
MA 120,212,200
MA 124,212,204
MV 152,212,224
MV 156,212,228
s_waitcnt lgkmcnt(4)
MA 160,216,192
RT 212,251,55296
MA 164,216,196
RT 214,251,63488
MA 168,216,200
MA 172,216,204
s_waitcnt lgkmcnt(4)
MA 176,220,192
RT 216,250,55808
MA 180,220,196
RT 218,250,64000
MA 184,220,200
MA 188,220,204
s_waitcnt lgkmcnt(4)
MA 192,208,192
RT 220,251,55808
MA 196,208,196
MA 200,208,200
MA 204,208,204
s_waitcnt lgkmcnt(3)
MA 208,212,192
RT 222,251,64000
MA 212,212,196
MA 216,212,200
MA 220,212,204
s_waitcnt lgkmcnt(2)
MA 224,216,192
MA 228,216,196
MA 232,216,200
MA 236,216,204
s_waitcnt lgkmcnt(0)
MA 240,220,192
MA 244,220,196
MA 248,220,200
MA 252,220,204
s_waitcnt vmcnt(0)
s_waitcnt lgkmcnt(0)
s_barrier
s_getpc_b64 s[80:81]
Ljt1:
s_add_u32 s80, s80, LT-Ljt1
s_addc_u32 s81, s81, 0
s_setpc_b64 s[80:81]
Lst1_0:
s_call_b64 s[80:81], LR2f_1
s_branch Lrt1_0
Lst1_1:
s_call_b64 s[80:81], LH_1
s_branch Lrt1_1
Lst1_2:
s_call_b64 s[80:81], LR1_1_1
s_branch Lrt1_2
Lst1_3:
s_call_b64 s[80:81], LR2_1_1
s_branch Lrt1_3
Lst1_4:
s_call_b64 s[80:81], LH_1
s_branch Lrt1_4
Lst1_5:
s_call_b64 s[80:81], LR1_1_0
s_branch Lrt1_5
Lst1_6:
s_call_b64 s[80:81], LR2_1_0
s_branch Lrt1_6
Lst1_7:
s_call_b64 s[80:81], LH_1
s_branch Lrt1_7
Lst1_8:
s_call_b64 s[80:81], LR1_1_1
s_branch Lrt1_8
Lst1_9:
s_call_b64 s[80:81], LR2_1_1
s_branch Lrt1_9
Lst1_10:
s_call_b64 s[80:81], LH_1
s_branch Lrt1_10
Lst1_11:
s_call_b64 s[80:81], LR1_1_0
s_branch Lrt1_11
Lst1_12:
s_call_b64 s[80:81], LR2_1_0
s_branch Lrt1_12
Lst1_13:
s_call_b64 s[80:81], LH_1
s_branch Lrt1_13
Lst1_14:
s_call_b64 s[80:81], LR1_1_1
s_branch Lrt1_14
Lst1_15:
s_call_b64 s[80:81], LR2_1_1
s_branch Lrt1_15
Lst1_16:
s_call_b64 s[80:81], LH_1
s_branch Lrt1_16
Lst1_17:
s_call_b64 s[80:81], LR1_1_0
s_branch Lrt1_17
Lst1_18:
s_call_b64 s[80:81], LR2_1_0
s_branch Lrt1_18
Lst1_19:
s_call_b64 s[80:81], LH_1
s_branch Lrt1_19
Lst1_20:
s_call_b64 s[80:81], LR1_1_1
s_branch Lrt1_20
Lst1_21:
s_call_b64 s[80:81], LR2_1_1
s_branch Lrt1_21
Lst1_22:
s_call_b64 s[80:81], LH_1
s_branch Lrt1_22
LR1_1_0:
s_nop 8
s_waitcnt lgkmcnt(0)
s_bitcmp1_b32 s65, 0
s_cbranch_scc1 Lsv1_10
ds_write_b128 v252, v[236:239] offset:22528
s_or_b32 s65, s65, 1
s_waitcnt lgkmcnt(0)
Lsv1_10:
v_max3_f32 v246, v160, v161, v162
v_max_f32 v246, v246, v163
v_mov_b32 v248, v246
s_nop 1
v_permlane16_swap_b32 v246, v248
v_max_f32 v246, v246, v248
v_mov_b32 v248, v246
s_nop 1
v_permlane32_swap_b32 v246, v248
v_max_f32 v246, v246, v248
v_max_f32 v246, 0, v246
v_sub_f32 v236, v236, v246
v_sub_f32 v160, v160, v246
v_sub_f32 v161, v161, v246
v_sub_f32 v162, v162, v246
v_sub_f32 v163, v163, v246
v_exp_f32 v248, -v246
s_nop 0
v_mul_f32 v241, v241, v248
v_max3_f32 v246, v164, v165, v166
v_max_f32 v246, v246, v167
v_mov_b32 v248, v246
s_nop 1
v_permlane16_swap_b32 v246, v248
v_max_f32 v246, v246, v248
v_mov_b32 v248, v246
s_nop 1
v_permlane32_swap_b32 v246, v248
v_max_f32 v246, v246, v248
v_max_f32 v246, 0, v246
v_sub_f32 v237, v237, v246
v_sub_f32 v164, v164, v246
v_sub_f32 v165, v165, v246
v_sub_f32 v166, v166, v246
v_sub_f32 v167, v167, v246
v_exp_f32 v248, -v246
s_nop 0
v_mul_f32 v242, v242, v248
v_max3_f32 v246, v168, v169, v170
v_max_f32 v246, v246, v171
v_mov_b32 v248, v246
s_nop 1
v_permlane16_swap_b32 v246, v248
v_max_f32 v246, v246, v248
v_mov_b32 v248, v246
s_nop 1
v_permlane32_swap_b32 v246, v248
v_max_f32 v246, v246, v248
v_max_f32 v246, 0, v246
v_sub_f32 v238, v238, v246
v_sub_f32 v168, v168, v246
v_sub_f32 v169, v169, v246
v_sub_f32 v170, v170, v246
v_sub_f32 v171, v171, v246
v_exp_f32 v248, -v246
s_nop 0
v_mul_f32 v243, v243, v248
v_max3_f32 v246, v172, v173, v174
v_max_f32 v246, v246, v175
v_mov_b32 v248, v246
s_nop 1
v_permlane16_swap_b32 v246, v248
v_max_f32 v246, v246, v248
v_mov_b32 v248, v246
s_nop 1
v_permlane32_swap_b32 v246, v248
v_max_f32 v246, v246, v248
v_max_f32 v246, 0, v246
v_sub_f32 v239, v239, v246
v_sub_f32 v172, v172, v246
v_sub_f32 v173, v173, v246
v_sub_f32 v174, v174, v246
v_sub_f32 v175, v175, v246
v_exp_f32 v248, -v246
s_nop 0
v_mul_f32 v244, v244, v248
v_max3_f32 v246, v232, v233, v234
v_max_f32 v246, v246, v235
v_mov_b32 v248, v246
s_nop 1
v_permlane16_swap_b32 v246, v248
v_max_f32 v246, v246, v248
v_mov_b32 v248, v246
s_nop 1
v_permlane32_swap_b32 v246, v248
v_max_f32 v246, v246, v248
v_max_f32 v246, 0, v246
v_sub_f32 v246, v240, v246
ds_write_b32 v252, v246 offset:28676
v_mov_b32 v248, 0x7149f2ca
ds_write_b32 v252, v248 offset:29704
s_waitcnt lgkmcnt(0)
s_nop 2
s_setpc_b64 s[80:81]
LR2_1_0:
s_nop 8
s_waitcnt lgkmcnt(0)
v_readfirstlane_b32 s77, v247
s_bitcmp1_b32 s65, 0
s_cbranch_scc1 Lsv1_200
ds_write_b128 v252, v[236:239] offset:22528
s_or_b32 s65, s65, 1
s_waitcnt lgkmcnt(0)
Lsv1_200:
v_max3_f32 v246, v176, v177, v178
v_max_f32 v246, v246, v179
v_mov_b32 v248, v246
s_nop 1
v_permlane16_swap_b32 v246, v248
v_max_f32 v246, v246, v248
v_mov_b32 v248, v246
s_nop 1
v_permlane32_swap_b32 v246, v248
v_max_f32 v246, v246, v248
v_max_f32 v246, 0, v246
v_sub_f32 v236, v236, v246
v_sub_f32 v176, v176, v246
v_sub_f32 v177, v177, v246
v_sub_f32 v178, v178, v246
v_sub_f32 v179, v179, v246
v_exp_f32 v248, -v246
s_nop 0
v_mul_f32 v241, v241, v248
v_mul_f32 v160, v160, v248
v_mul_f32 v161, v161, v248
v_mul_f32 v162, v162, v248
v_mul_f32 v163, v163, v248
v_max3_f32 v246, v180, v181, v182
v_max_f32 v246, v246, v183
v_mov_b32 v248, v246
s_nop 1
v_permlane16_swap_b32 v246, v248
v_max_f32 v246, v246, v248
v_mov_b32 v248, v246
s_nop 1
v_permlane32_swap_b32 v246, v248
v_max_f32 v246, v246, v248
v_max_f32 v246, 0, v246
v_sub_f32 v237, v237, v246
v_sub_f32 v180, v180, v246
v_sub_f32 v181, v181, v246
v_sub_f32 v182, v182, v246
v_sub_f32 v183, v183, v246
v_exp_f32 v248, -v246
s_nop 0
v_mul_f32 v242, v242, v248
v_mul_f32 v164, v164, v248
v_mul_f32 v165, v165, v248
v_mul_f32 v166, v166, v248
v_mul_f32 v167, v167, v248
v_max3_f32 v246, v184, v185, v186
v_max_f32 v246, v246, v187
v_mov_b32 v248, v246
s_nop 1
v_permlane16_swap_b32 v246, v248
v_max_f32 v246, v246, v248
v_mov_b32 v248, v246
s_nop 1
v_permlane32_swap_b32 v246, v248
v_max_f32 v246, v246, v248
v_max_f32 v246, 0, v246
v_sub_f32 v238, v238, v246
v_sub_f32 v184, v184, v246
v_sub_f32 v185, v185, v246
v_sub_f32 v186, v186, v246
v_sub_f32 v187, v187, v246
v_exp_f32 v248, -v246
s_nop 0
v_mul_f32 v243, v243, v248
v_mul_f32 v168, v168, v248
v_mul_f32 v169, v169, v248
v_mul_f32 v170, v170, v248
v_sub_f32 v171, v171, v246
v_max3_f32 v246, v188, v189, v190
v_max_f32 v246, v246, v191
v_mov_b32 v248, v246
s_nop 1
v_permlane16_swap_b32 v246, v248
v_max_f32 v246, v246, v248
v_mov_b32 v248, v246
s_nop 1
v_permlane32_swap_b32 v246, v248
v_max_f32 v246, v246, v248
v_max_f32 v246, 0, v246
v_sub_f32 v239, v239, v246
v_sub_f32 v188, v188, v246
v_sub_f32 v189, v189, v246
v_sub_f32 v190, v190, v246
v_sub_f32 v191, v191, v246
v_exp_f32 v248, -v246
s_nop 0
v_mul_f32 v244, v244, v248
v_sub_f32 v172, v172, v246
v_sub_f32 v173, v173, v246
v_sub_f32 v174, v174, v246
v_sub_f32 v175, v175, v246
s_cmp_eq_u32 s77, 0
s_cbranch_scc1 LR2s_1_0
ds_read_b32 v246, v252 offset:29696
ds_read_b32 v247, v252 offset:28672
ds_read_b32 v248, v252 offset:28676
s_waitcnt lgkmcnt(0)
v_min3_f32 v254, v246, v247, v248
v_sub_f32 v246, v254, v246
v_exp_f32 v246, v246
s_nop 0
ds_write_b32 v252, v246 offset:23552
s_waitcnt lgkmcnt(0)
ds_read_b32 v246, v252 offset:29700
ds_read_b32 v247, v252 offset:28680
ds_read_b32 v248, v252 offset:28684
s_waitcnt lgkmcnt(0)
v_min3_f32 v255, v246, v247, v248
v_sub_f32 v246, v255, v246
v_exp_f32 v246, v246
s_nop 0
ds_write_b32 v252, v246 offset:23556
s_waitcnt lgkmcnt(0)
v_sub_f32 v246, v240, v254
v_mov_b32 v240, v254
v_sub_f32 v232, v232, v246
v_sub_f32 v233, v233, v246
v_sub_f32 v234, v234, v246
v_sub_f32 v235, v235, v246
v_exp_f32 v248, -v246
s_nop 0
v_mul_f32 v245, v245, v248
s_or_b32 s65, s65, 2
s_waitcnt lgkmcnt(0)
s_barrier
s_barrier
LR2s_1_0:
s_waitcnt lgkmcnt(0)
s_nop 2
s_setpc_b64 s[80:81]
LR2f_1:
s_nop 8
s_waitcnt lgkmcnt(0)
v_readfirstlane_b32 s77, v247
s_bitcmp1_b32 s65, 0
s_cbranch_scc1 Lsv1_201
ds_write_b128 v252, v[236:239] offset:22528
s_or_b32 s65, s65, 1
s_waitcnt lgkmcnt(0)
Lsv1_201:
v_max3_f32 v246, v176, v177, v178
v_max_f32 v246, v246, v179
v_mov_b32 v248, v246
s_nop 1
v_permlane16_swap_b32 v246, v248
v_max_f32 v246, v246, v248
v_mov_b32 v248, v246
s_nop 1
v_permlane32_swap_b32 v246, v248
v_max_f32 v246, v246, v248
v_max_f32 v246, 0, v246
v_sub_f32 v236, v236, v246
v_sub_f32 v176, v176, v246
v_sub_f32 v177, v177, v246
v_sub_f32 v178, v178, v246
v_sub_f32 v179, v179, v246
v_exp_f32 v248, -v246
s_nop 0
v_mul_f32 v241, v241, v248
v_mul_f32 v160, v160, v248
v_mul_f32 v161, v161, v248
v_mul_f32 v162, v162, v248
v_mul_f32 v163, v163, v248
v_max3_f32 v246, v180, v181, v182
v_max_f32 v246, v246, v183
v_mov_b32 v248, v246
s_nop 1
v_permlane16_swap_b32 v246, v248
v_max_f32 v246, v246, v248
v_mov_b32 v248, v246
s_nop 1
v_permlane32_swap_b32 v246, v248
v_max_f32 v246, v246, v248
v_max_f32 v246, 0, v246
v_sub_f32 v237, v237, v246
v_sub_f32 v180, v180, v246
v_sub_f32 v181, v181, v246
v_sub_f32 v182, v182, v246
v_sub_f32 v183, v183, v246
v_exp_f32 v248, -v246
s_nop 0
v_mul_f32 v242, v242, v248
v_mul_f32 v164, v164, v248
v_mul_f32 v165, v165, v248
v_mul_f32 v166, v166, v248
v_mul_f32 v167, v167, v248
v_max3_f32 v246, v184, v185, v186
v_max_f32 v246, v246, v187
v_mov_b32 v248, v246
s_nop 1
v_permlane16_swap_b32 v246, v248
v_max_f32 v246, v246, v248
v_mov_b32 v248, v246
s_nop 1
v_permlane32_swap_b32 v246, v248
v_max_f32 v246, v246, v248
v_max_f32 v246, 0, v246
v_sub_f32 v238, v238, v246
v_sub_f32 v184, v184, v246
v_sub_f32 v185, v185, v246
v_sub_f32 v186, v186, v246
v_sub_f32 v187, v187, v246
v_exp_f32 v248, -v246
s_nop 0
v_mul_f32 v243, v243, v248
v_mul_f32 v168, v168, v248
v_mul_f32 v169, v169, v248
v_mul_f32 v170, v170, v248
v_mul_f32 v171, v171, v248
v_max3_f32 v246, v188, v189, v190
v_max_f32 v246, v246, v191
v_mov_b32 v248, v246
s_nop 1
v_permlane16_swap_b32 v246, v248
v_max_f32 v246, v246, v248
v_mov_b32 v248, v246
s_nop 1
v_permlane32_swap_b32 v246, v248
v_max_f32 v246, v246, v248
v_max_f32 v246, 0, v246
v_sub_f32 v239, v239, v246
v_sub_f32 v188, v188, v246
v_sub_f32 v189, v189, v246
v_sub_f32 v190, v190, v246
v_sub_f32 v191, v191, v246
v_exp_f32 v248, -v246
s_nop 0
v_mul_f32 v244, v244, v248
v_mul_f32 v172, v172, v248
v_mul_f32 v173, v173, v248
v_mul_f32 v174, v174, v248
v_mul_f32 v175, v175, v248
s_waitcnt lgkmcnt(0)
s_nop 2
s_setpc_b64 s[80:81]
LR1_1_1:
s_nop 8
s_waitcnt lgkmcnt(0)
s_bitcmp1_b32 s65, 0
s_cbranch_scc1 Lsv1_11
ds_write_b128 v252, v[236:239] offset:22528
s_or_b32 s65, s65, 1
s_waitcnt lgkmcnt(0)
Lsv1_11:
v_max3_f32 v246, v192, v193, v194
v_max_f32 v246, v246, v195
v_mov_b32 v248, v246
s_nop 1
v_permlane16_swap_b32 v246, v248
v_max_f32 v246, v246, v248
v_mov_b32 v248, v246
s_nop 1
v_permlane32_swap_b32 v246, v248
v_max_f32 v246, v246, v248
v_max_f32 v246, 0, v246
v_sub_f32 v236, v236, v246
v_sub_f32 v192, v192, v246
v_sub_f32 v193, v193, v246
v_sub_f32 v194, v194, v246
v_sub_f32 v195, v195, v246
v_exp_f32 v248, -v246
s_nop 0
v_mul_f32 v241, v241, v248
v_max3_f32 v246, v196, v197, v198
v_max_f32 v246, v246, v199
v_mov_b32 v248, v246
s_nop 1
v_permlane16_swap_b32 v246, v248
v_max_f32 v246, v246, v248
v_mov_b32 v248, v246
s_nop 1
v_permlane32_swap_b32 v246, v248
v_max_f32 v246, v246, v248
v_max_f32 v246, 0, v246
v_sub_f32 v237, v237, v246
v_sub_f32 v196, v196, v246
v_sub_f32 v197, v197, v246
v_sub_f32 v198, v198, v246
v_sub_f32 v199, v199, v246
v_exp_f32 v248, -v246
s_nop 0
v_mul_f32 v242, v242, v248
v_max3_f32 v246, v200, v201, v202
v_max_f32 v246, v246, v203
v_mov_b32 v248, v246
s_nop 1
v_permlane16_swap_b32 v246, v248
v_max_f32 v246, v246, v248
v_mov_b32 v248, v246
s_nop 1
v_permlane32_swap_b32 v246, v248
v_max_f32 v246, v246, v248
v_max_f32 v246, 0, v246
v_sub_f32 v238, v238, v246
v_sub_f32 v200, v200, v246
v_sub_f32 v201, v201, v246
v_sub_f32 v202, v202, v246
v_sub_f32 v203, v203, v246
v_exp_f32 v248, -v246
s_nop 0
v_mul_f32 v243, v243, v248
v_max3_f32 v246, v204, v205, v206
v_max_f32 v246, v246, v207
v_mov_b32 v248, v246
s_nop 1
v_permlane16_swap_b32 v246, v248
v_max_f32 v246, v246, v248
v_mov_b32 v248, v246
s_nop 1
v_permlane32_swap_b32 v246, v248
v_max_f32 v246, v246, v248
v_max_f32 v246, 0, v246
v_sub_f32 v239, v239, v246
v_sub_f32 v204, v204, v246
v_sub_f32 v205, v205, v246
v_sub_f32 v206, v206, v246
v_sub_f32 v207, v207, v246
v_exp_f32 v248, -v246
s_nop 0
v_mul_f32 v244, v244, v248
v_max3_f32 v246, v232, v233, v234
v_max_f32 v246, v246, v235
v_mov_b32 v248, v246
s_nop 1
v_permlane16_swap_b32 v246, v248
v_max_f32 v246, v246, v248
v_mov_b32 v248, v246
s_nop 1
v_permlane32_swap_b32 v246, v248
v_max_f32 v246, v246, v248
v_max_f32 v246, 0, v246
v_sub_f32 v246, v240, v246
ds_write_b32 v252, v246 offset:28676
v_mov_b32 v248, 0x7149f2ca
ds_write_b32 v252, v248 offset:29704
s_waitcnt lgkmcnt(0)
s_nop 2
s_setpc_b64 s[80:81]
LR2_1_1:
s_nop 8
s_waitcnt lgkmcnt(0)
v_readfirstlane_b32 s77, v247
s_bitcmp1_b32 s65, 0
s_cbranch_scc1 Lsv1_210
ds_write_b128 v252, v[236:239] offset:22528
s_or_b32 s65, s65, 1
s_waitcnt lgkmcnt(0)
Lsv1_210:
v_max3_f32 v246, v176, v177, v178
v_max_f32 v246, v246, v179
v_mov_b32 v248, v246
s_nop 1
v_permlane16_swap_b32 v246, v248
v_max_f32 v246, v246, v248
v_mov_b32 v248, v246
s_nop 1
v_permlane32_swap_b32 v246, v248
v_max_f32 v246, v246, v248
v_max_f32 v246, 0, v246
v_sub_f32 v236, v236, v246
v_sub_f32 v176, v176, v246
v_sub_f32 v177, v177, v246
v_sub_f32 v178, v178, v246
v_sub_f32 v179, v179, v246
v_exp_f32 v248, -v246
s_nop 0
v_mul_f32 v241, v241, v248
v_mul_f32 v192, v192, v248
v_mul_f32 v193, v193, v248
v_mul_f32 v194, v194, v248
v_mul_f32 v195, v195, v248
v_max3_f32 v246, v180, v181, v182
v_max_f32 v246, v246, v183
v_mov_b32 v248, v246
s_nop 1
v_permlane16_swap_b32 v246, v248
v_max_f32 v246, v246, v248
v_mov_b32 v248, v246
s_nop 1
v_permlane32_swap_b32 v246, v248
v_max_f32 v246, v246, v248
v_max_f32 v246, 0, v246
v_sub_f32 v237, v237, v246
v_sub_f32 v180, v180, v246
v_sub_f32 v181, v181, v246
v_sub_f32 v182, v182, v246
v_sub_f32 v183, v183, v246
v_exp_f32 v248, -v246
s_nop 0
v_mul_f32 v242, v242, v248
v_mul_f32 v196, v196, v248
v_mul_f32 v197, v197, v248
v_mul_f32 v198, v198, v248
v_mul_f32 v199, v199, v248
v_max3_f32 v246, v184, v185, v186
v_max_f32 v246, v246, v187
v_mov_b32 v248, v246
s_nop 1
v_permlane16_swap_b32 v246, v248
v_max_f32 v246, v246, v248
v_mov_b32 v248, v246
s_nop 1
v_permlane32_swap_b32 v246, v248
v_max_f32 v246, v246, v248
v_max_f32 v246, 0, v246
v_sub_f32 v238, v238, v246
v_sub_f32 v184, v184, v246
v_sub_f32 v185, v185, v246
v_sub_f32 v186, v186, v246
v_sub_f32 v187, v187, v246
v_exp_f32 v248, -v246
s_nop 0
v_mul_f32 v243, v243, v248
v_mul_f32 v200, v200, v248
v_mul_f32 v201, v201, v248
v_mul_f32 v202, v202, v248
v_sub_f32 v203, v203, v246
v_max3_f32 v246, v188, v189, v190
v_max_f32 v246, v246, v191
v_mov_b32 v248, v246
s_nop 1
v_permlane16_swap_b32 v246, v248
v_max_f32 v246, v246, v248
v_mov_b32 v248, v246
s_nop 1
v_permlane32_swap_b32 v246, v248
v_max_f32 v246, v246, v248
v_max_f32 v246, 0, v246
v_sub_f32 v239, v239, v246
v_sub_f32 v188, v188, v246
v_sub_f32 v189, v189, v246
v_sub_f32 v190, v190, v246
v_sub_f32 v191, v191, v246
v_exp_f32 v248, -v246
s_nop 0
v_mul_f32 v244, v244, v248
v_sub_f32 v204, v204, v246
v_sub_f32 v205, v205, v246
v_sub_f32 v206, v206, v246
v_sub_f32 v207, v207, v246
s_cmp_eq_u32 s77, 0
s_cbranch_scc1 LR2s_1_1
ds_read_b32 v246, v252 offset:29696
ds_read_b32 v247, v252 offset:28672
ds_read_b32 v248, v252 offset:28676
s_waitcnt lgkmcnt(0)
v_min3_f32 v254, v246, v247, v248
v_sub_f32 v246, v254, v246
v_exp_f32 v246, v246
s_nop 0
ds_write_b32 v252, v246 offset:23552
s_waitcnt lgkmcnt(0)
ds_read_b32 v246, v252 offset:29700
ds_read_b32 v247, v252 offset:28680
ds_read_b32 v248, v252 offset:28684
s_waitcnt lgkmcnt(0)
v_min3_f32 v255, v246, v247, v248
v_sub_f32 v246, v255, v246
v_exp_f32 v246, v246
s_nop 0
ds_write_b32 v252, v246 offset:23556
s_waitcnt lgkmcnt(0)
v_sub_f32 v246, v240, v254
v_mov_b32 v240, v254
v_sub_f32 v232, v232, v246
v_sub_f32 v233, v233, v246
v_sub_f32 v234, v234, v246
v_sub_f32 v235, v235, v246
v_exp_f32 v248, -v246
s_nop 0
v_mul_f32 v245, v245, v248
s_or_b32 s65, s65, 2
s_waitcnt lgkmcnt(0)
s_barrier
s_barrier
LR2s_1_1:
s_waitcnt lgkmcnt(0)
s_nop 2
s_setpc_b64 s[80:81]
LH_1:
s_nop 15
s_waitcnt lgkmcnt(0)
s_bitcmp1_b32 s65, 0
s_cbranch_scc0 LH2_1
ds_read_b32 v246, v252 offset:22528
s_waitcnt lgkmcnt(0)
v_sub_f32 v246, v236, v246
v_exp_f32 v246, v246
s_nop 1
.set ci_, 0
.rept 16
v_accvgpr_read_b32 v247, a[ci_+0]
v_accvgpr_read_b32 v248, a[ci_+1]
v_accvgpr_read_b32 v254, a[ci_+2]
v_accvgpr_read_b32 v255, a[ci_+3]
v_mul_f32 v247, v247, v246
v_mul_f32 v248, v248, v246
v_mul_f32 v254, v254, v246
v_mul_f32 v255, v255, v246
v_accvgpr_write_b32 a[ci_+0], v247
v_accvgpr_write_b32 a[ci_+1], v248
v_accvgpr_write_b32 a[ci_+2], v254
v_accvgpr_write_b32 a[ci_+3], v255
.set ci_, ci_+16
.endr
ds_read_b32 v246, v252 offset:22532
s_waitcnt lgkmcnt(0)
v_sub_f32 v246, v237, v246
v_exp_f32 v246, v246
s_nop 1
.set ci_, 4
.rept 16
v_accvgpr_read_b32 v247, a[ci_+0]
v_accvgpr_read_b32 v248, a[ci_+1]
v_accvgpr_read_b32 v254, a[ci_+2]
v_accvgpr_read_b32 v255, a[ci_+3]
v_mul_f32 v247, v247, v246
v_mul_f32 v248, v248, v246
v_mul_f32 v254, v254, v246
v_mul_f32 v255, v255, v246
v_accvgpr_write_b32 a[ci_+0], v247
v_accvgpr_write_b32 a[ci_+1], v248
v_accvgpr_write_b32 a[ci_+2], v254
v_accvgpr_write_b32 a[ci_+3], v255
.set ci_, ci_+16
.endr
ds_read_b32 v246, v252 offset:22536
s_waitcnt lgkmcnt(0)
v_sub_f32 v246, v238, v246
v_exp_f32 v246, v246
s_nop 1
.set ci_, 8
.rept 16
v_accvgpr_read_b32 v247, a[ci_+0]
v_accvgpr_read_b32 v248, a[ci_+1]
v_accvgpr_read_b32 v254, a[ci_+2]
v_accvgpr_read_b32 v255, a[ci_+3]
v_mul_f32 v247, v247, v246
v_mul_f32 v248, v248, v246
v_mul_f32 v254, v254, v246
v_mul_f32 v255, v255, v246
v_accvgpr_write_b32 a[ci_+0], v247
v_accvgpr_write_b32 a[ci_+1], v248
v_accvgpr_write_b32 a[ci_+2], v254
v_accvgpr_write_b32 a[ci_+3], v255
.set ci_, ci_+16
.endr
ds_read_b32 v246, v252 offset:22540
s_waitcnt lgkmcnt(0)
v_sub_f32 v246, v239, v246
v_exp_f32 v246, v246
s_nop 1
.set ci_, 12
.rept 16
v_accvgpr_read_b32 v247, a[ci_+0]
v_accvgpr_read_b32 v248, a[ci_+1]
v_accvgpr_read_b32 v254, a[ci_+2]
v_accvgpr_read_b32 v255, a[ci_+3]
v_mul_f32 v247, v247, v246
v_mul_f32 v248, v248, v246
v_mul_f32 v254, v254, v246
v_mul_f32 v255, v255, v246
v_accvgpr_write_b32 a[ci_+0], v247
v_accvgpr_write_b32 a[ci_+1], v248
v_accvgpr_write_b32 a[ci_+2], v254
v_accvgpr_write_b32 a[ci_+3], v255
.set ci_, ci_+16
.endr
LH2_1:
s_bitcmp1_b32 s65, 1
s_cbranch_scc0 LH3_1
ds_read_b32 v246, v252 offset:23552
s_waitcnt lgkmcnt(0)
v_mul_f32 v128, v128, v246
v_mul_f32 v129, v129, v246
v_mul_f32 v130, v130, v246
v_mul_f32 v131, v131, v246
v_mul_f32 v136, v136, v246
v_mul_f32 v137, v137, v246
v_mul_f32 v138, v138, v246
v_mul_f32 v139, v139, v246
v_mul_f32 v144, v144, v246
v_mul_f32 v145, v145, v246
v_mul_f32 v146, v146, v246
v_mul_f32 v147, v147, v246
v_mul_f32 v152, v152, v246
v_mul_f32 v153, v153, v246
v_mul_f32 v154, v154, v246
v_mul_f32 v155, v155, v246
ds_read_b32 v246, v252 offset:23556
s_waitcnt lgkmcnt(0)
v_mul_f32 v132, v132, v246
v_mul_f32 v133, v133, v246
v_mul_f32 v134, v134, v246
v_mul_f32 v135, v135, v246
v_mul_f32 v140, v140, v246
v_mul_f32 v141, v141, v246
v_mul_f32 v142, v142, v246
v_mul_f32 v143, v143, v246
v_mul_f32 v148, v148, v246
v_mul_f32 v149, v149, v246
v_mul_f32 v150, v150, v246
v_mul_f32 v151, v151, v246
v_mul_f32 v156, v156, v246
v_mul_f32 v157, v157, v246
v_mul_f32 v158, v158, v246
v_mul_f32 v159, v159, v246
LH3_1:
s_mov_b32 s65, 0
s_waitcnt lgkmcnt(0)
s_nop 2
s_setpc_b64 s[80:81]
LW2:
v_mbcnt_lo_u32_b32 v228, -1, 0
v_mbcnt_hi_u32_b32 v228, -1, v228
v_bfe_u32 v231, v228, 2, 3
v_lshrrev_b32 v232, 2, v231
v_sub_u32 v234, 4, v232
v_and_b32 v234, 3, v234
v_and_b32 v233, 3, v228
v_xor_b32 v233, v233, v234
v_lshlrev_b32 v233, 4, v233
v_lshrrev_b32 v234, 5, v228
v_lshl_add_u32 v233, v234, 6, v233
v_and_b32 v234, 3, v231
v_lshl_add_u32 v234, v232, 3, v234
v_add_u32 v234, 4, v234
v_lshl_add_u32 v225, v234, 13, v233
v_add_u32 v234, 16, v231
v_lshl_add_u32 v253, v234, 9, v233
v_add_u32 v234, 0, v231
v_mul_u32_u24 v234, 0x380, v234
v_add_u32 v226, v234, v233
s_and_b32 s77, s26, 1
s_mul_i32 s80, s77, 129024
s_sub_u32 s81, 129024, s80
s_add_u32 s67, s80, 14336
s_add_u32 s68, s81, 14336
s_add_u32 s69, s80, 43008
s_add_u32 s70, s81, 43008
s_add_u32 s71, s80, 71680
s_add_u32 s72, s81, 71680
s_add_u32 s73, s80, 100352
s_add_u32 s74, s81, 100352
s_add_u32 s75, s81, 114688
s_mov_b32 s76, 0
s_mov_b32 s62, 6
s_mov_b32 s63, 0x3db8aa3b
s_mov_b32 s66, 0xbdb8aa3b
s_mov_b32 s64, 0x41800000
s_mov_b32 s65, 1
s_mov_b32 s50, 3662336
s_mov_b32 s51, 0x20000
s_mov_b32 s54, 258048
s_mov_b32 s55, 0x20000
s_mov_b32 s42, 524288
s_mov_b32 s43, 0x20000
s_mov_b32 s46, 524288
s_mov_b32 s47, 0x20000
s_mov_b32 s58, Lcodeend-Lbase
s_mov_b32 s59, 0x20000
v_lshlrev_b32 v246, 7, v228
buffer_load_dword v247, v246, s[56:59], s78 offen
s_waitcnt lgkmcnt(0)
s_add_u32 s52, s16, s60
s_addc_u32 s53, s17, 0
s_and_b32 s53, s53, 0xffff
s_add_u32 s48, s22, s61
s_addc_u32 s49, s23, 0
s_and_b32 s49, s49, 0xffff
s_mov_b32 m0, 38912
s_nop 0
buffer_load_dwordx4 v226, s[52:55], s67 offen offset:0 lds
s_mov_b32 m0, 43008
s_nop 0
buffer_load_dwordx4 v226, s[52:55], s68 offen offset:0 lds
s_mov_b32 m0, 47104
s_nop 0
buffer_load_dwordx4 v226, s[52:55], s69 offen offset:0 lds
s_mov_b32 m0, 51200
s_nop 0
buffer_load_dwordx4 v226, s[52:55], s70 offen offset:0 lds
s_mov_b32 m0, 55296
s_nop 0
buffer_load_dwordx4 v226, s[52:55], s71 offen offset:0 lds
s_mov_b32 m0, 59392
s_nop 0
buffer_load_dwordx4 v226, s[52:55], s72 offen offset:0 lds
s_mov_b32 m0, 63488
s_nop 0
buffer_load_dwordx4 v226, s[52:55], s73 offen offset:0 lds
s_mov_b32 m0, 67584
s_nop 0
buffer_load_dwordx4 v226, s[52:55], s74 offen offset:0 lds
s_mov_b32 m0, 71680
s_nop 0
buffer_load_dwordx4 v226, s[52:55], s75 offen offset:0 lds
v_add_u32 v226, 0x80, v226
s_mov_b32 m0, 139264
s_nop 0
buffer_load_dwordx4 v225, s[48:51], s76 offen offset:0 lds
s_mov_b32 m0, 141184
s_nop 0
buffer_load_dwordx4 v225, s[48:51], s76 offen offset:128 lds
s_mov_b32 m0, 143104
s_nop 0
buffer_load_dwordx4 v225, s[48:51], s76 offen offset:256 lds
s_mov_b32 m0, 145024
s_nop 0
buffer_load_dwordx4 v225, s[48:51], s76 offen offset:384 lds
s_add_u32 s76, s76, 262144
s_mov_b32 m0, 155648
s_nop 0
buffer_load_dwordx4 v225, s[48:51], s76 offen offset:0 lds
s_mov_b32 m0, 157568
s_nop 0
buffer_load_dwordx4 v225, s[48:51], s76 offen offset:128 lds
s_mov_b32 m0, 159488
s_nop 0
buffer_load_dwordx4 v225, s[48:51], s76 offen offset:256 lds
s_mov_b32 m0, 161408
s_nop 0
buffer_load_dwordx4 v225, s[48:51], s76 offen offset:384 lds
s_add_u32 s76, s76, 262144
s_and_b32 s60, s26, 127
s_lshl_b32 s60, s60, 19
s_add_u32 s40, s18, s60
s_addc_u32 s41, s19, 0
s_and_b32 s41, s41, 0xffff
s_add_u32 s44, s20, s60
s_addc_u32 s45, s21, 0
s_and_b32 s45, s45, 0xffff
s_mov_b32 s60, 0
s_mov_b32 s61, 0
s_lshl_b32 s77, s26, 9
s_and_b32 s77, s77, 0xffffe000
s_add_u32 s77, s77, 100663296
s_add_u32 s38, s28, s77
s_addc_u32 s39, s29, 0
s_and_b32 s77, s26, 15
s_lshl_b32 s36, s77, 8
s_add_u32 s36, s36, 256
s_add_u32 s36, s36, s38
s_addc_u32 s37, s39, 0
s_lshl_b32 s77, s77, 2
s_add_u32 s38, s38, s77
s_addc_u32 s39, s39, 0
s_mov_b32 m0, 2048
s_nop 0
buffer_load_dwordx4 v226, s[52:55], s67 offen offset:0 lds
s_mov_b32 m0, 6144
s_nop 0
buffer_load_dwordx4 v226, s[52:55], s68 offen offset:0 lds
s_mov_b32 m0, 10240
s_nop 0
buffer_load_dwordx4 v226, s[52:55], s69 offen offset:0 lds
s_mov_b32 m0, 14336
s_nop 0
buffer_load_dwordx4 v226, s[52:55], s70 offen offset:0 lds
s_mov_b32 m0, 18432
s_nop 0
buffer_load_dwordx4 v226, s[52:55], s71 offen offset:0 lds
s_mov_b32 m0, 22528
s_nop 0
buffer_load_dwordx4 v226, s[52:55], s72 offen offset:0 lds
s_mov_b32 m0, 26624
s_nop 0
buffer_load_dwordx4 v226, s[52:55], s73 offen offset:0 lds
s_mov_b32 m0, 30720
s_nop 0
buffer_load_dwordx4 v226, s[52:55], s74 offen offset:0 lds
s_mov_b32 m0, 34816
s_nop 0
buffer_load_dwordx4 v226, s[52:55], s75 offen offset:0 lds
v_add_u32 v226, 0x80, v226
v_lshrrev_b32 v229, 4, v228
v_and_b32 v230, 15, v228
v_lshlrev_b32 v231, 4, v228
v_add_u32 v252, 0x10000, v231
v_lshrrev_b32 v231, 2, v230
v_sub_u32 v234, 4, v231
v_and_b32 v234, 3, v234
v_xor_b32 v234, v234, v229
v_lshlrev_b32 v234, 4, v234
v_and_b32 v231, 7, v230
v_lshl_add_u32 v234, v231, 6, v234
v_lshrrev_b32 v231, 3, v230
v_lshl_add_u32 v249, v231, 10, v234
v_add_u32 v227, 0x9000, v249
v_lshrrev_b32 v231, 2, v230
v_and_b32 v232, 1, v229
v_lshl_add_u32 v231, v232, 2, v231
v_lshlrev_b32 v231, 6, v231
v_lshrrev_b32 v232, 1, v229
v_lshl_add_u32 v231, v232, 10, v231
v_add_u32 v231, 0x18000, v231
v_sub_u32 v234, 4, v229
v_and_b32 v234, 3, v234
v_and_b32 v232, 3, v228
v_xor_b32 v233, v232, v234
v_lshl_add_u32 v224, v233, 4, v231
v_lshrrev_b32 v233, 1, v232
v_xor_b32 v233, v233, v234
v_lshl_add_u32 v233, v233, 4, v231
v_and_b32 v232, 1, v232
v_lshl_add_u32 v250, v232, 3, v233
v_xor_b32 v251, 32, v250
v_mov_b32 v241, 0
v_mov_b32 v242, 0
v_mov_b32 v243, 0
v_mov_b32 v244, 0
v_mov_b32 v245, 0
s_waitcnt vmcnt(9)
s_barrier
RT 160,224,36864
RT 162,224,45056
RT 164,224,53248
RT 166,224,61440
ds_read_b128 a[0:3], v227 offset:16384
ds_read_b128 a[4:7], v227 offset:16896
ds_read_b128 a[8:11], v227 offset:18432
ds_read_b128 a[12:15], v227 offset:18944
ds_read_b128 a[16:19], v227 offset:20480
ds_read_b128 a[20:23], v227 offset:20992
ds_read_b128 a[24:27], v227 offset:22528
ds_read_b128 a[28:31], v227 offset:23040
ds_read_b128 a[32:35], v227 offset:32768
ds_read_b128 a[36:39], v227 offset:33280
ds_read_b128 a[40:43], v227 offset:34816
ds_read_b128 a[44:47], v227 offset:35328
RT 168,224,36872
RT 170,224,45064
RT 172,224,53256
RT 174,224,61448
s_waitcnt lgkmcnt(15)
MG0 a,128,v,160,a,0
RT 176,224,37376
s_waitcnt lgkmcnt(15)
MG a,128,v,164,a,4
RT 178,224,45568
s_waitcnt lgkmcnt(15)
MG0 a,132,v,160,a,8
RT 180,224,53760
s_waitcnt lgkmcnt(15)
MG a,132,v,164,a,12
RT 182,224,61952
s_mov_b32 m0, 106496
s_waitcnt lgkmcnt(15)
MG0 a,136,v,160,a,16
buffer_load_dwordx4 v225, s[48:51], s76 offen offset:0 lds
s_waitcnt lgkmcnt(14)
MG a,136,v,164,a,20
s_waitcnt lgkmcnt(13)
MG0 a,140,v,160,a,24
s_waitcnt lgkmcnt(10)
MG a,140,v,164,a,28
s_mov_b32 m0, 108416
MG0 v,128,v,160,a,32
buffer_load_dwordx4 v225, s[48:51], s76 offen offset:128 lds
MG v,128,v,164,a,36
s_waitcnt lgkmcnt(9)
MG0 v,132,v,160,a,40
s_waitcnt lgkmcnt(8)
MG v,132,v,164,a,44
s_waitcnt lgkmcnt(4)
MG0 a,144,v,168,a,0
RT 160,224,37384
MG a,144,v,172,a,4
RT 162,224,45576
MG0 a,148,v,168,a,8
RT 164,224,53768
MG a,148,v,172,a,12
RT 166,224,61960
s_mov_b32 m0, 110336
MG0 a,152,v,168,a,16
buffer_load_dwordx4 v225, s[48:51], s76 offen offset:256 lds
MG a,152,v,172,a,20
MG0 a,156,v,168,a,24
MG a,156,v,172,a,28
s_mov_b32 m0, 112256
MG0 v,136,v,168,a,32
buffer_load_dwordx4 v225, s[48:51], s76 offen offset:384 lds
s_add_u32 s76, s76, 262144
MG v,136,v,172,a,36
MG0 v,140,v,168,a,40
MG v,140,v,172,a,44
s_waitcnt lgkmcnt(4)
MG0 a,160,v,176,a,0
RT 168,224,32768
MG a,160,v,180,a,4
RT 170,224,40960
MG0 a,164,v,176,a,8
RT 172,224,49152
MG a,164,v,180,a,12
RT 174,224,57344
s_mov_b32 m0, 122880
MG0 a,168,v,176,a,16
buffer_load_dwordx4 v225, s[48:51], s76 offen offset:0 lds
MG a,168,v,180,a,20
MG0 a,172,v,176,a,24
MG a,172,v,180,a,28
s_mov_b32 m0, 124800
MG0 v,144,v,176,a,32
buffer_load_dwordx4 v225, s[48:51], s76 offen offset:128 lds
MG v,144,v,180,a,36
MG0 v,148,v,176,a,40
MG v,148,v,180,a,44
s_waitcnt lgkmcnt(4)
MG0 a,176,v,160,a,0
RT 176,224,32776
MG a,176,v,164,a,4
RT 178,224,40968
MG0 a,180,v,160,a,8
RT 180,224,49160
MG a,180,v,164,a,12
RT 182,224,57352
s_mov_b32 m0, 126720
MG0 a,184,v,160,a,16
buffer_load_dwordx4 v225, s[48:51], s76 offen offset:256 lds
MG a,184,v,164,a,20
MG0 a,188,v,160,a,24
MG a,188,v,164,a,28
s_mov_b32 m0, 128640
MG0 v,152,v,160,a,32
buffer_load_dwordx4 v225, s[48:51], s76 offen offset:384 lds
s_add_u32 s76, s76, 262144
MG v,152,v,164,a,36
MG0 v,156,v,160,a,40
MG v,156,v,164,a,44
s_waitcnt lgkmcnt(4)
MG0 v,0,v,168,a,0
RT 160,224,33280
MG v,0,v,172,a,4
RT 162,224,41472
MG0 v,32,v,168,a,8
RT 164,224,49664
MG v,32,v,172,a,12
RT 166,224,57856
MG0 v,64,v,168,a,16
MG v,64,v,172,a,20
MG0 v,96,v,168,a,24
MG v,96,v,172,a,28
s_waitcnt lgkmcnt(4)
MG0 v,4,v,176,a,0
RT 168,224,33288
MG v,4,v,180,a,4
RT 170,224,41480
MG0 v,36,v,176,a,8
RT 172,224,49672
MG v,36,v,180,a,12
RT 174,224,57864
MG0 v,68,v,176,a,16
MG v,68,v,180,a,20
MG0 v,100,v,176,a,24
MG v,100,v,180,a,28
s_waitcnt lgkmcnt(4)
MG0 v,8,v,160,a,0
RT 176,224,34816
MG v,8,v,164,a,4
RT 178,224,43008
MG0 v,40,v,160,a,8
RT 180,224,51200
MG v,40,v,164,a,12
RT 182,224,59392
MG0 v,72,v,160,a,16
MG v,72,v,164,a,20
MG0 v,104,v,160,a,24
s_waitcnt vmcnt(8) lgkmcnt(4)
MG v,104,v,164,a,28
s_barrier
MG0 v,12,v,168,a,0
RT 160,224,34824
MG v,12,v,172,a,4
RT 162,224,43016
MG0 v,44,v,168,a,8
RT 164,224,51208
MG v,44,v,172,a,12
RT 166,224,59400
MG0 v,76,v,168,a,16
ds_read_b128 a[48:51], v249 offset:16384
s_mov_b32 m0, 38912
MG v,76,v,172,a,20
buffer_load_dwordx4 v226, s[52:55], s67 offen offset:0 lds
MG0 v,108,v,168,a,24
ds_read_b128 a[52:55], v249 offset:16896
MG v,108,v,172,a,28
s_waitcnt lgkmcnt(6)
MG0 v,16,v,176,a,0
RT 168,224,35328
MG v,16,v,180,a,4
RT 170,224,43520
MG0 v,48,v,176,a,8
RT 172,224,51712
MG v,48,v,180,a,12
RT 174,224,59904
MG0 v,80,v,176,a,16
ds_read_b128 a[56:59], v249 offset:18432
s_mov_b32 m0, 43008
MG v,80,v,180,a,20
buffer_load_dwordx4 v226, s[52:55], s68 offen offset:0 lds
MG0 v,112,v,176,a,24
ds_read_b128 a[60:63], v249 offset:18944
MG v,112,v,180,a,28
s_waitcnt lgkmcnt(8)
MG0 v,20,v,160,a,0
RT 176,224,35336
MG v,20,v,164,a,4
RT 178,224,43528
MG0 v,52,v,160,a,8
RT 180,224,51720
MG v,52,v,164,a,12
RT 182,224,59912
MG0 v,84,v,160,a,16
ds_read_b128 a[64:67], v249 offset:20480
s_mov_b32 m0, 47104
MG v,84,v,164,a,20
buffer_load_dwordx4 v226, s[52:55], s69 offen offset:0 lds
MG0 v,116,v,160,a,24
ds_read_b128 a[68:71], v249 offset:20992
MG v,116,v,164,a,28
s_waitcnt lgkmcnt(8)
MG0 v,24,v,168,a,0
RT 160,224,38912
MG v,24,v,172,a,4
RT 162,224,47104
MG0 v,56,v,168,a,8
RT 164,224,55296
MG v,56,v,172,a,12
RT 166,224,63488
MG0 v,88,v,168,a,16
ds_read_b128 a[72:75], v249 offset:22528
s_mov_b32 m0, 51200
MG v,88,v,172,a,20
buffer_load_dwordx4 v226, s[52:55], s70 offen offset:0 lds
MG0 v,120,v,168,a,24
ds_read_b128 a[76:79], v249 offset:23040
MG v,120,v,172,a,28
s_waitcnt lgkmcnt(8)
MG0 v,28,v,176,a,0
RT 168,224,38920
MG v,28,v,180,a,4
RT 170,224,47112
MG0 v,60,v,176,a,8
RT 172,224,55304
MG v,60,v,180,a,12
RT 174,224,63496
MG0 v,92,v,176,a,16
ds_read_b128 a[80:83], v249 offset:32768
s_mov_b32 m0, 55296
MG v,92,v,180,a,20
buffer_load_dwordx4 v226, s[52:55], s71 offen offset:0 lds
MG0 v,124,v,176,a,24
ds_read_b128 a[84:87], v249 offset:33280
MG v,124,v,180,a,28
s_waitcnt lgkmcnt(8)
MG0 a,192,v,160,a,0
RT 176,224,39424
MG a,192,v,164,a,4
RT 178,224,47616
MG0 a,196,v,160,a,8
RT 180,224,55808
MG a,196,v,164,a,12
RT 182,224,64000
MG0 a,200,v,160,a,16
ds_read_b128 a[88:91], v249 offset:34816
s_mov_b32 m0, 59392
MG a,200,v,164,a,20
buffer_load_dwordx4 v226, s[52:55], s72 offen offset:0 lds
MG0 a,204,v,160,a,24
ds_read_b128 a[92:95], v249 offset:35328
MG a,204,v,164,a,28
s_waitcnt lgkmcnt(8)
MG0 a,208,v,168,a,0
RT 160,224,39432
MG a,208,v,172,a,4
RT 162,224,47624
MG0 a,212,v,168,a,8
RT 164,224,55816
MG a,212,v,172,a,12
RT 166,224,64008
s_mov_b32 m0, 63488
MG0 a,216,v,168,a,16
buffer_load_dwordx4 v226, s[52:55], s73 offen offset:0 lds
MG a,216,v,172,a,20
MG0 a,220,v,168,a,24
s_waitcnt vmcnt(7) lgkmcnt(0)
MG a,220,v,172,a,28
s_barrier
MG0 a,224,v,176,a,0
RT 168,224,4096
MG a,224,v,180,a,4
RT 170,224,12288
MG0 a,228,v,176,a,8
RT 172,224,20480
MG a,228,v,180,a,12
RT 174,224,28672
s_mov_b32 m0, 67584
MG0 a,232,v,176,a,16
buffer_load_dwordx4 v226, s[52:55], s74 offen offset:0 lds
MG a,232,v,180,a,20
MG0 a,236,v,176,a,24
MG a,236,v,180,a,28
MG0 a,240,v,160,a,0
RT 176,224,4104
MG a,240,v,164,a,4
RT 178,224,12296
MG0 a,244,v,160,a,8
RT 180,224,20488
MG a,244,v,164,a,12
RT 182,224,28680
s_mov_b32 m0, 71680
MG0 a,248,v,160,a,16
buffer_load_dwordx4 v226, s[52:55], s75 offen offset:0 lds
v_add_u32 v226, 0x80, v226
MG a,248,v,164,a,20
MG0 a,252,v,160,a,24
MG a,252,v,164,a,28
s_waitcnt lgkmcnt(4)
MG a,128,v,168,a,48
RT 160,224,4608
MG a,128,v,172,a,52
RT 162,224,12800
MG a,132,v,168,a,56
RT 164,224,20992
MG a,132,v,172,a,60
RT 166,224,29184
s_mov_b32 m0, 139264
MG a,136,v,168,a,64
buffer_load_dwordx4 v225, s[48:51], s76 offen offset:0 lds
MG a,136,v,172,a,68
MG a,140,v,168,a,72
MG a,140,v,172,a,76
s_mov_b32 m0, 141184
MG v,128,v,168,a,80
buffer_load_dwordx4 v225, s[48:51], s76 offen offset:128 lds
MG v,128,v,172,a,84
MG v,132,v,168,a,88
MG v,132,v,172,a,92
s_waitcnt lgkmcnt(4)
MG a,144,v,176,a,48
RT 168,224,4616
MG a,144,v,180,a,52
RT 170,224,12808
MG a,148,v,176,a,56
RT 172,224,21000
MG a,148,v,180,a,60
RT 174,224,29192
s_mov_b32 m0, 143104
MG a,152,v,176,a,64
buffer_load_dwordx4 v225, s[48:51], s76 offen offset:256 lds
MG a,152,v,180,a,68
MG a,156,v,176,a,72
MG a,156,v,180,a,76
s_mov_b32 m0, 145024
MG v,136,v,176,a,80
buffer_load_dwordx4 v225, s[48:51], s76 offen offset:384 lds
s_add_u32 s76, s76, 262144
MG v,136,v,180,a,84
MG v,140,v,176,a,88
MG v,140,v,180,a,92
s_waitcnt lgkmcnt(4)
MG a,160,v,160,a,48
RT 176,224,0
MG a,160,v,164,a,52
RT 178,224,8192
MG a,164,v,160,a,56
RT 180,224,16384
MG a,164,v,164,a,60
RT 182,224,24576
s_mov_b32 m0, 155648
MG a,168,v,160,a,64
buffer_load_dwordx4 v225, s[48:51], s76 offen offset:0 lds
MG a,168,v,164,a,68
MG a,172,v,160,a,72
MG a,172,v,164,a,76
s_mov_b32 m0, 157568
MG v,144,v,160,a,80
buffer_load_dwordx4 v225, s[48:51], s76 offen offset:128 lds
MG v,144,v,164,a,84
MG v,148,v,160,a,88
MG v,148,v,164,a,92
s_waitcnt lgkmcnt(4)
MG a,176,v,168,a,48
RT 160,224,8
MG a,176,v,172,a,52
RT 162,224,8200
MG a,180,v,168,a,56
RT 164,224,16392
MG a,180,v,172,a,60
RT 166,224,24584
s_mov_b32 m0, 159488
MG a,184,v,168,a,64
buffer_load_dwordx4 v225, s[48:51], s76 offen offset:256 lds
MG a,184,v,172,a,68
MG a,188,v,168,a,72
MG a,188,v,172,a,76
s_mov_b32 m0, 161408
MG v,152,v,168,a,80
buffer_load_dwordx4 v225, s[48:51], s76 offen offset:384 lds
s_add_u32 s76, s76, 262144
MG v,152,v,172,a,84
MG v,156,v,168,a,88
MG v,156,v,172,a,92
s_waitcnt lgkmcnt(4)
MG v,0,v,176,a,48
RT 168,224,512
MG v,0,v,180,a,52
RT 170,224,8704
MG v,32,v,176,a,56
RT 172,224,16896
MG v,32,v,180,a,60
RT 174,224,25088
s_mov_b32 m0, 2048
MG v,64,v,176,a,64
buffer_load_dwordx4 v226, s[52:55], s67 offen offset:0 lds
MG v,64,v,180,a,68
MG v,96,v,176,a,72
MG v,96,v,180,a,76
s_waitcnt lgkmcnt(4)
MG v,4,v,160,a,48
RT 176,224,520
MG v,4,v,164,a,52
RT 178,224,8712
MG v,36,v,160,a,56
RT 180,224,16904
MG v,36,v,164,a,60
RT 182,224,25096
s_mov_b32 m0, 6144
MG v,68,v,160,a,64
buffer_load_dwordx4 v226, s[52:55], s68 offen offset:0 lds
MG v,68,v,164,a,68
MG v,100,v,160,a,72
MG v,100,v,164,a,76
s_waitcnt lgkmcnt(4)
MG v,8,v,168,a,48
RT 160,224,2048
MG v,8,v,172,a,52
RT 162,224,10240
MG v,40,v,168,a,56
RT 164,224,18432
MG v,40,v,172,a,60
RT 166,224,26624
s_mov_b32 m0, 10240
MG v,72,v,168,a,64
buffer_load_dwordx4 v226, s[52:55], s69 offen offset:0 lds
MG v,72,v,172,a,68
MG v,104,v,168,a,72
s_waitcnt vmcnt(11) lgkmcnt(4)
MG v,104,v,172,a,76
s_barrier
MG v,12,v,176,a,48
RT 168,224,2056
MG v,12,v,180,a,52
RT 170,224,10248
MG v,44,v,176,a,56
RT 172,224,18440
MG v,44,v,180,a,60
RT 174,224,26632
MG v,76,v,176,a,64
ds_read_b128 a[0:3], v227 offset:16384
s_mov_b32 m0, 14336
MG v,76,v,180,a,68
buffer_load_dwordx4 v226, s[52:55], s70 offen offset:0 lds
MG v,108,v,176,a,72
ds_read_b128 a[4:7], v227 offset:16896
MG v,108,v,180,a,76
s_waitcnt lgkmcnt(6)
MG v,16,v,160,a,48
RT 176,224,2560
MG v,16,v,164,a,52
RT 178,224,10752
MG v,48,v,160,a,56
RT 180,224,18944
MG v,48,v,164,a,60
RT 182,224,27136
MG v,80,v,160,a,64
ds_read_b128 a[8:11], v227 offset:18432
s_mov_b32 m0, 18432
MG v,80,v,164,a,68
buffer_load_dwordx4 v226, s[52:55], s71 offen offset:0 lds
MG v,112,v,160,a,72
ds_read_b128 a[12:15], v227 offset:18944
MG v,112,v,164,a,76
s_waitcnt lgkmcnt(8)
MG v,20,v,168,a,48
RT 160,224,2568
MG v,20,v,172,a,52
RT 162,224,10760
MG v,52,v,168,a,56
RT 164,224,18952
MG v,52,v,172,a,60
RT 166,224,27144
MG v,84,v,168,a,64
ds_read_b128 a[16:19], v227 offset:20480
s_mov_b32 m0, 22528
MG v,84,v,172,a,68
buffer_load_dwordx4 v226, s[52:55], s72 offen offset:0 lds
MG v,116,v,168,a,72
ds_read_b128 a[20:23], v227 offset:20992
MG v,116,v,172,a,76
s_waitcnt lgkmcnt(8)
MG v,24,v,176,a,48
RT 168,224,6144
MG v,24,v,180,a,52
RT 170,224,14336
MG v,56,v,176,a,56
RT 172,224,22528
MG v,56,v,180,a,60
RT 174,224,30720
MG v,88,v,176,a,64
ds_read_b128 a[24:27], v227 offset:22528
s_mov_b32 m0, 26624
MG v,88,v,180,a,68
buffer_load_dwordx4 v226, s[52:55], s73 offen offset:0 lds
MG v,120,v,176,a,72
ds_read_b128 a[28:31], v227 offset:23040
MG v,120,v,180,a,76
s_waitcnt lgkmcnt(8)
MG v,28,v,160,a,48
)ASM"
R"ASM(
RT 176,224,6152
MG v,28,v,164,a,52
RT 178,224,14344
MG v,60,v,160,a,56
RT 180,224,22536
MG v,60,v,164,a,60
RT 182,224,30728
MG v,92,v,160,a,64
ds_read_b128 a[32:35], v227 offset:32768
s_mov_b32 m0, 30720
MG v,92,v,164,a,68
buffer_load_dwordx4 v226, s[52:55], s74 offen offset:0 lds
MG v,124,v,160,a,72
ds_read_b128 a[36:39], v227 offset:33280
MG v,124,v,164,a,76
s_waitcnt lgkmcnt(8)
MG a,192,v,168,a,48
RT 160,224,6656
MG a,192,v,172,a,52
RT 162,224,14848
MG a,196,v,168,a,56
RT 164,224,23040
MG a,196,v,172,a,60
RT 166,224,31232
MG a,200,v,168,a,64
ds_read_b128 a[40:43], v227 offset:34816
s_mov_b32 m0, 34816
MG a,200,v,172,a,68
buffer_load_dwordx4 v226, s[52:55], s75 offen offset:0 lds
v_add_u32 v226, 0x80, v226
MG a,204,v,168,a,72
ds_read_b128 a[44:47], v227 offset:35328
MG a,204,v,172,a,76
s_waitcnt lgkmcnt(8)
MG a,208,v,176,a,48
RT 168,224,6664
MG a,208,v,180,a,52
RT 170,224,14856
MG a,212,v,176,a,56
RT 172,224,23048
MG a,212,v,180,a,60
RT 174,224,31240
MG a,216,v,176,a,64
MG a,216,v,180,a,68
MG a,220,v,176,a,72
s_waitcnt vmcnt(9) lgkmcnt(0)
MG a,220,v,180,a,76
s_barrier
MG a,224,v,160,a,48
RT 176,224,36864
MG a,224,v,164,a,52
RT 178,224,45056
MG a,228,v,160,a,56
RT 180,224,53248
MG a,228,v,164,a,60
RT 182,224,61440
s_mov_b32 m0, 106496
MG a,232,v,160,a,64
buffer_load_dwordx4 v225, s[48:51], s76 offen offset:0 lds
MG a,232,v,164,a,68
MG a,236,v,160,a,72
MG a,236,v,164,a,76
MG a,240,v,168,a,48
RT 160,224,36872
MG a,240,v,172,a,52
RT 162,224,45064
MG a,244,v,168,a,56
RT 164,224,53256
MG a,244,v,172,a,60
RT 166,224,61448
s_mov_b32 m0, 108416
MG a,248,v,168,a,64
buffer_load_dwordx4 v225, s[48:51], s76 offen offset:128 lds
MG a,248,v,172,a,68
MG a,252,v,168,a,72
MG a,252,v,172,a,76
s_waitcnt lgkmcnt(4)
MG a,128,v,176,a,0
RT 168,224,37376
MG a,128,v,180,a,4
RT 170,224,45568
MG a,132,v,176,a,8
RT 172,224,53760
MG a,132,v,180,a,12
RT 174,224,61952
s_mov_b32 m0, 110336
MG a,136,v,176,a,16
buffer_load_dwordx4 v225, s[48:51], s76 offen offset:256 lds
MG a,136,v,180,a,20
MG a,140,v,176,a,24
MG a,140,v,180,a,28
s_mov_b32 m0, 112256
MG v,128,v,176,a,32
buffer_load_dwordx4 v225, s[48:51], s76 offen offset:384 lds
s_add_u32 s76, s76, 262144
MG v,128,v,180,a,36
MG v,132,v,176,a,40
MG v,132,v,180,a,44
s_waitcnt lgkmcnt(4)
MG a,144,v,160,a,0
RT 176,224,37384
MG a,144,v,164,a,4
RT 178,224,45576
MG a,148,v,160,a,8
RT 180,224,53768
MG a,148,v,164,a,12
RT 182,224,61960
s_mov_b32 m0, 122880
MG a,152,v,160,a,16
buffer_load_dwordx4 v225, s[48:51], s76 offen offset:0 lds
MG a,152,v,164,a,20
MG a,156,v,160,a,24
MG a,156,v,164,a,28
s_mov_b32 m0, 124800
MG v,136,v,160,a,32
buffer_load_dwordx4 v225, s[48:51], s76 offen offset:128 lds
MG v,136,v,164,a,36
MG v,140,v,160,a,40
MG v,140,v,164,a,44
s_waitcnt lgkmcnt(4)
MG a,160,v,168,a,0
RT 160,224,32768
MG a,160,v,172,a,4
RT 162,224,40960
MG a,164,v,168,a,8
RT 164,224,49152
MG a,164,v,172,a,12
RT 166,224,57344
s_mov_b32 m0, 126720
MG a,168,v,168,a,16
buffer_load_dwordx4 v225, s[48:51], s76 offen offset:256 lds
MG a,168,v,172,a,20
MG a,172,v,168,a,24
MG a,172,v,172,a,28
s_mov_b32 m0, 128640
MG v,144,v,168,a,32
buffer_load_dwordx4 v225, s[48:51], s76 offen offset:384 lds
s_add_u32 s76, s76, 262144
MG v,144,v,172,a,36
MG v,148,v,168,a,40
MG v,148,v,172,a,44
s_waitcnt lgkmcnt(4)
MG a,176,v,176,a,0
RT 168,224,32776
MG a,176,v,180,a,4
RT 170,224,40968
MG a,180,v,176,a,8
RT 172,224,49160
MG a,180,v,180,a,12
RT 174,224,57352
s_mov_b32 m0, 38912
MG a,184,v,176,a,16
buffer_load_dwordx4 v226, s[52:55], s67 offen offset:0 lds
MG a,184,v,180,a,20
MG a,188,v,176,a,24
MG a,188,v,180,a,28
s_mov_b32 m0, 43008
MG v,152,v,176,a,32
buffer_load_dwordx4 v226, s[52:55], s68 offen offset:0 lds
MG v,152,v,180,a,36
MG v,156,v,176,a,40
MG v,156,v,180,a,44
s_waitcnt lgkmcnt(4)
MG v,0,v,160,a,0
RT 176,224,33280
MG v,0,v,164,a,4
RT 178,224,41472
MG v,32,v,160,a,8
RT 180,224,49664
MG v,32,v,164,a,12
RT 182,224,57856
s_mov_b32 m0, 47104
MG v,64,v,160,a,16
buffer_load_dwordx4 v226, s[52:55], s69 offen offset:0 lds
MG v,64,v,164,a,20
MG v,96,v,160,a,24
MG v,96,v,164,a,28
s_waitcnt lgkmcnt(4)
MG v,4,v,168,a,0
RT 160,224,33288
MG v,4,v,172,a,4
RT 162,224,41480
MG v,36,v,168,a,8
RT 164,224,49672
MG v,36,v,172,a,12
RT 166,224,57864
s_mov_b32 m0, 51200
MG v,68,v,168,a,16
buffer_load_dwordx4 v226, s[52:55], s70 offen offset:0 lds
MG v,68,v,172,a,20
MG v,100,v,168,a,24
MG v,100,v,172,a,28
s_waitcnt lgkmcnt(4)
MG v,8,v,176,a,0
RT 168,224,34816
MG v,8,v,180,a,4
RT 170,224,43008
MG v,40,v,176,a,8
RT 172,224,51200
MG v,40,v,180,a,12
RT 174,224,59392
s_mov_b32 m0, 55296
MG v,72,v,176,a,16
buffer_load_dwordx4 v226, s[52:55], s71 offen offset:0 lds
MG v,72,v,180,a,20
MG v,104,v,176,a,24
s_waitcnt vmcnt(13) lgkmcnt(4)
MG v,104,v,180,a,28
s_barrier
MG v,12,v,160,a,0
RT 176,224,34824
MG v,12,v,164,a,4
RT 178,224,43016
MG v,44,v,160,a,8
RT 180,224,51208
MG v,44,v,164,a,12
RT 182,224,59400
MG v,76,v,160,a,16
ds_read_b128 a[48:51], v249 offset:16384
s_mov_b32 m0, 59392
MG v,76,v,164,a,20
buffer_load_dwordx4 v226, s[52:55], s72 offen offset:0 lds
MG v,108,v,160,a,24
ds_read_b128 a[52:55], v249 offset:16896
MG v,108,v,164,a,28
s_waitcnt lgkmcnt(6)
MG v,16,v,168,a,0
RT 160,224,35328
MG v,16,v,172,a,4
RT 162,224,43520
MG v,48,v,168,a,8
RT 164,224,51712
MG v,48,v,172,a,12
RT 166,224,59904
MG v,80,v,168,a,16
ds_read_b128 a[56:59], v249 offset:18432
s_mov_b32 m0, 63488
MG v,80,v,172,a,20
buffer_load_dwordx4 v226, s[52:55], s73 offen offset:0 lds
MG v,112,v,168,a,24
ds_read_b128 a[60:63], v249 offset:18944
MG v,112,v,172,a,28
s_waitcnt lgkmcnt(8)
MG v,20,v,176,a,0
RT 168,224,35336
MG v,20,v,180,a,4
RT 170,224,43528
MG v,52,v,176,a,8
RT 172,224,51720
MG v,52,v,180,a,12
RT 174,224,59912
MG v,84,v,176,a,16
ds_read_b128 a[64:67], v249 offset:20480
s_mov_b32 m0, 67584
MG v,84,v,180,a,20
buffer_load_dwordx4 v226, s[52:55], s74 offen offset:0 lds
MG v,116,v,176,a,24
ds_read_b128 a[68:71], v249 offset:20992
MG v,116,v,180,a,28
s_waitcnt lgkmcnt(8)
MG v,24,v,160,a,0
RT 176,224,38912
MG v,24,v,164,a,4
RT 178,224,47104
MG v,56,v,160,a,8
RT 180,224,55296
MG v,56,v,164,a,12
RT 182,224,63488
MG v,88,v,160,a,16
ds_read_b128 a[72:75], v249 offset:22528
s_mov_b32 m0, 71680
MG v,88,v,164,a,20
buffer_load_dwordx4 v226, s[52:55], s75 offen offset:0 lds
v_add_u32 v226, 0x80, v226
MG v,120,v,160,a,24
ds_read_b128 a[76:79], v249 offset:23040
MG v,120,v,164,a,28
s_waitcnt lgkmcnt(8)
MG v,28,v,168,a,0
RT 160,224,38920
MG v,28,v,172,a,4
RT 162,224,47112
MG v,60,v,168,a,8
RT 164,224,55304
MG v,60,v,172,a,12
RT 166,224,63496
MG v,92,v,168,a,16
ds_read_b128 a[80:83], v249 offset:32768
MG v,92,v,172,a,20
MG v,124,v,168,a,24
ds_read_b128 a[84:87], v249 offset:33280
MG v,124,v,172,a,28
s_waitcnt lgkmcnt(8)
MG a,192,v,176,a,0
RT 168,224,39424
MG a,192,v,180,a,4
RT 170,224,47616
MG a,196,v,176,a,8
RT 172,224,55808
MG a,196,v,180,a,12
RT 174,224,64000
MG a,200,v,176,a,16
ds_read_b128 a[88:91], v249 offset:34816
MG a,200,v,180,a,20
MG a,204,v,176,a,24
ds_read_b128 a[92:95], v249 offset:35328
MG a,204,v,180,a,28
s_waitcnt lgkmcnt(8)
MG a,208,v,160,a,0
RT 176,224,39432
MG a,208,v,164,a,4
RT 178,224,47624
MG a,212,v,160,a,8
RT 180,224,55816
MG a,212,v,164,a,12
RT 182,224,64008
MG a,216,v,160,a,16
MG a,216,v,164,a,20
MG a,220,v,160,a,24
s_waitcnt vmcnt(9) lgkmcnt(0)
MG a,220,v,164,a,28
s_barrier
MG a,224,v,168,a,0
RT 160,224,4096
MG a,224,v,172,a,4
RT 162,224,12288
MG a,228,v,168,a,8
RT 164,224,20480
MG a,228,v,172,a,12
RT 166,224,28672
s_mov_b32 m0, 139264
MG a,232,v,168,a,16
buffer_load_dwordx4 v225, s[48:51], s76 offen offset:0 lds
MG a,232,v,172,a,20
MG a,236,v,168,a,24
MG a,236,v,172,a,28
MG a,240,v,176,a,0
RT 168,224,4104
MG a,240,v,180,a,4
RT 170,224,12296
MG a,244,v,176,a,8
RT 172,224,20488
MG a,244,v,180,a,12
RT 174,224,28680
s_mov_b32 m0, 141184
MG a,248,v,176,a,16
buffer_load_dwordx4 v225, s[48:51], s76 offen offset:128 lds
MG a,248,v,180,a,20
MG a,252,v,176,a,24
MG a,252,v,180,a,28
s_waitcnt lgkmcnt(4)
MG a,128,v,160,a,48
RT 176,224,4608
MG a,128,v,164,a,52
RT 178,224,12800
MG a,132,v,160,a,56
RT 180,224,20992
MG a,132,v,164,a,60
RT 182,224,29184
s_mov_b32 m0, 143104
MG a,136,v,160,a,64
buffer_load_dwordx4 v225, s[48:51], s76 offen offset:256 lds
MG a,136,v,164,a,68
MG a,140,v,160,a,72
MG a,140,v,164,a,76
s_mov_b32 m0, 145024
MG v,128,v,160,a,80
buffer_load_dwordx4 v225, s[48:51], s76 offen offset:384 lds
s_add_u32 s76, s76, 262144
MG v,128,v,164,a,84
MG v,132,v,160,a,88
MG v,132,v,164,a,92
s_waitcnt lgkmcnt(4)
MG a,144,v,168,a,48
RT 160,224,4616
MG a,144,v,172,a,52
RT 162,224,12808
MG a,148,v,168,a,56
RT 164,224,21000
MG a,148,v,172,a,60
RT 166,224,29192
s_mov_b32 m0, 155648
MG a,152,v,168,a,64
buffer_load_dwordx4 v225, s[48:51], s76 offen offset:0 lds
MG a,152,v,172,a,68
MG a,156,v,168,a,72
MG a,156,v,172,a,76
s_mov_b32 m0, 157568
MG v,136,v,168,a,80
buffer_load_dwordx4 v225, s[48:51], s76 offen offset:128 lds
MG v,136,v,172,a,84
MG v,140,v,168,a,88
MG v,140,v,172,a,92
s_waitcnt lgkmcnt(4)
MG a,160,v,176,a,48
RT 168,224,0
MG a,160,v,180,a,52
RT 170,224,8192
MG a,164,v,176,a,56
RT 172,224,16384
MG a,164,v,180,a,60
RT 174,224,24576
s_mov_b32 m0, 159488
MG a,168,v,176,a,64
buffer_load_dwordx4 v225, s[48:51], s76 offen offset:256 lds
MG a,168,v,180,a,68
MG a,172,v,176,a,72
MG a,172,v,180,a,76
s_mov_b32 m0, 161408
MG v,144,v,176,a,80
buffer_load_dwordx4 v225, s[48:51], s76 offen offset:384 lds
s_add_u32 s76, s76, 262144
MG v,144,v,180,a,84
MG v,148,v,176,a,88
MG v,148,v,180,a,92
s_waitcnt lgkmcnt(4)
MG a,176,v,160,a,48
RT 176,224,8
MG a,176,v,164,a,52
RT 178,224,8200
MG a,180,v,160,a,56
RT 180,224,16392
MG a,180,v,164,a,60
RT 182,224,24584
s_mov_b32 m0, 2048
MG a,184,v,160,a,64
buffer_load_dwordx4 v226, s[52:55], s67 offen offset:0 lds
MG a,184,v,164,a,68
MG a,188,v,160,a,72
MG a,188,v,164,a,76
s_mov_b32 m0, 6144
MG v,152,v,160,a,80
buffer_load_dwordx4 v226, s[52:55], s68 offen offset:0 lds
MG v,152,v,164,a,84
MG v,156,v,160,a,88
MG v,156,v,164,a,92
s_waitcnt lgkmcnt(4)
MG v,0,v,168,a,48
RT 160,224,512
MG v,0,v,172,a,52
RT 162,224,8704
MG v,32,v,168,a,56
RT 164,224,16896
MG v,32,v,172,a,60
RT 166,224,25088
s_mov_b32 m0, 10240
MG v,64,v,168,a,64
buffer_load_dwordx4 v226, s[52:55], s69 offen offset:0 lds
MG v,64,v,172,a,68
MG v,96,v,168,a,72
MG v,96,v,172,a,76
s_waitcnt lgkmcnt(4)
MG v,4,v,176,a,48
RT 168,224,520
MG v,4,v,180,a,52
RT 170,224,8712
MG v,36,v,176,a,56
RT 172,224,16904
MG v,36,v,180,a,60
RT 174,224,25096
s_mov_b32 m0, 14336
MG v,68,v,176,a,64
buffer_load_dwordx4 v226, s[52:55], s70 offen offset:0 lds
MG v,68,v,180,a,68
MG v,100,v,176,a,72
MG v,100,v,180,a,76
s_waitcnt lgkmcnt(4)
MG v,8,v,160,a,48
RT 176,224,2048
MG v,8,v,164,a,52
RT 178,224,10240
MG v,40,v,160,a,56
RT 180,224,18432
MG v,40,v,164,a,60
RT 182,224,26624
s_mov_b32 m0, 18432
MG v,72,v,160,a,64
buffer_load_dwordx4 v226, s[52:55], s71 offen offset:0 lds
MG v,72,v,164,a,68
MG v,104,v,160,a,72
s_waitcnt vmcnt(13) lgkmcnt(4)
MG v,104,v,164,a,76
s_barrier
MG v,12,v,168,a,48
RT 160,224,2056
MG v,12,v,172,a,52
RT 162,224,10248
MG v,44,v,168,a,56
RT 164,224,18440
MG v,44,v,172,a,60
RT 166,224,26632
MG v,76,v,168,a,64
ds_read_b128 a[0:3], v227 offset:16384
s_mov_b32 m0, 22528
MG v,76,v,172,a,68
buffer_load_dwordx4 v226, s[52:55], s72 offen offset:0 lds
MG v,108,v,168,a,72
ds_read_b128 a[4:7], v227 offset:16896
MG v,108,v,172,a,76
s_waitcnt lgkmcnt(6)
MG v,16,v,176,a,48
RT 168,224,2560
MG v,16,v,180,a,52
RT 170,224,10752
MG v,48,v,176,a,56
RT 172,224,18944
MG v,48,v,180,a,60
RT 174,224,27136
MG v,80,v,176,a,64
ds_read_b128 a[8:11], v227 offset:18432
s_mov_b32 m0, 26624
MG v,80,v,180,a,68
buffer_load_dwordx4 v226, s[52:55], s73 offen offset:0 lds
MG v,112,v,176,a,72
ds_read_b128 a[12:15], v227 offset:18944
MG v,112,v,180,a,76
s_waitcnt lgkmcnt(8)
MG v,20,v,160,a,48
RT 176,224,2568
MG v,20,v,164,a,52
RT 178,224,10760
MG v,52,v,160,a,56
RT 180,224,18952
MG v,52,v,164,a,60
RT 182,224,27144
MG v,84,v,160,a,64
ds_read_b128 a[16:19], v227 offset:20480
s_mov_b32 m0, 30720
MG v,84,v,164,a,68
buffer_load_dwordx4 v226, s[52:55], s74 offen offset:0 lds
MG v,116,v,160,a,72
ds_read_b128 a[20:23], v227 offset:20992
MG v,116,v,164,a,76
s_waitcnt lgkmcnt(8)
MG v,24,v,168,a,48
RT 160,224,6144
MG v,24,v,172,a,52
RT 162,224,14336
MG v,56,v,168,a,56
RT 164,224,22528
MG v,56,v,172,a,60
RT 166,224,30720
MG v,88,v,168,a,64
ds_read_b128 a[24:27], v227 offset:22528
s_mov_b32 m0, 34816
MG v,88,v,172,a,68
buffer_load_dwordx4 v226, s[52:55], s75 offen offset:0 lds
v_add_u32 v226, 0x80, v226
MG v,120,v,168,a,72
ds_read_b128 a[28:31], v227 offset:23040
MG v,120,v,172,a,76
s_waitcnt lgkmcnt(8)
MG v,28,v,176,a,48
RT 168,224,6152
MG v,28,v,180,a,52
RT 170,224,14344
MG v,60,v,176,a,56
RT 172,224,22536
MG v,60,v,180,a,60
RT 174,224,30728
MG v,92,v,176,a,64
ds_read_b128 a[32:35], v227 offset:32768
MG v,92,v,180,a,68
MG v,124,v,176,a,72
ds_read_b128 a[36:39], v227 offset:33280
MG v,124,v,180,a,76
s_waitcnt lgkmcnt(8)
MG a,192,v,160,a,48
RT 176,224,6656
MG a,192,v,164,a,52
RT 178,224,14848
MG a,196,v,160,a,56
RT 180,224,23040
MG a,196,v,164,a,60
RT 182,224,31232
MG a,200,v,160,a,64
ds_read_b128 a[40:43], v227 offset:34816
MG a,200,v,164,a,68
MG a,204,v,160,a,72
ds_read_b128 a[44:47], v227 offset:35328
MG a,204,v,164,a,76
s_waitcnt lgkmcnt(8)
MG a,208,v,168,a,48
RT 160,224,6664
MG a,208,v,172,a,52
RT 162,224,14856
MG a,212,v,168,a,56
RT 164,224,23048
MG a,212,v,172,a,60
RT 166,224,31240
MG a,216,v,168,a,64
MG a,216,v,172,a,68
MG a,220,v,168,a,72
s_waitcnt vmcnt(9) lgkmcnt(0)
MG a,220,v,172,a,76
s_barrier
MG a,224,v,176,a,48
RT 168,224,36864
MG a,224,v,180,a,52
RT 170,224,45056
MG a,228,v,176,a,56
RT 172,224,53248
MG a,228,v,180,a,60
RT 174,224,61440
s_mov_b32 m0, 106496
MG a,232,v,176,a,64
buffer_load_dwordx4 v225, s[48:51], s76 offen offset:0 lds
MG a,232,v,180,a,68
MG a,236,v,176,a,72
MG a,236,v,180,a,76
MG a,240,v,160,a,48
RT 176,224,36872
MG a,240,v,164,a,52
RT 178,224,45064
MG a,244,v,160,a,56
RT 180,224,53256
MG a,244,v,164,a,60
RT 182,224,61448
s_mov_b32 m0, 108416
MG a,248,v,160,a,64
buffer_load_dwordx4 v225, s[48:51], s76 offen offset:128 lds
MG a,248,v,164,a,68
MG a,252,v,160,a,72
MG a,252,v,164,a,76
s_waitcnt lgkmcnt(4)
MG a,128,v,168,a,0
RT 160,224,37376
MG a,128,v,172,a,4
RT 162,224,45568
MG a,132,v,168,a,8
RT 164,224,53760
MG a,132,v,172,a,12
RT 166,224,61952
s_mov_b32 m0, 110336
MG a,136,v,168,a,16
buffer_load_dwordx4 v225, s[48:51], s76 offen offset:256 lds
MG a,136,v,172,a,20
MG a,140,v,168,a,24
MG a,140,v,172,a,28
s_mov_b32 m0, 112256
MG v,128,v,168,a,32
buffer_load_dwordx4 v225, s[48:51], s76 offen offset:384 lds
s_add_u32 s76, s76, 262144
MG v,128,v,172,a,36
MG v,132,v,168,a,40
MG v,132,v,172,a,44
s_waitcnt lgkmcnt(4)
MG a,144,v,176,a,0
RT 168,224,37384
MG a,144,v,180,a,4
RT 170,224,45576
MG a,148,v,176,a,8
RT 172,224,53768
MG a,148,v,180,a,12
RT 174,224,61960
s_mov_b32 m0, 122880
MG a,152,v,176,a,16
buffer_load_dwordx4 v225, s[48:51], s76 offen offset:0 lds
MG a,152,v,180,a,20
MG a,156,v,176,a,24
MG a,156,v,180,a,28
s_mov_b32 m0, 124800
MG v,136,v,176,a,32
buffer_load_dwordx4 v225, s[48:51], s76 offen offset:128 lds
MG v,136,v,180,a,36
MG v,140,v,176,a,40
MG v,140,v,180,a,44
s_waitcnt lgkmcnt(4)
MG a,160,v,160,a,0
RT 176,224,32768
MG a,160,v,164,a,4
RT 178,224,40960
MG a,164,v,160,a,8
RT 180,224,49152
MG a,164,v,164,a,12
RT 182,224,57344
s_mov_b32 m0, 126720
MG a,168,v,160,a,16
buffer_load_dwordx4 v225, s[48:51], s76 offen offset:256 lds
MG a,168,v,164,a,20
MG a,172,v,160,a,24
MG a,172,v,164,a,28
s_mov_b32 m0, 128640
MG v,144,v,160,a,32
buffer_load_dwordx4 v225, s[48:51], s76 offen offset:384 lds
s_add_u32 s76, s76, 262144
MG v,144,v,164,a,36
MG v,148,v,160,a,40
MG v,148,v,164,a,44
s_waitcnt lgkmcnt(4)
MG a,176,v,168,a,0
RT 160,224,32776
MG a,176,v,172,a,4
RT 162,224,40968
MG a,180,v,168,a,8
RT 164,224,49160
MG a,180,v,172,a,12
RT 166,224,57352
s_mov_b32 m0, 38912
MG a,184,v,168,a,16
buffer_load_dwordx4 v226, s[52:55], s67 offen offset:0 lds
MG a,184,v,172,a,20
MG a,188,v,168,a,24
MG a,188,v,172,a,28
s_mov_b32 m0, 43008
MG v,152,v,168,a,32
buffer_load_dwordx4 v226, s[52:55], s68 offen offset:0 lds
MG v,152,v,172,a,36
MG v,156,v,168,a,40
MG v,156,v,172,a,44
s_waitcnt lgkmcnt(4)
MG v,0,v,176,a,0
RT 168,224,33280
MG v,0,v,180,a,4
RT 170,224,41472
MG v,32,v,176,a,8
RT 172,224,49664
MG v,32,v,180,a,12
RT 174,224,57856
s_mov_b32 m0, 47104
MG v,64,v,176,a,16
buffer_load_dwordx4 v226, s[52:55], s69 offen offset:0 lds
MG v,64,v,180,a,20
MG v,96,v,176,a,24
MG v,96,v,180,a,28
s_waitcnt lgkmcnt(4)
MG v,4,v,160,a,0
RT 176,224,33288
MG v,4,v,164,a,4
RT 178,224,41480
MG v,36,v,160,a,8
RT 180,224,49672
MG v,36,v,164,a,12
RT 182,224,57864
s_mov_b32 m0, 51200
MG v,68,v,160,a,16
buffer_load_dwordx4 v226, s[52:55], s70 offen offset:0 lds
MG v,68,v,164,a,20
MG v,100,v,160,a,24
MG v,100,v,164,a,28
s_waitcnt lgkmcnt(4)
MG v,8,v,168,a,0
RT 160,224,34816
MG v,8,v,172,a,4
RT 162,224,43008
MG v,40,v,168,a,8
RT 164,224,51200
MG v,40,v,172,a,12
RT 166,224,59392
s_mov_b32 m0, 55296
MG v,72,v,168,a,16
buffer_load_dwordx4 v226, s[52:55], s71 offen offset:0 lds
MG v,72,v,172,a,20
MG v,104,v,168,a,24
s_waitcnt vmcnt(13) lgkmcnt(4)
MG v,104,v,172,a,28
s_barrier
MG v,12,v,176,a,0
RT 168,224,34824
MG v,12,v,180,a,4
RT 170,224,43016
MG v,44,v,176,a,8
RT 172,224,51208
MG v,44,v,180,a,12
RT 174,224,59400
MG v,76,v,176,a,16
ds_read_b128 a[48:51], v249 offset:16384
s_mov_b32 m0, 59392
MG v,76,v,180,a,20
buffer_load_dwordx4 v226, s[52:55], s72 offen offset:0 lds
MG v,108,v,176,a,24
ds_read_b128 a[52:55], v249 offset:16896
MG v,108,v,180,a,28
s_waitcnt lgkmcnt(6)
MG v,16,v,160,a,0
RT 176,224,35328
MG v,16,v,164,a,4
RT 178,224,43520
MG v,48,v,160,a,8
RT 180,224,51712
MG v,48,v,164,a,12
RT 182,224,59904
MG v,80,v,160,a,16
ds_read_b128 a[56:59], v249 offset:18432
s_mov_b32 m0, 63488
MG v,80,v,164,a,20
buffer_load_dwordx4 v226, s[52:55], s73 offen offset:0 lds
MG v,112,v,160,a,24
ds_read_b128 a[60:63], v249 offset:18944
MG v,112,v,164,a,28
s_waitcnt lgkmcnt(8)
MG v,20,v,168,a,0
RT 160,224,35336
MG v,20,v,172,a,4
RT 162,224,43528
MG v,52,v,168,a,8
RT 164,224,51720
MG v,52,v,172,a,12
RT 166,224,59912
MG v,84,v,168,a,16
ds_read_b128 a[64:67], v249 offset:20480
s_mov_b32 m0, 67584
MG v,84,v,172,a,20
buffer_load_dwordx4 v226, s[52:55], s74 offen offset:0 lds
MG v,116,v,168,a,24
ds_read_b128 a[68:71], v249 offset:20992
MG v,116,v,172,a,28
s_waitcnt lgkmcnt(8)
MG v,24,v,176,a,0
RT 168,224,38912
MG v,24,v,180,a,4
RT 170,224,47104
MG v,56,v,176,a,8
RT 172,224,55296
MG v,56,v,180,a,12
RT 174,224,63488
MG v,88,v,176,a,16
ds_read_b128 a[72:75], v249 offset:22528
s_mov_b32 m0, 71680
MG v,88,v,180,a,20
buffer_load_dwordx4 v226, s[52:55], s75 offen offset:0 lds
v_add_u32 v226, 0x80, v226
MG v,120,v,176,a,24
ds_read_b128 a[76:79], v249 offset:23040
MG v,120,v,180,a,28
s_waitcnt lgkmcnt(8)
MG v,28,v,160,a,0
RT 176,224,38920
MG v,28,v,164,a,4
RT 178,224,47112
MG v,60,v,160,a,8
RT 180,224,55304
MG v,60,v,164,a,12
RT 182,224,63496
MG v,92,v,160,a,16
ds_read_b128 a[80:83], v249 offset:32768
MG v,92,v,164,a,20
MG v,124,v,160,a,24
ds_read_b128 a[84:87], v249 offset:33280
MG v,124,v,164,a,28
s_waitcnt lgkmcnt(8)
MG a,192,v,168,a,0
RT 160,224,39424
MG a,192,v,172,a,4
RT 162,224,47616
MG a,196,v,168,a,8
RT 164,224,55808
MG a,196,v,172,a,12
RT 166,224,64000
MG a,200,v,168,a,16
ds_read_b128 a[88:91], v249 offset:34816
MG a,200,v,172,a,20
MG a,204,v,168,a,24
ds_read_b128 a[92:95], v249 offset:35328
MG a,204,v,172,a,28
s_waitcnt lgkmcnt(8)
MG a,208,v,176,a,0
RT 168,224,39432
MG a,208,v,180,a,4
RT 170,224,47624
MG a,212,v,176,a,8
RT 172,224,55816
MG a,212,v,180,a,12
RT 174,224,64008
MG a,216,v,176,a,16
MG a,216,v,180,a,20
MG a,220,v,176,a,24
s_waitcnt vmcnt(9) lgkmcnt(0)
MG a,220,v,180,a,28
s_barrier
MG a,224,v,160,a,0
RT 176,224,4096
MG a,224,v,164,a,4
RT 178,224,12288
MG a,228,v,160,a,8
RT 180,224,20480
MG a,228,v,164,a,12
RT 182,224,28672
s_mov_b32 m0, 139264
MG a,232,v,160,a,16
buffer_load_dwordx4 v225, s[48:51], s76 offen offset:0 lds
MG a,232,v,164,a,20
MG a,236,v,160,a,24
MG a,236,v,164,a,28
MG a,240,v,168,a,0
RT 160,224,4104
MG a,240,v,172,a,4
RT 162,224,12296
MG a,244,v,168,a,8
RT 164,224,20488
MG a,244,v,172,a,12
RT 166,224,28680
s_mov_b32 m0, 141184
MG a,248,v,168,a,16
buffer_load_dwordx4 v225, s[48:51], s76 offen offset:128 lds
MG a,248,v,172,a,20
MG a,252,v,168,a,24
MG a,252,v,172,a,28
s_waitcnt lgkmcnt(4)
MG a,128,v,176,a,48
RT 168,224,4608
MG a,128,v,180,a,52
RT 170,224,12800
MG a,132,v,176,a,56
RT 172,224,20992
MG a,132,v,180,a,60
RT 174,224,29184
s_mov_b32 m0, 143104
MG a,136,v,176,a,64
buffer_load_dwordx4 v225, s[48:51], s76 offen offset:256 lds
MG a,136,v,180,a,68
MG a,140,v,176,a,72
MG a,140,v,180,a,76
s_mov_b32 m0, 145024
MG v,128,v,176,a,80
buffer_load_dwordx4 v225, s[48:51], s76 offen offset:384 lds
s_add_u32 s76, s76, 262144
MG v,128,v,180,a,84
MG v,132,v,176,a,88
MG v,132,v,180,a,92
s_waitcnt lgkmcnt(4)
MG a,144,v,160,a,48
RT 176,224,4616
MG a,144,v,164,a,52
RT 178,224,12808
MG a,148,v,160,a,56
RT 180,224,21000
MG a,148,v,164,a,60
RT 182,224,29192
s_mov_b32 m0, 155648
MG a,152,v,160,a,64
buffer_load_dwordx4 v225, s[48:51], s76 offen offset:0 lds
MG a,152,v,164,a,68
MG a,156,v,160,a,72
MG a,156,v,164,a,76
s_mov_b32 m0, 157568
MG v,136,v,160,a,80
buffer_load_dwordx4 v225, s[48:51], s76 offen offset:128 lds
MG v,136,v,164,a,84
MG v,140,v,160,a,88
MG v,140,v,164,a,92
s_waitcnt lgkmcnt(4)
MG a,160,v,168,a,48
RT 160,224,0
MG a,160,v,172,a,52
RT 162,224,8192
MG a,164,v,168,a,56
RT 164,224,16384
MG a,164,v,172,a,60
RT 166,224,24576
s_mov_b32 m0, 159488
MG a,168,v,168,a,64
buffer_load_dwordx4 v225, s[48:51], s76 offen offset:256 lds
MG a,168,v,172,a,68
MG a,172,v,168,a,72
MG a,172,v,172,a,76
s_mov_b32 m0, 161408
MG v,144,v,168,a,80
buffer_load_dwordx4 v225, s[48:51], s76 offen offset:384 lds
s_add_u32 s76, s76, 262144
MG v,144,v,172,a,84
MG v,148,v,168,a,88
MG v,148,v,172,a,92
s_waitcnt lgkmcnt(4)
MG a,176,v,176,a,48
RT 168,224,8
MG a,176,v,180,a,52
RT 170,224,8200
MG a,180,v,176,a,56
RT 172,224,16392
MG a,180,v,180,a,60
RT 174,224,24584
s_mov_b32 m0, 8192
MG a,184,v,176,a,64
buffer_load_dwordx4 v253, s[40:43], s60 offen offset:0 nt lds
MG a,184,v,180,a,68
MG a,188,v,176,a,72
MG a,188,v,180,a,76
s_mov_b32 m0, 10112
MG v,152,v,176,a,80
buffer_load_dwordx4 v253, s[40:43], s60 offen offset:128 nt lds
MG v,152,v,180,a,84
MG v,156,v,176,a,88
MG v,156,v,180,a,92
s_waitcnt lgkmcnt(4)
MG v,0,v,160,a,48
RT 176,224,512
MG v,0,v,164,a,52
RT 178,224,8704
MG v,32,v,160,a,56
RT 180,224,16896
MG v,32,v,164,a,60
RT 182,224,25088
s_mov_b32 m0, 12032
MG v,64,v,160,a,64
buffer_load_dwordx4 v253, s[40:43], s60 offen offset:256 nt lds
MG v,64,v,164,a,68
MG v,96,v,160,a,72
MG v,96,v,164,a,76
s_waitcnt lgkmcnt(4)
MG v,4,v,168,a,48
RT 160,224,520
MG v,4,v,172,a,52
RT 162,224,8712
MG v,36,v,168,a,56
RT 164,224,16904
MG v,36,v,172,a,60
RT 166,224,25096
s_mov_b32 m0, 13952
MG v,68,v,168,a,64
buffer_load_dwordx4 v253, s[40:43], s60 offen offset:384 nt lds
s_add_u32 s60, s60, 16384
MG v,68,v,172,a,68
MG v,100,v,168,a,72
MG v,100,v,172,a,76
s_waitcnt lgkmcnt(4)
MG v,8,v,176,a,48
RT 168,224,2048
MG v,8,v,180,a,52
RT 170,224,10240
MG v,40,v,176,a,56
RT 172,224,18432
MG v,40,v,180,a,60
RT 174,224,26624
s_mov_b32 m0, 24576
MG v,72,v,176,a,64
buffer_load_dwordx4 v253, s[40:43], s60 offen offset:0 nt lds
MG v,72,v,180,a,68
MG v,104,v,176,a,72
s_waitcnt vmcnt(13) lgkmcnt(4)
MG v,104,v,180,a,76
s_barrier
MG v,12,v,160,a,48
RT 176,224,2056
MG v,12,v,164,a,52
RT 178,224,10248
MG v,44,v,160,a,56
RT 180,224,18440
MG v,44,v,164,a,60
RT 182,224,26632
MG v,76,v,160,a,64
ds_read_b128 a[0:3], v227 offset:16384
s_mov_b32 m0, 26496
MG v,76,v,164,a,68
buffer_load_dwordx4 v253, s[40:43], s60 offen offset:128 nt lds
MG v,108,v,160,a,72
ds_read_b128 a[4:7], v227 offset:16896
MG v,108,v,164,a,76
s_waitcnt lgkmcnt(6)
MG v,16,v,168,a,48
RT 160,224,2560
MG v,16,v,172,a,52
RT 162,224,10752
MG v,48,v,168,a,56
RT 164,224,18944
MG v,48,v,172,a,60
RT 166,224,27136
MG v,80,v,168,a,64
ds_read_b128 a[8:11], v227 offset:18432
s_mov_b32 m0, 28416
MG v,80,v,172,a,68
buffer_load_dwordx4 v253, s[40:43], s60 offen offset:256 nt lds
MG v,112,v,168,a,72
ds_read_b128 a[12:15], v227 offset:18944
MG v,112,v,172,a,76
s_waitcnt lgkmcnt(8)
MG v,20,v,176,a,48
RT 168,224,2568
MG v,20,v,180,a,52
RT 170,224,10760
MG v,52,v,176,a,56
RT 172,224,18952
MG v,52,v,180,a,60
RT 174,224,27144
MG v,84,v,176,a,64
ds_read_b128 a[16:19], v227 offset:20480
s_mov_b32 m0, 30336
MG v,84,v,180,a,68
buffer_load_dwordx4 v253, s[40:43], s60 offen offset:384 nt lds
s_add_u32 s60, s60, 16384
MG v,116,v,176,a,72
ds_read_b128 a[20:23], v227 offset:20992
MG v,116,v,180,a,76
s_waitcnt lgkmcnt(8)
MG v,24,v,160,a,48
RT 176,224,6144
MG v,24,v,164,a,52
RT 178,224,14336
MG v,56,v,160,a,56
RT 180,224,22528
MG v,56,v,164,a,60
RT 182,224,30720
MG v,88,v,160,a,64
ds_read_b128 a[24:27], v227 offset:22528
MG v,88,v,164,a,68
MG v,120,v,160,a,72
ds_read_b128 a[28:31], v227 offset:23040
MG v,120,v,164,a,76
s_waitcnt lgkmcnt(8)
MG v,28,v,168,a,48
RT 160,224,6152
MG v,28,v,172,a,52
RT 162,224,14344
MG v,60,v,168,a,56
RT 164,224,22536
MG v,60,v,172,a,60
RT 166,224,30728
MG v,92,v,168,a,64
ds_read_b128 a[32:35], v227 offset:32768
MG v,92,v,172,a,68
MG v,124,v,168,a,72
ds_read_b128 a[36:39], v227 offset:33280
MG v,124,v,172,a,76
s_waitcnt lgkmcnt(8)
MG a,192,v,176,a,48
RT 168,224,6656
MG a,192,v,180,a,52
RT 170,224,14848
MG a,196,v,176,a,56
RT 172,224,23040
MG a,196,v,180,a,60
RT 174,224,31232
MG a,200,v,176,a,64
ds_read_b128 a[40:43], v227 offset:34816
MG a,200,v,180,a,68
MG a,204,v,176,a,72
ds_read_b128 a[44:47], v227 offset:35328
MG a,204,v,180,a,76
s_waitcnt lgkmcnt(8)
MG a,208,v,160,a,48
RT 176,224,6664
MG a,208,v,164,a,52
RT 178,224,14856
MG a,212,v,160,a,56
RT 180,224,23048
MG a,212,v,164,a,60
RT 182,224,31240
MG a,216,v,160,a,64
MG a,216,v,164,a,68
MG a,220,v,160,a,72
s_waitcnt vmcnt(8) lgkmcnt(0)
MG a,220,v,164,a,76
s_barrier
MG a,224,v,168,a,48
RT 160,224,36864
MG a,224,v,172,a,52
RT 162,224,45056
MG a,228,v,168,a,56
RT 164,224,53248
MG a,228,v,172,a,60
RT 166,224,61440
s_mov_b32 m0, 106496
MG a,232,v,168,a,64
buffer_load_dwordx4 v253, s[44:47], s61 offen offset:0 nt lds
MG a,232,v,172,a,68
MG a,236,v,168,a,72
MG a,236,v,172,a,76
MG a,240,v,176,a,48
RT 168,224,36872
MG a,240,v,180,a,52
RT 170,224,45064
MG a,244,v,176,a,56
RT 172,224,53256
MG a,244,v,180,a,60
RT 174,224,61448
s_mov_b32 m0, 108416
MG a,248,v,176,a,64
buffer_load_dwordx4 v253, s[44:47], s61 offen offset:128 nt lds
MG a,248,v,180,a,68
MG a,252,v,176,a,72
MG a,252,v,180,a,76
s_waitcnt lgkmcnt(4)
MG a,128,v,160,a,0
RT 176,224,37376
MG a,128,v,164,a,4
RT 178,224,45568
MG a,132,v,160,a,8
RT 180,224,53760
MG a,132,v,164,a,12
RT 182,224,61952
s_mov_b32 m0, 110336
MG a,136,v,160,a,16
buffer_load_dwordx4 v253, s[44:47], s61 offen offset:256 nt lds
MG a,136,v,164,a,20
MG a,140,v,160,a,24
MG a,140,v,164,a,28
MG v,128,v,160,a,32
MG v,128,v,164,a,36
MG v,132,v,160,a,40
MG v,132,v,164,a,44
s_waitcnt lgkmcnt(4)
MG a,144,v,168,a,0
RT 160,224,37384
MG a,144,v,172,a,4
RT 162,224,45576
MG a,148,v,168,a,8
RT 164,224,53768
MG a,148,v,172,a,12
RT 166,224,61960
s_mov_b32 m0, 112256
MG a,152,v,168,a,16
buffer_load_dwordx4 v253, s[44:47], s61 offen offset:384 nt lds
s_add_u32 s61, s61, 16384
MG a,152,v,172,a,20
MG a,156,v,168,a,24
MG a,156,v,172,a,28
MG v,136,v,168,a,32
MG v,136,v,172,a,36
MG v,140,v,168,a,40
MG v,140,v,172,a,44
s_waitcnt lgkmcnt(4)
MG a,160,v,176,a,0
RT 168,224,32768
MG a,160,v,180,a,4
RT 170,224,40960
MG a,164,v,176,a,8
RT 172,224,49152
MG a,164,v,180,a,12
RT 174,224,57344
s_mov_b32 m0, 40960
MG a,168,v,176,a,16
buffer_load_dwordx4 v253, s[40:43], s60 offen offset:0 nt lds
MG a,168,v,180,a,20
CV 128,128,129
MG a,172,v,176,a,24
CV 129,130,131
CV 130,136,137
MG a,172,v,180,a,28
CV 131,138,139
MG v,144,v,176,a,32
ds_write_b128 v252, v[128:131] offset:4096
MG v,144,v,180,a,36
CV 132,132,133
CV 133,134,135
MG v,148,v,176,a,40
CV 134,140,141
MG v,148,v,180,a,44
s_waitcnt lgkmcnt(5)
CV 135,142,143
MG a,176,v,160,a,0
RT 176,224,32776
MG a,176,v,164,a,4
RT 178,224,40968
MG a,180,v,160,a,8
RT 180,224,49160
MG a,180,v,164,a,12
RT 182,224,57352
s_mov_b32 m0, 42880
MG a,184,v,160,a,16
buffer_load_dwordx4 v253, s[40:43], s60 offen offset:128 nt lds
MG a,184,v,164,a,20
ds_write_b128 v252, v[132:135] offset:12288
MG a,188,v,160,a,24
MG a,188,v,164,a,28
MG v,152,v,160,a,32
MG v,152,v,164,a,36
MG v,156,v,160,a,40
MG v,156,v,164,a,44
s_waitcnt lgkmcnt(6)
MG v,0,v,168,a,0
RT 160,224,33280
MG v,0,v,172,a,4
RT 162,224,41472
MG v,32,v,168,a,8
RT 164,224,49664
MG v,32,v,172,a,12
RT 166,224,57856
s_mov_b32 m0, 44800
MG v,64,v,168,a,16
buffer_load_dwordx4 v253, s[40:43], s60 offen offset:256 nt lds
MG v,64,v,172,a,20
CV 144,144,145
CV 145,146,147
MG v,96,v,168,a,24
CV 146,152,153
MG v,96,v,172,a,28
s_waitcnt lgkmcnt(5)
CV 147,154,155
MG v,4,v,176,a,0
RT 168,224,33288
MG v,4,v,180,a,4
RT 170,224,41480
MG v,36,v,176,a,8
RT 172,224,49672
MG v,36,v,180,a,12
RT 174,224,57864
s_mov_b32 m0, 46720
MG v,68,v,176,a,16
buffer_load_dwordx4 v253, s[40:43], s60 offen offset:384 nt lds
s_add_u32 s60, s60, 16384
MG v,68,v,180,a,20
ds_write_b128 v252, v[144:147] offset:5120
MG v,100,v,176,a,24
CV 148,148,149
CV 149,150,151
MG v,100,v,180,a,28
s_waitcnt lgkmcnt(5)
CV 150,156,157
CV 151,158,159
MG v,8,v,160,a,0
RT 176,224,34816
MG v,8,v,164,a,4
RT 178,224,43008
MG v,40,v,160,a,8
RT 180,224,51200
MG v,40,v,164,a,12
RT 182,224,59392
s_mov_b32 m0, 122880
MG v,72,v,160,a,16
buffer_load_dwordx4 v253, s[44:47], s61 offen offset:0 nt lds
MG v,72,v,164,a,20
MG v,104,v,160,a,24
ds_write_b128 v252, v[148:151] offset:13312
MG v,104,v,164,a,28
s_waitcnt lgkmcnt(6)
CV 0,0,1
CV 1,2,3
MG v,12,v,168,a,0
RT 160,224,34824
CV 2,4,5
MG v,12,v,172,a,4
RT 162,224,43016
CV 3,6,7
MG v,44,v,168,a,8
RT 164,224,51208
CV 32,32,33
MG v,44,v,172,a,12
RT 166,224,59400
s_mov_b32 m0, 124800
MG v,76,v,168,a,16
buffer_load_dwordx4 v253, s[44:47], s61 offen offset:128 nt lds
MG v,76,v,172,a,20
CV 33,34,35
CV 34,36,37
MG v,108,v,168,a,24
CV 35,38,39
CV 64,64,65
MG v,108,v,172,a,28
s_waitcnt lgkmcnt(5)
CV 65,66,67
MG v,16,v,176,a,0
RT 168,224,35328
CV 66,68,69
MG v,16,v,180,a,4
RT 170,224,43520
CV 67,70,71
MG v,48,v,176,a,8
RT 172,224,51712
CV 96,96,97
MG v,48,v,180,a,12
RT 174,224,59904
s_mov_b32 m0, 126720
MG v,80,v,176,a,16
buffer_load_dwordx4 v253, s[44:47], s61 offen offset:256 nt lds
MG v,80,v,180,a,20
CV 97,98,99
CV 98,100,101
MG v,112,v,176,a,24
CV 99,102,103
CV 4,8,9
MG v,112,v,180,a,28
s_waitcnt lgkmcnt(4)
CV 5,10,11
CV 6,12,13
MG v,20,v,160,a,0
RT 176,224,35336
CV 7,14,15
MG v,20,v,164,a,4
RT 178,224,43528
CV 36,40,41
MG v,52,v,160,a,8
RT 180,224,51720
CV 37,42,43
MG v,52,v,164,a,12
RT 182,224,59912
s_mov_b32 m0, 128640
MG v,84,v,160,a,16
buffer_load_dwordx4 v253, s[44:47], s61 offen offset:384 nt lds
s_add_u32 s61, s61, 16384
MG v,84,v,164,a,20
CV 38,44,45
CV 39,46,47
MG v,116,v,160,a,24
CV 68,72,73
MG v,116,v,164,a,28
s_waitcnt lgkmcnt(4)
CV 69,74,75
CV 70,76,77
MG v,24,v,168,a,0
RT 160,224,38912
CV 71,78,79
MG v,24,v,172,a,4
RT 162,224,47104
CV 100,104,105
MG v,56,v,168,a,8
RT 164,224,55296
CV 101,106,107
MG v,56,v,172,a,12
RT 166,224,63488
s_mov_b32 m0, 57344
MG v,88,v,168,a,16
buffer_load_dwordx4 v253, s[40:43], s60 offen offset:0 nt lds
MG v,88,v,172,a,20
CV 102,108,109
CV 103,110,111
MG v,120,v,168,a,24
CV 8,16,17
CV 9,18,19
MG v,120,v,172,a,28
s_waitcnt lgkmcnt(4)
CV 10,20,21
CV 11,22,23
MG v,28,v,176,a,0
RT 168,224,38920
CV 40,48,49
MG v,28,v,180,a,4
RT 170,224,47112
CV 41,50,51
MG v,60,v,176,a,8
RT 172,224,55304
CV 42,52,53
MG v,60,v,180,a,12
RT 174,224,63496
s_mov_b32 m0, 59264
MG v,92,v,176,a,16
buffer_load_dwordx4 v253, s[40:43], s60 offen offset:128 nt lds
MG v,92,v,180,a,20
CV 43,54,55
MG v,124,v,176,a,24
CV 72,80,81
CV 73,82,83
MG v,124,v,180,a,28
s_waitcnt lgkmcnt(4)
CV 74,84,85
CV 75,86,87
MG a,192,v,160,a,0
RT 176,224,39424
CV 104,112,113
MG a,192,v,164,a,4
RT 178,224,47616
CV 105,114,115
MG a,196,v,160,a,8
RT 180,224,55808
CV 106,116,117
MG a,196,v,164,a,12
RT 182,224,64000
s_mov_b32 m0, 61184
MG a,200,v,160,a,16
buffer_load_dwordx4 v253, s[40:43], s60 offen offset:256 nt lds
MG a,200,v,164,a,20
CV 107,118,119
CV 12,24,25
MG a,204,v,160,a,24
CV 13,26,27
CV 14,28,29
MG a,204,v,164,a,28
s_waitcnt lgkmcnt(4)
CV 15,30,31
MG a,208,v,168,a,0
RT 160,224,39432
CV 44,56,57
MG a,208,v,172,a,4
RT 162,224,47624
CV 45,58,59
MG a,212,v,168,a,8
RT 164,224,55816
CV 46,60,61
MG a,212,v,172,a,12
RT 166,224,64008
s_mov_b32 m0, 63104
MG a,216,v,168,a,16
buffer_load_dwordx4 v253, s[40:43], s60 offen offset:384 nt lds
s_add_u32 s60, s60, 16384
MG a,216,v,172,a,20
CV 47,62,63
CV 76,88,89
MG a,220,v,168,a,24
CV 77,90,91
CV 78,92,93
MG a,220,v,172,a,28
s_waitcnt lgkmcnt(6)
CV 79,94,95
CV 108,120,121
MG a,224,v,176,a,0
s_waitcnt lgkmcnt(4)
CV 109,122,123
CV 110,124,125
MG a,224,v,180,a,4
CV 111,126,127
MG a,228,v,176,a,8
AR 192,128
AR 193,129
MG a,228,v,180,a,12
AR 194,130
AR 195,131
MG a,232,v,176,a,16
AR 196,144
AR 197,145
MG a,232,v,180,a,20
AR 198,146
AR 199,147
MG a,236,v,176,a,24
CV 16,192,193
MG a,236,v,180,a,28
s_waitcnt lgkmcnt(2)
CV 17,194,195
CV 18,196,197
MG a,240,v,160,a,0
s_waitcnt lgkmcnt(0)
CV 19,198,199
AR 200,132
MG a,240,v,164,a,4
AR 201,133
AR 202,134
MG a,244,v,160,a,8
AR 203,135
AR 204,148
MG a,244,v,164,a,12
AR 205,149
MG a,248,v,160,a,16
AR 206,150
AR 207,151
MG a,248,v,164,a,20
CV 48,200,201
CV 49,202,203
MG a,252,v,160,a,24
CV 50,204,205
CV 51,206,207
MG a,252,v,164,a,28
s_waitcnt vmcnt(20)
AR 192,136
AR 193,137
s_barrier
RB 176,249,0
RB 180,249,512
RB 224,252,8192
RB 208,249,2048
RB 212,249,2560
RB 228,252,9216
AR 194,138
AR 195,139
AR 196,152
AR 197,153
AR 198,154
AR 199,155
CV 80,192,193
CV 81,194,195
CV 82,196,197
CV 83,198,199
AR 200,140
AR 201,141
AR 202,142
AR 203,143
AR 204,156
AR 205,157
AR 206,158
AR 207,159
CV 112,200,201
CV 113,202,203
CV 114,204,205
CV 115,206,207
AR 192,160
AR 193,161
AR 194,162
AR 195,163
AR 196,176
AR 197,177
AR 198,178
AR 199,179
CV 20,192,193
CV 21,194,195
CV 22,196,197
CV 23,198,199
AR 200,164
AR 201,165
AR 202,166
AR 203,167
AR 204,180
AR 205,181
AR 206,182
AR 207,183
CV 52,200,201
CV 53,202,203
CV 54,204,205
CV 55,206,207
AR 192,168
AR 193,169
AR 194,170
AR 195,171
AR 196,184
AR 197,185
AR 198,186
AR 199,187
CV 84,192,193
CV 85,194,195
CV 86,196,197
CV 87,198,199
AR 200,172
AR 201,173
AR 202,174
AR 203,175
AR 204,188
AR 205,189
AR 206,190
AR 207,191
CV 116,200,201
CV 117,202,203
CV 118,204,205
CV 119,206,207
AR 192,192
AR 193,193
AR 194,194
AR 195,195
AR 196,208
AR 197,209
AR 198,210
AR 199,211
CV 24,192,193
CV 25,194,195
CV 26,196,197
CV 27,198,199
AR 200,196
AR 201,197
AR 202,198
AR 203,199
AR 204,212
AR 205,213
AR 206,214
AR 207,215
CV 56,200,201
CV 57,202,203
CV 58,204,205
CV 59,206,207
AR 192,200
AR 193,201
AR 194,202
AR 195,203
AR 196,216
AR 197,217
AR 198,218
AR 199,219
CV 88,192,193
CV 89,194,195
CV 90,196,197
CV 91,198,199
AR 200,204
AR 201,205
AR 202,206
AR 203,207
AR 204,220
AR 205,221
AR 206,222
AR 207,223
CV 120,200,201
CV 121,202,203
CV 122,204,205
s_waitcnt lgkmcnt(5)
MV0 160,176,0
RB 216,249,4096
CV 123,206,207
s_waitcnt lgkmcnt(5)
MV 160,180,4
RB 220,249,4608
AR 192,224
MV0 164,176,32
AR 193,225
AR 194,226
MV 164,180,36
AR 195,227
AR 196,240
MV0 168,176,64
AR 197,241
AR 198,242
MV 168,180,68
AR 199,243
CV 28,192,193
MV0 172,176,96
CV 29,194,195
CV 30,196,197
MV 172,180,100
s_waitcnt lgkmcnt(2)
CV 31,198,199
AR 200,228
MV0 232,176,224
RB 224,252,10240
AR 201,229
MV 232,180,228
AR 202,230
AR 203,231
MV 160,208,8
RB 176,249,6144
AR 204,244
MV 160,212,12
RB 180,249,6656
AR 205,245
MV 164,208,40
RB 228,252,11264
AR 206,246
MV 164,212,44
AR 207,247
CV 60,200,201
MV 168,208,72
CV 61,202,203
CV 62,204,205
MV 168,212,76
CV 63,206,207
AR 192,232
MV 172,208,104
AR 193,233
AR 194,234
MV 172,212,108
s_waitcnt lgkmcnt(0)
AR 195,235
AR 196,248
MV 232,208,224
RB 224,252,12288
AR 197,249
MV 232,212,228
AR 198,250
AR 199,251
MV 160,216,16
RB 208,249,8192
CV 92,192,193
MV 160,220,20
RB 212,249,8704
CV 93,194,195
MV 164,216,48
RB 228,252,13312
CV 94,196,197
MV 164,220,52
CV 95,198,199
AR 200,236
MV 168,216,80
AR 201,237
AR 202,238
MV 168,220,84
AR 203,239
AR 204,252
MV 172,216,112
AR 205,253
AR 206,254
MV 172,220,116
s_waitcnt lgkmcnt(0)
AR 207,255
CV 124,200,201
MV 232,216,224
RB 216,249,10240
CV 125,202,203
MV 232,220,228
CV 126,204,205
CV 127,206,207
MV 160,176,24
RB 220,249,10752
MV 160,180,28
RB 224,252,14336
MV 164,176,56
RB 228,252,15360
MV 164,180,60
MV 168,176,88
MV 168,180,92
MV 172,176,120
MV 172,180,124
s_waitcnt lgkmcnt(0)
MV 232,176,224
RB 224,249,12288
MV 232,180,228
MV0 176,208,0
RB 228,249,12800
MV 176,212,4
MV0 180,208,32
MV 180,212,36
MV0 184,208,64
v_max3_f32 v192, v160, v161, v162
v_max3_f32 v193, v164, v165, v166
MV 184,212,68
v_max3_f32 v194, v168, v169, v170
v_max3_f32 v195, v172, v173, v174
MV0 188,208,96
RB 208,249,14336
v_max3_f32 v196, v232, v233, v234
MV 188,212,100
v_max_f32 v192, v192, v163
v_max_f32 v193, v193, v167
MV 176,216,8
RB 212,249,14848
v_max_f32 v194, v194, v171
MV 176,220,12
v_max_f32 v195, v195, v175
v_max_f32 v196, v196, v235
MV 180,216,40
v_mov_b32 v197, v192
v_mov_b32 v198, v193
MV 180,220,44
v_mov_b32 v199, v194
v_mov_b32 v200, v195
MV 184,216,72
v_mov_b32 v201, v196
v_permlane16_swap_b32 v192, v197
MV 184,220,76
v_permlane16_swap_b32 v193, v198
v_permlane16_swap_b32 v194, v199
MV 188,216,104
v_permlane16_swap_b32 v195, v200
v_permlane16_swap_b32 v196, v201
MV 188,220,108
s_waitcnt lgkmcnt(3)
v_max_f32 v192, v192, v197
v_max_f32 v193, v193, v198
MV 176,224,16
s_waitcnt lgkmcnt(2)
v_max_f32 v194, v194, v199
v_max_f32 v195, v195, v200
MV 176,228,20
v_max_f32 v196, v196, v201
v_mov_b32 v197, v192
MV 180,224,48
v_mov_b32 v198, v193
v_mov_b32 v199, v194
MV 180,228,52
v_mov_b32 v200, v195
v_mov_b32 v201, v196
MV 184,224,80
v_permlane32_swap_b32 v192, v197
v_permlane32_swap_b32 v193, v198
MV 184,228,84
v_permlane32_swap_b32 v194, v199
v_permlane32_swap_b32 v195, v200
MV 188,224,112
v_permlane32_swap_b32 v196, v201
v_max_f32 v192, v192, v197
MV 188,228,116
s_waitcnt lgkmcnt(1)
v_max_f32 v193, v193, v198
v_max_f32 v194, v194, v199
MV 176,208,24
s_waitcnt lgkmcnt(0)
v_max_f32 v195, v195, v200
v_max_f32 v196, v196, v201
MV 176,212,28
v_mul_f32 v196, s66, v196
v_mul_f32 v236, s66, v192
MV 180,208,56
v_mul_f32 v237, s66, v193
v_mul_f32 v238, s66, v194
MV 180,212,60
v_mul_f32 v239, s66, v195
MV 184,208,88
ds_write_b32 v252, v196 offset:28680
MV 184,212,92
v_fma_f32 v160, v160, s63, v236
v_fma_f32 v161, v161, s63, v236
MV 188,208,120
v_fma_f32 v162, v162, s63, v236
v_fma_f32 v163, v163, s63, v236
MV 188,212,124
s_waitcnt vmcnt(12) lgkmcnt(0)
v_fma_f32 v164, v164, s63, v237
v_fma_f32 v165, v165, s63, v237
s_barrier
ds_read_b32 v246, v252 offset:28680
ds_read_b32 v248, v252 offset:28684
s_mov_b32 m0, 139264
s_nop 0
buffer_load_dwordx4 v253, s[44:47], s61 offen offset:0 nt lds
s_mov_b32 m0, 141184
s_nop 0
buffer_load_dwordx4 v253, s[44:47], s61 offen offset:128 nt lds
s_mov_b32 m0, 143104
s_nop 0
buffer_load_dwordx4 v253, s[44:47], s61 offen offset:256 nt lds
s_mov_b32 m0, 145024
s_nop 0
buffer_load_dwordx4 v253, s[44:47], s61 offen offset:384 nt lds
s_add_u32 s61, s61, 16384
s_mov_b32 s48, s24
s_and_b32 s49, s25, 0xffff
s_mov_b32 s50, 3670016
s_and_b32 s76, s26, 127
s_mul_i32 s76, s76, 7
v_fma_f32 v166, v166, s63, v237
v_fma_f32 v167, v167, s63, v237
v_fma_f32 v168, v168, s63, v238
v_fma_f32 v169, v169, s63, v238
v_fma_f32 v170, v170, s63, v238
v_fma_f32 v171, v171, s63, v238
v_fma_f32 v172, v172, s63, v239
v_fma_f32 v173, v173, s63, v239
v_fma_f32 v174, v174, s63, v239
v_fma_f32 v175, v175, s63, v239
v_exp_f32 v160, v160
v_exp_f32 v161, v161
v_exp_f32 v162, v162
v_exp_f32 v163, v163
v_exp_f32 v164, v164
v_exp_f32 v165, v165
v_exp_f32 v166, v166
v_exp_f32 v167, v167
v_exp_f32 v168, v168
v_exp_f32 v169, v169
v_exp_f32 v170, v170
v_exp_f32 v171, v171
v_exp_f32 v172, v172
v_exp_f32 v173, v173
v_exp_f32 v174, v174
v_exp_f32 v175, v175
s_waitcnt lgkmcnt(0)
v_min_f32 v240, v246, v248
ds_write_b32 v252, v240 offset:29700
v_mov_b32 v247, 0
v_fma_f32 v176, v176, s63, v236
v_fma_f32 v177, v177, s63, v236
v_fma_f32 v178, v178, s63, v236
v_fma_f32 v179, v179, s63, v236
v_fma_f32 v182, v182, s63, v237
v_fma_f32 v180, v180, s63, v237
v_fma_f32 v181, v181, s63, v237
v_fma_f32 v183, v183, s63, v237
v_fma_f32 v184, v184, s63, v238
v_fma_f32 v185, v185, s63, v238
v_fma_f32 v186, v186, s63, v238
v_fma_f32 v187, v187, s63, v238
v_fma_f32 v188, v188, s63, v239
v_fma_f32 v189, v189, s63, v239
v_fma_f32 v190, v190, s63, v239
v_fma_f32 v191, v191, s63, v239
v_max3_f32 v246, v176, v177, v178
v_max3_f32 v246, v246, v179, v182
v_max3_f32 v246, v246, v180, v181
v_max3_f32 v246, v246, v183, v184
v_max3_f32 v246, v246, v185, v186
v_max3_f32 v246, v246, v187, v188
v_max3_f32 v246, v246, v189, v190
v_max3_f32 v246, v246, v191, v247
v_cmp_lt_f32 vcc, s64, v246
v_fma_f32 v232, v232, s63, v240
v_fma_f32 v233, v233, s63, v240
v_fma_f32 v234, v234, s63, v240
v_fma_f32 v235, v235, s63, v240
s_cbranch_vccnz Lst2_0
Lrt2_0:
v_add_f32 v241, v241, v160
v_add_f32 v241, v241, v161
v_add_f32 v241, v241, v162
v_add_f32 v241, v241, v163
v_add_f32 v242, v242, v164
v_add_f32 v242, v242, v165
v_add_f32 v242, v242, v166
v_add_f32 v242, v242, v167
v_add_f32 v243, v243, v168
v_add_f32 v243, v243, v169
v_exp_f32 v176, v176
v_exp_f32 v177, v177
v_add_f32 v241, v241, v176
v_add_f32 v241, v241, v177
v_exp_f32 v178, v178
v_exp_f32 v179, v179
v_exp_f32 v180, v180
v_add_f32 v241, v241, v178
v_add_f32 v241, v241, v179
v_exp_f32 v181, v181
v_exp_f32 v182, v182
v_exp_f32 v183, v183
CV 160,160,161
CV 161,162,163
CV 162,176,177
CV 163,178,179
v_add_f32 v242, v242, v180
v_add_f32 v242, v242, v181
v_add_f32 v242, v242, v182
v_add_f32 v242, v242, v183
CV 164,164,165
CV 165,166,167
CV 166,180,181
CV 167,182,183
v_add_f32 v243, v243, v170
v_exp_f32 v232, v232
s_nop 0
v_add_f32 v245, v245, v232
v_exp_f32 v233, v233
v_exp_f32 v234, v234
v_add_f32 v245, v245, v233
RB 176,249,16384
RB 180,249,16896
RB 224,252,8192
RB 208,249,18432
RB 212,249,18944
RB 228,252,9216
v_add_f32 v245, v245, v234
CV 254,232,233
v_exp_f32 v235, v235
s_nop 0
v_add_f32 v245, v245, v235
CV 255,234,235
v_add_f32 v243, v243, v171
CV 168,168,169
ds_write_b64 v252, v[254:255] offset:17408
CV 169,170,171
v_exp_f32 v184, v184
s_mov_b32 s65, 0
s_barrier
s_waitcnt lgkmcnt(6)
MV0 192,176,0
RB 216,249,20480
v_add_f32 v244, v244, v172
s_waitcnt lgkmcnt(6)
MV 192,180,4
RB 220,249,20992
v_add_f32 v244, v244, v173
MV0 196,176,32
s_cmp_lg_u32 s65, 0
v_exp_f32 v185, v185
MV 196,180,36
s_cbranch_scc1 Lst2_1
Lrt2_1:
v_exp_f32 v186, v186
MV0 200,176,64
v_exp_f32 v187, v187
MV 200,180,68
v_exp_f32 v188, v188
MV0 204,176,96
s_mov_b32 m0, 8192
v_exp_f32 v189, v189
MV 204,180,100
s_waitcnt lgkmcnt(3)
v_exp_f32 v190, v190
MV0 232,176,224
RB 224,252,10240
v_add_f32 v244, v244, v174
MV 232,180,228
v_exp_f32 v191, v191
MV 192,208,8
RB 176,249,22528
v_add_f32 v244, v244, v175
MV 192,212,12
RB 180,249,23040
v_add_f32 v243, v243, v184
MV 196,208,40
RB 228,252,11264
v_add_f32 v243, v243, v185
MV 196,212,44
buffer_load_dwordx4 v253, s[40:43], s60 offen offset:0 nt lds
MV 200,208,72
s_mov_b32 m0, 10112
v_add_f32 v243, v243, v186
v_add_f32 v243, v243, v187
MV 200,212,76
buffer_load_dwordx4 v253, s[40:43], s60 offen offset:128 nt lds
MV 204,208,104
v_add_f32 v244, v244, v188
v_add_f32 v244, v244, v189
MV 204,212,108
s_waitcnt lgkmcnt(0)
v_add_f32 v244, v244, v190
v_add_f32 v244, v244, v191
MV 232,208,224
RB 224,252,12288
CV 170,184,185
MV 232,212,228
CV 172,172,173
CV 173,174,175
MV 192,216,16
RB 208,249,24576
CV 171,186,187
MV 192,220,20
RB 212,249,25088
CV 174,188,189
MV 196,216,48
RB 228,252,13312
CV 175,190,191
MV 196,220,52
s_mov_b32 m0, 12032
MV 200,216,80
buffer_load_dwordx4 v253, s[40:43], s60 offen offset:256 nt lds
MV 200,220,84
MV 204,216,112
MV 204,220,116
s_waitcnt lgkmcnt(0)
MV 232,216,224
RB 216,249,26624
MV 232,220,228
MV 192,176,24
RB 220,249,27136
MV 192,180,28
RB 224,252,14336
MV 196,176,56
RB 228,252,15360
MV 196,180,60
s_mov_b32 m0, 13952
MV 200,176,88
buffer_load_dwordx4 v253, s[40:43], s60 offen offset:384 nt lds
s_add_u32 s60, s60, 16384
MV 200,180,92
v_fma_f32 v192, v192, s63, v236
v_fma_f32 v193, v193, s63, v236
MV 204,176,120
MV 204,180,124
s_waitcnt lgkmcnt(0)
v_fma_f32 v194, v194, s63, v236
v_fma_f32 v195, v195, s63, v236
MV 232,176,224
RB 224,249,28672
v_fma_f32 v196, v196, s63, v237
MV 232,180,228
v_fma_f32 v197, v197, s63, v237
v_fma_f32 v198, v198, s63, v237
MV0 176,208,0
RB 228,249,29184
v_fma_f32 v199, v199, s63, v237
MV 176,212,4
v_fma_f32 v200, v200, s63, v238
v_fma_f32 v201, v201, s63, v238
MV0 180,208,32
v_fma_f32 v202, v202, s63, v238
v_fma_f32 v203, v203, s63, v238
MV 180,212,36
v_fma_f32 v204, v204, s63, v239
MV0 184,208,64
v_fma_f32 v205, v205, s63, v239
v_fma_f32 v206, v206, s63, v239
MV 184,212,68
v_fma_f32 v207, v207, s63, v239
v_fma_f32 v232, v232, s63, v240
MV0 188,208,96
RB 208,249,30720
v_fma_f32 v233, v233, s63, v240
MV 188,212,100
v_fma_f32 v234, v234, s63, v240
v_fma_f32 v235, v235, s63, v240
MV 176,216,8
RB 212,249,31232
v_max3_f32 v246, v192, v193, v194
MV 176,220,12
v_max3_f32 v246, v246, v195, v196
v_max3_f32 v246, v246, v197, v198
MV 180,216,40
v_max3_f32 v246, v246, v199, v200
v_max3_f32 v246, v246, v201, v202
MV 180,220,44
v_max3_f32 v246, v246, v203, v204
MV 184,216,72
v_max3_f32 v246, v246, v205, v206
v_max3_f32 v246, v246, v207, v232
MV 184,220,76
v_max3_f32 v246, v246, v233, v234
v_max_f32 v246, v246, v235
MV 188,216,104
RT 216,250,0
v_cmp_lt_f32 vcc, s64, v246
MV 188,220,108
s_waitcnt lgkmcnt(3)
MV 176,224,16
RT 218,250,8192
MV 176,228,20
RT 220,251,0
MV 180,224,48
RT 222,251,8192
MV 180,228,52
s_cbranch_vccnz Lst2_2
Lrt2_2:
v_exp_f32 v192, v192
MV 184,224,80
v_exp_f32 v193, v193
MV 184,228,84
v_exp_f32 v194, v194
MV 188,224,112
RT 224,250,512
v_add_f32 v241, v241, v192
MV 188,228,116
s_waitcnt lgkmcnt(5)
v_exp_f32 v195, v195
MV 176,208,24
RT 226,250,8704
v_add_f32 v241, v241, v193
MV 176,212,28
RT 228,251,512
v_add_f32 v241, v241, v194
MV 180,208,56
RT 230,251,8704
v_add_f32 v241, v241, v195
MV 180,212,60
v_exp_f32 v196, v196
MV 184,208,88
v_exp_f32 v197, v197
MV 184,212,92
v_fma_f32 v176, v176, s63, v236
v_fma_f32 v177, v177, s63, v236
MV 188,208,120
s_waitcnt vmcnt(12) lgkmcnt(6)
v_fma_f32 v178, v178, s63, v236
v_fma_f32 v179, v179, s63, v236
MV 188,212,124
s_barrier
v_fma_f32 v182, v182, s63, v237
MA0 0,216,160
RT 208,250,2048
v_fma_f32 v180, v180, s63, v237
MA0 4,216,164
RT 210,250,10240
v_fma_f32 v181, v181, s63, v237
MA0 8,216,168
RT 212,251,2048
v_fma_f32 v183, v183, s63, v237
MA0 12,216,172
s_waitcnt lgkmcnt(7)
v_fma_f32 v184, v184, s63, v238
v_fma_f32 v185, v185, s63, v238
MA0 16,220,160
RT 214,251,10240
v_fma_f32 v186, v186, s63, v238
MA0 20,220,164
RT 216,250,4096
v_fma_f32 v187, v187, s63, v238
MA0 24,220,168
RT 218,250,12288
v_fma_f32 v188, v188, s63, v239
MA0 28,220,172
s_waitcnt lgkmcnt(8)
v_fma_f32 v189, v189, s63, v239
v_fma_f32 v190, v190, s63, v239
MA0 32,224,160
ds_read_b32 v247, v252 offset:29704
v_fma_f32 v191, v191, s63, v239
MA0 36,224,164
v_max3_f32 v246, v176, v177, v178
v_max3_f32 v246, v246, v179, v182
MA0 40,224,168
RT 220,251,4096
v_max3_f32 v246, v246, v180, v181
MA0 44,224,172
s_waitcnt lgkmcnt(8)
v_max3_f32 v246, v246, v183, v184
v_max3_f32 v246, v246, v185, v186
MA0 48,228,160
RT 222,251,12288
v_max3_f32 v246, v246, v187, v188
MA0 52,228,164
RB 224,252,16384
v_max3_f32 v246, v246, v189, v190
MA0 56,228,168
s_mov_b32 m0, 155648
v_exp_f32 v198, v198
MA0 60,228,172
s_waitcnt lgkmcnt(3)
v_exp_f32 v199, v199
MA0 64,208,160
RB 228,252,17408
v_max3_f32 v246, v246, v191, v247
MA0 68,208,164
buffer_load_dwordx4 v253, s[44:47], s61 offen offset:0 nt lds
MA0 72,208,168
v_cmp_lt_f32 vcc, s64, v246
v_add_f32 v242, v242, v196
MA0 76,208,172
v_add_f32 v242, v242, v197
v_add_f32 v242, v242, v198
MA0 80,212,160
RT 208,250,4608
v_add_f32 v242, v242, v199
MA0 84,212,164
RT 210,250,12800
MA0 88,212,168
s_mov_b32 m0, 157568
v_exp_f32 v200, v200
MA0 92,212,172
v_exp_f32 v201, v201
MA0 128,216,160
RT 212,251,4608
v_add_f32 v243, v243, v200
MA0 132,216,164
RT 214,251,12800
v_add_f32 v243, v243, v201
MA0 136,216,168
buffer_load_dwordx4 v253, s[44:47], s61 offen offset:128 nt lds
MA0 140,216,172
s_waitcnt lgkmcnt(4)
v_exp_f32 v202, v202
MV0 128,216,224
s_cbranch_vccnz Lst2_3
Lrt2_3:
v_exp_f32 v176, v176
MV0 132,216,228
v_exp_f32 v177, v177
MA0 144,220,160
RT 216,250,2560
v_add_f32 v241, v241, v176
MA0 148,220,164
RT 218,250,10752
v_add_f32 v241, v241, v177
MA0 152,220,168
s_mov_b32 m0, 159488
v_exp_f32 v178, v178
MA0 156,220,172
buffer_load_dwordx4 v253, s[44:47], s61 offen offset:256 nt lds
MV0 136,220,224
v_exp_f32 v179, v179
MV0 140,220,228
s_waitcnt lgkmcnt(4)
v_exp_f32 v180, v180
MA0 160,208,160
RT 220,251,2560
v_add_f32 v241, v241, v178
MA0 164,208,164
RT 222,251,10752
v_add_f32 v241, v241, v179
MA0 168,208,168
v_exp_f32 v181, v181
MA0 172,208,172
v_exp_f32 v182, v182
MV0 144,208,224
s_mov_b32 m0, 161408
v_exp_f32 v183, v183
MV0 148,208,228
s_waitcnt lgkmcnt(4)
CV 192,192,193
CV 193,194,195
MA0 176,212,160
RT 208,250,6144
CV 194,176,177
MA0 180,212,164
RT 210,250,14336
CV 195,178,179
MA0 184,212,168
buffer_load_dwordx4 v253, s[44:47], s61 offen offset:384 nt lds
s_add_u32 s61, s61, 16384
MA0 188,212,172
v_add_f32 v242, v242, v180
v_add_f32 v242, v242, v181
MV0 152,212,224
v_add_f32 v242, v242, v182
v_add_f32 v242, v242, v183
MV0 156,212,228
s_waitcnt lgkmcnt(4)
CV 196,196,197
MA0 96,216,160
RT 212,251,6144
CV 197,198,199
MA0 100,216,164
RT 214,251,14336
CV 198,180,181
MA0 104,216,168
CV 199,182,183
v_add_f32 v243, v243, v202
MA0 108,216,172
s_waitcnt lgkmcnt(4)
v_exp_f32 v232, v232
MA0 112,220,160
RT 216,250,6656
v_add_f32 v245, v245, v232
MA0 116,220,164
RT 218,250,14848
MA0 120,220,168
v_exp_f32 v233, v233
MA0 124,220,172
s_waitcnt lgkmcnt(4)
v_exp_f32 v234, v234
MA0 192,208,160
RT 220,251,6656
v_add_f32 v245, v245, v233
MA0 196,208,164
RB 176,249,32768
v_add_f32 v245, v245, v234
MA0 200,208,168
RB 180,249,33280
CV 254,232,233
MA0 204,208,172
s_waitcnt lgkmcnt(5)
v_exp_f32 v235, v235
MA0 208,212,160
RT 222,251,14848
v_add_f32 v245, v245, v235
MA0 212,212,164
RB 224,252,8192
CV 255,234,235
MA0 216,212,168
RB 208,249,34816
MA0 220,212,172
s_waitcnt lgkmcnt(6)
v_exp_f32 v203, v203
MA0 224,216,160
RB 212,249,35328
v_add_f32 v243, v243, v203
MA0 228,216,164
RB 228,252,9216
CV 200,200,201
MA0 232,216,168
ds_write_b64 v252, v[254:255] offset:19456
CV 201,202,203
MA0 236,216,172
s_waitcnt lgkmcnt(5)
v_exp_f32 v204, v204
MA0 240,220,160
v_exp_f32 v205, v205
MA0 244,220,164
v_exp_f32 v206, v206
MA0 248,220,168
v_exp_f32 v207, v207
MA0 252,220,172
v_exp_f32 v184, v184
.p2align 6
Lloop2:
MV0 160,176,0
RB 216,249,36864
v_add_f32 v244, v244, v204
MV 160,180,4
RB 220,249,37376
v_add_f32 v244, v244, v205
MV0 164,176,32
s_cmp_lg_u32 s65, 0
v_exp_f32 v185, v185
MV 164,180,36
s_cbranch_scc1 Lst2_4
Lrt2_4:
v_exp_f32 v186, v186
MV0 168,176,64
s_lshl_b32 s78, s62, 20
v_exp_f32 v187, v187
MV 168,180,68
s_add_u32 s78, s78, Lpeel2-Lbase
v_exp_f32 v188, v188
MV0 172,176,96
s_mov_b32 m0, 24576
v_exp_f32 v189, v189
MV 172,180,100
s_waitcnt lgkmcnt(3)
v_exp_f32 v190, v190
MV0 232,176,224
RB 224,252,10240
v_add_f32 v244, v244, v206
MV 232,180,228
v_exp_f32 v191, v191
MV 160,208,8
RB 176,249,38912
v_add_f32 v244, v244, v207
MV 160,212,12
RB 180,249,39424
v_add_f32 v243, v243, v184
MV 164,208,40
RB 228,252,11264
v_add_f32 v243, v243, v185
MV 164,212,44
buffer_load_dwordx4 v253, s[40:43], s60 offen offset:0 nt lds
MV 168,208,72
s_mov_b32 m0, 26496
v_add_f32 v243, v243, v186
v_add_f32 v243, v243, v187
MV 168,212,76
buffer_load_dwordx4 v253, s[40:43], s60 offen offset:128 nt lds
MV 172,208,104
v_add_f32 v244, v244, v188
v_add_f32 v244, v244, v189
MV 172,212,108
s_waitcnt lgkmcnt(0)
v_add_f32 v244, v244, v190
v_add_f32 v244, v244, v191
MV 232,208,224
RB 224,252,12288
CV 202,184,185
MV 232,212,228
CV 204,204,205
CV 205,206,207
MV 160,216,16
RB 208,249,40960
CV 203,186,187
MV 160,220,20
RB 212,249,41472
CV 206,188,189
MV 164,216,48
RB 228,252,13312
CV 207,190,191
MV 164,220,52
s_mov_b32 m0, 28416
MV 168,216,80
buffer_load_dwordx4 v253, s[40:43], s60 offen offset:256 nt lds
MV 168,220,84
MV 172,216,112
MV 172,220,116
s_waitcnt lgkmcnt(0)
MV 232,216,224
RB 216,249,43008
v_subrev_u32 v248, 0x10000, v252
MV 232,220,228
MV 160,176,24
RB 220,249,43520
v_lshlrev_b32 v248, 3, v248
MV 160,180,28
RB 224,252,14336
MV 164,176,56
RB 228,252,15360
MV 164,180,60
s_mov_b32 m0, 30336
MV 168,176,88
buffer_load_dwordx4 v253, s[40:43], s60 offen offset:384 nt lds
s_add_u32 s60, s60, 16384
MV 168,180,92
s_mov_b32 m0, 97792
v_fma_f32 v160, v160, s63, v236
v_fma_f32 v161, v161, s63, v236
MV 172,176,120
buffer_load_dword v248, s[56:59], s78 offen lds
MV 172,180,124
s_waitcnt lgkmcnt(0)
v_fma_f32 v162, v162, s63, v236
v_fma_f32 v163, v163, s63, v236
MV 232,176,224
RB 224,249,45056
v_fma_f32 v164, v164, s63, v237
MV 232,180,228
v_fma_f32 v165, v165, s63, v237
v_fma_f32 v166, v166, s63, v237
MV0 176,208,0
RB 228,249,45568
v_fma_f32 v167, v167, s63, v237
MV 176,212,4
v_fma_f32 v168, v168, s63, v238
v_fma_f32 v169, v169, s63, v238
MV0 180,208,32
v_fma_f32 v170, v170, s63, v238
v_fma_f32 v171, v171, s63, v238
MV 180,212,36
v_fma_f32 v172, v172, s63, v239
MV0 184,208,64
v_fma_f32 v173, v173, s63, v239
v_fma_f32 v174, v174, s63, v239
MV 184,212,68
v_fma_f32 v175, v175, s63, v239
v_fma_f32 v232, v232, s63, v240
MV0 188,208,96
RB 208,249,47104
v_fma_f32 v233, v233, s63, v240
MV 188,212,100
v_fma_f32 v234, v234, s63, v240
v_fma_f32 v235, v235, s63, v240
MV 176,216,8
RB 212,249,47616
v_max3_f32 v246, v160, v161, v162
MV 176,220,12
v_max3_f32 v246, v246, v163, v164
v_max3_f32 v246, v246, v165, v166
MV 180,216,40
v_max3_f32 v246, v246, v167, v168
v_max3_f32 v246, v246, v169, v170
MV 180,220,44
v_max3_f32 v246, v246, v171, v172
MV 184,216,72
v_max3_f32 v246, v246, v173, v174
v_max3_f32 v246, v246, v175, v232
MV 184,220,76
v_max3_f32 v246, v246, v233, v234
v_max_f32 v246, v246, v235
MV 188,216,104
RT 216,250,16384
v_cmp_lt_f32 vcc, s64, v246
MV 188,220,108
s_waitcnt lgkmcnt(3)
MV 176,224,16
RT 218,250,24576
MV 176,228,20
RT 220,251,16384
MV 180,224,48
RT 222,251,24576
MV 180,228,52
s_cbranch_vccnz Lst2_5
Lrt2_5:
v_exp_f32 v160, v160
MV 184,224,80
v_exp_f32 v161, v161
MV 184,228,84
v_exp_f32 v162, v162
MV 188,224,112
RT 224,250,16896
v_add_f32 v241, v241, v160
MV 188,228,116
s_waitcnt lgkmcnt(5)
v_exp_f32 v163, v163
MV 176,208,24
RT 226,250,25088
v_add_f32 v241, v241, v161
MV 176,212,28
RT 228,251,16896
v_add_f32 v241, v241, v162
MV 180,208,56
RT 230,251,25088
v_add_f32 v241, v241, v163
MV 180,212,60
v_exp_f32 v164, v164
MV 184,208,88
v_exp_f32 v165, v165
MV 184,212,92
v_fma_f32 v176, v176, s63, v236
v_fma_f32 v177, v177, s63, v236
MV 188,208,120
s_waitcnt vmcnt(13) lgkmcnt(6)
v_fma_f32 v178, v178, s63, v236
v_fma_f32 v179, v179, s63, v236
MV 188,212,124
s_barrier
v_fma_f32 v182, v182, s63, v237
MA 0,216,192
RT 208,250,18432
v_fma_f32 v180, v180, s63, v237
MA 4,216,196
RT 210,250,26624
v_fma_f32 v181, v181, s63, v237
MA 8,216,200
RT 212,251,18432
v_fma_f32 v183, v183, s63, v237
MA 12,216,204
s_waitcnt lgkmcnt(7)
v_fma_f32 v184, v184, s63, v238
v_fma_f32 v185, v185, s63, v238
MA 16,220,192
RT 214,251,26624
v_fma_f32 v186, v186, s63, v238
MA 20,220,196
RT 216,250,20480
v_fma_f32 v187, v187, s63, v238
MA 24,220,200
RT 218,250,28672
v_fma_f32 v188, v188, s63, v239
MA 28,220,204
s_waitcnt lgkmcnt(8)
v_fma_f32 v189, v189, s63, v239
v_fma_f32 v190, v190, s63, v239
MA 32,224,192
ds_read_b32 v247, v252 offset:29704
v_fma_f32 v191, v191, s63, v239
MA 36,224,196
v_max3_f32 v246, v176, v177, v178
v_max3_f32 v246, v246, v179, v182
MA 40,224,200
RT 220,251,20480
v_max3_f32 v246, v246, v180, v181
MA 44,224,204
s_waitcnt lgkmcnt(8)
v_max3_f32 v246, v246, v183, v184
v_max3_f32 v246, v246, v185, v186
MA 48,228,192
RT 222,251,28672
v_max3_f32 v246, v246, v187, v188
MA 52,228,196
RB 224,252,18432
v_max3_f32 v246, v246, v189, v190
MA 56,228,200
s_mov_b32 m0, 106496
v_exp_f32 v166, v166
MA 60,228,204
s_waitcnt lgkmcnt(3)
v_exp_f32 v167, v167
MA 64,208,192
RB 228,252,19456
v_max3_f32 v246, v246, v191, v247
MA 68,208,196
buffer_load_dwordx4 v253, s[44:47], s61 offen offset:0 nt lds
MA 72,208,200
v_cmp_lt_f32 vcc, s64, v246
v_add_f32 v242, v242, v164
MA 76,208,204
v_add_f32 v242, v242, v165
v_add_f32 v242, v242, v166
MA 80,212,192
RT 208,250,20992
v_add_f32 v242, v242, v167
MA 84,212,196
RT 210,250,29184
MA 88,212,200
s_mov_b32 m0, 108416
v_exp_f32 v168, v168
MA 92,212,204
v_exp_f32 v169, v169
MA 128,216,192
RT 212,251,20992
v_add_f32 v243, v243, v168
MA 132,216,196
RT 214,251,29184
v_add_f32 v243, v243, v169
MA 136,216,200
buffer_load_dwordx4 v253, s[44:47], s61 offen offset:128 nt lds
MA 140,216,204
s_waitcnt lgkmcnt(4)
v_exp_f32 v170, v170
MV 128,216,224
s_cbranch_vccnz Lst2_6
Lrt2_6:
v_exp_f32 v176, v176
MV 132,216,228
v_exp_f32 v177, v177
MA 144,220,192
RT 216,250,18944
v_add_f32 v241, v241, v176
MA 148,220,196
RT 218,250,27136
v_add_f32 v241, v241, v177
MA 152,220,200
s_mov_b32 m0, 110336
v_exp_f32 v178, v178
MA 156,220,204
buffer_load_dwordx4 v253, s[44:47], s61 offen offset:256 nt lds
MV 136,220,224
v_exp_f32 v179, v179
MV 140,220,228
s_waitcnt lgkmcnt(4)
v_exp_f32 v180, v180
MA 160,208,192
RT 220,251,18944
v_add_f32 v241, v241, v178
MA 164,208,196
RT 222,251,27136
v_add_f32 v241, v241, v179
MA 168,208,200
v_exp_f32 v181, v181
MA 172,208,204
v_exp_f32 v182, v182
MV 144,208,224
s_mov_b32 m0, 112256
v_exp_f32 v183, v183
MV 148,208,228
s_waitcnt lgkmcnt(4)
CV 160,160,161
CV 161,162,163
MA 176,212,192
RT 208,250,22528
CV 162,176,177
MA 180,212,196
RT 210,250,30720
CV 163,178,179
MA 184,212,200
buffer_load_dwordx4 v253, s[44:47], s61 offen offset:384 nt lds
s_add_u32 s61, s61, 16384
MA 188,212,204
v_add_f32 v242, v242, v180
v_add_f32 v242, v242, v181
MV 152,212,224
v_add_f32 v242, v242, v182
v_add_f32 v242, v242, v183
MV 156,212,228
s_waitcnt lgkmcnt(4)
CV 164,164,165
MA 96,216,192
RT 212,251,22528
CV 165,166,167
MA 100,216,196
RT 214,251,30720
CV 166,180,181
MA 104,216,200
CV 167,182,183
v_add_f32 v243, v243, v170
MA 108,216,204
s_waitcnt lgkmcnt(4)
v_exp_f32 v232, v232
MA 112,220,192
RT 216,250,23040
v_add_f32 v245, v245, v232
MA 116,220,196
RT 218,250,31232
MA 120,220,200
v_exp_f32 v233, v233
MA 124,220,204
s_waitcnt lgkmcnt(4)
v_exp_f32 v234, v234
MA 192,208,192
RT 220,251,23040
v_add_f32 v245, v245, v233
MA 196,208,196
RB 176,249,49152
v_add_f32 v245, v245, v234
MA 200,208,200
RB 180,249,49664
CV 254,232,233
MA 204,208,204
s_waitcnt lgkmcnt(5)
v_exp_f32 v235, v235
MA 208,212,192
RT 222,251,31232
v_add_f32 v245, v245, v235
MA 212,212,196
RB 224,252,8192
CV 255,234,235
MA 216,212,200
RB 208,249,51200
MA 220,212,204
s_waitcnt lgkmcnt(6)
v_exp_f32 v171, v171
MA 224,216,192
RB 212,249,51712
v_add_f32 v243, v243, v171
MA 228,216,196
RB 228,252,9216
CV 168,168,169
MA 232,216,200
ds_write_b64 v252, v[254:255] offset:17408
CV 169,170,171
MA 236,216,204
s_waitcnt lgkmcnt(5)
v_exp_f32 v172, v172
MA 240,220,192
v_exp_f32 v173, v173
MA 244,220,196
v_exp_f32 v174, v174
MA 248,220,200
v_exp_f32 v175, v175
MA 252,220,204
v_exp_f32 v184, v184
MV0 192,176,0
RB 216,249,53248
v_add_f32 v244, v244, v172
MV 192,180,4
RB 220,249,53760
v_add_f32 v244, v244, v173
MV0 196,176,32
s_cmp_lg_u32 s65, 0
v_exp_f32 v185, v185
MV 196,180,36
s_cbranch_scc1 Lst2_7
Lrt2_7:
v_exp_f32 v186, v186
MV0 200,176,64
s_lshl_b32 s78, s62, 20
v_exp_f32 v187, v187
MV 200,180,68
s_add_u32 s78, s78, Lpeel2-Lbase+8192
v_exp_f32 v188, v188
MV0 204,176,96
s_mov_b32 m0, 40960
v_exp_f32 v189, v189
MV 204,180,100
s_waitcnt lgkmcnt(3)
v_exp_f32 v190, v190
MV0 232,176,224
RB 224,252,10240
v_add_f32 v244, v244, v174
MV 232,180,228
v_exp_f32 v191, v191
MV 192,208,8
RB 176,249,55296
v_add_f32 v244, v244, v175
MV 192,212,12
RB 180,249,55808
v_add_f32 v243, v243, v184
MV 196,208,40
RB 228,252,11264
v_add_f32 v243, v243, v185
MV 196,212,44
buffer_load_dwordx4 v253, s[40:43], s60 offen offset:0 nt lds
MV 200,208,72
s_mov_b32 m0, 42880
v_add_f32 v243, v243, v186
v_add_f32 v243, v243, v187
MV 200,212,76
buffer_load_dwordx4 v253, s[40:43], s60 offen offset:128 nt lds
MV 204,208,104
v_add_f32 v244, v244, v188
v_add_f32 v244, v244, v189
MV 204,212,108
s_waitcnt lgkmcnt(0)
v_add_f32 v244, v244, v190
v_add_f32 v244, v244, v191
MV 232,208,224
RB 224,252,12288
CV 170,184,185
MV 232,212,228
CV 172,172,173
)ASM"
R"ASM(
CV 173,174,175
MV 192,216,16
RB 208,249,57344
CV 171,186,187
MV 192,220,20
RB 212,249,57856
CV 174,188,189
MV 196,216,48
RB 228,252,13312
CV 175,190,191
MV 196,220,52
s_mov_b32 m0, 44800
MV 200,216,80
buffer_load_dwordx4 v253, s[40:43], s60 offen offset:256 nt lds
MV 200,220,84
MV 204,216,112
MV 204,220,116
s_waitcnt lgkmcnt(0)
MV 232,216,224
RB 216,249,59392
v_subrev_u32 v248, 0x10000, v252
MV 232,220,228
MV 192,176,24
RB 220,249,59904
v_lshlrev_b32 v248, 3, v248
MV 192,180,28
RB 224,252,14336
MV 196,176,56
RB 228,252,15360
MV 196,180,60
s_mov_b32 m0, 46720
MV 200,176,88
buffer_load_dwordx4 v253, s[40:43], s60 offen offset:384 nt lds
s_add_u32 s60, s60, 16384
MV 200,180,92
s_mov_b32 m0, 97792
v_fma_f32 v192, v192, s63, v236
v_fma_f32 v193, v193, s63, v236
MV 204,176,120
buffer_load_dword v248, s[56:59], s78 offen lds
MV 204,180,124
s_waitcnt lgkmcnt(0)
v_fma_f32 v194, v194, s63, v236
v_fma_f32 v195, v195, s63, v236
MV 232,176,224
RB 224,249,61440
v_fma_f32 v196, v196, s63, v237
MV 232,180,228
v_fma_f32 v197, v197, s63, v237
v_fma_f32 v198, v198, s63, v237
MV0 176,208,0
RB 228,249,61952
v_fma_f32 v199, v199, s63, v237
MV 176,212,4
v_fma_f32 v200, v200, s63, v238
v_fma_f32 v201, v201, s63, v238
MV0 180,208,32
v_fma_f32 v202, v202, s63, v238
v_fma_f32 v203, v203, s63, v238
MV 180,212,36
v_fma_f32 v204, v204, s63, v239
MV0 184,208,64
v_fma_f32 v205, v205, s63, v239
v_fma_f32 v206, v206, s63, v239
MV 184,212,68
v_fma_f32 v207, v207, s63, v239
v_fma_f32 v232, v232, s63, v240
MV0 188,208,96
RB 208,249,63488
v_fma_f32 v233, v233, s63, v240
MV 188,212,100
v_fma_f32 v234, v234, s63, v240
v_fma_f32 v235, v235, s63, v240
MV 176,216,8
RB 212,249,64000
v_max3_f32 v246, v192, v193, v194
MV 176,220,12
v_max3_f32 v246, v246, v195, v196
v_max3_f32 v246, v246, v197, v198
MV 180,216,40
v_max3_f32 v246, v246, v199, v200
v_max3_f32 v246, v246, v201, v202
MV 180,220,44
v_max3_f32 v246, v246, v203, v204
MV 184,216,72
v_max3_f32 v246, v246, v205, v206
v_max3_f32 v246, v246, v207, v232
MV 184,220,76
v_max3_f32 v246, v246, v233, v234
v_max_f32 v246, v246, v235
MV 188,216,104
RT 216,250,32768
v_cmp_lt_f32 vcc, s64, v246
MV 188,220,108
s_waitcnt lgkmcnt(3)
MV 176,224,16
RT 218,250,40960
MV 176,228,20
RT 220,251,32768
MV 180,224,48
RT 222,251,40960
MV 180,228,52
s_cbranch_vccnz Lst2_8
Lrt2_8:
v_exp_f32 v192, v192
MV 184,224,80
v_exp_f32 v193, v193
MV 184,228,84
v_exp_f32 v194, v194
MV 188,224,112
RT 224,250,33280
v_add_f32 v241, v241, v192
MV 188,228,116
s_waitcnt lgkmcnt(5)
v_exp_f32 v195, v195
MV 176,208,24
RT 226,250,41472
v_add_f32 v241, v241, v193
MV 176,212,28
RT 228,251,33280
v_add_f32 v241, v241, v194
MV 180,208,56
RT 230,251,41472
v_add_f32 v241, v241, v195
MV 180,212,60
v_exp_f32 v196, v196
MV 184,208,88
v_exp_f32 v197, v197
MV 184,212,92
v_fma_f32 v176, v176, s63, v236
v_fma_f32 v177, v177, s63, v236
MV 188,208,120
s_waitcnt vmcnt(14) lgkmcnt(6)
v_fma_f32 v178, v178, s63, v236
v_fma_f32 v179, v179, s63, v236
MV 188,212,124
s_barrier
v_fma_f32 v182, v182, s63, v237
MA 0,216,160
RT 208,250,34816
v_fma_f32 v180, v180, s63, v237
MA 4,216,164
RT 210,250,43008
v_fma_f32 v181, v181, s63, v237
MA 8,216,168
RT 212,251,34816
v_fma_f32 v183, v183, s63, v237
MA 12,216,172
s_waitcnt lgkmcnt(7)
v_fma_f32 v184, v184, s63, v238
v_fma_f32 v185, v185, s63, v238
MA 16,220,160
RT 214,251,43008
v_fma_f32 v186, v186, s63, v238
MA 20,220,164
RT 216,250,36864
v_fma_f32 v187, v187, s63, v238
MA 24,220,168
RT 218,250,45056
v_fma_f32 v188, v188, s63, v239
MA 28,220,172
s_waitcnt lgkmcnt(8)
v_fma_f32 v189, v189, s63, v239
v_fma_f32 v190, v190, s63, v239
MA 32,224,160
ds_read_b32 v247, v252 offset:29704
v_fma_f32 v191, v191, s63, v239
MA 36,224,164
v_max3_f32 v246, v176, v177, v178
v_max3_f32 v246, v246, v179, v182
MA 40,224,168
RT 220,251,36864
v_max3_f32 v246, v246, v180, v181
MA 44,224,172
s_waitcnt lgkmcnt(8)
v_max3_f32 v246, v246, v183, v184
v_max3_f32 v246, v246, v185, v186
MA 48,228,160
RT 222,251,45056
v_max3_f32 v246, v246, v187, v188
MA 52,228,164
RB 224,252,16384
v_max3_f32 v246, v246, v189, v190
MA 56,228,168
s_mov_b32 m0, 122880
v_exp_f32 v198, v198
MA 60,228,172
s_waitcnt lgkmcnt(3)
v_exp_f32 v199, v199
MA 64,208,160
RB 228,252,17408
v_max3_f32 v246, v246, v191, v247
MA 68,208,164
buffer_load_dwordx4 v253, s[44:47], s61 offen offset:0 nt lds
MA 72,208,168
v_cmp_lt_f32 vcc, s64, v246
v_add_f32 v242, v242, v196
MA 76,208,172
v_add_f32 v242, v242, v197
v_add_f32 v242, v242, v198
MA 80,212,160
RT 208,250,37376
v_add_f32 v242, v242, v199
MA 84,212,164
RT 210,250,45568
MA 88,212,168
s_mov_b32 m0, 124800
v_exp_f32 v200, v200
MA 92,212,172
v_exp_f32 v201, v201
MA 128,216,160
RT 212,251,37376
v_add_f32 v243, v243, v200
MA 132,216,164
RT 214,251,45568
v_add_f32 v243, v243, v201
MA 136,216,168
buffer_load_dwordx4 v253, s[44:47], s61 offen offset:128 nt lds
MA 140,216,172
s_waitcnt lgkmcnt(4)
v_exp_f32 v202, v202
MV 128,216,224
s_cbranch_vccnz Lst2_9
Lrt2_9:
v_exp_f32 v176, v176
MV 132,216,228
v_exp_f32 v177, v177
MA 144,220,160
RT 216,250,35328
v_add_f32 v241, v241, v176
MA 148,220,164
RT 218,250,43520
v_add_f32 v241, v241, v177
MA 152,220,168
s_mov_b32 m0, 126720
v_exp_f32 v178, v178
MA 156,220,172
buffer_load_dwordx4 v253, s[44:47], s61 offen offset:256 nt lds
MV 136,220,224
v_exp_f32 v179, v179
MV 140,220,228
s_waitcnt lgkmcnt(4)
v_exp_f32 v180, v180
MA 160,208,160
RT 220,251,35328
v_add_f32 v241, v241, v178
MA 164,208,164
RT 222,251,43520
v_add_f32 v241, v241, v179
MA 168,208,168
v_exp_f32 v181, v181
MA 172,208,172
v_exp_f32 v182, v182
MV 144,208,224
s_mov_b32 m0, 128640
v_exp_f32 v183, v183
MV 148,208,228
s_waitcnt lgkmcnt(4)
CV 192,192,193
CV 193,194,195
MA 176,212,160
RT 208,250,38912
CV 194,176,177
MA 180,212,164
RT 210,250,47104
CV 195,178,179
MA 184,212,168
buffer_load_dwordx4 v253, s[44:47], s61 offen offset:384 nt lds
s_add_u32 s61, s61, 16384
MA 188,212,172
v_add_f32 v242, v242, v180
v_add_f32 v242, v242, v181
MV 152,212,224
v_add_f32 v242, v242, v182
v_add_f32 v242, v242, v183
MV 156,212,228
s_waitcnt lgkmcnt(4)
CV 196,196,197
MA 96,216,160
RT 212,251,38912
CV 197,198,199
MA 100,216,164
RT 214,251,47104
CV 198,180,181
MA 104,216,168
CV 199,182,183
v_add_f32 v243, v243, v202
MA 108,216,172
s_waitcnt lgkmcnt(4)
v_exp_f32 v232, v232
MA 112,220,160
RT 216,250,39424
v_add_f32 v245, v245, v232
MA 116,220,164
RT 218,250,47616
MA 120,220,168
v_exp_f32 v233, v233
MA 124,220,172
s_waitcnt lgkmcnt(4)
v_exp_f32 v234, v234
MA 192,208,160
RT 220,251,39424
v_add_f32 v245, v245, v233
MA 196,208,164
RB 176,249,0
v_add_f32 v245, v245, v234
MA 200,208,168
RB 180,249,512
CV 254,232,233
MA 204,208,172
s_waitcnt lgkmcnt(5)
v_exp_f32 v235, v235
MA 208,212,160
RT 222,251,47616
v_add_f32 v245, v245, v235
MA 212,212,164
RB 224,252,8192
CV 255,234,235
MA 216,212,168
RB 208,249,2048
MA 220,212,172
s_waitcnt lgkmcnt(6)
v_exp_f32 v203, v203
MA 224,216,160
RB 212,249,2560
v_add_f32 v243, v243, v203
MA 228,216,164
RB 228,252,9216
CV 200,200,201
MA 232,216,168
ds_write_b64 v252, v[254:255] offset:19456
CV 201,202,203
MA 236,216,172
s_waitcnt lgkmcnt(5)
v_exp_f32 v204, v204
MA 240,220,160
v_exp_f32 v205, v205
MA 244,220,164
v_exp_f32 v206, v206
MA 248,220,168
v_exp_f32 v207, v207
MA 252,220,172
v_exp_f32 v184, v184
MV0 160,176,0
RB 216,249,4096
v_add_f32 v244, v244, v204
MV 160,180,4
RB 220,249,4608
v_add_f32 v244, v244, v205
MV0 164,176,32
s_cmp_lg_u32 s65, 0
v_exp_f32 v185, v185
MV 164,180,36
s_cbranch_scc1 Lst2_10
Lrt2_10:
v_exp_f32 v186, v186
MV0 168,176,64
s_lshl_b32 s78, s62, 20
v_exp_f32 v187, v187
MV 168,180,68
s_add_u32 s78, s78, LT-Lbase+16384
v_exp_f32 v188, v188
MV0 172,176,96
s_mov_b32 m0, 57344
v_exp_f32 v189, v189
MV 172,180,100
s_waitcnt lgkmcnt(3)
v_exp_f32 v190, v190
MV0 232,176,224
RB 224,252,10240
v_add_f32 v244, v244, v206
MV 232,180,228
v_exp_f32 v191, v191
MV 160,208,8
RB 176,249,6144
v_add_f32 v244, v244, v207
MV 160,212,12
RB 180,249,6656
v_add_f32 v243, v243, v184
MV 164,208,40
RB 228,252,11264
v_add_f32 v243, v243, v185
MV 164,212,44
buffer_load_dwordx4 v253, s[40:43], s60 offen offset:0 nt lds
MV 168,208,72
s_mov_b32 m0, 59264
v_add_f32 v243, v243, v186
v_add_f32 v243, v243, v187
MV 168,212,76
buffer_load_dwordx4 v253, s[40:43], s60 offen offset:128 nt lds
MV 172,208,104
v_add_f32 v244, v244, v188
v_add_f32 v244, v244, v189
MV 172,212,108
s_waitcnt lgkmcnt(0)
v_add_f32 v244, v244, v190
v_add_f32 v244, v244, v191
MV 232,208,224
RB 224,252,12288
CV 202,184,185
MV 232,212,228
CV 204,204,205
CV 205,206,207
MV 160,216,16
RB 208,249,8192
CV 203,186,187
MV 160,220,20
RB 212,249,8704
CV 206,188,189
MV 164,216,48
RB 228,252,13312
CV 207,190,191
MV 164,220,52
s_mov_b32 m0, 61184
MV 168,216,80
buffer_load_dwordx4 v253, s[40:43], s60 offen offset:256 nt lds
MV 168,220,84
MV 172,216,112
MV 172,220,116
s_waitcnt lgkmcnt(0)
MV 232,216,224
RB 216,249,10240
v_subrev_u32 v248, 0x10000, v252
MV 232,220,228
MV 160,176,24
RB 220,249,10752
v_lshlrev_b32 v248, 3, v248
MV 160,180,28
RB 224,252,14336
MV 164,176,56
RB 228,252,15360
MV 164,180,60
s_mov_b32 m0, 63104
MV 168,176,88
buffer_load_dwordx4 v253, s[40:43], s60 offen offset:384 nt lds
s_add_u32 s60, s60, 16384
MV 168,180,92
s_mov_b32 m0, 97792
v_fma_f32 v160, v160, s63, v236
v_fma_f32 v161, v161, s63, v236
MV 172,176,120
buffer_load_dword v248, s[56:59], s78 offen lds
MV 172,180,124
s_waitcnt lgkmcnt(0)
v_fma_f32 v162, v162, s63, v236
v_fma_f32 v163, v163, s63, v236
MV 232,176,224
RB 224,249,12288
v_fma_f32 v164, v164, s63, v237
MV 232,180,228
v_fma_f32 v165, v165, s63, v237
v_fma_f32 v166, v166, s63, v237
MV0 176,208,0
RB 228,249,12800
v_fma_f32 v167, v167, s63, v237
MV 176,212,4
v_fma_f32 v168, v168, s63, v238
v_fma_f32 v169, v169, s63, v238
MV0 180,208,32
v_fma_f32 v170, v170, s63, v238
v_fma_f32 v171, v171, s63, v238
MV 180,212,36
v_fma_f32 v172, v172, s63, v239
MV0 184,208,64
v_fma_f32 v173, v173, s63, v239
v_fma_f32 v174, v174, s63, v239
MV 184,212,68
v_fma_f32 v175, v175, s63, v239
v_fma_f32 v232, v232, s63, v240
MV0 188,208,96
RB 208,249,14336
v_fma_f32 v233, v233, s63, v240
MV 188,212,100
v_fma_f32 v234, v234, s63, v240
v_fma_f32 v235, v235, s63, v240
MV 176,216,8
RB 212,249,14848
v_max3_f32 v246, v160, v161, v162
MV 176,220,12
v_max3_f32 v246, v246, v163, v164
v_max3_f32 v246, v246, v165, v166
MV 180,216,40
v_max3_f32 v246, v246, v167, v168
v_max3_f32 v246, v246, v169, v170
MV 180,220,44
v_max3_f32 v246, v246, v171, v172
MV 184,216,72
v_max3_f32 v246, v246, v173, v174
v_max3_f32 v246, v246, v175, v232
MV 184,220,76
v_max3_f32 v246, v246, v233, v234
v_max_f32 v246, v246, v235
MV 188,216,104
RT 216,250,49152
v_cmp_lt_f32 vcc, s64, v246
MV 188,220,108
s_waitcnt lgkmcnt(3)
MV 176,224,16
RT 218,250,57344
MV 176,228,20
RT 220,251,49152
MV 180,224,48
RT 222,251,57344
MV 180,228,52
s_cbranch_vccnz Lst2_11
Lrt2_11:
v_exp_f32 v160, v160
MV 184,224,80
v_exp_f32 v161, v161
MV 184,228,84
v_exp_f32 v162, v162
MV 188,224,112
RT 224,250,49664
v_add_f32 v241, v241, v160
MV 188,228,116
s_waitcnt lgkmcnt(5)
v_exp_f32 v163, v163
MV 176,208,24
RT 226,250,57856
v_add_f32 v241, v241, v161
MV 176,212,28
RT 228,251,49664
v_add_f32 v241, v241, v162
MV 180,208,56
RT 230,251,57856
v_add_f32 v241, v241, v163
MV 180,212,60
v_exp_f32 v164, v164
MV 184,208,88
v_exp_f32 v165, v165
MV 184,212,92
v_fma_f32 v176, v176, s63, v236
v_fma_f32 v177, v177, s63, v236
MV 188,208,120
s_waitcnt vmcnt(14) lgkmcnt(6)
v_fma_f32 v178, v178, s63, v236
v_fma_f32 v179, v179, s63, v236
MV 188,212,124
s_barrier
v_fma_f32 v182, v182, s63, v237
MA 0,216,192
RT 208,250,51200
v_fma_f32 v180, v180, s63, v237
MA 4,216,196
RT 210,250,59392
v_fma_f32 v181, v181, s63, v237
MA 8,216,200
RT 212,251,51200
v_fma_f32 v183, v183, s63, v237
MA 12,216,204
s_waitcnt lgkmcnt(7)
v_fma_f32 v184, v184, s63, v238
v_fma_f32 v185, v185, s63, v238
MA 16,220,192
RT 214,251,59392
v_fma_f32 v186, v186, s63, v238
MA 20,220,196
RT 216,250,53248
v_fma_f32 v187, v187, s63, v238
MA 24,220,200
RT 218,250,61440
v_fma_f32 v188, v188, s63, v239
MA 28,220,204
s_waitcnt lgkmcnt(8)
v_fma_f32 v189, v189, s63, v239
v_fma_f32 v190, v190, s63, v239
MA 32,224,192
ds_read_b32 v247, v252 offset:29704
v_fma_f32 v191, v191, s63, v239
MA 36,224,196
v_max3_f32 v246, v176, v177, v178
v_max3_f32 v246, v246, v179, v182
MA 40,224,200
RT 220,251,53248
v_max3_f32 v246, v246, v180, v181
MA 44,224,204
s_waitcnt lgkmcnt(8)
v_max3_f32 v246, v246, v183, v184
v_max3_f32 v246, v246, v185, v186
MA 48,228,192
RT 222,251,61440
v_max3_f32 v246, v246, v187, v188
MA 52,228,196
RB 224,252,18432
v_max3_f32 v246, v246, v189, v190
MA 56,228,200
s_mov_b32 m0, 139264
v_exp_f32 v166, v166
MA 60,228,204
s_waitcnt lgkmcnt(3)
v_exp_f32 v167, v167
MA 64,208,192
RB 228,252,19456
v_max3_f32 v246, v246, v191, v247
MA 68,208,196
buffer_load_dwordx4 v253, s[44:47], s61 offen offset:0 nt lds
MA 72,208,200
v_cmp_lt_f32 vcc, s64, v246
v_add_f32 v242, v242, v164
MA 76,208,204
v_add_f32 v242, v242, v165
v_add_f32 v242, v242, v166
MA 80,212,192
RT 208,250,53760
v_add_f32 v242, v242, v167
MA 84,212,196
RT 210,250,61952
MA 88,212,200
s_mov_b32 m0, 141184
v_exp_f32 v168, v168
MA 92,212,204
v_exp_f32 v169, v169
MA 128,216,192
RT 212,251,53760
v_add_f32 v243, v243, v168
MA 132,216,196
RT 214,251,61952
v_add_f32 v243, v243, v169
MA 136,216,200
buffer_load_dwordx4 v253, s[44:47], s61 offen offset:128 nt lds
MA 140,216,204
s_waitcnt lgkmcnt(4)
v_exp_f32 v170, v170
MV 128,216,224
s_cbranch_vccnz Lst2_12
Lrt2_12:
v_exp_f32 v176, v176
MV 132,216,228
v_exp_f32 v177, v177
MA 144,220,192
RT 216,250,51712
v_add_f32 v241, v241, v176
MA 148,220,196
RT 218,250,59904
v_add_f32 v241, v241, v177
MA 152,220,200
s_mov_b32 m0, 143104
v_exp_f32 v178, v178
MA 156,220,204
buffer_load_dwordx4 v253, s[44:47], s61 offen offset:256 nt lds
MV 136,220,224
v_exp_f32 v179, v179
MV 140,220,228
s_waitcnt lgkmcnt(4)
v_exp_f32 v180, v180
MA 160,208,192
RT 220,251,51712
v_add_f32 v241, v241, v178
MA 164,208,196
RT 222,251,59904
v_add_f32 v241, v241, v179
MA 168,208,200
v_exp_f32 v181, v181
MA 172,208,204
v_exp_f32 v182, v182
MV 144,208,224
s_mov_b32 m0, 145024
v_exp_f32 v183, v183
MV 148,208,228
s_waitcnt lgkmcnt(4)
CV 160,160,161
CV 161,162,163
MA 176,212,192
RT 208,250,55296
CV 162,176,177
MA 180,212,196
RT 210,250,63488
CV 163,178,179
MA 184,212,200
buffer_load_dwordx4 v253, s[44:47], s61 offen offset:384 nt lds
s_add_u32 s61, s61, 16384
MA 188,212,204
v_add_f32 v242, v242, v180
v_add_f32 v242, v242, v181
MV 152,212,224
v_add_f32 v242, v242, v182
v_add_f32 v242, v242, v183
MV 156,212,228
s_waitcnt lgkmcnt(4)
CV 164,164,165
MA 96,216,192
RT 212,251,55296
CV 165,166,167
MA 100,216,196
RT 214,251,63488
CV 166,180,181
MA 104,216,200
CV 167,182,183
v_add_f32 v243, v243, v170
MA 108,216,204
s_waitcnt lgkmcnt(4)
v_exp_f32 v232, v232
MA 112,220,192
RT 216,250,55808
v_add_f32 v245, v245, v232
MA 116,220,196
RT 218,250,64000
MA 120,220,200
v_exp_f32 v233, v233
MA 124,220,204
s_waitcnt lgkmcnt(4)
v_exp_f32 v234, v234
MA 192,208,192
RT 220,251,55808
v_add_f32 v245, v245, v233
MA 196,208,196
RB 176,249,16384
v_add_f32 v245, v245, v234
MA 200,208,200
RB 180,249,16896
CV 254,232,233
MA 204,208,204
s_waitcnt lgkmcnt(5)
v_exp_f32 v235, v235
MA 208,212,192
RT 222,251,64000
v_add_f32 v245, v245, v235
MA 212,212,196
RB 224,252,8192
CV 255,234,235
MA 216,212,200
RB 208,249,18432
MA 220,212,204
s_waitcnt lgkmcnt(6)
v_exp_f32 v171, v171
MA 224,216,192
RB 212,249,18944
v_add_f32 v243, v243, v171
MA 228,216,196
RB 228,252,9216
CV 168,168,169
MA 232,216,200
ds_write_b64 v252, v[254:255] offset:17408
CV 169,170,171
MA 236,216,204
s_waitcnt lgkmcnt(5)
v_exp_f32 v172, v172
MA 240,220,192
v_exp_f32 v173, v173
MA 244,220,196
v_exp_f32 v174, v174
MA 248,220,200
v_exp_f32 v175, v175
MA 252,220,204
v_exp_f32 v184, v184
MV0 192,176,0
RB 216,249,20480
v_add_f32 v244, v244, v172
MV 192,180,4
RB 220,249,20992
v_add_f32 v244, v244, v173
MV0 196,176,32
s_cmp_lg_u32 s65, 0
v_exp_f32 v185, v185
MV 196,180,36
s_cbranch_scc1 Lst2_13
Lrt2_13:
v_exp_f32 v186, v186
MV0 200,176,64
s_lshr_b32 s78, s76, 2
s_lshl_b32 s78, s78, 7
v_exp_f32 v187, v187
MV 200,180,68
s_and_b32 s67, s76, 3
s_add_u32 s78, s78, s67
v_exp_f32 v188, v188
MV0 204,176,96
s_mov_b32 m0, 8192
v_exp_f32 v189, v189
MV 204,180,100
s_waitcnt lgkmcnt(3)
v_exp_f32 v190, v190
MV0 232,176,224
RB 224,252,10240
v_add_f32 v244, v244, v174
MV 232,180,228
v_exp_f32 v191, v191
MV 192,208,8
RB 176,249,22528
v_add_f32 v244, v244, v175
MV 192,212,12
RB 180,249,23040
v_add_f32 v243, v243, v184
MV 196,208,40
RB 228,252,11264
v_add_f32 v243, v243, v185
MV 196,212,44
buffer_load_dwordx4 v253, s[40:43], s60 offen offset:0 nt lds
MV 200,208,72
s_mov_b32 m0, 10112
v_add_f32 v243, v243, v186
v_add_f32 v243, v243, v187
MV 200,212,76
buffer_load_dwordx4 v253, s[40:43], s60 offen offset:128 nt lds
MV 204,208,104
s_lshl_b32 s78, s78, 7
s_add_u32 s76, s76, 1
v_add_f32 v244, v244, v188
v_add_f32 v244, v244, v189
MV 204,212,108
s_waitcnt lgkmcnt(0)
v_add_f32 v244, v244, v190
v_add_f32 v244, v244, v191
MV 232,208,224
RB 224,252,12288
CV 170,184,185
MV 232,212,228
CV 172,172,173
CV 173,174,175
MV 192,216,16
RB 208,249,24576
CV 171,186,187
MV 192,220,20
RB 212,249,25088
CV 174,188,189
MV 196,216,48
RB 228,252,13312
CV 175,190,191
MV 196,220,52
s_mov_b32 m0, 12032
MV 200,216,80
buffer_load_dwordx4 v253, s[40:43], s60 offen offset:256 nt lds
MV 200,220,84
MV 204,216,112
MV 204,220,116
s_waitcnt lgkmcnt(0)
MV 232,216,224
RB 216,249,26624
MV 232,220,228
MV 192,176,24
RB 220,249,27136
MV 192,180,28
RB 224,252,14336
MV 196,176,56
RB 228,252,15360
MV 196,180,60
s_mov_b32 m0, 13952
MV 200,176,88
buffer_load_dwordx4 v253, s[40:43], s60 offen offset:384 nt lds
s_add_u32 s60, s60, 16384
MV 200,180,92
s_mov_b32 m0, 97792
v_fma_f32 v192, v192, s63, v236
v_fma_f32 v193, v193, s63, v236
MV 204,176,120
buffer_load_dword v253, s[48:51], s78 offen lds
MV 204,180,124
s_waitcnt lgkmcnt(0)
v_fma_f32 v194, v194, s63, v236
v_fma_f32 v195, v195, s63, v236
MV 232,176,224
RB 224,249,28672
v_fma_f32 v196, v196, s63, v237
MV 232,180,228
v_fma_f32 v197, v197, s63, v237
v_fma_f32 v198, v198, s63, v237
MV0 176,208,0
RB 228,249,29184
v_fma_f32 v199, v199, s63, v237
MV 176,212,4
v_fma_f32 v200, v200, s63, v238
v_fma_f32 v201, v201, s63, v238
MV0 180,208,32
v_fma_f32 v202, v202, s63, v238
v_fma_f32 v203, v203, s63, v238
MV 180,212,36
v_fma_f32 v204, v204, s63, v239
MV0 184,208,64
v_fma_f32 v205, v205, s63, v239
v_fma_f32 v206, v206, s63, v239
MV 184,212,68
v_fma_f32 v207, v207, s63, v239
v_fma_f32 v232, v232, s63, v240
MV0 188,208,96
RB 208,249,30720
v_fma_f32 v233, v233, s63, v240
MV 188,212,100
v_fma_f32 v234, v234, s63, v240
v_fma_f32 v235, v235, s63, v240
MV 176,216,8
RB 212,249,31232
v_max3_f32 v246, v192, v193, v194
MV 176,220,12
v_max3_f32 v246, v246, v195, v196
v_max3_f32 v246, v246, v197, v198
MV 180,216,40
v_max3_f32 v246, v246, v199, v200
v_max3_f32 v246, v246, v201, v202
MV 180,220,44
v_max3_f32 v246, v246, v203, v204
MV 184,216,72
v_max3_f32 v246, v246, v205, v206
v_max3_f32 v246, v246, v207, v232
MV 184,220,76
v_max3_f32 v246, v246, v233, v234
v_max_f32 v246, v246, v235
MV 188,216,104
RT 216,250,0
v_cmp_lt_f32 vcc, s64, v246
MV 188,220,108
s_waitcnt lgkmcnt(3)
MV 176,224,16
RT 218,250,8192
MV 176,228,20
RT 220,251,0
MV 180,224,48
RT 222,251,8192
MV 180,228,52
s_cbranch_vccnz Lst2_14
Lrt2_14:
v_exp_f32 v192, v192
MV 184,224,80
v_exp_f32 v193, v193
MV 184,228,84
v_exp_f32 v194, v194
MV 188,224,112
RT 224,250,512
v_add_f32 v241, v241, v192
MV 188,228,116
s_waitcnt lgkmcnt(5)
v_exp_f32 v195, v195
MV 176,208,24
RT 226,250,8704
v_add_f32 v241, v241, v193
MV 176,212,28
RT 228,251,512
v_add_f32 v241, v241, v194
MV 180,208,56
RT 230,251,8704
v_add_f32 v241, v241, v195
MV 180,212,60
v_exp_f32 v196, v196
MV 184,208,88
v_exp_f32 v197, v197
MV 184,212,92
v_fma_f32 v176, v176, s63, v236
v_fma_f32 v177, v177, s63, v236
MV 188,208,120
s_waitcnt vmcnt(14) lgkmcnt(6)
v_fma_f32 v178, v178, s63, v236
v_fma_f32 v179, v179, s63, v236
MV 188,212,124
s_barrier
v_fma_f32 v182, v182, s63, v237
MA 0,216,160
RT 208,250,2048
v_fma_f32 v180, v180, s63, v237
MA 4,216,164
RT 210,250,10240
v_fma_f32 v181, v181, s63, v237
MA 8,216,168
RT 212,251,2048
v_fma_f32 v183, v183, s63, v237
MA 12,216,172
s_waitcnt lgkmcnt(7)
v_fma_f32 v184, v184, s63, v238
v_fma_f32 v185, v185, s63, v238
MA 16,220,160
RT 214,251,10240
v_fma_f32 v186, v186, s63, v238
MA 20,220,164
RT 216,250,4096
v_fma_f32 v187, v187, s63, v238
MA 24,220,168
RT 218,250,12288
v_fma_f32 v188, v188, s63, v239
MA 28,220,172
s_waitcnt lgkmcnt(8)
v_fma_f32 v189, v189, s63, v239
v_fma_f32 v190, v190, s63, v239
MA 32,224,160
ds_read_b32 v247, v252 offset:29704
v_fma_f32 v191, v191, s63, v239
MA 36,224,164
v_max3_f32 v246, v176, v177, v178
v_max3_f32 v246, v246, v179, v182
MA 40,224,168
RT 220,251,4096
v_max3_f32 v246, v246, v180, v181
MA 44,224,172
s_waitcnt lgkmcnt(8)
v_max3_f32 v246, v246, v183, v184
v_max3_f32 v246, v246, v185, v186
MA 48,228,160
RT 222,251,12288
v_max3_f32 v246, v246, v187, v188
MA 52,228,164
RB 224,252,16384
v_max3_f32 v246, v246, v189, v190
MA 56,228,168
s_mov_b32 m0, 155648
v_exp_f32 v198, v198
MA 60,228,172
s_waitcnt lgkmcnt(3)
v_exp_f32 v199, v199
MA 64,208,160
RB 228,252,17408
v_max3_f32 v246, v246, v191, v247
MA 68,208,164
buffer_load_dwordx4 v253, s[44:47], s61 offen offset:0 nt lds
MA 72,208,168
v_cmp_lt_f32 vcc, s64, v246
v_add_f32 v242, v242, v196
MA 76,208,172
v_add_f32 v242, v242, v197
v_add_f32 v242, v242, v198
MA 80,212,160
RT 208,250,4608
v_add_f32 v242, v242, v199
MA 84,212,164
RT 210,250,12800
MA 88,212,168
s_mov_b32 m0, 157568
v_exp_f32 v200, v200
MA 92,212,172
v_exp_f32 v201, v201
MA 128,216,160
RT 212,251,4608
v_add_f32 v243, v243, v200
MA 132,216,164
RT 214,251,12800
v_add_f32 v243, v243, v201
MA 136,216,168
buffer_load_dwordx4 v253, s[44:47], s61 offen offset:128 nt lds
MA 140,216,172
s_waitcnt lgkmcnt(4)
v_exp_f32 v202, v202
MV 128,216,224
s_cbranch_vccnz Lst2_15
Lrt2_15:
v_exp_f32 v176, v176
MV 132,216,228
v_exp_f32 v177, v177
MA 144,220,160
RT 216,250,2560
v_add_f32 v241, v241, v176
MA 148,220,164
RT 218,250,10752
v_add_f32 v241, v241, v177
MA 152,220,168
s_mov_b32 m0, 159488
v_exp_f32 v178, v178
MA 156,220,172
buffer_load_dwordx4 v253, s[44:47], s61 offen offset:256 nt lds
MV 136,220,224
v_exp_f32 v179, v179
MV 140,220,228
s_waitcnt lgkmcnt(4)
v_exp_f32 v180, v180
MA 160,208,160
RT 220,251,2560
v_add_f32 v241, v241, v178
MA 164,208,164
RT 222,251,10752
v_add_f32 v241, v241, v179
MA 168,208,168
v_exp_f32 v181, v181
MA 172,208,172
v_exp_f32 v182, v182
MV 144,208,224
s_mov_b32 m0, 161408
v_exp_f32 v183, v183
MV 148,208,228
s_waitcnt lgkmcnt(4)
CV 192,192,193
CV 193,194,195
MA 176,212,160
RT 208,250,6144
CV 194,176,177
MA 180,212,164
RT 210,250,14336
CV 195,178,179
MA 184,212,168
buffer_load_dwordx4 v253, s[44:47], s61 offen offset:384 nt lds
s_add_u32 s61, s61, 16384
MA 188,212,172
v_add_f32 v242, v242, v180
v_add_f32 v242, v242, v181
MV 152,212,224
v_add_f32 v242, v242, v182
v_add_f32 v242, v242, v183
MV 156,212,228
s_waitcnt lgkmcnt(4)
CV 196,196,197
MA 96,216,160
RT 212,251,6144
CV 197,198,199
MA 100,216,164
RT 214,251,14336
CV 198,180,181
MA 104,216,168
CV 199,182,183
v_add_f32 v243, v243, v202
MA 108,216,172
s_waitcnt lgkmcnt(4)
v_exp_f32 v232, v232
MA 112,220,160
RT 216,250,6656
v_add_f32 v245, v245, v232
MA 116,220,164
RT 218,250,14848
MA 120,220,168
v_exp_f32 v233, v233
MA 124,220,172
s_waitcnt lgkmcnt(4)
v_exp_f32 v234, v234
MA 192,208,160
RT 220,251,6656
v_add_f32 v245, v245, v233
MA 196,208,164
RB 176,249,32768
v_add_f32 v245, v245, v234
MA 200,208,168
RB 180,249,33280
CV 254,232,233
MA 204,208,172
s_waitcnt lgkmcnt(5)
v_exp_f32 v235, v235
MA 208,212,160
RT 222,251,14848
v_add_f32 v245, v245, v235
MA 212,212,164
RB 224,252,8192
CV 255,234,235
MA 216,212,168
RB 208,249,34816
MA 220,212,172
s_waitcnt lgkmcnt(6)
v_exp_f32 v203, v203
MA 224,216,160
RB 212,249,35328
v_add_f32 v243, v243, v203
MA 228,216,164
RB 228,252,9216
CV 200,200,201
MA 232,216,168
ds_write_b64 v252, v[254:255] offset:19456
CV 201,202,203
MA 236,216,172
s_waitcnt lgkmcnt(5)
v_exp_f32 v204, v204
MA 240,220,160
v_exp_f32 v205, v205
MA 244,220,164
v_exp_f32 v206, v206
MA 248,220,168
v_exp_f32 v207, v207
MA 252,220,172
v_exp_f32 v184, v184
s_sub_u32 s62, s62, 1
s_cbranch_scc0 Lloop2
Lpeel2:
s_mov_b32 s48, s24
s_and_b32 s49, s25, 0xffff
s_mov_b32 s50, 3670016
s_and_b32 s77, s26, 15
s_mul_i32 s67, s77, 229376
s_xor_b32 s77, s77, 1
s_mul_i32 s69, s77, 229376
s_mov_b32 s68, 2048
v_bfe_u32 v248, v252, 6, 3
v_add_u32 v248, 16, v248
v_mul_u32_u24 v248, 0x380, v248
v_and_b32 v253, 0x7f, v253
v_add_u32 v253, v253, v248
MV0 160,176,0
RB 216,249,36864
v_add_f32 v244, v244, v204
MV 160,180,4
RB 220,249,37376
v_add_f32 v244, v244, v205
MV0 164,176,32
s_cmp_lg_u32 s65, 0
v_exp_f32 v185, v185
MV 164,180,36
s_cbranch_scc1 Lst2_16
Lrt2_16:
v_exp_f32 v186, v186
MV0 168,176,64
v_exp_f32 v187, v187
MV 168,180,68
v_exp_f32 v188, v188
MV0 172,176,96
v_exp_f32 v189, v189
MV 172,180,100
s_waitcnt lgkmcnt(3)
v_exp_f32 v190, v190
MV0 232,176,224
RB 224,252,10240
v_add_f32 v244, v244, v206
MV 232,180,228
v_exp_f32 v191, v191
MV 160,208,8
RB 176,249,38912
v_add_f32 v244, v244, v207
MV 160,212,12
RB 180,249,39424
v_add_f32 v243, v243, v184
MV 164,208,40
RB 228,252,11264
v_add_f32 v243, v243, v185
MV 164,212,44
MV 168,208,72
v_add_f32 v243, v243, v186
v_add_f32 v243, v243, v187
MV 168,212,76
MV 172,208,104
v_add_f32 v244, v244, v188
v_add_f32 v244, v244, v189
MV 172,212,108
s_waitcnt lgkmcnt(0)
v_add_f32 v244, v244, v190
v_add_f32 v244, v244, v191
MV 232,208,224
RB 224,252,12288
CV 202,184,185
MV 232,212,228
CV 204,204,205
CV 205,206,207
MV 160,216,16
RB 208,249,40960
CV 203,186,187
MV 160,220,20
RB 212,249,41472
CV 206,188,189
MV 164,216,48
RB 228,252,13312
CV 207,190,191
MV 164,220,52
MV 168,216,80
MV 168,220,84
MV 172,216,112
MV 172,220,116
s_waitcnt lgkmcnt(0)
MV 232,216,224
RB 216,249,43008
MV 232,220,228
MV 160,176,24
RB 220,249,43520
MV 160,180,28
RB 224,252,14336
MV 164,176,56
RB 228,252,15360
MV 164,180,60
MV 168,176,88
MV 168,180,92
s_nop 1
v_fma_f32 v160, v160, s63, v236
v_fma_f32 v161, v161, s63, v236
MV 172,176,120
MV 172,180,124
s_waitcnt lgkmcnt(0)
v_fma_f32 v162, v162, s63, v236
v_fma_f32 v163, v163, s63, v236
MV 232,176,224
RB 224,249,45056
v_fma_f32 v164, v164, s63, v237
MV 232,180,228
v_fma_f32 v165, v165, s63, v237
v_fma_f32 v166, v166, s63, v237
MV0 176,208,0
RB 228,249,45568
v_fma_f32 v167, v167, s63, v237
MV 176,212,4
v_fma_f32 v168, v168, s63, v238
v_fma_f32 v169, v169, s63, v238
MV0 180,208,32
v_fma_f32 v170, v170, s63, v238
v_fma_f32 v171, v171, s63, v238
MV 180,212,36
v_fma_f32 v172, v172, s63, v239
MV0 184,208,64
v_fma_f32 v173, v173, s63, v239
v_fma_f32 v174, v174, s63, v239
MV 184,212,68
v_fma_f32 v175, v175, s63, v239
v_fma_f32 v232, v232, s63, v240
MV0 188,208,96
RB 208,249,47104
v_fma_f32 v233, v233, s63, v240
MV 188,212,100
v_fma_f32 v234, v234, s63, v240
v_fma_f32 v235, v235, s63, v240
MV 176,216,8
RB 212,249,47616
v_max3_f32 v246, v160, v161, v162
MV 176,220,12
v_max3_f32 v246, v246, v163, v164
v_max3_f32 v246, v246, v165, v166
MV 180,216,40
v_max3_f32 v246, v246, v167, v168
v_max3_f32 v246, v246, v169, v170
MV 180,220,44
v_max3_f32 v246, v246, v171, v172
MV 184,216,72
v_max3_f32 v246, v246, v173, v174
v_max3_f32 v246, v246, v175, v232
MV 184,220,76
v_max3_f32 v246, v246, v233, v234
v_max_f32 v246, v246, v235
MV 188,216,104
RT 216,250,16384
v_cmp_lt_f32 vcc, s64, v246
MV 188,220,108
s_waitcnt lgkmcnt(3)
MV 176,224,16
RT 218,250,24576
MV 176,228,20
RT 220,251,16384
MV 180,224,48
RT 222,251,24576
MV 180,228,52
s_cbranch_vccnz Lst2_17
Lrt2_17:
v_exp_f32 v160, v160
MV 184,224,80
v_exp_f32 v161, v161
MV 184,228,84
v_exp_f32 v162, v162
MV 188,224,112
RT 224,250,16896
v_add_f32 v241, v241, v160
MV 188,228,116
s_waitcnt lgkmcnt(5)
v_exp_f32 v163, v163
MV 176,208,24
RT 226,250,25088
v_add_f32 v241, v241, v161
MV 176,212,28
RT 228,251,16896
v_add_f32 v241, v241, v162
MV 180,208,56
RT 230,251,25088
v_add_f32 v241, v241, v163
MV 180,212,60
v_exp_f32 v164, v164
MV 184,208,88
v_exp_f32 v165, v165
MV 184,212,92
v_fma_f32 v176, v176, s63, v236
v_fma_f32 v177, v177, s63, v236
MV 188,208,120
s_waitcnt vmcnt(5) lgkmcnt(6)
v_fma_f32 v178, v178, s63, v236
v_fma_f32 v179, v179, s63, v236
MV 188,212,124
s_barrier
v_fma_f32 v182, v182, s63, v237
MA 0,216,192
RT 208,250,18432
v_fma_f32 v180, v180, s63, v237
MA 4,216,196
RT 210,250,26624
v_fma_f32 v181, v181, s63, v237
MA 8,216,200
RT 212,251,18432
v_fma_f32 v183, v183, s63, v237
MA 12,216,204
s_waitcnt lgkmcnt(7)
v_fma_f32 v184, v184, s63, v238
v_fma_f32 v185, v185, s63, v238
MA 16,220,192
RT 214,251,26624
v_fma_f32 v186, v186, s63, v238
MA 20,220,196
RT 216,250,20480
v_fma_f32 v187, v187, s63, v238
MA 24,220,200
RT 218,250,28672
v_fma_f32 v188, v188, s63, v239
MA 28,220,204
s_waitcnt lgkmcnt(8)
v_fma_f32 v189, v189, s63, v239
v_fma_f32 v190, v190, s63, v239
MA 32,224,192
ds_read_b32 v247, v252 offset:29704
v_fma_f32 v191, v191, s63, v239
MA 36,224,196
v_max3_f32 v246, v176, v177, v178
v_max3_f32 v246, v246, v179, v182
MA 40,224,200
RT 220,251,20480
v_max3_f32 v246, v246, v180, v181
MA 44,224,204
s_waitcnt lgkmcnt(8)
v_max3_f32 v246, v246, v183, v184
v_max3_f32 v246, v246, v185, v186
MA 48,228,192
RT 222,251,28672
v_max3_f32 v246, v246, v187, v188
MA 52,228,196
RB 224,252,18432
v_max3_f32 v246, v246, v189, v190
MA 56,228,200
s_mov_b32 m0, 2048
v_exp_f32 v166, v166
MA 60,228,204
s_waitcnt lgkmcnt(3)
v_exp_f32 v167, v167
MA 64,208,192
RB 228,252,19456
v_max3_f32 v246, v246, v191, v247
MA 68,208,196
buffer_load_dwordx4 v253, s[48:51], s67 offen offset:0 lds
MA 72,208,200
v_cmp_lt_f32 vcc, s64, v246
v_add_f32 v242, v242, v164
MA 76,208,204
v_add_f32 v242, v242, v165
v_add_f32 v242, v242, v166
MA 80,212,192
RT 208,250,20992
v_add_f32 v242, v242, v167
MA 84,212,196
RT 210,250,29184
MA 88,212,200
s_mov_b32 m0, 6016
v_exp_f32 v168, v168
MA 92,212,204
v_exp_f32 v169, v169
MA 128,216,192
RT 212,251,20992
v_add_f32 v243, v243, v168
MA 132,216,196
RT 214,251,29184
v_add_f32 v243, v243, v169
MA 136,216,200
buffer_load_dwordx4 v253, s[48:51], s67 offen offset:128 lds
MA 140,216,204
s_waitcnt lgkmcnt(4)
v_exp_f32 v170, v170
MV 128,216,224
s_cbranch_vccnz Lst2_18
Lrt2_18:
v_exp_f32 v176, v176
MV 132,216,228
v_exp_f32 v177, v177
MA 144,220,192
RT 216,250,18944
v_add_f32 v241, v241, v176
MA 148,220,196
RT 218,250,27136
v_add_f32 v241, v241, v177
MA 152,220,200
s_mov_b32 m0, 9984
v_exp_f32 v178, v178
MA 156,220,204
buffer_load_dwordx4 v253, s[48:51], s67 offen offset:256 lds
MV 136,220,224
v_exp_f32 v179, v179
MV 140,220,228
s_waitcnt lgkmcnt(4)
v_exp_f32 v180, v180
MA 160,208,192
RT 220,251,18944
v_add_f32 v241, v241, v178
MA 164,208,196
RT 222,251,27136
v_add_f32 v241, v241, v179
MA 168,208,200
v_exp_f32 v181, v181
MA 172,208,204
v_exp_f32 v182, v182
MV 144,208,224
s_mov_b32 m0, 13952
v_exp_f32 v183, v183
MV 148,208,228
s_waitcnt lgkmcnt(4)
CV 160,160,161
CV 161,162,163
MA 176,212,192
RT 208,250,22528
CV 162,176,177
MA 180,212,196
RT 210,250,30720
CV 163,178,179
MA 184,212,200
buffer_load_dwordx4 v253, s[48:51], s67 offen offset:384 lds
MA 188,212,204
v_add_f32 v242, v242, v180
v_add_f32 v242, v242, v181
MV 152,212,224
v_add_f32 v242, v242, v182
v_add_f32 v242, v242, v183
MV 156,212,228
s_waitcnt lgkmcnt(4)
CV 164,164,165
MA 96,216,192
RT 212,251,22528
CV 165,166,167
MA 100,216,196
RT 214,251,30720
CV 166,180,181
MA 104,216,200
CV 167,182,183
v_add_f32 v243, v243, v170
MA 108,216,204
s_waitcnt lgkmcnt(4)
v_exp_f32 v232, v232
MA 112,220,192
RT 216,250,23040
v_add_f32 v245, v245, v232
MA 116,220,196
RT 218,250,31232
MA 120,220,200
v_exp_f32 v233, v233
MA 124,220,204
s_waitcnt lgkmcnt(4)
v_exp_f32 v234, v234
MA 192,208,192
RT 220,251,23040
v_add_f32 v245, v245, v233
MA 196,208,196
RB 176,249,49152
v_add_f32 v245, v245, v234
MA 200,208,200
RB 180,249,49664
CV 254,232,233
MA 204,208,204
s_waitcnt lgkmcnt(5)
v_exp_f32 v235, v235
MA 208,212,192
RT 222,251,31232
v_add_f32 v245, v245, v235
MA 212,212,196
RB 224,252,8192
CV 255,234,235
MA 216,212,200
RB 208,249,51200
MA 220,212,204
s_waitcnt lgkmcnt(6)
v_exp_f32 v171, v171
MA 224,216,192
RB 212,249,51712
v_add_f32 v243, v243, v171
MA 228,216,196
RB 228,252,9216
CV 168,168,169
MA 232,216,200
ds_write_b64 v252, v[254:255] offset:17408
CV 169,170,171
MA 236,216,204
s_waitcnt lgkmcnt(5)
v_exp_f32 v172, v172
MA 240,220,192
v_exp_f32 v173, v173
MA 244,220,196
v_exp_f32 v174, v174
MA 248,220,200
v_exp_f32 v175, v175
MA 252,220,204
v_exp_f32 v184, v184
MV0 192,176,0
RB 216,249,53248
v_add_f32 v244, v244, v172
MV 192,180,4
RB 220,249,53760
v_add_f32 v244, v244, v173
MV0 196,176,32
s_cmp_lg_u32 s65, 0
v_exp_f32 v185, v185
MV 196,180,36
s_cbranch_scc1 Lst2_19
Lrt2_19:
v_exp_f32 v186, v186
MV0 200,176,64
v_exp_f32 v187, v187
MV 200,180,68
v_exp_f32 v188, v188
MV0 204,176,96
s_mov_b32 m0, 17920
v_exp_f32 v189, v189
MV 204,180,100
s_waitcnt lgkmcnt(3)
v_exp_f32 v190, v190
MV0 232,176,224
RB 224,252,10240
v_add_f32 v244, v244, v174
MV 232,180,228
v_exp_f32 v191, v191
MV 192,208,8
RB 176,249,55296
v_add_f32 v244, v244, v175
MV 192,212,12
RB 180,249,55808
v_add_f32 v243, v243, v184
MV 196,208,40
RB 228,252,11264
v_add_f32 v243, v243, v185
MV 196,212,44
buffer_load_dwordx4 v253, s[48:51], s67 offen offset:512 lds
MV 200,208,72
s_mov_b32 m0, 21888
v_add_f32 v243, v243, v186
v_add_f32 v243, v243, v187
MV 200,212,76
buffer_load_dwordx4 v253, s[48:51], s67 offen offset:640 lds
MV 204,208,104
v_add_f32 v244, v244, v188
v_add_f32 v244, v244, v189
MV 204,212,108
s_waitcnt lgkmcnt(0)
v_add_f32 v244, v244, v190
v_add_f32 v244, v244, v191
MV 232,208,224
RB 224,252,12288
CV 170,184,185
MV 232,212,228
CV 172,172,173
CV 173,174,175
MV 192,216,16
RB 208,249,57344
CV 171,186,187
MV 192,220,20
RB 212,249,57856
CV 174,188,189
MV 196,216,48
RB 228,252,13312
CV 175,190,191
MV 196,220,52
s_mov_b32 m0, 25856
MV 200,216,80
buffer_load_dwordx4 v253, s[48:51], s67 offen offset:768 lds
s_add_u32 s67, s67, 28672
MV 200,220,84
MV 204,216,112
MV 204,220,116
s_waitcnt lgkmcnt(0)
MV 232,216,224
RB 216,249,59392
MV 232,220,228
MV 192,176,24
RB 220,249,59904
MV 192,180,28
RB 224,252,14336
MV 196,176,56
RB 228,252,15360
MV 196,180,60
s_mov_b32 m0, 30720
MV 200,176,88
buffer_load_dwordx4 v253, s[48:51], s67 offen offset:0 lds
MV 200,180,92
v_fma_f32 v192, v192, s63, v236
v_fma_f32 v193, v193, s63, v236
MV 204,176,120
MV 204,180,124
s_waitcnt lgkmcnt(0)
v_fma_f32 v194, v194, s63, v236
v_fma_f32 v195, v195, s63, v236
MV 232,176,224
RB 224,249,61440
v_fma_f32 v196, v196, s63, v237
MV 232,180,228
v_fma_f32 v197, v197, s63, v237
v_fma_f32 v198, v198, s63, v237
MV0 176,208,0
RB 228,249,61952
v_fma_f32 v199, v199, s63, v237
MV 176,212,4
v_fma_f32 v200, v200, s63, v238
v_fma_f32 v201, v201, s63, v238
MV0 180,208,32
v_fma_f32 v202, v202, s63, v238
v_fma_f32 v203, v203, s63, v238
MV 180,212,36
v_fma_f32 v204, v204, s63, v239
MV0 184,208,64
v_fma_f32 v205, v205, s63, v239
v_fma_f32 v206, v206, s63, v239
MV 184,212,68
v_fma_f32 v207, v207, s63, v239
v_fma_f32 v232, v232, s63, v240
MV0 188,208,96
RB 208,249,63488
v_fma_f32 v233, v233, s63, v240
MV 188,212,100
v_fma_f32 v234, v234, s63, v240
v_fma_f32 v235, v235, s63, v240
MV 176,216,8
RB 212,249,64000
v_max3_f32 v246, v192, v193, v194
MV 176,220,12
v_max3_f32 v246, v246, v195, v196
v_max3_f32 v246, v246, v197, v198
MV 180,216,40
v_max3_f32 v246, v246, v199, v200
v_max3_f32 v246, v246, v201, v202
MV 180,220,44
v_max3_f32 v246, v246, v203, v204
MV 184,216,72
v_max3_f32 v246, v246, v205, v206
v_max3_f32 v246, v246, v207, v232
MV 184,220,76
v_max3_f32 v246, v246, v233, v234
v_max_f32 v246, v246, v235
MV 188,216,104
RT 216,250,32768
v_cmp_lt_f32 vcc, s64, v246
MV 188,220,108
s_waitcnt lgkmcnt(3)
MV 176,224,16
RT 218,250,40960
MV 176,228,20
RT 220,251,32768
MV 180,224,48
RT 222,251,40960
MV 180,228,52
s_cbranch_vccnz Lst2_20
Lrt2_20:
v_exp_f32 v192, v192
MV 184,224,80
v_exp_f32 v193, v193
MV 184,228,84
v_exp_f32 v194, v194
MV 188,224,112
RT 224,250,33280
v_add_f32 v241, v241, v192
MV 188,228,116
s_waitcnt lgkmcnt(5)
v_exp_f32 v195, v195
MV 176,208,24
RT 226,250,41472
v_add_f32 v241, v241, v193
MV 176,212,28
RT 228,251,33280
v_add_f32 v241, v241, v194
MV 180,208,56
RT 230,251,41472
v_add_f32 v241, v241, v195
MV 180,212,60
v_exp_f32 v196, v196
MV 184,208,88
v_exp_f32 v197, v197
MV 184,212,92
v_fma_f32 v176, v176, s63, v236
v_fma_f32 v177, v177, s63, v236
MV 188,208,120
s_waitcnt vmcnt(8) lgkmcnt(6)
v_fma_f32 v178, v178, s63, v236
v_fma_f32 v179, v179, s63, v236
MV 188,212,124
s_barrier
v_fma_f32 v182, v182, s63, v237
MA 0,216,160
RT 208,250,34816
v_fma_f32 v180, v180, s63, v237
MA 4,216,164
RT 210,250,43008
v_fma_f32 v181, v181, s63, v237
MA 8,216,168
RT 212,251,34816
v_fma_f32 v183, v183, s63, v237
MA 12,216,172
s_waitcnt lgkmcnt(7)
v_fma_f32 v184, v184, s63, v238
v_fma_f32 v185, v185, s63, v238
MA 16,220,160
RT 214,251,43008
v_fma_f32 v186, v186, s63, v238
MA 20,220,164
RT 216,250,36864
v_fma_f32 v187, v187, s63, v238
MA 24,220,168
RT 218,250,45056
v_fma_f32 v188, v188, s63, v239
MA 28,220,172
s_waitcnt lgkmcnt(8)
v_fma_f32 v189, v189, s63, v239
v_fma_f32 v190, v190, s63, v239
MA 32,224,160
ds_read_b32 v247, v252 offset:29704
v_fma_f32 v191, v191, s63, v239
MA 36,224,164
v_max3_f32 v246, v176, v177, v178
v_max3_f32 v246, v246, v179, v182
MA 40,224,168
RT 220,251,36864
v_max3_f32 v246, v246, v180, v181
MA 44,224,172
s_waitcnt lgkmcnt(8)
v_max3_f32 v246, v246, v183, v184
v_max3_f32 v246, v246, v185, v186
MA 48,228,160
RT 222,251,45056
v_max3_f32 v246, v246, v187, v188
MA 52,228,164
RB 224,252,16384
v_max3_f32 v246, v246, v189, v190
MA 56,228,168
s_mov_b32 m0, 34688
v_exp_f32 v198, v198
MA 60,228,172
s_waitcnt lgkmcnt(3)
v_exp_f32 v199, v199
MA 64,208,160
RB 228,252,17408
v_max3_f32 v246, v246, v191, v247
MA 68,208,164
buffer_load_dwordx4 v253, s[48:51], s67 offen offset:128 lds
MA 72,208,168
v_cmp_lt_f32 vcc, s64, v246
v_add_f32 v242, v242, v196
MA 76,208,172
v_add_f32 v242, v242, v197
v_add_f32 v242, v242, v198
MA 80,212,160
RT 208,250,37376
v_add_f32 v242, v242, v199
MA 84,212,164
RT 210,250,45568
MA 88,212,168
s_mov_b32 m0, 38656
v_exp_f32 v200, v200
MA 92,212,172
v_exp_f32 v201, v201
MA 128,216,160
RT 212,251,37376
v_add_f32 v243, v243, v200
MA 132,216,164
RT 214,251,45568
v_add_f32 v243, v243, v201
MA 136,216,168
buffer_load_dwordx4 v253, s[48:51], s67 offen offset:256 lds
MA 140,216,172
s_waitcnt lgkmcnt(4)
v_exp_f32 v202, v202
MV 128,216,224
s_cbranch_vccnz Lst2_21
Lrt2_21:
v_exp_f32 v176, v176
MV 132,216,228
v_exp_f32 v177, v177
MA 144,220,160
RT 216,250,35328
v_add_f32 v241, v241, v176
MA 148,220,164
RT 218,250,43520
v_add_f32 v241, v241, v177
MA 152,220,168
s_mov_b32 m0, 42624
v_exp_f32 v178, v178
MA 156,220,172
buffer_load_dwordx4 v253, s[48:51], s67 offen offset:384 lds
MV 136,220,224
v_exp_f32 v179, v179
MV 140,220,228
s_waitcnt lgkmcnt(4)
v_exp_f32 v180, v180
MA 160,208,160
RT 220,251,35328
v_add_f32 v241, v241, v178
MA 164,208,164
RT 222,251,43520
v_add_f32 v241, v241, v179
MA 168,208,168
v_exp_f32 v181, v181
MA 172,208,172
v_exp_f32 v182, v182
MV 144,208,224
s_mov_b32 m0, 46592
v_exp_f32 v183, v183
MV 148,208,228
s_waitcnt lgkmcnt(4)
CV 192,192,193
CV 193,194,195
MA 176,212,160
RT 208,250,38912
CV 194,176,177
MA 180,212,164
RT 210,250,47104
CV 195,178,179
MA 184,212,168
buffer_load_dwordx4 v253, s[48:51], s67 offen offset:512 lds
MA 188,212,172
v_add_f32 v242, v242, v180
v_add_f32 v242, v242, v181
MV 152,212,224
v_add_f32 v242, v242, v182
v_add_f32 v242, v242, v183
MV 156,212,228
s_waitcnt lgkmcnt(4)
CV 196,196,197
MA 96,216,160
RT 212,251,38912
CV 197,198,199
MA 100,216,164
RT 214,251,47104
CV 198,180,181
MA 104,216,168
CV 199,182,183
v_add_f32 v243, v243, v202
MA 108,216,172
s_waitcnt lgkmcnt(4)
v_exp_f32 v232, v232
MA 112,220,160
RT 216,250,39424
v_add_f32 v245, v245, v232
MA 116,220,164
RT 218,250,47616
MA 120,220,168
v_exp_f32 v233, v233
MA 124,220,172
s_waitcnt lgkmcnt(4)
v_exp_f32 v234, v234
MA 192,208,160
RT 220,251,39424
v_add_f32 v245, v245, v233
MA 196,208,164
RB 176,249,0
v_add_f32 v245, v245, v234
MA 200,208,168
RB 180,249,512
CV 254,232,233
MA 204,208,172
s_waitcnt lgkmcnt(5)
v_exp_f32 v235, v235
MA 208,212,160
RT 222,251,47616
v_add_f32 v245, v245, v235
MA 212,212,164
RB 224,252,8192
CV 255,234,235
MA 216,212,168
RB 208,249,2048
MA 220,212,172
s_waitcnt lgkmcnt(6)
v_exp_f32 v203, v203
MA 224,216,160
RB 212,249,2560
v_add_f32 v243, v243, v203
MA 228,216,164
RB 228,252,9216
CV 200,200,201
MA 232,216,168
ds_write_b64 v252, v[254:255] offset:19456
CV 201,202,203
MA 236,216,172
s_waitcnt lgkmcnt(5)
v_exp_f32 v204, v204
MA 240,220,160
v_exp_f32 v205, v205
MA 244,220,164
v_exp_f32 v206, v206
MA 248,220,168
v_exp_f32 v207, v207
MA 252,220,172
s_waitcnt lgkmcnt(1)
v_exp_f32 v184, v184
s_mov_b32 m0, 50560
s_nop 0
buffer_load_dwordx4 v253, s[48:51], s67 offen offset:640 lds
s_mov_b32 m0, 54528
s_nop 0
buffer_load_dwordx4 v253, s[48:51], s67 offen offset:768 lds
s_add_u32 s67, s67, 28672
v_add_f32 v244, v244, v204
v_add_f32 v244, v244, v205
s_cmp_lg_u32 s65, 0
v_exp_f32 v185, v185
s_cbranch_scc1 Lst2_22
Lrt2_22:
v_exp_f32 v186, v186
v_exp_f32 v187, v187
v_exp_f32 v188, v188
v_exp_f32 v189, v189
v_exp_f32 v190, v190
v_add_f32 v244, v244, v206
v_exp_f32 v191, v191
v_add_f32 v244, v244, v207
v_add_f32 v243, v243, v184
v_add_f32 v243, v243, v185
v_add_f32 v243, v243, v186
v_add_f32 v243, v243, v187
v_add_f32 v244, v244, v188
v_add_f32 v244, v244, v189
v_add_f32 v244, v244, v190
v_add_f32 v244, v244, v191
CV 202,184,185
CV 204,204,205
CV 205,206,207
CV 203,186,187
CV 206,188,189
CV 207,190,191
RT 216,250,49152
RT 218,250,57344
RT 220,251,49152
RT 222,251,57344
RT 224,250,49664
RT 226,250,57856
RT 228,251,49664
RT 230,251,57856
v_mov_b32 v176, v241
v_mov_b32 v177, v242
v_mov_b32 v178, v243
v_mov_b32 v179, v244
v_mov_b32 v180, v245
v_permlane16_swap_b32 v241, v176
v_permlane16_swap_b32 v242, v177
v_permlane16_swap_b32 v243, v178
v_permlane16_swap_b32 v244, v179
v_permlane16_swap_b32 v245, v180
v_add_f32 v241, v241, v176
v_add_f32 v242, v242, v177
v_add_f32 v243, v243, v178
v_add_f32 v244, v244, v179
v_add_f32 v245, v245, v180
v_mov_b32 v176, v241
v_mov_b32 v177, v242
v_mov_b32 v178, v243
v_mov_b32 v179, v244
v_mov_b32 v180, v245
v_permlane32_swap_b32 v241, v176
v_permlane32_swap_b32 v242, v177
v_permlane32_swap_b32 v243, v178
v_permlane32_swap_b32 v244, v179
v_permlane32_swap_b32 v245, v180
v_add_f32 v241, v241, v176
v_add_f32 v242, v242, v177
v_add_f32 v243, v243, v178
v_add_f32 v244, v244, v179
v_add_f32 v245, v245, v180
ds_write_b32 v252, v245 offset:30728
v_rcp_f32 v241, v241
v_rcp_f32 v242, v242
v_rcp_f32 v243, v243
v_rcp_f32 v244, v244
s_waitcnt lgkmcnt(0)
s_barrier
RB 236,252,30720
MA 0,216,192
RT 208,250,51200
MA 4,216,196
RT 210,250,59392
MA 8,216,200
RT 212,251,51200
MA 12,216,204
MA 16,220,192
RT 214,251,59392
MA 20,220,196
RT 216,250,53248
MA 24,220,200
RT 218,250,61440
MA 28,220,204
MA 32,224,192
MA 36,224,196
MA 40,224,200
RT 220,251,53248
MA 44,224,204
MA 48,228,192
RT 222,251,61440
MA 52,228,196
RB 224,252,18432
MA 56,228,200
s_waitcnt lgkmcnt(9)
v_add_f32 v236, v236, v237
MA 60,228,204
s_waitcnt lgkmcnt(7)
v_add_f32 v237, v238, v239
MA 64,208,192
RB 228,252,19456
MA 68,208,196
v_rcp_f32 v236, v236
MA 72,208,200
v_rcp_f32 v237, v237
MA 76,208,204
s_waitcnt lgkmcnt(6)
MA 80,212,192
RT 208,250,53760
MA 84,212,196
RT 210,250,61952
MA 88,212,200
MA 92,212,204
s_waitcnt lgkmcnt(6)
MA 128,216,192
RT 212,251,53760
MA 132,216,196
RT 214,251,61952
MA 136,216,200
MA 140,216,204
s_waitcnt lgkmcnt(5)
MV 128,216,224
s_waitcnt lgkmcnt(4)
MV 132,216,228
MA 144,220,192
RT 216,250,51712
MA 148,220,196
RT 218,250,59904
MA 152,220,200
MA 156,220,204
MV 136,220,224
MV 140,220,228
s_waitcnt lgkmcnt(4)
MA 160,208,192
RT 220,251,51712
MA 164,208,196
RT 222,251,59904
MA 168,208,200
MA 172,208,204
MV 144,208,224
MV 148,208,228
s_waitcnt lgkmcnt(4)
MA 176,212,192
RT 208,250,55296
MA 180,212,196
RT 210,250,63488
MA 184,212,200
MA 188,212,204
MV 152,212,224
MV 156,212,228
s_waitcnt lgkmcnt(4)
MA 96,216,192
RT 212,251,55296
MA 100,216,196
RT 214,251,63488
MA 104,216,200
MA 108,216,204
s_waitcnt lgkmcnt(4)
MA 112,220,192
RT 216,250,55808
MA 116,220,196
RT 218,250,64000
MA 120,220,200
MA 124,220,204
s_waitcnt lgkmcnt(4)
MA 192,208,192
RT 220,251,55808
MA 196,208,196
MA 200,208,200
MA 204,208,204
s_waitcnt lgkmcnt(3)
MA 208,212,192
RT 222,251,64000
MA 212,212,196
MA 216,212,200
MA 220,212,204
s_waitcnt lgkmcnt(2)
MA 224,216,192
MA 228,216,196
MA 232,216,200
MA 236,216,204
s_waitcnt lgkmcnt(0)
MA 240,220,192
MA 244,220,196
MA 248,220,200
MA 252,220,204
s_waitcnt vmcnt(0)
s_waitcnt lgkmcnt(0)
s_barrier
s_getpc_b64 s[80:81]
Ljt2:
s_add_u32 s80, s80, LT-Ljt2
s_addc_u32 s81, s81, 0
s_setpc_b64 s[80:81]
Lst2_0:
s_call_b64 s[80:81], LR2f_2
s_branch Lrt2_0
Lst2_1:
s_call_b64 s[80:81], LH_2
s_branch Lrt2_1
Lst2_2:
s_call_b64 s[80:81], LR1_2_1
s_branch Lrt2_2
Lst2_3:
s_call_b64 s[80:81], LR2_2_1
s_branch Lrt2_3
Lst2_4:
s_call_b64 s[80:81], LH_2
s_branch Lrt2_4
Lst2_5:
s_call_b64 s[80:81], LR1_2_0
s_branch Lrt2_5
Lst2_6:
s_call_b64 s[80:81], LR2_2_0
s_branch Lrt2_6
Lst2_7:
s_call_b64 s[80:81], LH_2
s_branch Lrt2_7
Lst2_8:
s_call_b64 s[80:81], LR1_2_1
s_branch Lrt2_8
Lst2_9:
s_call_b64 s[80:81], LR2_2_1
s_branch Lrt2_9
Lst2_10:
s_call_b64 s[80:81], LH_2
s_branch Lrt2_10
Lst2_11:
s_call_b64 s[80:81], LR1_2_0
s_branch Lrt2_11
Lst2_12:
s_call_b64 s[80:81], LR2_2_0
s_branch Lrt2_12
Lst2_13:
s_call_b64 s[80:81], LH_2
s_branch Lrt2_13
Lst2_14:
s_call_b64 s[80:81], LR1_2_1
s_branch Lrt2_14
Lst2_15:
s_call_b64 s[80:81], LR2_2_1
s_branch Lrt2_15
Lst2_16:
s_call_b64 s[80:81], LH_2
s_branch Lrt2_16
Lst2_17:
s_call_b64 s[80:81], LR1_2_0
s_branch Lrt2_17
Lst2_18:
s_call_b64 s[80:81], LR2_2_0
s_branch Lrt2_18
Lst2_19:
s_call_b64 s[80:81], LH_2
s_branch Lrt2_19
Lst2_20:
s_call_b64 s[80:81], LR1_2_1
s_branch Lrt2_20
Lst2_21:
s_call_b64 s[80:81], LR2_2_1
s_branch Lrt2_21
Lst2_22:
s_call_b64 s[80:81], LH_2
s_branch Lrt2_22
LR1_2_0:
s_nop 8
s_waitcnt lgkmcnt(0)
s_bitcmp1_b32 s65, 0
s_cbranch_scc1 Lsv2_10
ds_write_b128 v252, v[236:239] offset:24576
s_or_b32 s65, s65, 1
s_waitcnt lgkmcnt(0)
Lsv2_10:
v_max3_f32 v246, v160, v161, v162
v_max_f32 v246, v246, v163
v_mov_b32 v248, v246
s_nop 1
v_permlane16_swap_b32 v246, v248
v_max_f32 v246, v246, v248
v_mov_b32 v248, v246
s_nop 1
v_permlane32_swap_b32 v246, v248
v_max_f32 v246, v246, v248
v_max_f32 v246, 0, v246
v_sub_f32 v236, v236, v246
v_sub_f32 v160, v160, v246
v_sub_f32 v161, v161, v246
v_sub_f32 v162, v162, v246
v_sub_f32 v163, v163, v246
v_exp_f32 v248, -v246
s_nop 0
v_mul_f32 v241, v241, v248
v_max3_f32 v246, v164, v165, v166
v_max_f32 v246, v246, v167
v_mov_b32 v248, v246
s_nop 1
v_permlane16_swap_b32 v246, v248
v_max_f32 v246, v246, v248
v_mov_b32 v248, v246
s_nop 1
v_permlane32_swap_b32 v246, v248
v_max_f32 v246, v246, v248
v_max_f32 v246, 0, v246
v_sub_f32 v237, v237, v246
v_sub_f32 v164, v164, v246
v_sub_f32 v165, v165, v246
v_sub_f32 v166, v166, v246
v_sub_f32 v167, v167, v246
v_exp_f32 v248, -v246
s_nop 0
v_mul_f32 v242, v242, v248
v_max3_f32 v246, v168, v169, v170
v_max_f32 v246, v246, v171
v_mov_b32 v248, v246
s_nop 1
v_permlane16_swap_b32 v246, v248
v_max_f32 v246, v246, v248
v_mov_b32 v248, v246
s_nop 1
v_permlane32_swap_b32 v246, v248
v_max_f32 v246, v246, v248
v_max_f32 v246, 0, v246
v_sub_f32 v238, v238, v246
v_sub_f32 v168, v168, v246
v_sub_f32 v169, v169, v246
v_sub_f32 v170, v170, v246
v_sub_f32 v171, v171, v246
v_exp_f32 v248, -v246
s_nop 0
v_mul_f32 v243, v243, v248
v_max3_f32 v246, v172, v173, v174
v_max_f32 v246, v246, v175
v_mov_b32 v248, v246
s_nop 1
v_permlane16_swap_b32 v246, v248
v_max_f32 v246, v246, v248
v_mov_b32 v248, v246
s_nop 1
v_permlane32_swap_b32 v246, v248
v_max_f32 v246, v246, v248
v_max_f32 v246, 0, v246
v_sub_f32 v239, v239, v246
v_sub_f32 v172, v172, v246
v_sub_f32 v173, v173, v246
v_sub_f32 v174, v174, v246
v_sub_f32 v175, v175, v246
v_exp_f32 v248, -v246
s_nop 0
v_mul_f32 v244, v244, v248
v_max3_f32 v246, v232, v233, v234
v_max_f32 v246, v246, v235
v_mov_b32 v248, v246
s_nop 1
v_permlane16_swap_b32 v246, v248
v_max_f32 v246, v246, v248
v_mov_b32 v248, v246
s_nop 1
v_permlane32_swap_b32 v246, v248
v_max_f32 v246, v246, v248
v_max_f32 v246, 0, v246
v_sub_f32 v246, v240, v246
ds_write_b32 v252, v246 offset:28680
v_mov_b32 v248, 0x7149f2ca
ds_write_b32 v252, v248 offset:29704
s_waitcnt lgkmcnt(0)
s_nop 2
s_setpc_b64 s[80:81]
LR2_2_0:
s_nop 8
s_waitcnt lgkmcnt(0)
v_readfirstlane_b32 s77, v247
s_bitcmp1_b32 s65, 0
s_cbranch_scc1 Lsv2_200
ds_write_b128 v252, v[236:239] offset:24576
s_or_b32 s65, s65, 1
s_waitcnt lgkmcnt(0)
Lsv2_200:
v_max3_f32 v246, v176, v177, v178
v_max_f32 v246, v246, v179
v_mov_b32 v248, v246
s_nop 1
v_permlane16_swap_b32 v246, v248
v_max_f32 v246, v246, v248
v_mov_b32 v248, v246
s_nop 1
v_permlane32_swap_b32 v246, v248
v_max_f32 v246, v246, v248
v_max_f32 v246, 0, v246
v_sub_f32 v236, v236, v246
v_sub_f32 v176, v176, v246
v_sub_f32 v177, v177, v246
v_sub_f32 v178, v178, v246
v_sub_f32 v179, v179, v246
v_exp_f32 v248, -v246
s_nop 0
v_mul_f32 v241, v241, v248
v_mul_f32 v160, v160, v248
v_mul_f32 v161, v161, v248
v_mul_f32 v162, v162, v248
v_mul_f32 v163, v163, v248
v_max3_f32 v246, v180, v181, v182
v_max_f32 v246, v246, v183
v_mov_b32 v248, v246
s_nop 1
v_permlane16_swap_b32 v246, v248
v_max_f32 v246, v246, v248
v_mov_b32 v248, v246
s_nop 1
v_permlane32_swap_b32 v246, v248
v_max_f32 v246, v246, v248
v_max_f32 v246, 0, v246
v_sub_f32 v237, v237, v246
v_sub_f32 v180, v180, v246
v_sub_f32 v181, v181, v246
v_sub_f32 v182, v182, v246
v_sub_f32 v183, v183, v246
v_exp_f32 v248, -v246
s_nop 0
v_mul_f32 v242, v242, v248
v_mul_f32 v164, v164, v248
v_mul_f32 v165, v165, v248
v_mul_f32 v166, v166, v248
v_mul_f32 v167, v167, v248
v_max3_f32 v246, v184, v185, v186
v_max_f32 v246, v246, v187
v_mov_b32 v248, v246
s_nop 1
v_permlane16_swap_b32 v246, v248
v_max_f32 v246, v246, v248
v_mov_b32 v248, v246
s_nop 1
v_permlane32_swap_b32 v246, v248
v_max_f32 v246, v246, v248
v_max_f32 v246, 0, v246
v_sub_f32 v238, v238, v246
v_sub_f32 v184, v184, v246
v_sub_f32 v185, v185, v246
v_sub_f32 v186, v186, v246
v_sub_f32 v187, v187, v246
v_exp_f32 v248, -v246
s_nop 0
v_mul_f32 v243, v243, v248
v_mul_f32 v168, v168, v248
v_mul_f32 v169, v169, v248
v_mul_f32 v170, v170, v248
v_sub_f32 v171, v171, v246
v_max3_f32 v246, v188, v189, v190
v_max_f32 v246, v246, v191
v_mov_b32 v248, v246
s_nop 1
v_permlane16_swap_b32 v246, v248
v_max_f32 v246, v246, v248
v_mov_b32 v248, v246
s_nop 1
v_permlane32_swap_b32 v246, v248
v_max_f32 v246, v246, v248
v_max_f32 v246, 0, v246
v_sub_f32 v239, v239, v246
v_sub_f32 v188, v188, v246
v_sub_f32 v189, v189, v246
v_sub_f32 v190, v190, v246
v_sub_f32 v191, v191, v246
v_exp_f32 v248, -v246
s_nop 0
v_mul_f32 v244, v244, v248
v_sub_f32 v172, v172, v246
v_sub_f32 v173, v173, v246
v_sub_f32 v174, v174, v246
v_sub_f32 v175, v175, v246
s_cmp_eq_u32 s77, 0
s_cbranch_scc1 LR2s_2_0
ds_read_b32 v246, v252 offset:29696
ds_read_b32 v247, v252 offset:28672
ds_read_b32 v248, v252 offset:28676
s_waitcnt lgkmcnt(0)
v_min3_f32 v254, v246, v247, v248
v_sub_f32 v246, v254, v246
v_exp_f32 v246, v246
s_nop 0
ds_write_b32 v252, v246 offset:25600
s_waitcnt lgkmcnt(0)
ds_read_b32 v246, v252 offset:29700
ds_read_b32 v247, v252 offset:28680
ds_read_b32 v248, v252 offset:28684
s_waitcnt lgkmcnt(0)
v_min3_f32 v255, v246, v247, v248
v_sub_f32 v246, v255, v246
v_exp_f32 v246, v246
s_nop 0
ds_write_b32 v252, v246 offset:25604
s_waitcnt lgkmcnt(0)
v_sub_f32 v246, v240, v255
v_mov_b32 v240, v255
v_sub_f32 v232, v232, v246
v_sub_f32 v233, v233, v246
v_sub_f32 v234, v234, v246
v_sub_f32 v235, v235, v246
v_exp_f32 v248, -v246
s_nop 0
v_mul_f32 v245, v245, v248
s_or_b32 s65, s65, 2
s_waitcnt lgkmcnt(0)
s_barrier
s_barrier
LR2s_2_0:
s_waitcnt lgkmcnt(0)
s_nop 2
s_setpc_b64 s[80:81]
LR2f_2:
s_nop 8
s_waitcnt lgkmcnt(0)
v_readfirstlane_b32 s77, v247
s_bitcmp1_b32 s65, 0
s_cbranch_scc1 Lsv2_201
ds_write_b128 v252, v[236:239] offset:24576
s_or_b32 s65, s65, 1
s_waitcnt lgkmcnt(0)
Lsv2_201:
v_max3_f32 v246, v176, v177, v178
v_max_f32 v246, v246, v179
v_mov_b32 v248, v246
s_nop 1
v_permlane16_swap_b32 v246, v248
v_max_f32 v246, v246, v248
v_mov_b32 v248, v246
s_nop 1
v_permlane32_swap_b32 v246, v248
v_max_f32 v246, v246, v248
v_max_f32 v246, 0, v246
v_sub_f32 v236, v236, v246
v_sub_f32 v176, v176, v246
v_sub_f32 v177, v177, v246
v_sub_f32 v178, v178, v246
v_sub_f32 v179, v179, v246
v_exp_f32 v248, -v246
s_nop 0
v_mul_f32 v241, v241, v248
v_mul_f32 v160, v160, v248
v_mul_f32 v161, v161, v248
v_mul_f32 v162, v162, v248
v_mul_f32 v163, v163, v248
v_max3_f32 v246, v180, v181, v182
v_max_f32 v246, v246, v183
v_mov_b32 v248, v246
s_nop 1
v_permlane16_swap_b32 v246, v248
v_max_f32 v246, v246, v248
v_mov_b32 v248, v246
s_nop 1
v_permlane32_swap_b32 v246, v248
v_max_f32 v246, v246, v248
v_max_f32 v246, 0, v246
v_sub_f32 v237, v237, v246
v_sub_f32 v180, v180, v246
v_sub_f32 v181, v181, v246
v_sub_f32 v182, v182, v246
v_sub_f32 v183, v183, v246
v_exp_f32 v248, -v246
s_nop 0
v_mul_f32 v242, v242, v248
v_mul_f32 v164, v164, v248
v_mul_f32 v165, v165, v248
v_mul_f32 v166, v166, v248
v_mul_f32 v167, v167, v248
v_max3_f32 v246, v184, v185, v186
v_max_f32 v246, v246, v187
v_mov_b32 v248, v246
s_nop 1
v_permlane16_swap_b32 v246, v248
v_max_f32 v246, v246, v248
v_mov_b32 v248, v246
s_nop 1
v_permlane32_swap_b32 v246, v248
v_max_f32 v246, v246, v248
v_max_f32 v246, 0, v246
v_sub_f32 v238, v238, v246
v_sub_f32 v184, v184, v246
v_sub_f32 v185, v185, v246
v_sub_f32 v186, v186, v246
v_sub_f32 v187, v187, v246
v_exp_f32 v248, -v246
s_nop 0
v_mul_f32 v243, v243, v248
v_mul_f32 v168, v168, v248
v_mul_f32 v169, v169, v248
v_mul_f32 v170, v170, v248
v_mul_f32 v171, v171, v248
v_max3_f32 v246, v188, v189, v190
v_max_f32 v246, v246, v191
v_mov_b32 v248, v246
s_nop 1
v_permlane16_swap_b32 v246, v248
v_max_f32 v246, v246, v248
v_mov_b32 v248, v246
s_nop 1
v_permlane32_swap_b32 v246, v248
v_max_f32 v246, v246, v248
v_max_f32 v246, 0, v246
v_sub_f32 v239, v239, v246
v_sub_f32 v188, v188, v246
v_sub_f32 v189, v189, v246
v_sub_f32 v190, v190, v246
v_sub_f32 v191, v191, v246
v_exp_f32 v248, -v246
s_nop 0
v_mul_f32 v244, v244, v248
v_mul_f32 v172, v172, v248
v_mul_f32 v173, v173, v248
v_mul_f32 v174, v174, v248
v_mul_f32 v175, v175, v248
s_waitcnt lgkmcnt(0)
s_nop 2
s_setpc_b64 s[80:81]
LR1_2_1:
s_nop 8
s_waitcnt lgkmcnt(0)
s_bitcmp1_b32 s65, 0
s_cbranch_scc1 Lsv2_11
ds_write_b128 v252, v[236:239] offset:24576
s_or_b32 s65, s65, 1
s_waitcnt lgkmcnt(0)
Lsv2_11:
v_max3_f32 v246, v192, v193, v194
v_max_f32 v246, v246, v195
v_mov_b32 v248, v246
s_nop 1
v_permlane16_swap_b32 v246, v248
v_max_f32 v246, v246, v248
v_mov_b32 v248, v246
s_nop 1
v_permlane32_swap_b32 v246, v248
v_max_f32 v246, v246, v248
v_max_f32 v246, 0, v246
v_sub_f32 v236, v236, v246
v_sub_f32 v192, v192, v246
v_sub_f32 v193, v193, v246
v_sub_f32 v194, v194, v246
v_sub_f32 v195, v195, v246
v_exp_f32 v248, -v246
s_nop 0
v_mul_f32 v241, v241, v248
v_max3_f32 v246, v196, v197, v198
v_max_f32 v246, v246, v199
v_mov_b32 v248, v246
s_nop 1
v_permlane16_swap_b32 v246, v248
v_max_f32 v246, v246, v248
v_mov_b32 v248, v246
s_nop 1
v_permlane32_swap_b32 v246, v248
v_max_f32 v246, v246, v248
v_max_f32 v246, 0, v246
v_sub_f32 v237, v237, v246
v_sub_f32 v196, v196, v246
v_sub_f32 v197, v197, v246
v_sub_f32 v198, v198, v246
v_sub_f32 v199, v199, v246
v_exp_f32 v248, -v246
s_nop 0
v_mul_f32 v242, v242, v248
v_max3_f32 v246, v200, v201, v202
v_max_f32 v246, v246, v203
v_mov_b32 v248, v246
s_nop 1
v_permlane16_swap_b32 v246, v248
v_max_f32 v246, v246, v248
v_mov_b32 v248, v246
s_nop 1
v_permlane32_swap_b32 v246, v248
v_max_f32 v246, v246, v248
v_max_f32 v246, 0, v246
v_sub_f32 v238, v238, v246
v_sub_f32 v200, v200, v246
v_sub_f32 v201, v201, v246
v_sub_f32 v202, v202, v246
v_sub_f32 v203, v203, v246
v_exp_f32 v248, -v246
s_nop 0
v_mul_f32 v243, v243, v248
v_max3_f32 v246, v204, v205, v206
v_max_f32 v246, v246, v207
v_mov_b32 v248, v246
s_nop 1
v_permlane16_swap_b32 v246, v248
v_max_f32 v246, v246, v248
v_mov_b32 v248, v246
s_nop 1
v_permlane32_swap_b32 v246, v248
v_max_f32 v246, v246, v248
v_max_f32 v246, 0, v246
v_sub_f32 v239, v239, v246
v_sub_f32 v204, v204, v246
v_sub_f32 v205, v205, v246
v_sub_f32 v206, v206, v246
v_sub_f32 v207, v207, v246
v_exp_f32 v248, -v246
s_nop 0
v_mul_f32 v244, v244, v248
v_max3_f32 v246, v232, v233, v234
v_max_f32 v246, v246, v235
v_mov_b32 v248, v246
s_nop 1
v_permlane16_swap_b32 v246, v248
v_max_f32 v246, v246, v248
v_mov_b32 v248, v246
s_nop 1
v_permlane32_swap_b32 v246, v248
v_max_f32 v246, v246, v248
v_max_f32 v246, 0, v246
v_sub_f32 v246, v240, v246
ds_write_b32 v252, v246 offset:28680
v_mov_b32 v248, 0x7149f2ca
ds_write_b32 v252, v248 offset:29704
s_waitcnt lgkmcnt(0)
s_nop 2
s_setpc_b64 s[80:81]
LR2_2_1:
s_nop 8
s_waitcnt lgkmcnt(0)
v_readfirstlane_b32 s77, v247
s_bitcmp1_b32 s65, 0
s_cbranch_scc1 Lsv2_210
ds_write_b128 v252, v[236:239] offset:24576
s_or_b32 s65, s65, 1
s_waitcnt lgkmcnt(0)
Lsv2_210:
v_max3_f32 v246, v176, v177, v178
v_max_f32 v246, v246, v179
v_mov_b32 v248, v246
s_nop 1
v_permlane16_swap_b32 v246, v248
v_max_f32 v246, v246, v248
v_mov_b32 v248, v246
s_nop 1
v_permlane32_swap_b32 v246, v248
v_max_f32 v246, v246, v248
v_max_f32 v246, 0, v246
v_sub_f32 v236, v236, v246
v_sub_f32 v176, v176, v246
v_sub_f32 v177, v177, v246
v_sub_f32 v178, v178, v246
v_sub_f32 v179, v179, v246
v_exp_f32 v248, -v246
s_nop 0
v_mul_f32 v241, v241, v248
v_mul_f32 v192, v192, v248
v_mul_f32 v193, v193, v248
v_mul_f32 v194, v194, v248
v_mul_f32 v195, v195, v248
v_max3_f32 v246, v180, v181, v182
v_max_f32 v246, v246, v183
v_mov_b32 v248, v246
s_nop 1
v_permlane16_swap_b32 v246, v248
v_max_f32 v246, v246, v248
v_mov_b32 v248, v246
s_nop 1
v_permlane32_swap_b32 v246, v248
v_max_f32 v246, v246, v248
v_max_f32 v246, 0, v246
v_sub_f32 v237, v237, v246
v_sub_f32 v180, v180, v246
v_sub_f32 v181, v181, v246
v_sub_f32 v182, v182, v246
v_sub_f32 v183, v183, v246
v_exp_f32 v248, -v246
s_nop 0
v_mul_f32 v242, v242, v248
v_mul_f32 v196, v196, v248
v_mul_f32 v197, v197, v248
v_mul_f32 v198, v198, v248
v_mul_f32 v199, v199, v248
v_max3_f32 v246, v184, v185, v186
v_max_f32 v246, v246, v187
v_mov_b32 v248, v246
s_nop 1
v_permlane16_swap_b32 v246, v248
v_max_f32 v246, v246, v248
v_mov_b32 v248, v246
s_nop 1
v_permlane32_swap_b32 v246, v248
v_max_f32 v246, v246, v248
v_max_f32 v246, 0, v246
v_sub_f32 v238, v238, v246
v_sub_f32 v184, v184, v246
v_sub_f32 v185, v185, v246
v_sub_f32 v186, v186, v246
v_sub_f32 v187, v187, v246
v_exp_f32 v248, -v246
s_nop 0
v_mul_f32 v243, v243, v248
v_mul_f32 v200, v200, v248
v_mul_f32 v201, v201, v248
v_mul_f32 v202, v202, v248
v_sub_f32 v203, v203, v246
v_max3_f32 v246, v188, v189, v190
v_max_f32 v246, v246, v191
v_mov_b32 v248, v246
s_nop 1
v_permlane16_swap_b32 v246, v248
v_max_f32 v246, v246, v248
v_mov_b32 v248, v246
s_nop 1
v_permlane32_swap_b32 v246, v248
v_max_f32 v246, v246, v248
v_max_f32 v246, 0, v246
v_sub_f32 v239, v239, v246
v_sub_f32 v188, v188, v246
v_sub_f32 v189, v189, v246
v_sub_f32 v190, v190, v246
v_sub_f32 v191, v191, v246
v_exp_f32 v248, -v246
s_nop 0
v_mul_f32 v244, v244, v248
v_sub_f32 v204, v204, v246
v_sub_f32 v205, v205, v246
v_sub_f32 v206, v206, v246
v_sub_f32 v207, v207, v246
s_cmp_eq_u32 s77, 0
s_cbranch_scc1 LR2s_2_1
ds_read_b32 v246, v252 offset:29696
ds_read_b32 v247, v252 offset:28672
ds_read_b32 v248, v252 offset:28676
s_waitcnt lgkmcnt(0)
v_min3_f32 v254, v246, v247, v248
v_sub_f32 v246, v254, v246
v_exp_f32 v246, v246
s_nop 0
ds_write_b32 v252, v246 offset:25600
s_waitcnt lgkmcnt(0)
ds_read_b32 v246, v252 offset:29700
ds_read_b32 v247, v252 offset:28680
ds_read_b32 v248, v252 offset:28684
s_waitcnt lgkmcnt(0)
v_min3_f32 v255, v246, v247, v248
v_sub_f32 v246, v255, v246
v_exp_f32 v246, v246
s_nop 0
ds_write_b32 v252, v246 offset:25604
s_waitcnt lgkmcnt(0)
v_sub_f32 v246, v240, v255
v_mov_b32 v240, v255
v_sub_f32 v232, v232, v246
v_sub_f32 v233, v233, v246
v_sub_f32 v234, v234, v246
v_sub_f32 v235, v235, v246
v_exp_f32 v248, -v246
s_nop 0
v_mul_f32 v245, v245, v248
s_or_b32 s65, s65, 2
s_waitcnt lgkmcnt(0)
s_barrier
s_barrier
LR2s_2_1:
s_waitcnt lgkmcnt(0)
s_nop 2
s_setpc_b64 s[80:81]
LH_2:
s_nop 15
s_waitcnt lgkmcnt(0)
s_bitcmp1_b32 s65, 0
s_cbranch_scc0 LH2_2
ds_read_b32 v246, v252 offset:24576
s_waitcnt lgkmcnt(0)
v_sub_f32 v246, v236, v246
v_exp_f32 v246, v246
s_nop 1
)ASM"
R"ASM(
.set ci_, 0
.rept 16
v_accvgpr_read_b32 v247, a[ci_+0]
v_accvgpr_read_b32 v248, a[ci_+1]
v_accvgpr_read_b32 v254, a[ci_+2]
v_accvgpr_read_b32 v255, a[ci_+3]
v_mul_f32 v247, v247, v246
v_mul_f32 v248, v248, v246
v_mul_f32 v254, v254, v246
v_mul_f32 v255, v255, v246
v_accvgpr_write_b32 a[ci_+0], v247
v_accvgpr_write_b32 a[ci_+1], v248
v_accvgpr_write_b32 a[ci_+2], v254
v_accvgpr_write_b32 a[ci_+3], v255
.set ci_, ci_+16
.endr
ds_read_b32 v246, v252 offset:24580
s_waitcnt lgkmcnt(0)
v_sub_f32 v246, v237, v246
v_exp_f32 v246, v246
s_nop 1
.set ci_, 4
.rept 16
v_accvgpr_read_b32 v247, a[ci_+0]
v_accvgpr_read_b32 v248, a[ci_+1]
v_accvgpr_read_b32 v254, a[ci_+2]
v_accvgpr_read_b32 v255, a[ci_+3]
v_mul_f32 v247, v247, v246
v_mul_f32 v248, v248, v246
v_mul_f32 v254, v254, v246
v_mul_f32 v255, v255, v246
v_accvgpr_write_b32 a[ci_+0], v247
v_accvgpr_write_b32 a[ci_+1], v248
v_accvgpr_write_b32 a[ci_+2], v254
v_accvgpr_write_b32 a[ci_+3], v255
.set ci_, ci_+16
.endr
ds_read_b32 v246, v252 offset:24584
s_waitcnt lgkmcnt(0)
v_sub_f32 v246, v238, v246
v_exp_f32 v246, v246
s_nop 1
.set ci_, 8
.rept 16
v_accvgpr_read_b32 v247, a[ci_+0]
v_accvgpr_read_b32 v248, a[ci_+1]
v_accvgpr_read_b32 v254, a[ci_+2]
v_accvgpr_read_b32 v255, a[ci_+3]
v_mul_f32 v247, v247, v246
v_mul_f32 v248, v248, v246
v_mul_f32 v254, v254, v246
v_mul_f32 v255, v255, v246
v_accvgpr_write_b32 a[ci_+0], v247
v_accvgpr_write_b32 a[ci_+1], v248
v_accvgpr_write_b32 a[ci_+2], v254
v_accvgpr_write_b32 a[ci_+3], v255
.set ci_, ci_+16
.endr
ds_read_b32 v246, v252 offset:24588
s_waitcnt lgkmcnt(0)
v_sub_f32 v246, v239, v246
v_exp_f32 v246, v246
s_nop 1
.set ci_, 12
.rept 16
v_accvgpr_read_b32 v247, a[ci_+0]
v_accvgpr_read_b32 v248, a[ci_+1]
v_accvgpr_read_b32 v254, a[ci_+2]
v_accvgpr_read_b32 v255, a[ci_+3]
v_mul_f32 v247, v247, v246
v_mul_f32 v248, v248, v246
v_mul_f32 v254, v254, v246
v_mul_f32 v255, v255, v246
v_accvgpr_write_b32 a[ci_+0], v247
v_accvgpr_write_b32 a[ci_+1], v248
v_accvgpr_write_b32 a[ci_+2], v254
v_accvgpr_write_b32 a[ci_+3], v255
.set ci_, ci_+16
.endr
LH2_2:
s_bitcmp1_b32 s65, 1
s_cbranch_scc0 LH3_2
ds_read_b32 v246, v252 offset:25600
s_waitcnt lgkmcnt(0)
v_mul_f32 v128, v128, v246
v_mul_f32 v129, v129, v246
v_mul_f32 v130, v130, v246
v_mul_f32 v131, v131, v246
v_mul_f32 v136, v136, v246
v_mul_f32 v137, v137, v246
v_mul_f32 v138, v138, v246
v_mul_f32 v139, v139, v246
v_mul_f32 v144, v144, v246
v_mul_f32 v145, v145, v246
v_mul_f32 v146, v146, v246
v_mul_f32 v147, v147, v246
v_mul_f32 v152, v152, v246
v_mul_f32 v153, v153, v246
v_mul_f32 v154, v154, v246
v_mul_f32 v155, v155, v246
ds_read_b32 v246, v252 offset:25604
s_waitcnt lgkmcnt(0)
v_mul_f32 v132, v132, v246
v_mul_f32 v133, v133, v246
v_mul_f32 v134, v134, v246
v_mul_f32 v135, v135, v246
v_mul_f32 v140, v140, v246
v_mul_f32 v141, v141, v246
v_mul_f32 v142, v142, v246
v_mul_f32 v143, v143, v246
v_mul_f32 v148, v148, v246
v_mul_f32 v149, v149, v246
v_mul_f32 v150, v150, v246
v_mul_f32 v151, v151, v246
v_mul_f32 v156, v156, v246
v_mul_f32 v157, v157, v246
v_mul_f32 v158, v158, v246
v_mul_f32 v159, v159, v246
LH3_2:
s_mov_b32 s65, 0
s_waitcnt lgkmcnt(0)
s_nop 2
s_setpc_b64 s[80:81]
LW3:
v_mbcnt_lo_u32_b32 v228, -1, 0
v_mbcnt_hi_u32_b32 v228, -1, v228
v_bfe_u32 v231, v228, 2, 3
v_lshrrev_b32 v232, 2, v231
v_sub_u32 v234, 2, v232
v_and_b32 v234, 3, v234
v_and_b32 v233, 3, v228
v_xor_b32 v233, v233, v234
v_lshlrev_b32 v233, 4, v233
v_lshrrev_b32 v234, 5, v228
v_lshl_add_u32 v233, v234, 6, v233
v_and_b32 v234, 3, v231
v_lshl_add_u32 v234, v232, 3, v234
v_add_u32 v234, 20, v234
v_lshl_add_u32 v225, v234, 13, v233
v_add_u32 v234, 24, v231
v_lshl_add_u32 v253, v234, 9, v233
v_add_u32 v234, 8, v231
v_mul_u32_u24 v234, 0x380, v234
v_add_u32 v226, v234, v233
s_and_b32 s77, s26, 1
s_mul_i32 s80, s77, 129024
s_sub_u32 s81, 129024, s80
s_add_u32 s67, s80, 14336
s_add_u32 s68, s81, 14336
s_add_u32 s69, s80, 43008
s_add_u32 s70, s81, 43008
s_add_u32 s71, s80, 71680
s_add_u32 s72, s81, 71680
s_add_u32 s73, s80, 100352
s_add_u32 s74, s81, 100352
s_add_u32 s75, s81, 114688
s_mov_b32 s76, 0
s_mov_b32 s62, 6
s_mov_b32 s63, 0x3db8aa3b
s_mov_b32 s66, 0xbdb8aa3b
s_mov_b32 s64, 0x41800000
s_mov_b32 s65, 1
s_mov_b32 s50, 3662336
s_mov_b32 s51, 0x20000
s_mov_b32 s54, 258048
s_mov_b32 s55, 0x20000
s_mov_b32 s42, 524288
s_mov_b32 s43, 0x20000
s_mov_b32 s46, 524288
s_mov_b32 s47, 0x20000
s_mov_b32 s58, Lcodeend-Lbase
s_mov_b32 s59, 0x20000
v_lshlrev_b32 v246, 7, v228
buffer_load_dword v247, v246, s[56:59], s78 offen
s_waitcnt lgkmcnt(0)
s_add_u32 s52, s16, s60
s_addc_u32 s53, s17, 0
s_and_b32 s53, s53, 0xffff
s_add_u32 s48, s22, s61
s_addc_u32 s49, s23, 0
s_and_b32 s49, s49, 0xffff
s_mov_b32 m0, 39936
s_nop 0
buffer_load_dwordx4 v226, s[52:55], s67 offen offset:0 lds
s_mov_b32 m0, 44032
s_nop 0
buffer_load_dwordx4 v226, s[52:55], s68 offen offset:0 lds
s_mov_b32 m0, 48128
s_nop 0
buffer_load_dwordx4 v226, s[52:55], s69 offen offset:0 lds
s_mov_b32 m0, 52224
s_nop 0
buffer_load_dwordx4 v226, s[52:55], s70 offen offset:0 lds
s_mov_b32 m0, 56320
s_nop 0
buffer_load_dwordx4 v226, s[52:55], s71 offen offset:0 lds
s_mov_b32 m0, 60416
s_nop 0
buffer_load_dwordx4 v226, s[52:55], s72 offen offset:0 lds
s_mov_b32 m0, 64512
s_nop 0
buffer_load_dwordx4 v226, s[52:55], s73 offen offset:0 lds
s_mov_b32 m0, 68608
s_nop 0
buffer_load_dwordx4 v226, s[52:55], s74 offen offset:0 lds
s_mov_b32 m0, 72704
s_nop 0
buffer_load_dwordx4 v226, s[52:55], s75 offen offset:0 lds
v_add_u32 v226, 0x80, v226
s_mov_b32 m0, 140288
s_nop 0
buffer_load_dwordx4 v225, s[48:51], s76 offen offset:0 lds
s_mov_b32 m0, 142208
s_nop 0
buffer_load_dwordx4 v225, s[48:51], s76 offen offset:128 lds
s_mov_b32 m0, 144128
s_nop 0
buffer_load_dwordx4 v225, s[48:51], s76 offen offset:256 lds
s_mov_b32 m0, 146048
s_nop 0
buffer_load_dwordx4 v225, s[48:51], s76 offen offset:384 lds
s_add_u32 s76, s76, 262144
s_mov_b32 m0, 156672
s_nop 0
buffer_load_dwordx4 v225, s[48:51], s76 offen offset:0 lds
s_mov_b32 m0, 158592
s_nop 0
buffer_load_dwordx4 v225, s[48:51], s76 offen offset:128 lds
s_mov_b32 m0, 160512
s_nop 0
buffer_load_dwordx4 v225, s[48:51], s76 offen offset:256 lds
s_mov_b32 m0, 162432
s_nop 0
buffer_load_dwordx4 v225, s[48:51], s76 offen offset:384 lds
s_add_u32 s76, s76, 262144
s_and_b32 s60, s26, 127
s_lshl_b32 s60, s60, 19
s_add_u32 s40, s18, s60
s_addc_u32 s41, s19, 0
s_and_b32 s41, s41, 0xffff
s_add_u32 s44, s20, s60
s_addc_u32 s45, s21, 0
s_and_b32 s45, s45, 0xffff
s_mov_b32 s60, 0
s_mov_b32 s61, 0
s_lshl_b32 s77, s26, 9
s_and_b32 s77, s77, 0xffffe000
s_add_u32 s77, s77, 100663296
s_add_u32 s38, s28, s77
s_addc_u32 s39, s29, 0
s_and_b32 s77, s26, 15
s_lshl_b32 s36, s77, 8
s_add_u32 s36, s36, 256
s_add_u32 s36, s36, s38
s_addc_u32 s37, s39, 0
s_lshl_b32 s77, s77, 2
s_add_u32 s38, s38, s77
s_addc_u32 s39, s39, 0
s_mov_b32 m0, 3072
s_nop 0
buffer_load_dwordx4 v226, s[52:55], s67 offen offset:0 lds
s_mov_b32 m0, 7168
s_nop 0
buffer_load_dwordx4 v226, s[52:55], s68 offen offset:0 lds
s_mov_b32 m0, 11264
s_nop 0
buffer_load_dwordx4 v226, s[52:55], s69 offen offset:0 lds
s_mov_b32 m0, 15360
s_nop 0
buffer_load_dwordx4 v226, s[52:55], s70 offen offset:0 lds
s_mov_b32 m0, 19456
s_nop 0
buffer_load_dwordx4 v226, s[52:55], s71 offen offset:0 lds
s_mov_b32 m0, 23552
s_nop 0
buffer_load_dwordx4 v226, s[52:55], s72 offen offset:0 lds
s_mov_b32 m0, 27648
s_nop 0
buffer_load_dwordx4 v226, s[52:55], s73 offen offset:0 lds
s_mov_b32 m0, 31744
s_nop 0
buffer_load_dwordx4 v226, s[52:55], s74 offen offset:0 lds
s_mov_b32 m0, 35840
s_nop 0
buffer_load_dwordx4 v226, s[52:55], s75 offen offset:0 lds
v_add_u32 v226, 0x80, v226
v_lshrrev_b32 v229, 4, v228
v_and_b32 v230, 15, v228
v_lshlrev_b32 v231, 4, v228
v_add_u32 v252, 0x10000, v231
v_lshrrev_b32 v231, 2, v230
v_sub_u32 v234, 4, v231
v_and_b32 v234, 3, v234
v_xor_b32 v234, v234, v229
v_lshlrev_b32 v234, 4, v234
v_and_b32 v231, 7, v230
v_lshl_add_u32 v234, v231, 6, v234
v_lshrrev_b32 v231, 3, v230
v_lshl_add_u32 v249, v231, 10, v234
v_add_u32 v227, 0x9000, v249
v_lshrrev_b32 v231, 2, v230
v_and_b32 v232, 1, v229
v_lshl_add_u32 v231, v232, 2, v231
v_lshlrev_b32 v231, 6, v231
v_lshrrev_b32 v232, 1, v229
v_lshl_add_u32 v231, v232, 10, v231
v_add_u32 v231, 0x18000, v231
v_sub_u32 v234, 4, v229
v_and_b32 v234, 3, v234
v_and_b32 v232, 3, v228
v_xor_b32 v233, v232, v234
v_lshl_add_u32 v224, v233, 4, v231
v_lshrrev_b32 v233, 1, v232
v_xor_b32 v233, v233, v234
v_lshl_add_u32 v233, v233, 4, v231
v_and_b32 v232, 1, v232
v_lshl_add_u32 v250, v232, 3, v233
v_xor_b32 v251, 32, v250
v_mov_b32 v241, 0
v_mov_b32 v242, 0
v_mov_b32 v243, 0
v_mov_b32 v244, 0
v_mov_b32 v245, 0
s_waitcnt vmcnt(9)
s_barrier
RT 160,224,38912
RT 162,224,47104
RT 164,224,55296
RT 166,224,63488
ds_read_b128 a[0:3], v227 offset:24576
ds_read_b128 a[4:7], v227 offset:25088
ds_read_b128 a[8:11], v227 offset:26624
ds_read_b128 a[12:15], v227 offset:27136
ds_read_b128 a[16:19], v227 offset:28672
ds_read_b128 a[20:23], v227 offset:29184
ds_read_b128 a[24:27], v227 offset:30720
ds_read_b128 a[28:31], v227 offset:31232
ds_read_b128 a[32:35], v227 offset:32768
ds_read_b128 a[36:39], v227 offset:33280
ds_read_b128 a[40:43], v227 offset:34816
ds_read_b128 a[44:47], v227 offset:35328
RT 168,224,38920
RT 170,224,47112
RT 172,224,55304
RT 174,224,63496
s_waitcnt lgkmcnt(15)
MG0 a,192,v,160,a,0
RT 176,224,39424
s_waitcnt lgkmcnt(15)
MG a,192,v,164,a,4
RT 178,224,47616
s_waitcnt lgkmcnt(15)
MG0 a,196,v,160,a,8
RT 180,224,55808
s_waitcnt lgkmcnt(15)
MG a,196,v,164,a,12
RT 182,224,64000
s_mov_b32 m0, 107520
s_waitcnt lgkmcnt(15)
MG0 a,200,v,160,a,16
buffer_load_dwordx4 v225, s[48:51], s76 offen offset:0 lds
s_waitcnt lgkmcnt(14)
MG a,200,v,164,a,20
s_waitcnt lgkmcnt(13)
MG0 a,204,v,160,a,24
s_waitcnt lgkmcnt(10)
MG a,204,v,164,a,28
s_mov_b32 m0, 109440
MG0 v,128,v,160,a,32
buffer_load_dwordx4 v225, s[48:51], s76 offen offset:128 lds
MG v,128,v,164,a,36
s_waitcnt lgkmcnt(9)
MG0 v,132,v,160,a,40
s_waitcnt lgkmcnt(8)
MG v,132,v,164,a,44
s_waitcnt lgkmcnt(4)
MG0 a,208,v,168,a,0
RT 160,224,39432
MG a,208,v,172,a,4
RT 162,224,47624
MG0 a,212,v,168,a,8
RT 164,224,55816
MG a,212,v,172,a,12
RT 166,224,64008
s_mov_b32 m0, 111360
MG0 a,216,v,168,a,16
buffer_load_dwordx4 v225, s[48:51], s76 offen offset:256 lds
MG a,216,v,172,a,20
MG0 a,220,v,168,a,24
MG a,220,v,172,a,28
s_mov_b32 m0, 113280
MG0 v,136,v,168,a,32
buffer_load_dwordx4 v225, s[48:51], s76 offen offset:384 lds
s_add_u32 s76, s76, 262144
MG v,136,v,172,a,36
MG0 v,140,v,168,a,40
MG v,140,v,172,a,44
s_waitcnt lgkmcnt(4)
MG0 a,224,v,176,a,0
RT 168,224,32768
MG a,224,v,180,a,4
RT 170,224,40960
MG0 a,228,v,176,a,8
RT 172,224,49152
MG a,228,v,180,a,12
RT 174,224,57344
s_mov_b32 m0, 123904
MG0 a,232,v,176,a,16
buffer_load_dwordx4 v225, s[48:51], s76 offen offset:0 lds
MG a,232,v,180,a,20
MG0 a,236,v,176,a,24
MG a,236,v,180,a,28
s_mov_b32 m0, 125824
MG0 v,144,v,176,a,32
buffer_load_dwordx4 v225, s[48:51], s76 offen offset:128 lds
MG v,144,v,180,a,36
MG0 v,148,v,176,a,40
MG v,148,v,180,a,44
s_waitcnt lgkmcnt(4)
MG0 a,240,v,160,a,0
RT 176,224,32776
MG a,240,v,164,a,4
RT 178,224,40968
MG0 a,244,v,160,a,8
RT 180,224,49160
MG a,244,v,164,a,12
RT 182,224,57352
s_mov_b32 m0, 127744
MG0 a,248,v,160,a,16
buffer_load_dwordx4 v225, s[48:51], s76 offen offset:256 lds
MG a,248,v,164,a,20
MG0 a,252,v,160,a,24
MG a,252,v,164,a,28
s_mov_b32 m0, 129664
MG0 v,152,v,160,a,32
buffer_load_dwordx4 v225, s[48:51], s76 offen offset:384 lds
s_add_u32 s76, s76, 262144
MG v,152,v,164,a,36
MG0 v,156,v,160,a,40
MG v,156,v,164,a,44
s_waitcnt lgkmcnt(4)
MG0 v,0,v,168,a,0
RT 160,224,33280
MG v,0,v,172,a,4
RT 162,224,41472
MG0 v,32,v,168,a,8
RT 164,224,49664
MG v,32,v,172,a,12
RT 166,224,57856
MG0 v,64,v,168,a,16
MG v,64,v,172,a,20
MG0 v,96,v,168,a,24
MG v,96,v,172,a,28
s_waitcnt lgkmcnt(4)
MG0 v,4,v,176,a,0
RT 168,224,33288
MG v,4,v,180,a,4
RT 170,224,41480
MG0 v,36,v,176,a,8
RT 172,224,49672
MG v,36,v,180,a,12
RT 174,224,57864
MG0 v,68,v,176,a,16
MG v,68,v,180,a,20
MG0 v,100,v,176,a,24
MG v,100,v,180,a,28
s_waitcnt lgkmcnt(4)
MG0 v,8,v,160,a,0
RT 176,224,34816
MG v,8,v,164,a,4
RT 178,224,43008
MG0 v,40,v,160,a,8
RT 180,224,51200
MG v,40,v,164,a,12
RT 182,224,59392
MG0 v,72,v,160,a,16
MG v,72,v,164,a,20
MG0 v,104,v,160,a,24
s_waitcnt vmcnt(8) lgkmcnt(4)
MG v,104,v,164,a,28
s_barrier
MG0 v,12,v,168,a,0
RT 160,224,34824
MG v,12,v,172,a,4
RT 162,224,43016
MG0 v,44,v,168,a,8
RT 164,224,51208
MG v,44,v,172,a,12
RT 166,224,59400
MG0 v,76,v,168,a,16
ds_read_b128 a[48:51], v249 offset:24576
s_mov_b32 m0, 39936
MG v,76,v,172,a,20
buffer_load_dwordx4 v226, s[52:55], s67 offen offset:0 lds
MG0 v,108,v,168,a,24
ds_read_b128 a[52:55], v249 offset:25088
MG v,108,v,172,a,28
s_waitcnt lgkmcnt(6)
MG0 v,16,v,176,a,0
RT 168,224,35328
MG v,16,v,180,a,4
RT 170,224,43520
MG0 v,48,v,176,a,8
RT 172,224,51712
MG v,48,v,180,a,12
RT 174,224,59904
MG0 v,80,v,176,a,16
ds_read_b128 a[56:59], v249 offset:26624
s_mov_b32 m0, 44032
MG v,80,v,180,a,20
buffer_load_dwordx4 v226, s[52:55], s68 offen offset:0 lds
MG0 v,112,v,176,a,24
ds_read_b128 a[60:63], v249 offset:27136
MG v,112,v,180,a,28
s_waitcnt lgkmcnt(8)
MG0 v,20,v,160,a,0
RT 176,224,35336
MG v,20,v,164,a,4
RT 178,224,43528
MG0 v,52,v,160,a,8
RT 180,224,51720
MG v,52,v,164,a,12
RT 182,224,59912
MG0 v,84,v,160,a,16
ds_read_b128 a[64:67], v249 offset:28672
s_mov_b32 m0, 48128
MG v,84,v,164,a,20
buffer_load_dwordx4 v226, s[52:55], s69 offen offset:0 lds
MG0 v,116,v,160,a,24
ds_read_b128 a[68:71], v249 offset:29184
MG v,116,v,164,a,28
s_waitcnt lgkmcnt(8)
MG0 v,24,v,168,a,0
RT 160,224,36864
MG v,24,v,172,a,4
RT 162,224,45056
MG0 v,56,v,168,a,8
RT 164,224,53248
MG v,56,v,172,a,12
RT 166,224,61440
MG0 v,88,v,168,a,16
ds_read_b128 a[72:75], v249 offset:30720
s_mov_b32 m0, 52224
MG v,88,v,172,a,20
buffer_load_dwordx4 v226, s[52:55], s70 offen offset:0 lds
MG0 v,120,v,168,a,24
ds_read_b128 a[76:79], v249 offset:31232
MG v,120,v,172,a,28
s_waitcnt lgkmcnt(8)
MG0 v,28,v,176,a,0
RT 168,224,36872
MG v,28,v,180,a,4
RT 170,224,45064
MG0 v,60,v,176,a,8
RT 172,224,53256
MG v,60,v,180,a,12
RT 174,224,61448
MG0 v,92,v,176,a,16
ds_read_b128 a[80:83], v249 offset:32768
s_mov_b32 m0, 56320
MG v,92,v,180,a,20
buffer_load_dwordx4 v226, s[52:55], s71 offen offset:0 lds
MG0 v,124,v,176,a,24
ds_read_b128 a[84:87], v249 offset:33280
MG v,124,v,180,a,28
s_waitcnt lgkmcnt(8)
MG0 a,128,v,160,a,0
RT 176,224,37376
MG a,128,v,164,a,4
RT 178,224,45568
MG0 a,132,v,160,a,8
RT 180,224,53760
MG a,132,v,164,a,12
RT 182,224,61952
MG0 a,136,v,160,a,16
ds_read_b128 a[88:91], v249 offset:34816
s_mov_b32 m0, 60416
MG a,136,v,164,a,20
buffer_load_dwordx4 v226, s[52:55], s72 offen offset:0 lds
MG0 a,140,v,160,a,24
ds_read_b128 a[92:95], v249 offset:35328
MG a,140,v,164,a,28
s_waitcnt lgkmcnt(8)
MG0 a,144,v,168,a,0
RT 160,224,37384
MG a,144,v,172,a,4
RT 162,224,45576
MG0 a,148,v,168,a,8
RT 164,224,53768
MG a,148,v,172,a,12
RT 166,224,61960
s_mov_b32 m0, 64512
MG0 a,152,v,168,a,16
buffer_load_dwordx4 v226, s[52:55], s73 offen offset:0 lds
MG a,152,v,172,a,20
MG0 a,156,v,168,a,24
s_waitcnt vmcnt(7) lgkmcnt(0)
MG a,156,v,172,a,28
s_barrier
MG0 a,160,v,176,a,0
RT 168,224,6144
MG a,160,v,180,a,4
RT 170,224,14336
MG0 a,164,v,176,a,8
RT 172,224,22528
MG a,164,v,180,a,12
RT 174,224,30720
s_mov_b32 m0, 68608
MG0 a,168,v,176,a,16
buffer_load_dwordx4 v226, s[52:55], s74 offen offset:0 lds
MG a,168,v,180,a,20
MG0 a,172,v,176,a,24
MG a,172,v,180,a,28
MG0 a,176,v,160,a,0
RT 176,224,6152
MG a,176,v,164,a,4
RT 178,224,14344
MG0 a,180,v,160,a,8
RT 180,224,22536
MG a,180,v,164,a,12
RT 182,224,30728
s_mov_b32 m0, 72704
MG0 a,184,v,160,a,16
buffer_load_dwordx4 v226, s[52:55], s75 offen offset:0 lds
v_add_u32 v226, 0x80, v226
MG a,184,v,164,a,20
MG0 a,188,v,160,a,24
MG a,188,v,164,a,28
s_waitcnt lgkmcnt(4)
MG a,192,v,168,a,48
RT 160,224,6656
MG a,192,v,172,a,52
RT 162,224,14848
MG a,196,v,168,a,56
RT 164,224,23040
MG a,196,v,172,a,60
RT 166,224,31232
s_mov_b32 m0, 140288
MG a,200,v,168,a,64
buffer_load_dwordx4 v225, s[48:51], s76 offen offset:0 lds
MG a,200,v,172,a,68
MG a,204,v,168,a,72
MG a,204,v,172,a,76
s_mov_b32 m0, 142208
MG v,128,v,168,a,80
buffer_load_dwordx4 v225, s[48:51], s76 offen offset:128 lds
MG v,128,v,172,a,84
MG v,132,v,168,a,88
MG v,132,v,172,a,92
s_waitcnt lgkmcnt(4)
MG a,208,v,176,a,48
RT 168,224,6664
MG a,208,v,180,a,52
RT 170,224,14856
MG a,212,v,176,a,56
RT 172,224,23048
MG a,212,v,180,a,60
RT 174,224,31240
s_mov_b32 m0, 144128
MG a,216,v,176,a,64
buffer_load_dwordx4 v225, s[48:51], s76 offen offset:256 lds
MG a,216,v,180,a,68
MG a,220,v,176,a,72
MG a,220,v,180,a,76
s_mov_b32 m0, 146048
MG v,136,v,176,a,80
buffer_load_dwordx4 v225, s[48:51], s76 offen offset:384 lds
s_add_u32 s76, s76, 262144
MG v,136,v,180,a,84
MG v,140,v,176,a,88
MG v,140,v,180,a,92
s_waitcnt lgkmcnt(4)
MG a,224,v,160,a,48
RT 176,224,0
MG a,224,v,164,a,52
RT 178,224,8192
MG a,228,v,160,a,56
RT 180,224,16384
MG a,228,v,164,a,60
RT 182,224,24576
s_mov_b32 m0, 156672
MG a,232,v,160,a,64
buffer_load_dwordx4 v225, s[48:51], s76 offen offset:0 lds
MG a,232,v,164,a,68
MG a,236,v,160,a,72
MG a,236,v,164,a,76
s_mov_b32 m0, 158592
MG v,144,v,160,a,80
buffer_load_dwordx4 v225, s[48:51], s76 offen offset:128 lds
MG v,144,v,164,a,84
MG v,148,v,160,a,88
MG v,148,v,164,a,92
s_waitcnt lgkmcnt(4)
MG a,240,v,168,a,48
RT 160,224,8
MG a,240,v,172,a,52
RT 162,224,8200
MG a,244,v,168,a,56
RT 164,224,16392
MG a,244,v,172,a,60
RT 166,224,24584
s_mov_b32 m0, 160512
MG a,248,v,168,a,64
buffer_load_dwordx4 v225, s[48:51], s76 offen offset:256 lds
MG a,248,v,172,a,68
MG a,252,v,168,a,72
MG a,252,v,172,a,76
s_mov_b32 m0, 162432
MG v,152,v,168,a,80
buffer_load_dwordx4 v225, s[48:51], s76 offen offset:384 lds
s_add_u32 s76, s76, 262144
MG v,152,v,172,a,84
MG v,156,v,168,a,88
MG v,156,v,172,a,92
s_waitcnt lgkmcnt(4)
MG v,0,v,176,a,48
RT 168,224,512
MG v,0,v,180,a,52
RT 170,224,8704
MG v,32,v,176,a,56
RT 172,224,16896
MG v,32,v,180,a,60
RT 174,224,25088
s_mov_b32 m0, 3072
MG v,64,v,176,a,64
buffer_load_dwordx4 v226, s[52:55], s67 offen offset:0 lds
MG v,64,v,180,a,68
MG v,96,v,176,a,72
MG v,96,v,180,a,76
s_waitcnt lgkmcnt(4)
MG v,4,v,160,a,48
RT 176,224,520
MG v,4,v,164,a,52
RT 178,224,8712
MG v,36,v,160,a,56
RT 180,224,16904
MG v,36,v,164,a,60
RT 182,224,25096
s_mov_b32 m0, 7168
MG v,68,v,160,a,64
buffer_load_dwordx4 v226, s[52:55], s68 offen offset:0 lds
MG v,68,v,164,a,68
MG v,100,v,160,a,72
MG v,100,v,164,a,76
s_waitcnt lgkmcnt(4)
MG v,8,v,168,a,48
RT 160,224,2048
MG v,8,v,172,a,52
RT 162,224,10240
MG v,40,v,168,a,56
RT 164,224,18432
MG v,40,v,172,a,60
RT 166,224,26624
s_mov_b32 m0, 11264
MG v,72,v,168,a,64
buffer_load_dwordx4 v226, s[52:55], s69 offen offset:0 lds
MG v,72,v,172,a,68
MG v,104,v,168,a,72
s_waitcnt vmcnt(11) lgkmcnt(4)
MG v,104,v,172,a,76
s_barrier
MG v,12,v,176,a,48
RT 168,224,2056
MG v,12,v,180,a,52
RT 170,224,10248
MG v,44,v,176,a,56
RT 172,224,18440
MG v,44,v,180,a,60
RT 174,224,26632
MG v,76,v,176,a,64
ds_read_b128 a[0:3], v227 offset:24576
s_mov_b32 m0, 15360
MG v,76,v,180,a,68
buffer_load_dwordx4 v226, s[52:55], s70 offen offset:0 lds
MG v,108,v,176,a,72
ds_read_b128 a[4:7], v227 offset:25088
MG v,108,v,180,a,76
s_waitcnt lgkmcnt(6)
MG v,16,v,160,a,48
RT 176,224,2560
MG v,16,v,164,a,52
RT 178,224,10752
MG v,48,v,160,a,56
RT 180,224,18944
MG v,48,v,164,a,60
RT 182,224,27136
MG v,80,v,160,a,64
ds_read_b128 a[8:11], v227 offset:26624
s_mov_b32 m0, 19456
MG v,80,v,164,a,68
buffer_load_dwordx4 v226, s[52:55], s71 offen offset:0 lds
MG v,112,v,160,a,72
ds_read_b128 a[12:15], v227 offset:27136
MG v,112,v,164,a,76
s_waitcnt lgkmcnt(8)
MG v,20,v,168,a,48
RT 160,224,2568
MG v,20,v,172,a,52
RT 162,224,10760
MG v,52,v,168,a,56
RT 164,224,18952
MG v,52,v,172,a,60
RT 166,224,27144
MG v,84,v,168,a,64
ds_read_b128 a[16:19], v227 offset:28672
s_mov_b32 m0, 23552
MG v,84,v,172,a,68
buffer_load_dwordx4 v226, s[52:55], s72 offen offset:0 lds
MG v,116,v,168,a,72
ds_read_b128 a[20:23], v227 offset:29184
MG v,116,v,172,a,76
s_waitcnt lgkmcnt(8)
MG v,24,v,176,a,48
RT 168,224,4096
MG v,24,v,180,a,52
RT 170,224,12288
MG v,56,v,176,a,56
RT 172,224,20480
MG v,56,v,180,a,60
RT 174,224,28672
MG v,88,v,176,a,64
ds_read_b128 a[24:27], v227 offset:30720
s_mov_b32 m0, 27648
MG v,88,v,180,a,68
buffer_load_dwordx4 v226, s[52:55], s73 offen offset:0 lds
MG v,120,v,176,a,72
ds_read_b128 a[28:31], v227 offset:31232
MG v,120,v,180,a,76
s_waitcnt lgkmcnt(8)
MG v,28,v,160,a,48
RT 176,224,4104
MG v,28,v,164,a,52
RT 178,224,12296
MG v,60,v,160,a,56
RT 180,224,20488
MG v,60,v,164,a,60
RT 182,224,28680
MG v,92,v,160,a,64
ds_read_b128 a[32:35], v227 offset:32768
s_mov_b32 m0, 31744
MG v,92,v,164,a,68
buffer_load_dwordx4 v226, s[52:55], s74 offen offset:0 lds
MG v,124,v,160,a,72
ds_read_b128 a[36:39], v227 offset:33280
MG v,124,v,164,a,76
s_waitcnt lgkmcnt(8)
MG a,128,v,168,a,48
RT 160,224,4608
MG a,128,v,172,a,52
RT 162,224,12800
MG a,132,v,168,a,56
RT 164,224,20992
MG a,132,v,172,a,60
RT 166,224,29184
MG a,136,v,168,a,64
ds_read_b128 a[40:43], v227 offset:34816
s_mov_b32 m0, 35840
MG a,136,v,172,a,68
buffer_load_dwordx4 v226, s[52:55], s75 offen offset:0 lds
v_add_u32 v226, 0x80, v226
MG a,140,v,168,a,72
ds_read_b128 a[44:47], v227 offset:35328
MG a,140,v,172,a,76
s_waitcnt lgkmcnt(8)
MG a,144,v,176,a,48
RT 168,224,4616
MG a,144,v,180,a,52
RT 170,224,12808
MG a,148,v,176,a,56
RT 172,224,21000
MG a,148,v,180,a,60
RT 174,224,29192
MG a,152,v,176,a,64
MG a,152,v,180,a,68
MG a,156,v,176,a,72
s_waitcnt vmcnt(9) lgkmcnt(0)
MG a,156,v,180,a,76
s_barrier
MG a,160,v,160,a,48
RT 176,224,38912
MG a,160,v,164,a,52
RT 178,224,47104
MG a,164,v,160,a,56
RT 180,224,55296
MG a,164,v,164,a,60
RT 182,224,63488
s_mov_b32 m0, 107520
MG a,168,v,160,a,64
buffer_load_dwordx4 v225, s[48:51], s76 offen offset:0 lds
MG a,168,v,164,a,68
MG a,172,v,160,a,72
MG a,172,v,164,a,76
MG a,176,v,168,a,48
RT 160,224,38920
MG a,176,v,172,a,52
RT 162,224,47112
MG a,180,v,168,a,56
RT 164,224,55304
MG a,180,v,172,a,60
RT 166,224,63496
s_mov_b32 m0, 109440
MG a,184,v,168,a,64
buffer_load_dwordx4 v225, s[48:51], s76 offen offset:128 lds
MG a,184,v,172,a,68
MG a,188,v,168,a,72
MG a,188,v,172,a,76
s_waitcnt lgkmcnt(4)
MG a,192,v,176,a,0
RT 168,224,39424
MG a,192,v,180,a,4
RT 170,224,47616
MG a,196,v,176,a,8
RT 172,224,55808
MG a,196,v,180,a,12
RT 174,224,64000
s_mov_b32 m0, 111360
MG a,200,v,176,a,16
buffer_load_dwordx4 v225, s[48:51], s76 offen offset:256 lds
MG a,200,v,180,a,20
MG a,204,v,176,a,24
MG a,204,v,180,a,28
s_mov_b32 m0, 113280
MG v,128,v,176,a,32
buffer_load_dwordx4 v225, s[48:51], s76 offen offset:384 lds
s_add_u32 s76, s76, 262144
MG v,128,v,180,a,36
MG v,132,v,176,a,40
MG v,132,v,180,a,44
s_waitcnt lgkmcnt(4)
MG a,208,v,160,a,0
RT 176,224,39432
MG a,208,v,164,a,4
RT 178,224,47624
MG a,212,v,160,a,8
RT 180,224,55816
MG a,212,v,164,a,12
RT 182,224,64008
s_mov_b32 m0, 123904
MG a,216,v,160,a,16
buffer_load_dwordx4 v225, s[48:51], s76 offen offset:0 lds
MG a,216,v,164,a,20
MG a,220,v,160,a,24
MG a,220,v,164,a,28
s_mov_b32 m0, 125824
MG v,136,v,160,a,32
buffer_load_dwordx4 v225, s[48:51], s76 offen offset:128 lds
MG v,136,v,164,a,36
MG v,140,v,160,a,40
MG v,140,v,164,a,44
s_waitcnt lgkmcnt(4)
MG a,224,v,168,a,0
RT 160,224,32768
MG a,224,v,172,a,4
RT 162,224,40960
MG a,228,v,168,a,8
RT 164,224,49152
MG a,228,v,172,a,12
RT 166,224,57344
s_mov_b32 m0, 127744
MG a,232,v,168,a,16
buffer_load_dwordx4 v225, s[48:51], s76 offen offset:256 lds
MG a,232,v,172,a,20
MG a,236,v,168,a,24
MG a,236,v,172,a,28
s_mov_b32 m0, 129664
MG v,144,v,168,a,32
buffer_load_dwordx4 v225, s[48:51], s76 offen offset:384 lds
s_add_u32 s76, s76, 262144
MG v,144,v,172,a,36
MG v,148,v,168,a,40
MG v,148,v,172,a,44
s_waitcnt lgkmcnt(4)
MG a,240,v,176,a,0
RT 168,224,32776
MG a,240,v,180,a,4
RT 170,224,40968
MG a,244,v,176,a,8
RT 172,224,49160
MG a,244,v,180,a,12
RT 174,224,57352
s_mov_b32 m0, 39936
MG a,248,v,176,a,16
buffer_load_dwordx4 v226, s[52:55], s67 offen offset:0 lds
MG a,248,v,180,a,20
MG a,252,v,176,a,24
MG a,252,v,180,a,28
s_mov_b32 m0, 44032
MG v,152,v,176,a,32
buffer_load_dwordx4 v226, s[52:55], s68 offen offset:0 lds
MG v,152,v,180,a,36
MG v,156,v,176,a,40
MG v,156,v,180,a,44
s_waitcnt lgkmcnt(4)
MG v,0,v,160,a,0
RT 176,224,33280
MG v,0,v,164,a,4
RT 178,224,41472
MG v,32,v,160,a,8
RT 180,224,49664
MG v,32,v,164,a,12
RT 182,224,57856
s_mov_b32 m0, 48128
MG v,64,v,160,a,16
buffer_load_dwordx4 v226, s[52:55], s69 offen offset:0 lds
MG v,64,v,164,a,20
MG v,96,v,160,a,24
MG v,96,v,164,a,28
s_waitcnt lgkmcnt(4)
MG v,4,v,168,a,0
RT 160,224,33288
MG v,4,v,172,a,4
RT 162,224,41480
MG v,36,v,168,a,8
RT 164,224,49672
MG v,36,v,172,a,12
RT 166,224,57864
s_mov_b32 m0, 52224
MG v,68,v,168,a,16
buffer_load_dwordx4 v226, s[52:55], s70 offen offset:0 lds
MG v,68,v,172,a,20
MG v,100,v,168,a,24
MG v,100,v,172,a,28
s_waitcnt lgkmcnt(4)
MG v,8,v,176,a,0
RT 168,224,34816
MG v,8,v,180,a,4
RT 170,224,43008
MG v,40,v,176,a,8
RT 172,224,51200
MG v,40,v,180,a,12
RT 174,224,59392
s_mov_b32 m0, 56320
MG v,72,v,176,a,16
buffer_load_dwordx4 v226, s[52:55], s71 offen offset:0 lds
MG v,72,v,180,a,20
MG v,104,v,176,a,24
s_waitcnt vmcnt(13) lgkmcnt(4)
MG v,104,v,180,a,28
s_barrier
MG v,12,v,160,a,0
RT 176,224,34824
MG v,12,v,164,a,4
RT 178,224,43016
MG v,44,v,160,a,8
RT 180,224,51208
MG v,44,v,164,a,12
RT 182,224,59400
MG v,76,v,160,a,16
ds_read_b128 a[48:51], v249 offset:24576
s_mov_b32 m0, 60416
MG v,76,v,164,a,20
buffer_load_dwordx4 v226, s[52:55], s72 offen offset:0 lds
MG v,108,v,160,a,24
ds_read_b128 a[52:55], v249 offset:25088
MG v,108,v,164,a,28
s_waitcnt lgkmcnt(6)
MG v,16,v,168,a,0
RT 160,224,35328
MG v,16,v,172,a,4
RT 162,224,43520
MG v,48,v,168,a,8
RT 164,224,51712
MG v,48,v,172,a,12
RT 166,224,59904
MG v,80,v,168,a,16
ds_read_b128 a[56:59], v249 offset:26624
s_mov_b32 m0, 64512
MG v,80,v,172,a,20
buffer_load_dwordx4 v226, s[52:55], s73 offen offset:0 lds
MG v,112,v,168,a,24
ds_read_b128 a[60:63], v249 offset:27136
MG v,112,v,172,a,28
s_waitcnt lgkmcnt(8)
MG v,20,v,176,a,0
RT 168,224,35336
MG v,20,v,180,a,4
RT 170,224,43528
MG v,52,v,176,a,8
RT 172,224,51720
MG v,52,v,180,a,12
RT 174,224,59912
MG v,84,v,176,a,16
ds_read_b128 a[64:67], v249 offset:28672
s_mov_b32 m0, 68608
MG v,84,v,180,a,20
buffer_load_dwordx4 v226, s[52:55], s74 offen offset:0 lds
MG v,116,v,176,a,24
ds_read_b128 a[68:71], v249 offset:29184
MG v,116,v,180,a,28
s_waitcnt lgkmcnt(8)
MG v,24,v,160,a,0
RT 176,224,36864
MG v,24,v,164,a,4
RT 178,224,45056
MG v,56,v,160,a,8
RT 180,224,53248
MG v,56,v,164,a,12
RT 182,224,61440
MG v,88,v,160,a,16
ds_read_b128 a[72:75], v249 offset:30720
s_mov_b32 m0, 72704
MG v,88,v,164,a,20
buffer_load_dwordx4 v226, s[52:55], s75 offen offset:0 lds
v_add_u32 v226, 0x80, v226
MG v,120,v,160,a,24
ds_read_b128 a[76:79], v249 offset:31232
MG v,120,v,164,a,28
s_waitcnt lgkmcnt(8)
MG v,28,v,168,a,0
RT 160,224,36872
MG v,28,v,172,a,4
RT 162,224,45064
MG v,60,v,168,a,8
RT 164,224,53256
MG v,60,v,172,a,12
RT 166,224,61448
MG v,92,v,168,a,16
ds_read_b128 a[80:83], v249 offset:32768
MG v,92,v,172,a,20
MG v,124,v,168,a,24
ds_read_b128 a[84:87], v249 offset:33280
MG v,124,v,172,a,28
s_waitcnt lgkmcnt(8)
MG a,128,v,176,a,0
RT 168,224,37376
MG a,128,v,180,a,4
RT 170,224,45568
MG a,132,v,176,a,8
RT 172,224,53760
MG a,132,v,180,a,12
RT 174,224,61952
MG a,136,v,176,a,16
ds_read_b128 a[88:91], v249 offset:34816
MG a,136,v,180,a,20
MG a,140,v,176,a,24
ds_read_b128 a[92:95], v249 offset:35328
MG a,140,v,180,a,28
s_waitcnt lgkmcnt(8)
MG a,144,v,160,a,0
RT 176,224,37384
MG a,144,v,164,a,4
RT 178,224,45576
MG a,148,v,160,a,8
RT 180,224,53768
MG a,148,v,164,a,12
RT 182,224,61960
MG a,152,v,160,a,16
MG a,152,v,164,a,20
MG a,156,v,160,a,24
s_waitcnt vmcnt(9) lgkmcnt(0)
MG a,156,v,164,a,28
s_barrier
MG a,160,v,168,a,0
RT 160,224,6144
MG a,160,v,172,a,4
RT 162,224,14336
MG a,164,v,168,a,8
RT 164,224,22528
MG a,164,v,172,a,12
RT 166,224,30720
s_mov_b32 m0, 140288
MG a,168,v,168,a,16
buffer_load_dwordx4 v225, s[48:51], s76 offen offset:0 lds
MG a,168,v,172,a,20
MG a,172,v,168,a,24
MG a,172,v,172,a,28
MG a,176,v,176,a,0
RT 168,224,6152
MG a,176,v,180,a,4
RT 170,224,14344
MG a,180,v,176,a,8
RT 172,224,22536
MG a,180,v,180,a,12
RT 174,224,30728
s_mov_b32 m0, 142208
MG a,184,v,176,a,16
buffer_load_dwordx4 v225, s[48:51], s76 offen offset:128 lds
MG a,184,v,180,a,20
MG a,188,v,176,a,24
MG a,188,v,180,a,28
s_waitcnt lgkmcnt(4)
MG a,192,v,160,a,48
RT 176,224,6656
MG a,192,v,164,a,52
RT 178,224,14848
MG a,196,v,160,a,56
RT 180,224,23040
MG a,196,v,164,a,60
RT 182,224,31232
s_mov_b32 m0, 144128
MG a,200,v,160,a,64
buffer_load_dwordx4 v225, s[48:51], s76 offen offset:256 lds
MG a,200,v,164,a,68
MG a,204,v,160,a,72
MG a,204,v,164,a,76
s_mov_b32 m0, 146048
MG v,128,v,160,a,80
buffer_load_dwordx4 v225, s[48:51], s76 offen offset:384 lds
s_add_u32 s76, s76, 262144
MG v,128,v,164,a,84
MG v,132,v,160,a,88
MG v,132,v,164,a,92
s_waitcnt lgkmcnt(4)
MG a,208,v,168,a,48
RT 160,224,6664
MG a,208,v,172,a,52
RT 162,224,14856
MG a,212,v,168,a,56
RT 164,224,23048
MG a,212,v,172,a,60
RT 166,224,31240
s_mov_b32 m0, 156672
MG a,216,v,168,a,64
buffer_load_dwordx4 v225, s[48:51], s76 offen offset:0 lds
MG a,216,v,172,a,68
MG a,220,v,168,a,72
MG a,220,v,172,a,76
s_mov_b32 m0, 158592
MG v,136,v,168,a,80
buffer_load_dwordx4 v225, s[48:51], s76 offen offset:128 lds
MG v,136,v,172,a,84
MG v,140,v,168,a,88
MG v,140,v,172,a,92
s_waitcnt lgkmcnt(4)
MG a,224,v,176,a,48
RT 168,224,0
MG a,224,v,180,a,52
RT 170,224,8192
MG a,228,v,176,a,56
RT 172,224,16384
MG a,228,v,180,a,60
RT 174,224,24576
s_mov_b32 m0, 160512
MG a,232,v,176,a,64
buffer_load_dwordx4 v225, s[48:51], s76 offen offset:256 lds
MG a,232,v,180,a,68
MG a,236,v,176,a,72
MG a,236,v,180,a,76
s_mov_b32 m0, 162432
MG v,144,v,176,a,80
buffer_load_dwordx4 v225, s[48:51], s76 offen offset:384 lds
s_add_u32 s76, s76, 262144
MG v,144,v,180,a,84
MG v,148,v,176,a,88
MG v,148,v,180,a,92
s_waitcnt lgkmcnt(4)
MG a,240,v,160,a,48
RT 176,224,8
MG a,240,v,164,a,52
RT 178,224,8200
MG a,244,v,160,a,56
RT 180,224,16392
MG a,244,v,164,a,60
RT 182,224,24584
s_mov_b32 m0, 3072
MG a,248,v,160,a,64
buffer_load_dwordx4 v226, s[52:55], s67 offen offset:0 lds
MG a,248,v,164,a,68
MG a,252,v,160,a,72
MG a,252,v,164,a,76
s_mov_b32 m0, 7168
MG v,152,v,160,a,80
buffer_load_dwordx4 v226, s[52:55], s68 offen offset:0 lds
MG v,152,v,164,a,84
MG v,156,v,160,a,88
MG v,156,v,164,a,92
s_waitcnt lgkmcnt(4)
MG v,0,v,168,a,48
RT 160,224,512
MG v,0,v,172,a,52
RT 162,224,8704
MG v,32,v,168,a,56
RT 164,224,16896
MG v,32,v,172,a,60
RT 166,224,25088
s_mov_b32 m0, 11264
MG v,64,v,168,a,64
buffer_load_dwordx4 v226, s[52:55], s69 offen offset:0 lds
MG v,64,v,172,a,68
MG v,96,v,168,a,72
MG v,96,v,172,a,76
s_waitcnt lgkmcnt(4)
MG v,4,v,176,a,48
RT 168,224,520
MG v,4,v,180,a,52
RT 170,224,8712
MG v,36,v,176,a,56
RT 172,224,16904
MG v,36,v,180,a,60
RT 174,224,25096
s_mov_b32 m0, 15360
MG v,68,v,176,a,64
buffer_load_dwordx4 v226, s[52:55], s70 offen offset:0 lds
MG v,68,v,180,a,68
MG v,100,v,176,a,72
MG v,100,v,180,a,76
s_waitcnt lgkmcnt(4)
MG v,8,v,160,a,48
RT 176,224,2048
MG v,8,v,164,a,52
RT 178,224,10240
MG v,40,v,160,a,56
RT 180,224,18432
MG v,40,v,164,a,60
RT 182,224,26624
s_mov_b32 m0, 19456
MG v,72,v,160,a,64
buffer_load_dwordx4 v226, s[52:55], s71 offen offset:0 lds
MG v,72,v,164,a,68
MG v,104,v,160,a,72
s_waitcnt vmcnt(13) lgkmcnt(4)
MG v,104,v,164,a,76
s_barrier
MG v,12,v,168,a,48
RT 160,224,2056
MG v,12,v,172,a,52
RT 162,224,10248
MG v,44,v,168,a,56
RT 164,224,18440
MG v,44,v,172,a,60
RT 166,224,26632
MG v,76,v,168,a,64
ds_read_b128 a[0:3], v227 offset:24576
s_mov_b32 m0, 23552
MG v,76,v,172,a,68
buffer_load_dwordx4 v226, s[52:55], s72 offen offset:0 lds
MG v,108,v,168,a,72
ds_read_b128 a[4:7], v227 offset:25088
MG v,108,v,172,a,76
s_waitcnt lgkmcnt(6)
MG v,16,v,176,a,48
RT 168,224,2560
MG v,16,v,180,a,52
RT 170,224,10752
MG v,48,v,176,a,56
RT 172,224,18944
MG v,48,v,180,a,60
RT 174,224,27136
MG v,80,v,176,a,64
ds_read_b128 a[8:11], v227 offset:26624
s_mov_b32 m0, 27648
MG v,80,v,180,a,68
buffer_load_dwordx4 v226, s[52:55], s73 offen offset:0 lds
MG v,112,v,176,a,72
ds_read_b128 a[12:15], v227 offset:27136
MG v,112,v,180,a,76
s_waitcnt lgkmcnt(8)
MG v,20,v,160,a,48
RT 176,224,2568
MG v,20,v,164,a,52
RT 178,224,10760
MG v,52,v,160,a,56
RT 180,224,18952
MG v,52,v,164,a,60
RT 182,224,27144
MG v,84,v,160,a,64
ds_read_b128 a[16:19], v227 offset:28672
s_mov_b32 m0, 31744
MG v,84,v,164,a,68
buffer_load_dwordx4 v226, s[52:55], s74 offen offset:0 lds
MG v,116,v,160,a,72
ds_read_b128 a[20:23], v227 offset:29184
MG v,116,v,164,a,76
s_waitcnt lgkmcnt(8)
MG v,24,v,168,a,48
RT 160,224,4096
MG v,24,v,172,a,52
RT 162,224,12288
MG v,56,v,168,a,56
RT 164,224,20480
MG v,56,v,172,a,60
RT 166,224,28672
MG v,88,v,168,a,64
ds_read_b128 a[24:27], v227 offset:30720
s_mov_b32 m0, 35840
MG v,88,v,172,a,68
buffer_load_dwordx4 v226, s[52:55], s75 offen offset:0 lds
v_add_u32 v226, 0x80, v226
MG v,120,v,168,a,72
ds_read_b128 a[28:31], v227 offset:31232
MG v,120,v,172,a,76
s_waitcnt lgkmcnt(8)
MG v,28,v,176,a,48
RT 168,224,4104
MG v,28,v,180,a,52
RT 170,224,12296
MG v,60,v,176,a,56
RT 172,224,20488
MG v,60,v,180,a,60
RT 174,224,28680
MG v,92,v,176,a,64
ds_read_b128 a[32:35], v227 offset:32768
MG v,92,v,180,a,68
MG v,124,v,176,a,72
ds_read_b128 a[36:39], v227 offset:33280
MG v,124,v,180,a,76
s_waitcnt lgkmcnt(8)
MG a,128,v,160,a,48
RT 176,224,4608
MG a,128,v,164,a,52
RT 178,224,12800
MG a,132,v,160,a,56
RT 180,224,20992
MG a,132,v,164,a,60
RT 182,224,29184
MG a,136,v,160,a,64
ds_read_b128 a[40:43], v227 offset:34816
MG a,136,v,164,a,68
MG a,140,v,160,a,72
ds_read_b128 a[44:47], v227 offset:35328
MG a,140,v,164,a,76
s_waitcnt lgkmcnt(8)
MG a,144,v,168,a,48
RT 160,224,4616
MG a,144,v,172,a,52
RT 162,224,12808
MG a,148,v,168,a,56
RT 164,224,21000
MG a,148,v,172,a,60
RT 166,224,29192
MG a,152,v,168,a,64
MG a,152,v,172,a,68
MG a,156,v,168,a,72
s_waitcnt vmcnt(9) lgkmcnt(0)
MG a,156,v,172,a,76
s_barrier
MG a,160,v,176,a,48
RT 168,224,38912
MG a,160,v,180,a,52
RT 170,224,47104
MG a,164,v,176,a,56
RT 172,224,55296
MG a,164,v,180,a,60
RT 174,224,63488
s_mov_b32 m0, 107520
MG a,168,v,176,a,64
buffer_load_dwordx4 v225, s[48:51], s76 offen offset:0 lds
MG a,168,v,180,a,68
MG a,172,v,176,a,72
MG a,172,v,180,a,76
MG a,176,v,160,a,48
RT 176,224,38920
MG a,176,v,164,a,52
RT 178,224,47112
MG a,180,v,160,a,56
RT 180,224,55304
MG a,180,v,164,a,60
RT 182,224,63496
s_mov_b32 m0, 109440
MG a,184,v,160,a,64
buffer_load_dwordx4 v225, s[48:51], s76 offen offset:128 lds
MG a,184,v,164,a,68
MG a,188,v,160,a,72
MG a,188,v,164,a,76
s_waitcnt lgkmcnt(4)
MG a,192,v,168,a,0
RT 160,224,39424
MG a,192,v,172,a,4
RT 162,224,47616
MG a,196,v,168,a,8
RT 164,224,55808
MG a,196,v,172,a,12
RT 166,224,64000
s_mov_b32 m0, 111360
MG a,200,v,168,a,16
buffer_load_dwordx4 v225, s[48:51], s76 offen offset:256 lds
MG a,200,v,172,a,20
MG a,204,v,168,a,24
MG a,204,v,172,a,28
s_mov_b32 m0, 113280
MG v,128,v,168,a,32
buffer_load_dwordx4 v225, s[48:51], s76 offen offset:384 lds
s_add_u32 s76, s76, 262144
MG v,128,v,172,a,36
MG v,132,v,168,a,40
MG v,132,v,172,a,44
s_waitcnt lgkmcnt(4)
MG a,208,v,176,a,0
RT 168,224,39432
MG a,208,v,180,a,4
RT 170,224,47624
MG a,212,v,176,a,8
RT 172,224,55816
MG a,212,v,180,a,12
RT 174,224,64008
s_mov_b32 m0, 123904
MG a,216,v,176,a,16
buffer_load_dwordx4 v225, s[48:51], s76 offen offset:0 lds
MG a,216,v,180,a,20
MG a,220,v,176,a,24
MG a,220,v,180,a,28
s_mov_b32 m0, 125824
MG v,136,v,176,a,32
buffer_load_dwordx4 v225, s[48:51], s76 offen offset:128 lds
MG v,136,v,180,a,36
MG v,140,v,176,a,40
MG v,140,v,180,a,44
s_waitcnt lgkmcnt(4)
MG a,224,v,160,a,0
RT 176,224,32768
MG a,224,v,164,a,4
RT 178,224,40960
MG a,228,v,160,a,8
RT 180,224,49152
MG a,228,v,164,a,12
RT 182,224,57344
s_mov_b32 m0, 127744
MG a,232,v,160,a,16
buffer_load_dwordx4 v225, s[48:51], s76 offen offset:256 lds
MG a,232,v,164,a,20
MG a,236,v,160,a,24
MG a,236,v,164,a,28
s_mov_b32 m0, 129664
MG v,144,v,160,a,32
buffer_load_dwordx4 v225, s[48:51], s76 offen offset:384 lds
s_add_u32 s76, s76, 262144
MG v,144,v,164,a,36
MG v,148,v,160,a,40
MG v,148,v,164,a,44
s_waitcnt lgkmcnt(4)
MG a,240,v,168,a,0
RT 160,224,32776
MG a,240,v,172,a,4
RT 162,224,40968
MG a,244,v,168,a,8
RT 164,224,49160
MG a,244,v,172,a,12
RT 166,224,57352
s_mov_b32 m0, 39936
MG a,248,v,168,a,16
buffer_load_dwordx4 v226, s[52:55], s67 offen offset:0 lds
MG a,248,v,172,a,20
MG a,252,v,168,a,24
MG a,252,v,172,a,28
s_mov_b32 m0, 44032
MG v,152,v,168,a,32
buffer_load_dwordx4 v226, s[52:55], s68 offen offset:0 lds
MG v,152,v,172,a,36
MG v,156,v,168,a,40
MG v,156,v,172,a,44
s_waitcnt lgkmcnt(4)
MG v,0,v,176,a,0
RT 168,224,33280
MG v,0,v,180,a,4
RT 170,224,41472
MG v,32,v,176,a,8
RT 172,224,49664
MG v,32,v,180,a,12
RT 174,224,57856
s_mov_b32 m0, 48128
MG v,64,v,176,a,16
buffer_load_dwordx4 v226, s[52:55], s69 offen offset:0 lds
MG v,64,v,180,a,20
MG v,96,v,176,a,24
MG v,96,v,180,a,28
s_waitcnt lgkmcnt(4)
MG v,4,v,160,a,0
RT 176,224,33288
MG v,4,v,164,a,4
RT 178,224,41480
MG v,36,v,160,a,8
RT 180,224,49672
MG v,36,v,164,a,12
RT 182,224,57864
s_mov_b32 m0, 52224
MG v,68,v,160,a,16
buffer_load_dwordx4 v226, s[52:55], s70 offen offset:0 lds
MG v,68,v,164,a,20
MG v,100,v,160,a,24
MG v,100,v,164,a,28
s_waitcnt lgkmcnt(4)
MG v,8,v,168,a,0
RT 160,224,34816
MG v,8,v,172,a,4
RT 162,224,43008
MG v,40,v,168,a,8
RT 164,224,51200
MG v,40,v,172,a,12
RT 166,224,59392
s_mov_b32 m0, 56320
MG v,72,v,168,a,16
buffer_load_dwordx4 v226, s[52:55], s71 offen offset:0 lds
MG v,72,v,172,a,20
MG v,104,v,168,a,24
s_waitcnt vmcnt(13) lgkmcnt(4)
MG v,104,v,172,a,28
s_barrier
MG v,12,v,176,a,0
RT 168,224,34824
MG v,12,v,180,a,4
RT 170,224,43016
MG v,44,v,176,a,8
RT 172,224,51208
MG v,44,v,180,a,12
RT 174,224,59400
MG v,76,v,176,a,16
ds_read_b128 a[48:51], v249 offset:24576
s_mov_b32 m0, 60416
MG v,76,v,180,a,20
buffer_load_dwordx4 v226, s[52:55], s72 offen offset:0 lds
MG v,108,v,176,a,24
ds_read_b128 a[52:55], v249 offset:25088
MG v,108,v,180,a,28
s_waitcnt lgkmcnt(6)
MG v,16,v,160,a,0
RT 176,224,35328
MG v,16,v,164,a,4
RT 178,224,43520
MG v,48,v,160,a,8
RT 180,224,51712
MG v,48,v,164,a,12
RT 182,224,59904
MG v,80,v,160,a,16
ds_read_b128 a[56:59], v249 offset:26624
s_mov_b32 m0, 64512
MG v,80,v,164,a,20
buffer_load_dwordx4 v226, s[52:55], s73 offen offset:0 lds
MG v,112,v,160,a,24
ds_read_b128 a[60:63], v249 offset:27136
MG v,112,v,164,a,28
s_waitcnt lgkmcnt(8)
MG v,20,v,168,a,0
RT 160,224,35336
MG v,20,v,172,a,4
RT 162,224,43528
MG v,52,v,168,a,8
RT 164,224,51720
MG v,52,v,172,a,12
RT 166,224,59912
MG v,84,v,168,a,16
ds_read_b128 a[64:67], v249 offset:28672
s_mov_b32 m0, 68608
MG v,84,v,172,a,20
buffer_load_dwordx4 v226, s[52:55], s74 offen offset:0 lds
MG v,116,v,168,a,24
ds_read_b128 a[68:71], v249 offset:29184
MG v,116,v,172,a,28
s_waitcnt lgkmcnt(8)
MG v,24,v,176,a,0
RT 168,224,36864
MG v,24,v,180,a,4
RT 170,224,45056
MG v,56,v,176,a,8
RT 172,224,53248
MG v,56,v,180,a,12
RT 174,224,61440
MG v,88,v,176,a,16
ds_read_b128 a[72:75], v249 offset:30720
s_mov_b32 m0, 72704
MG v,88,v,180,a,20
buffer_load_dwordx4 v226, s[52:55], s75 offen offset:0 lds
v_add_u32 v226, 0x80, v226
MG v,120,v,176,a,24
ds_read_b128 a[76:79], v249 offset:31232
MG v,120,v,180,a,28
s_waitcnt lgkmcnt(8)
MG v,28,v,160,a,0
RT 176,224,36872
MG v,28,v,164,a,4
RT 178,224,45064
MG v,60,v,160,a,8
RT 180,224,53256
MG v,60,v,164,a,12
RT 182,224,61448
MG v,92,v,160,a,16
ds_read_b128 a[80:83], v249 offset:32768
MG v,92,v,164,a,20
MG v,124,v,160,a,24
ds_read_b128 a[84:87], v249 offset:33280
MG v,124,v,164,a,28
s_waitcnt lgkmcnt(8)
MG a,128,v,168,a,0
RT 160,224,37376
MG a,128,v,172,a,4
RT 162,224,45568
MG a,132,v,168,a,8
RT 164,224,53760
MG a,132,v,172,a,12
RT 166,224,61952
MG a,136,v,168,a,16
ds_read_b128 a[88:91], v249 offset:34816
MG a,136,v,172,a,20
MG a,140,v,168,a,24
ds_read_b128 a[92:95], v249 offset:35328
MG a,140,v,172,a,28
s_waitcnt lgkmcnt(8)
MG a,144,v,176,a,0
RT 168,224,37384
MG a,144,v,180,a,4
RT 170,224,45576
MG a,148,v,176,a,8
RT 172,224,53768
MG a,148,v,180,a,12
RT 174,224,61960
MG a,152,v,176,a,16
MG a,152,v,180,a,20
MG a,156,v,176,a,24
s_waitcnt vmcnt(9) lgkmcnt(0)
MG a,156,v,180,a,28
s_barrier
MG a,160,v,160,a,0
RT 176,224,6144
MG a,160,v,164,a,4
RT 178,224,14336
MG a,164,v,160,a,8
RT 180,224,22528
MG a,164,v,164,a,12
RT 182,224,30720
s_mov_b32 m0, 140288
MG a,168,v,160,a,16
buffer_load_dwordx4 v225, s[48:51], s76 offen offset:0 lds
MG a,168,v,164,a,20
MG a,172,v,160,a,24
MG a,172,v,164,a,28
MG a,176,v,168,a,0
RT 160,224,6152
MG a,176,v,172,a,4
RT 162,224,14344
MG a,180,v,168,a,8
RT 164,224,22536
MG a,180,v,172,a,12
RT 166,224,30728
s_mov_b32 m0, 142208
MG a,184,v,168,a,16
buffer_load_dwordx4 v225, s[48:51], s76 offen offset:128 lds
MG a,184,v,172,a,20
MG a,188,v,168,a,24
MG a,188,v,172,a,28
s_waitcnt lgkmcnt(4)
MG a,192,v,176,a,48
RT 168,224,6656
MG a,192,v,180,a,52
RT 170,224,14848
MG a,196,v,176,a,56
RT 172,224,23040
MG a,196,v,180,a,60
RT 174,224,31232
s_mov_b32 m0, 144128
MG a,200,v,176,a,64
buffer_load_dwordx4 v225, s[48:51], s76 offen offset:256 lds
MG a,200,v,180,a,68
MG a,204,v,176,a,72
MG a,204,v,180,a,76
s_mov_b32 m0, 146048
MG v,128,v,176,a,80
buffer_load_dwordx4 v225, s[48:51], s76 offen offset:384 lds
s_add_u32 s76, s76, 262144
MG v,128,v,180,a,84
MG v,132,v,176,a,88
MG v,132,v,180,a,92
s_waitcnt lgkmcnt(4)
MG a,208,v,160,a,48
RT 176,224,6664
MG a,208,v,164,a,52
RT 178,224,14856
MG a,212,v,160,a,56
RT 180,224,23048
MG a,212,v,164,a,60
RT 182,224,31240
s_mov_b32 m0, 156672
MG a,216,v,160,a,64
buffer_load_dwordx4 v225, s[48:51], s76 offen offset:0 lds
MG a,216,v,164,a,68
MG a,220,v,160,a,72
MG a,220,v,164,a,76
s_mov_b32 m0, 158592
MG v,136,v,160,a,80
buffer_load_dwordx4 v225, s[48:51], s76 offen offset:128 lds
MG v,136,v,164,a,84
MG v,140,v,160,a,88
MG v,140,v,164,a,92
s_waitcnt lgkmcnt(4)
MG a,224,v,168,a,48
RT 160,224,0
MG a,224,v,172,a,52
RT 162,224,8192
MG a,228,v,168,a,56
RT 164,224,16384
MG a,228,v,172,a,60
RT 166,224,24576
s_mov_b32 m0, 160512
MG a,232,v,168,a,64
buffer_load_dwordx4 v225, s[48:51], s76 offen offset:256 lds
MG a,232,v,172,a,68
MG a,236,v,168,a,72
MG a,236,v,172,a,76
s_mov_b32 m0, 162432
MG v,144,v,168,a,80
buffer_load_dwordx4 v225, s[48:51], s76 offen offset:384 lds
s_add_u32 s76, s76, 262144
MG v,144,v,172,a,84
MG v,148,v,168,a,88
MG v,148,v,172,a,92
s_waitcnt lgkmcnt(4)
MG a,240,v,176,a,48
RT 168,224,8
MG a,240,v,180,a,52
RT 170,224,8200
MG a,244,v,176,a,56
RT 172,224,16392
MG a,244,v,180,a,60
RT 174,224,24584
s_mov_b32 m0, 9216
MG a,248,v,176,a,64
buffer_load_dwordx4 v253, s[40:43], s60 offen offset:0 nt lds
MG a,248,v,180,a,68
MG a,252,v,176,a,72
MG a,252,v,180,a,76
s_mov_b32 m0, 11136
MG v,152,v,176,a,80
buffer_load_dwordx4 v253, s[40:43], s60 offen offset:128 nt lds
MG v,152,v,180,a,84
MG v,156,v,176,a,88
MG v,156,v,180,a,92
s_waitcnt lgkmcnt(4)
MG v,0,v,160,a,48
RT 176,224,512
MG v,0,v,164,a,52
RT 178,224,8704
MG v,32,v,160,a,56
RT 180,224,16896
MG v,32,v,164,a,60
RT 182,224,25088
s_mov_b32 m0, 13056
MG v,64,v,160,a,64
buffer_load_dwordx4 v253, s[40:43], s60 offen offset:256 nt lds
MG v,64,v,164,a,68
MG v,96,v,160,a,72
MG v,96,v,164,a,76
s_waitcnt lgkmcnt(4)
MG v,4,v,168,a,48
RT 160,224,520
MG v,4,v,172,a,52
RT 162,224,8712
MG v,36,v,168,a,56
RT 164,224,16904
MG v,36,v,172,a,60
RT 166,224,25096
s_mov_b32 m0, 14976
MG v,68,v,168,a,64
buffer_load_dwordx4 v253, s[40:43], s60 offen offset:384 nt lds
s_add_u32 s60, s60, 16384
MG v,68,v,172,a,68
MG v,100,v,168,a,72
MG v,100,v,172,a,76
s_waitcnt lgkmcnt(4)
MG v,8,v,176,a,48
RT 168,224,2048
MG v,8,v,180,a,52
RT 170,224,10240
MG v,40,v,176,a,56
RT 172,224,18432
MG v,40,v,180,a,60
RT 174,224,26624
s_mov_b32 m0, 25600
MG v,72,v,176,a,64
buffer_load_dwordx4 v253, s[40:43], s60 offen offset:0 nt lds
MG v,72,v,180,a,68
MG v,104,v,176,a,72
s_waitcnt vmcnt(13) lgkmcnt(4)
MG v,104,v,180,a,76
s_barrier
MG v,12,v,160,a,48
RT 176,224,2056
MG v,12,v,164,a,52
RT 178,224,10248
MG v,44,v,160,a,56
RT 180,224,18440
MG v,44,v,164,a,60
RT 182,224,26632
MG v,76,v,160,a,64
ds_read_b128 a[0:3], v227 offset:24576
s_mov_b32 m0, 27520
MG v,76,v,164,a,68
buffer_load_dwordx4 v253, s[40:43], s60 offen offset:128 nt lds
MG v,108,v,160,a,72
ds_read_b128 a[4:7], v227 offset:25088
MG v,108,v,164,a,76
s_waitcnt lgkmcnt(6)
MG v,16,v,168,a,48
RT 160,224,2560
MG v,16,v,172,a,52
RT 162,224,10752
MG v,48,v,168,a,56
RT 164,224,18944
MG v,48,v,172,a,60
RT 166,224,27136
MG v,80,v,168,a,64
ds_read_b128 a[8:11], v227 offset:26624
s_mov_b32 m0, 29440
MG v,80,v,172,a,68
buffer_load_dwordx4 v253, s[40:43], s60 offen offset:256 nt lds
MG v,112,v,168,a,72
ds_read_b128 a[12:15], v227 offset:27136
MG v,112,v,172,a,76
s_waitcnt lgkmcnt(8)
MG v,20,v,176,a,48
RT 168,224,2568
MG v,20,v,180,a,52
RT 170,224,10760
MG v,52,v,176,a,56
RT 172,224,18952
MG v,52,v,180,a,60
RT 174,224,27144
MG v,84,v,176,a,64
ds_read_b128 a[16:19], v227 offset:28672
s_mov_b32 m0, 31360
MG v,84,v,180,a,68
buffer_load_dwordx4 v253, s[40:43], s60 offen offset:384 nt lds
s_add_u32 s60, s60, 16384
MG v,116,v,176,a,72
ds_read_b128 a[20:23], v227 offset:29184
MG v,116,v,180,a,76
s_waitcnt lgkmcnt(8)
MG v,24,v,160,a,48
RT 176,224,4096
MG v,24,v,164,a,52
RT 178,224,12288
MG v,56,v,160,a,56
RT 180,224,20480
MG v,56,v,164,a,60
RT 182,224,28672
MG v,88,v,160,a,64
ds_read_b128 a[24:27], v227 offset:30720
MG v,88,v,164,a,68
MG v,120,v,160,a,72
ds_read_b128 a[28:31], v227 offset:31232
MG v,120,v,164,a,76
s_waitcnt lgkmcnt(8)
MG v,28,v,168,a,48
RT 160,224,4104
MG v,28,v,172,a,52
RT 162,224,12296
MG v,60,v,168,a,56
RT 164,224,20488
MG v,60,v,172,a,60
RT 166,224,28680
MG v,92,v,168,a,64
ds_read_b128 a[32:35], v227 offset:32768
MG v,92,v,172,a,68
MG v,124,v,168,a,72
ds_read_b128 a[36:39], v227 offset:33280
MG v,124,v,172,a,76
s_waitcnt lgkmcnt(8)
MG a,128,v,176,a,48
RT 168,224,4608
MG a,128,v,180,a,52
RT 170,224,12800
MG a,132,v,176,a,56
RT 172,224,20992
MG a,132,v,180,a,60
RT 174,224,29184
MG a,136,v,176,a,64
ds_read_b128 a[40:43], v227 offset:34816
MG a,136,v,180,a,68
MG a,140,v,176,a,72
ds_read_b128 a[44:47], v227 offset:35328
MG a,140,v,180,a,76
s_waitcnt lgkmcnt(8)
MG a,144,v,160,a,48
RT 176,224,4616
MG a,144,v,164,a,52
RT 178,224,12808
MG a,148,v,160,a,56
RT 180,224,21000
MG a,148,v,164,a,60
RT 182,224,29192
MG a,152,v,160,a,64
MG a,152,v,164,a,68
MG a,156,v,160,a,72
s_waitcnt vmcnt(8) lgkmcnt(0)
MG a,156,v,164,a,76
s_barrier
MG a,160,v,168,a,48
RT 160,224,38912
MG a,160,v,172,a,52
RT 162,224,47104
MG a,164,v,168,a,56
RT 164,224,55296
MG a,164,v,172,a,60
RT 166,224,63488
s_mov_b32 m0, 107520
MG a,168,v,168,a,64
buffer_load_dwordx4 v253, s[44:47], s61 offen offset:0 nt lds
MG a,168,v,172,a,68
MG a,172,v,168,a,72
MG a,172,v,172,a,76
MG a,176,v,176,a,48
RT 168,224,38920
MG a,176,v,180,a,52
RT 170,224,47112
MG a,180,v,176,a,56
RT 172,224,55304
MG a,180,v,180,a,60
RT 174,224,63496
s_mov_b32 m0, 109440
MG a,184,v,176,a,64
buffer_load_dwordx4 v253, s[44:47], s61 offen offset:128 nt lds
MG a,184,v,180,a,68
MG a,188,v,176,a,72
MG a,188,v,180,a,76
s_waitcnt lgkmcnt(4)
MG a,192,v,160,a,0
RT 176,224,39424
MG a,192,v,164,a,4
RT 178,224,47616
MG a,196,v,160,a,8
RT 180,224,55808
MG a,196,v,164,a,12
RT 182,224,64000
s_mov_b32 m0, 111360
MG a,200,v,160,a,16
buffer_load_dwordx4 v253, s[44:47], s61 offen offset:256 nt lds
MG a,200,v,164,a,20
MG a,204,v,160,a,24
MG a,204,v,164,a,28
MG v,128,v,160,a,32
MG v,128,v,164,a,36
MG v,132,v,160,a,40
MG v,132,v,164,a,44
s_waitcnt lgkmcnt(4)
MG a,208,v,168,a,0
RT 160,224,39432
MG a,208,v,172,a,4
RT 162,224,47624
MG a,212,v,168,a,8
RT 164,224,55816
MG a,212,v,172,a,12
RT 166,224,64008
s_mov_b32 m0, 113280
MG a,216,v,168,a,16
buffer_load_dwordx4 v253, s[44:47], s61 offen offset:384 nt lds
s_add_u32 s61, s61, 16384
MG a,216,v,172,a,20
MG a,220,v,168,a,24
MG a,220,v,172,a,28
MG v,136,v,168,a,32
MG v,136,v,172,a,36
MG v,140,v,168,a,40
MG v,140,v,172,a,44
s_waitcnt lgkmcnt(4)
MG a,224,v,176,a,0
RT 168,224,32768
MG a,224,v,180,a,4
RT 170,224,40960
MG a,228,v,176,a,8
RT 172,224,49152
MG a,228,v,180,a,12
RT 174,224,57344
s_mov_b32 m0, 41984
MG a,232,v,176,a,16
buffer_load_dwordx4 v253, s[40:43], s60 offen offset:0 nt lds
MG a,232,v,180,a,20
CV 128,128,129
MG a,236,v,176,a,24
CV 129,130,131
CV 130,136,137
MG a,236,v,180,a,28
CV 131,138,139
MG v,144,v,176,a,32
ds_write_b128 v252, v[128:131] offset:6144
MG v,144,v,180,a,36
CV 132,132,133
CV 133,134,135
MG v,148,v,176,a,40
CV 134,140,141
MG v,148,v,180,a,44
s_waitcnt lgkmcnt(5)
CV 135,142,143
MG a,240,v,160,a,0
RT 176,224,32776
MG a,240,v,164,a,4
RT 178,224,40968
MG a,244,v,160,a,8
RT 180,224,49160
MG a,244,v,164,a,12
RT 182,224,57352
s_mov_b32 m0, 43904
MG a,248,v,160,a,16
buffer_load_dwordx4 v253, s[40:43], s60 offen offset:128 nt lds
MG a,248,v,164,a,20
ds_write_b128 v252, v[132:135] offset:14336
MG a,252,v,160,a,24
MG a,252,v,164,a,28
MG v,152,v,160,a,32
MG v,152,v,164,a,36
MG v,156,v,160,a,40
MG v,156,v,164,a,44
s_waitcnt lgkmcnt(6)
MG v,0,v,168,a,0
RT 160,224,33280
MG v,0,v,172,a,4
RT 162,224,41472
MG v,32,v,168,a,8
RT 164,224,49664
MG v,32,v,172,a,12
RT 166,224,57856
s_mov_b32 m0, 45824
MG v,64,v,168,a,16
buffer_load_dwordx4 v253, s[40:43], s60 offen offset:256 nt lds
MG v,64,v,172,a,20
CV 144,144,145
CV 145,146,147
MG v,96,v,168,a,24
CV 146,152,153
MG v,96,v,172,a,28
s_waitcnt lgkmcnt(5)
CV 147,154,155
MG v,4,v,176,a,0
RT 168,224,33288
MG v,4,v,180,a,4
RT 170,224,41480
MG v,36,v,176,a,8
RT 172,224,49672
MG v,36,v,180,a,12
RT 174,224,57864
s_mov_b32 m0, 47744
MG v,68,v,176,a,16
buffer_load_dwordx4 v253, s[40:43], s60 offen offset:384 nt lds
s_add_u32 s60, s60, 16384
MG v,68,v,180,a,20
ds_write_b128 v252, v[144:147] offset:7168
MG v,100,v,176,a,24
CV 148,148,149
CV 149,150,151
MG v,100,v,180,a,28
s_waitcnt lgkmcnt(5)
CV 150,156,157
CV 151,158,159
MG v,8,v,160,a,0
RT 176,224,34816
MG v,8,v,164,a,4
RT 178,224,43008
MG v,40,v,160,a,8
RT 180,224,51200
MG v,40,v,164,a,12
RT 182,224,59392
s_mov_b32 m0, 123904
MG v,72,v,160,a,16
buffer_load_dwordx4 v253, s[44:47], s61 offen offset:0 nt lds
MG v,72,v,164,a,20
MG v,104,v,160,a,24
ds_write_b128 v252, v[148:151] offset:15360
MG v,104,v,164,a,28
s_waitcnt lgkmcnt(6)
CV 0,0,1
CV 1,2,3
MG v,12,v,168,a,0
RT 160,224,34824
CV 2,4,5
MG v,12,v,172,a,4
RT 162,224,43016
CV 3,6,7
MG v,44,v,168,a,8
RT 164,224,51208
CV 32,32,33
MG v,44,v,172,a,12
RT 166,224,59400
s_mov_b32 m0, 125824
MG v,76,v,168,a,16
buffer_load_dwordx4 v253, s[44:47], s61 offen offset:128 nt lds
MG v,76,v,172,a,20
CV 33,34,35
CV 34,36,37
MG v,108,v,168,a,24
CV 35,38,39
CV 64,64,65
MG v,108,v,172,a,28
s_waitcnt lgkmcnt(5)
CV 65,66,67
MG v,16,v,176,a,0
RT 168,224,35328
CV 66,68,69
MG v,16,v,180,a,4
RT 170,224,43520
CV 67,70,71
MG v,48,v,176,a,8
RT 172,224,51712
CV 96,96,97
MG v,48,v,180,a,12
RT 174,224,59904
s_mov_b32 m0, 127744
MG v,80,v,176,a,16
buffer_load_dwordx4 v253, s[44:47], s61 offen offset:256 nt lds
MG v,80,v,180,a,20
CV 97,98,99
CV 98,100,101
MG v,112,v,176,a,24
CV 99,102,103
CV 4,8,9
MG v,112,v,180,a,28
s_waitcnt lgkmcnt(4)
CV 5,10,11
CV 6,12,13
MG v,20,v,160,a,0
RT 176,224,35336
CV 7,14,15
MG v,20,v,164,a,4
RT 178,224,43528
CV 36,40,41
MG v,52,v,160,a,8
RT 180,224,51720
CV 37,42,43
MG v,52,v,164,a,12
RT 182,224,59912
s_mov_b32 m0, 129664
MG v,84,v,160,a,16
buffer_load_dwordx4 v253, s[44:47], s61 offen offset:384 nt lds
s_add_u32 s61, s61, 16384
MG v,84,v,164,a,20
CV 38,44,45
CV 39,46,47
MG v,116,v,160,a,24
CV 68,72,73
MG v,116,v,164,a,28
s_waitcnt lgkmcnt(4)
CV 69,74,75
CV 70,76,77
MG v,24,v,168,a,0
RT 160,224,36864
CV 71,78,79
MG v,24,v,172,a,4
RT 162,224,45056
CV 100,104,105
MG v,56,v,168,a,8
RT 164,224,53248
CV 101,106,107
MG v,56,v,172,a,12
RT 166,224,61440
s_mov_b32 m0, 58368
MG v,88,v,168,a,16
buffer_load_dwordx4 v253, s[40:43], s60 offen offset:0 nt lds
MG v,88,v,172,a,20
CV 102,108,109
CV 103,110,111
MG v,120,v,168,a,24
CV 8,16,17
CV 9,18,19
MG v,120,v,172,a,28
s_waitcnt lgkmcnt(4)
CV 10,20,21
CV 11,22,23
MG v,28,v,176,a,0
RT 168,224,36872
CV 40,48,49
MG v,28,v,180,a,4
RT 170,224,45064
CV 41,50,51
MG v,60,v,176,a,8
RT 172,224,53256
CV 42,52,53
MG v,60,v,180,a,12
RT 174,224,61448
s_mov_b32 m0, 60288
MG v,92,v,176,a,16
buffer_load_dwordx4 v253, s[40:43], s60 offen offset:128 nt lds
MG v,92,v,180,a,20
CV 43,54,55
MG v,124,v,176,a,24
CV 72,80,81
CV 73,82,83
MG v,124,v,180,a,28
s_waitcnt lgkmcnt(4)
CV 74,84,85
CV 75,86,87
MG a,128,v,160,a,0
RT 176,224,37376
CV 104,112,113
MG a,128,v,164,a,4
RT 178,224,45568
CV 105,114,115
MG a,132,v,160,a,8
RT 180,224,53760
CV 106,116,117
MG a,132,v,164,a,12
RT 182,224,61952
s_mov_b32 m0, 62208
MG a,136,v,160,a,16
buffer_load_dwordx4 v253, s[40:43], s60 offen offset:256 nt lds
MG a,136,v,164,a,20
CV 107,118,119
CV 12,24,25
MG a,140,v,160,a,24
CV 13,26,27
CV 14,28,29
MG a,140,v,164,a,28
s_waitcnt lgkmcnt(4)
CV 15,30,31
MG a,144,v,168,a,0
RT 160,224,37384
CV 44,56,57
MG a,144,v,172,a,4
RT 162,224,45576
CV 45,58,59
MG a,148,v,168,a,8
RT 164,224,53768
CV 46,60,61
MG a,148,v,172,a,12
RT 166,224,61960
s_mov_b32 m0, 64128
MG a,152,v,168,a,16
buffer_load_dwordx4 v253, s[40:43], s60 offen offset:384 nt lds
s_add_u32 s60, s60, 16384
MG a,152,v,172,a,20
CV 47,62,63
CV 76,88,89
MG a,156,v,168,a,24
CV 77,90,91
CV 78,92,93
MG a,156,v,172,a,28
s_waitcnt lgkmcnt(6)
CV 79,94,95
CV 108,120,121
MG a,160,v,176,a,0
s_waitcnt lgkmcnt(4)
CV 109,122,123
CV 110,124,125
MG a,160,v,180,a,4
CV 111,126,127
MG a,164,v,176,a,8
MG a,164,v,180,a,12
AR 192,128
AR 193,129
MG a,168,v,176,a,16
AR 194,130
AR 195,131
MG a,168,v,180,a,20
AR 196,144
AR 197,145
MG a,172,v,176,a,24
AR 198,146
MG a,172,v,180,a,28
s_waitcnt lgkmcnt(2)
AR 199,147
CV 16,192,193
MG a,176,v,160,a,0
s_waitcnt lgkmcnt(0)
CV 17,194,195
CV 18,196,197
MG a,176,v,164,a,4
CV 19,198,199
AR 200,132
MG a,180,v,160,a,8
AR 201,133
AR 202,134
MG a,180,v,164,a,12
AR 203,135
MG a,184,v,160,a,16
AR 204,148
AR 205,149
MG a,184,v,164,a,20
AR 206,150
AR 207,151
MG a,188,v,160,a,24
CV 48,200,201
CV 49,202,203
MG a,188,v,164,a,28
s_waitcnt vmcnt(20)
CV 50,204,205
CV 51,206,207
s_barrier
RB 176,249,8192
RB 180,249,8704
RB 224,252,8192
RB 208,249,10240
RB 212,249,10752
RB 228,252,9216
AR 192,136
AR 193,137
AR 194,138
AR 195,139
AR 196,152
AR 197,153
AR 198,154
AR 199,155
CV 80,192,193
CV 81,194,195
CV 82,196,197
CV 83,198,199
AR 200,140
AR 201,141
AR 202,142
AR 203,143
AR 204,156
AR 205,157
AR 206,158
AR 207,159
CV 112,200,201
CV 113,202,203
CV 114,204,205
CV 115,206,207
AR 192,160
AR 193,161
AR 194,162
AR 195,163
AR 196,176
AR 197,177
AR 198,178
AR 199,179
CV 20,192,193
CV 21,194,195
CV 22,196,197
CV 23,198,199
AR 200,164
AR 201,165
AR 202,166
AR 203,167
AR 204,180
AR 205,181
AR 206,182
AR 207,183
CV 52,200,201
CV 53,202,203
CV 54,204,205
CV 55,206,207
AR 192,168
AR 193,169
AR 194,170
AR 195,171
AR 196,184
AR 197,185
AR 198,186
AR 199,187
CV 84,192,193
CV 85,194,195
CV 86,196,197
CV 87,198,199
AR 200,172
AR 201,173
AR 202,174
AR 203,175
AR 204,188
AR 205,189
AR 206,190
AR 207,191
CV 116,200,201
CV 117,202,203
CV 118,204,205
CV 119,206,207
AR 192,192
AR 193,193
AR 194,194
AR 195,195
AR 196,208
AR 197,209
AR 198,210
AR 199,211
CV 24,192,193
CV 25,194,195
CV 26,196,197
CV 27,198,199
AR 200,196
AR 201,197
AR 202,198
AR 203,199
AR 204,212
AR 205,213
AR 206,214
AR 207,215
CV 56,200,201
CV 57,202,203
CV 58,204,205
CV 59,206,207
AR 192,200
AR 193,201
AR 194,202
AR 195,203
AR 196,216
AR 197,217
AR 198,218
AR 199,219
CV 88,192,193
CV 89,194,195
CV 90,196,197
CV 91,198,199
AR 200,204
AR 201,205
AR 202,206
AR 203,207
AR 204,220
AR 205,221
AR 206,222
AR 207,223
CV 120,200,201
CV 121,202,203
CV 122,204,205
s_waitcnt lgkmcnt(5)
MV0 160,176,0
RB 216,249,12288
CV 123,206,207
s_waitcnt lgkmcnt(5)
MV 160,180,4
RB 220,249,12800
AR 192,224
MV0 164,176,32
AR 193,225
AR 194,226
MV 164,180,36
AR 195,227
AR 196,240
MV0 168,176,64
AR 197,241
AR 198,242
MV 168,180,68
AR 199,243
CV 28,192,193
MV0 172,176,96
CV 29,194,195
CV 30,196,197
MV 172,180,100
s_waitcnt lgkmcnt(2)
CV 31,198,199
AR 200,228
MV0 232,176,224
RB 224,252,10240
AR 201,229
MV 232,180,228
AR 202,230
AR 203,231
MV 160,208,8
RB 176,249,14336
AR 204,244
MV 160,212,12
RB 180,249,14848
AR 205,245
MV 164,208,40
RB 228,252,11264
AR 206,246
MV 164,212,44
AR 207,247
CV 60,200,201
MV 168,208,72
CV 61,202,203
CV 62,204,205
MV 168,212,76
CV 63,206,207
AR 192,232
MV 172,208,104
AR 193,233
AR 194,234
MV 172,212,108
s_waitcnt lgkmcnt(0)
AR 195,235
AR 196,248
MV 232,208,224
RB 224,252,12288
AR 197,249
MV 232,212,228
AR 198,250
AR 199,251
MV 160,216,16
RB 208,249,0
CV 92,192,193
MV 160,220,20
RB 212,249,512
CV 93,194,195
MV 164,216,48
RB 228,252,13312
CV 94,196,197
MV 164,220,52
CV 95,198,199
AR 200,236
MV 168,216,80
AR 201,237
AR 202,238
MV 168,220,84
AR 203,239
AR 204,252
MV 172,216,112
AR 205,253
AR 206,254
MV 172,220,116
s_waitcnt lgkmcnt(0)
AR 207,255
CV 124,200,201
MV 232,216,224
RB 216,249,2048
CV 125,202,203
MV 232,220,228
CV 126,204,205
CV 127,206,207
MV 160,176,24
RB 220,249,2560
MV 160,180,28
RB 224,252,14336
MV 164,176,56
RB 228,252,15360
MV 164,180,60
MV 168,176,88
MV 168,180,92
MV 172,176,120
MV 172,180,124
s_waitcnt lgkmcnt(0)
MV 232,176,224
RB 224,249,4096
MV 232,180,228
MV0 176,208,0
RB 228,249,4608
MV 176,212,4
MV0 180,208,32
MV 180,212,36
MV0 184,208,64
v_max3_f32 v192, v160, v161, v162
v_max3_f32 v193, v164, v165, v166
MV 184,212,68
v_max3_f32 v194, v168, v169, v170
v_max3_f32 v195, v172, v173, v174
MV0 188,208,96
RB 208,249,6144
v_max3_f32 v196, v232, v233, v234
MV 188,212,100
v_max_f32 v192, v192, v163
v_max_f32 v193, v193, v167
MV 176,216,8
RB 212,249,6656
v_max_f32 v194, v194, v171
MV 176,220,12
v_max_f32 v195, v195, v175
v_max_f32 v196, v196, v235
MV 180,216,40
v_mov_b32 v197, v192
v_mov_b32 v198, v193
MV 180,220,44
v_mov_b32 v199, v194
v_mov_b32 v200, v195
MV 184,216,72
v_mov_b32 v201, v196
v_permlane16_swap_b32 v192, v197
MV 184,220,76
v_permlane16_swap_b32 v193, v198
v_permlane16_swap_b32 v194, v199
MV 188,216,104
v_permlane16_swap_b32 v195, v200
v_permlane16_swap_b32 v196, v201
MV 188,220,108
s_waitcnt lgkmcnt(3)
v_max_f32 v192, v192, v197
v_max_f32 v193, v193, v198
MV 176,224,16
s_waitcnt lgkmcnt(2)
v_max_f32 v194, v194, v199
v_max_f32 v195, v195, v200
MV 176,228,20
v_max_f32 v196, v196, v201
v_mov_b32 v197, v192
MV 180,224,48
v_mov_b32 v198, v193
v_mov_b32 v199, v194
MV 180,228,52
v_mov_b32 v200, v195
v_mov_b32 v201, v196
MV 184,224,80
v_permlane32_swap_b32 v192, v197
v_permlane32_swap_b32 v193, v198
MV 184,228,84
v_permlane32_swap_b32 v194, v199
v_permlane32_swap_b32 v195, v200
MV 188,224,112
v_permlane32_swap_b32 v196, v201
v_max_f32 v192, v192, v197
MV 188,228,116
s_waitcnt lgkmcnt(1)
v_max_f32 v193, v193, v198
v_max_f32 v194, v194, v199
MV 176,208,24
s_waitcnt lgkmcnt(0)
v_max_f32 v195, v195, v200
v_max_f32 v196, v196, v201
MV 176,212,28
v_mul_f32 v196, s66, v196
v_mul_f32 v236, s66, v192
MV 180,208,56
v_mul_f32 v237, s66, v193
v_mul_f32 v238, s66, v194
MV 180,212,60
v_mul_f32 v239, s66, v195
MV 184,208,88
ds_write_b32 v252, v196 offset:28684
MV 184,212,92
v_fma_f32 v160, v160, s63, v236
v_fma_f32 v161, v161, s63, v236
MV 188,208,120
v_fma_f32 v162, v162, s63, v236
v_fma_f32 v163, v163, s63, v236
MV 188,212,124
s_waitcnt vmcnt(12) lgkmcnt(0)
v_fma_f32 v164, v164, s63, v237
v_fma_f32 v165, v165, s63, v237
s_barrier
ds_read_b32 v246, v252 offset:28680
ds_read_b32 v248, v252 offset:28684
s_mov_b32 m0, 140288
s_nop 0
buffer_load_dwordx4 v253, s[44:47], s61 offen offset:0 nt lds
s_mov_b32 m0, 142208
s_nop 0
buffer_load_dwordx4 v253, s[44:47], s61 offen offset:128 nt lds
s_mov_b32 m0, 144128
s_nop 0
buffer_load_dwordx4 v253, s[44:47], s61 offen offset:256 nt lds
s_mov_b32 m0, 146048
s_nop 0
buffer_load_dwordx4 v253, s[44:47], s61 offen offset:384 nt lds
s_add_u32 s61, s61, 16384
s_mov_b32 s48, s24
s_and_b32 s49, s25, 0xffff
s_mov_b32 s50, 3670016
s_and_b32 s76, s26, 127
s_mul_i32 s76, s76, 7
v_fma_f32 v166, v166, s63, v237
v_fma_f32 v167, v167, s63, v237
v_fma_f32 v168, v168, s63, v238
v_fma_f32 v169, v169, s63, v238
v_fma_f32 v170, v170, s63, v238
v_fma_f32 v171, v171, s63, v238
v_fma_f32 v172, v172, s63, v239
v_fma_f32 v173, v173, s63, v239
v_fma_f32 v174, v174, s63, v239
v_fma_f32 v175, v175, s63, v239
v_exp_f32 v160, v160
v_exp_f32 v161, v161
v_exp_f32 v162, v162
v_exp_f32 v163, v163
v_exp_f32 v164, v164
v_exp_f32 v165, v165
v_exp_f32 v166, v166
v_exp_f32 v167, v167
v_exp_f32 v168, v168
v_exp_f32 v169, v169
v_exp_f32 v170, v170
v_exp_f32 v171, v171
v_exp_f32 v172, v172
v_exp_f32 v173, v173
v_exp_f32 v174, v174
v_exp_f32 v175, v175
s_waitcnt lgkmcnt(0)
v_min_f32 v240, v246, v248
v_mov_b32 v247, 0
)ASM"
R"ASM(
v_fma_f32 v176, v176, s63, v236
v_fma_f32 v177, v177, s63, v236
v_fma_f32 v178, v178, s63, v236
v_fma_f32 v179, v179, s63, v236
v_fma_f32 v182, v182, s63, v237
v_fma_f32 v180, v180, s63, v237
v_fma_f32 v181, v181, s63, v237
v_fma_f32 v183, v183, s63, v237
v_fma_f32 v184, v184, s63, v238
v_fma_f32 v185, v185, s63, v238
v_fma_f32 v186, v186, s63, v238
v_fma_f32 v187, v187, s63, v238
v_fma_f32 v188, v188, s63, v239
v_fma_f32 v189, v189, s63, v239
v_fma_f32 v190, v190, s63, v239
v_fma_f32 v191, v191, s63, v239
v_max3_f32 v246, v176, v177, v178
v_max3_f32 v246, v246, v179, v182
v_max3_f32 v246, v246, v180, v181
v_max3_f32 v246, v246, v183, v184
v_max3_f32 v246, v246, v185, v186
v_max3_f32 v246, v246, v187, v188
v_max3_f32 v246, v246, v189, v190
v_max3_f32 v246, v246, v191, v247
v_cmp_lt_f32 vcc, s64, v246
v_fma_f32 v232, v232, s63, v240
v_fma_f32 v233, v233, s63, v240
v_fma_f32 v234, v234, s63, v240
v_fma_f32 v235, v235, s63, v240
s_cbranch_vccnz Lst3_0
Lrt3_0:
v_add_f32 v241, v241, v160
v_add_f32 v241, v241, v161
v_add_f32 v241, v241, v162
v_add_f32 v241, v241, v163
v_add_f32 v242, v242, v164
v_add_f32 v242, v242, v165
v_add_f32 v242, v242, v166
v_add_f32 v242, v242, v167
v_add_f32 v243, v243, v168
v_add_f32 v243, v243, v169
v_exp_f32 v176, v176
v_exp_f32 v177, v177
v_add_f32 v241, v241, v176
v_add_f32 v241, v241, v177
v_exp_f32 v178, v178
v_exp_f32 v179, v179
v_exp_f32 v180, v180
v_add_f32 v241, v241, v178
v_add_f32 v241, v241, v179
v_exp_f32 v181, v181
v_exp_f32 v182, v182
v_exp_f32 v183, v183
CV 163,162,163
CV 162,160,161
CV 160,176,177
CV 161,178,179
v_add_f32 v242, v242, v180
v_add_f32 v242, v242, v181
v_add_f32 v242, v242, v182
v_add_f32 v242, v242, v183
CV 167,166,167
CV 166,164,165
CV 164,180,181
CV 165,182,183
v_add_f32 v243, v243, v170
v_exp_f32 v232, v232
s_nop 0
v_add_f32 v245, v245, v232
v_exp_f32 v233, v233
v_exp_f32 v234, v234
v_add_f32 v245, v245, v233
RB 176,249,24576
RB 180,249,25088
RB 224,252,8192
RB 208,249,26624
RB 212,249,27136
RB 228,252,9216
v_add_f32 v245, v245, v234
CV 254,232,233
v_exp_f32 v235, v235
s_nop 0
v_add_f32 v245, v245, v235
CV 255,234,235
v_add_f32 v243, v243, v171
CV 171,170,171
ds_write_b64 v252, v[254:255] offset:17416
CV 170,168,169
v_exp_f32 v184, v184
s_mov_b32 s65, 0
s_barrier
s_waitcnt lgkmcnt(6)
MV0 192,176,0
RB 216,249,28672
v_add_f32 v244, v244, v172
s_waitcnt lgkmcnt(6)
MV 192,180,4
RB 220,249,29184
v_add_f32 v244, v244, v173
MV0 196,176,32
s_cmp_lg_u32 s65, 0
v_exp_f32 v185, v185
MV 196,180,36
s_cbranch_scc1 Lst3_1
Lrt3_1:
v_exp_f32 v186, v186
MV0 200,176,64
v_exp_f32 v187, v187
MV 200,180,68
v_exp_f32 v188, v188
MV0 204,176,96
s_mov_b32 m0, 9216
v_exp_f32 v189, v189
MV 204,180,100
s_waitcnt lgkmcnt(3)
v_exp_f32 v190, v190
MV0 232,176,224
RB 224,252,10240
v_add_f32 v244, v244, v174
MV 232,180,228
v_exp_f32 v191, v191
MV 192,208,8
RB 176,249,30720
v_add_f32 v244, v244, v175
MV 192,212,12
RB 180,249,31232
v_add_f32 v243, v243, v184
MV 196,208,40
RB 228,252,11264
v_add_f32 v243, v243, v185
MV 196,212,44
buffer_load_dwordx4 v253, s[40:43], s60 offen offset:0 nt lds
MV 200,208,72
s_mov_b32 m0, 11136
v_add_f32 v243, v243, v186
v_add_f32 v243, v243, v187
MV 200,212,76
buffer_load_dwordx4 v253, s[40:43], s60 offen offset:128 nt lds
MV 204,208,104
v_add_f32 v244, v244, v188
v_add_f32 v244, v244, v189
MV 204,212,108
s_waitcnt lgkmcnt(0)
v_add_f32 v244, v244, v190
v_add_f32 v244, v244, v191
MV 232,208,224
RB 224,252,12288
CV 168,184,185
MV 232,212,228
CV 175,174,175
CV 174,172,173
MV 192,216,16
RB 208,249,16384
CV 169,186,187
MV 192,220,20
RB 212,249,16896
CV 172,188,189
MV 196,216,48
RB 228,252,13312
CV 173,190,191
MV 196,220,52
s_mov_b32 m0, 13056
MV 200,216,80
buffer_load_dwordx4 v253, s[40:43], s60 offen offset:256 nt lds
MV 200,220,84
MV 204,216,112
MV 204,220,116
s_waitcnt lgkmcnt(0)
MV 232,216,224
RB 216,249,18432
MV 232,220,228
MV 192,176,24
RB 220,249,18944
MV 192,180,28
RB 224,252,14336
MV 196,176,56
RB 228,252,15360
MV 196,180,60
s_mov_b32 m0, 14976
MV 200,176,88
buffer_load_dwordx4 v253, s[40:43], s60 offen offset:384 nt lds
s_add_u32 s60, s60, 16384
MV 200,180,92
v_fma_f32 v192, v192, s63, v236
v_fma_f32 v193, v193, s63, v236
MV 204,176,120
MV 204,180,124
s_waitcnt lgkmcnt(0)
v_fma_f32 v194, v194, s63, v236
v_fma_f32 v195, v195, s63, v236
MV 232,176,224
RB 224,249,20480
v_fma_f32 v196, v196, s63, v237
MV 232,180,228
v_fma_f32 v197, v197, s63, v237
v_fma_f32 v198, v198, s63, v237
MV0 176,208,0
RB 228,249,20992
v_fma_f32 v199, v199, s63, v237
MV 176,212,4
v_fma_f32 v200, v200, s63, v238
v_fma_f32 v201, v201, s63, v238
MV0 180,208,32
v_fma_f32 v202, v202, s63, v238
v_fma_f32 v203, v203, s63, v238
MV 180,212,36
v_fma_f32 v204, v204, s63, v239
MV0 184,208,64
v_fma_f32 v205, v205, s63, v239
v_fma_f32 v206, v206, s63, v239
MV 184,212,68
v_fma_f32 v207, v207, s63, v239
v_fma_f32 v232, v232, s63, v240
MV0 188,208,96
RB 208,249,22528
v_fma_f32 v233, v233, s63, v240
MV 188,212,100
v_fma_f32 v234, v234, s63, v240
v_fma_f32 v235, v235, s63, v240
MV 176,216,8
RB 212,249,23040
v_max3_f32 v246, v192, v193, v194
MV 176,220,12
v_max3_f32 v246, v246, v195, v196
v_max3_f32 v246, v246, v197, v198
MV 180,216,40
v_max3_f32 v246, v246, v199, v200
v_max3_f32 v246, v246, v201, v202
MV 180,220,44
v_max3_f32 v246, v246, v203, v204
MV 184,216,72
v_max3_f32 v246, v246, v205, v206
v_max3_f32 v246, v246, v207, v232
MV 184,220,76
v_max3_f32 v246, v246, v233, v234
v_max_f32 v246, v246, v235
MV 188,216,104
RT 216,250,0
v_cmp_lt_f32 vcc, s64, v246
MV 188,220,108
s_waitcnt lgkmcnt(3)
MV 176,224,16
RT 218,250,8192
MV 176,228,20
RT 220,251,0
MV 180,224,48
RT 222,251,8192
MV 180,228,52
s_cbranch_vccnz Lst3_2
Lrt3_2:
v_exp_f32 v192, v192
MV 184,224,80
v_exp_f32 v193, v193
MV 184,228,84
v_exp_f32 v194, v194
MV 188,224,112
RT 224,250,512
v_add_f32 v241, v241, v192
MV 188,228,116
s_waitcnt lgkmcnt(5)
v_exp_f32 v195, v195
MV 176,208,24
RT 226,250,8704
v_add_f32 v241, v241, v193
MV 176,212,28
RT 228,251,512
v_add_f32 v241, v241, v194
MV 180,208,56
RT 230,251,8704
v_add_f32 v241, v241, v195
MV 180,212,60
v_exp_f32 v196, v196
MV 184,208,88
v_exp_f32 v197, v197
MV 184,212,92
v_fma_f32 v176, v176, s63, v236
v_fma_f32 v177, v177, s63, v236
MV 188,208,120
s_waitcnt vmcnt(12) lgkmcnt(6)
v_fma_f32 v178, v178, s63, v236
v_fma_f32 v179, v179, s63, v236
MV 188,212,124
s_barrier
v_fma_f32 v182, v182, s63, v237
MA0 0,216,160
RT 208,250,2048
v_fma_f32 v180, v180, s63, v237
MA0 4,216,164
RT 210,250,10240
v_fma_f32 v181, v181, s63, v237
MA0 8,216,168
RT 212,251,2048
v_fma_f32 v183, v183, s63, v237
MA0 12,216,172
s_waitcnt lgkmcnt(7)
v_fma_f32 v184, v184, s63, v238
v_fma_f32 v185, v185, s63, v238
MA0 16,220,160
RT 214,251,10240
v_fma_f32 v186, v186, s63, v238
MA0 20,220,164
RT 216,250,6144
v_fma_f32 v187, v187, s63, v238
MA0 24,220,168
RT 218,250,14336
v_fma_f32 v188, v188, s63, v239
MA0 28,220,172
s_waitcnt lgkmcnt(8)
v_fma_f32 v189, v189, s63, v239
v_fma_f32 v190, v190, s63, v239
MA0 32,224,160
ds_read_b32 v247, v252 offset:29704
v_fma_f32 v191, v191, s63, v239
MA0 36,224,164
v_max3_f32 v246, v176, v177, v178
v_max3_f32 v246, v246, v179, v182
MA0 40,224,168
RT 220,251,6144
v_max3_f32 v246, v246, v180, v181
MA0 44,224,172
s_waitcnt lgkmcnt(8)
v_max3_f32 v246, v246, v183, v184
v_max3_f32 v246, v246, v185, v186
MA0 48,228,160
RT 222,251,14336
v_max3_f32 v246, v246, v187, v188
MA0 52,228,164
RB 224,252,16384
v_max3_f32 v246, v246, v189, v190
MA0 56,228,168
s_mov_b32 m0, 156672
v_exp_f32 v198, v198
MA0 60,228,172
s_waitcnt lgkmcnt(3)
v_exp_f32 v199, v199
MA0 64,208,160
RB 228,252,17408
v_max3_f32 v246, v246, v191, v247
MA0 68,208,164
buffer_load_dwordx4 v253, s[44:47], s61 offen offset:0 nt lds
MA0 72,208,168
v_cmp_lt_f32 vcc, s64, v246
v_add_f32 v242, v242, v196
MA0 76,208,172
v_add_f32 v242, v242, v197
v_add_f32 v242, v242, v198
MA0 80,212,160
RT 208,250,6656
v_add_f32 v242, v242, v199
MA0 84,212,164
RT 210,250,14848
MA0 88,212,168
s_mov_b32 m0, 158592
v_exp_f32 v200, v200
MA0 92,212,172
v_exp_f32 v201, v201
MA0 192,216,160
RT 212,251,6656
v_add_f32 v243, v243, v200
MA0 196,216,164
RT 214,251,14848
v_add_f32 v243, v243, v201
MA0 200,216,168
buffer_load_dwordx4 v253, s[44:47], s61 offen offset:128 nt lds
MA0 204,216,172
s_waitcnt lgkmcnt(4)
v_exp_f32 v202, v202
MV0 128,216,224
s_cbranch_vccnz Lst3_3
Lrt3_3:
v_exp_f32 v176, v176
MV0 132,216,228
v_exp_f32 v177, v177
MA0 208,220,160
RT 216,250,2560
v_add_f32 v241, v241, v176
MA0 212,220,164
RT 218,250,10752
v_add_f32 v241, v241, v177
MA0 216,220,168
s_mov_b32 m0, 160512
v_exp_f32 v178, v178
MA0 220,220,172
buffer_load_dwordx4 v253, s[44:47], s61 offen offset:256 nt lds
MV0 136,220,224
v_exp_f32 v179, v179
MV0 140,220,228
s_waitcnt lgkmcnt(4)
v_exp_f32 v180, v180
MA0 224,208,160
RT 220,251,2560
v_add_f32 v241, v241, v178
MA0 228,208,164
RT 222,251,10752
v_add_f32 v241, v241, v179
MA0 232,208,168
v_exp_f32 v181, v181
MA0 236,208,172
v_exp_f32 v182, v182
MV0 144,208,224
s_mov_b32 m0, 162432
v_exp_f32 v183, v183
MV0 148,208,228
s_waitcnt lgkmcnt(4)
CV 195,194,195
CV 194,192,193
MA0 240,212,160
RT 208,250,4096
CV 192,176,177
MA0 244,212,164
RT 210,250,12288
CV 193,178,179
MA0 248,212,168
buffer_load_dwordx4 v253, s[44:47], s61 offen offset:384 nt lds
s_add_u32 s61, s61, 16384
MA0 252,212,172
v_add_f32 v242, v242, v180
v_add_f32 v242, v242, v181
MV0 152,212,224
v_add_f32 v242, v242, v182
v_add_f32 v242, v242, v183
MV0 156,212,228
s_waitcnt lgkmcnt(4)
CV 199,198,199
MA0 96,216,160
RT 212,251,4096
CV 198,196,197
MA0 100,216,164
RT 214,251,12288
CV 196,180,181
MA0 104,216,168
CV 197,182,183
v_add_f32 v243, v243, v202
MA0 108,216,172
s_waitcnt lgkmcnt(4)
v_exp_f32 v232, v232
MA0 112,220,160
RT 216,250,4608
v_add_f32 v245, v245, v232
MA0 116,220,164
RT 218,250,12800
MA0 120,220,168
v_exp_f32 v233, v233
MA0 124,220,172
s_waitcnt lgkmcnt(4)
v_exp_f32 v234, v234
MA0 128,208,160
RT 220,251,4608
v_add_f32 v245, v245, v233
MA0 132,208,164
RB 176,249,40960
v_add_f32 v245, v245, v234
MA0 136,208,168
RB 180,249,41472
CV 254,232,233
MA0 140,208,172
s_waitcnt lgkmcnt(5)
v_exp_f32 v235, v235
MA0 144,212,160
RT 222,251,12800
v_add_f32 v245, v245, v235
MA0 148,212,164
RB 224,252,8192
CV 255,234,235
MA0 152,212,168
RB 208,249,43008
MA0 156,212,172
s_waitcnt lgkmcnt(6)
v_exp_f32 v203, v203
MA0 160,216,160
RB 212,249,43520
v_add_f32 v243, v243, v203
MA0 164,216,164
RB 228,252,9216
CV 203,202,203
MA0 168,216,168
ds_write_b64 v252, v[254:255] offset:19464
CV 202,200,201
MA0 172,216,172
s_waitcnt lgkmcnt(5)
v_exp_f32 v204, v204
MA0 176,220,160
v_exp_f32 v205, v205
MA0 180,220,164
v_exp_f32 v206, v206
MA0 184,220,168
v_exp_f32 v207, v207
MA0 188,220,172
v_exp_f32 v184, v184
.p2align 6
Lloop3:
MV0 160,176,0
RB 216,249,45056
v_add_f32 v244, v244, v204
MV 160,180,4
RB 220,249,45568
v_add_f32 v244, v244, v205
MV0 164,176,32
s_cmp_lg_u32 s65, 0
v_exp_f32 v185, v185
MV 164,180,36
s_cbranch_scc1 Lst3_4
Lrt3_4:
v_exp_f32 v186, v186
MV0 168,176,64
s_lshl_b32 s78, s62, 20
v_exp_f32 v187, v187
MV 168,180,68
s_add_u32 s78, s78, Lpeel3-Lbase
v_exp_f32 v188, v188
MV0 172,176,96
s_mov_b32 m0, 25600
v_exp_f32 v189, v189
MV 172,180,100
s_waitcnt lgkmcnt(3)
v_exp_f32 v190, v190
MV0 232,176,224
RB 224,252,10240
v_add_f32 v244, v244, v206
MV 232,180,228
v_exp_f32 v191, v191
MV 160,208,8
RB 176,249,47104
v_add_f32 v244, v244, v207
MV 160,212,12
RB 180,249,47616
v_add_f32 v243, v243, v184
MV 164,208,40
RB 228,252,11264
v_add_f32 v243, v243, v185
MV 164,212,44
buffer_load_dwordx4 v253, s[40:43], s60 offen offset:0 nt lds
MV 168,208,72
s_mov_b32 m0, 27520
v_add_f32 v243, v243, v186
v_add_f32 v243, v243, v187
MV 168,212,76
buffer_load_dwordx4 v253, s[40:43], s60 offen offset:128 nt lds
MV 172,208,104
v_add_f32 v244, v244, v188
v_add_f32 v244, v244, v189
MV 172,212,108
s_waitcnt lgkmcnt(0)
v_add_f32 v244, v244, v190
v_add_f32 v244, v244, v191
MV 232,208,224
RB 224,252,12288
CV 200,184,185
MV 232,212,228
CV 207,206,207
CV 206,204,205
MV 160,216,16
RB 208,249,32768
CV 201,186,187
MV 160,220,20
RB 212,249,33280
CV 204,188,189
MV 164,216,48
RB 228,252,13312
CV 205,190,191
MV 164,220,52
s_mov_b32 m0, 29440
MV 168,216,80
buffer_load_dwordx4 v253, s[40:43], s60 offen offset:256 nt lds
MV 168,220,84
MV 172,216,112
MV 172,220,116
s_waitcnt lgkmcnt(0)
MV 232,216,224
RB 216,249,34816
v_subrev_u32 v248, 0x10000, v252
MV 232,220,228
MV 160,176,24
RB 220,249,35328
v_lshlrev_b32 v248, 3, v248
MV 160,180,28
RB 224,252,14336
MV 164,176,56
RB 228,252,15360
MV 164,180,60
s_mov_b32 m0, 31360
MV 168,176,88
buffer_load_dwordx4 v253, s[40:43], s60 offen offset:384 nt lds
s_add_u32 s60, s60, 16384
MV 168,180,92
s_mov_b32 m0, 98048
v_fma_f32 v160, v160, s63, v236
v_fma_f32 v161, v161, s63, v236
MV 172,176,120
buffer_load_dword v248, s[56:59], s78 offen lds
MV 172,180,124
s_waitcnt lgkmcnt(0)
v_fma_f32 v162, v162, s63, v236
v_fma_f32 v163, v163, s63, v236
MV 232,176,224
RB 224,249,36864
v_fma_f32 v164, v164, s63, v237
MV 232,180,228
v_fma_f32 v165, v165, s63, v237
v_fma_f32 v166, v166, s63, v237
MV0 176,208,0
RB 228,249,37376
v_fma_f32 v167, v167, s63, v237
MV 176,212,4
v_fma_f32 v168, v168, s63, v238
v_fma_f32 v169, v169, s63, v238
MV0 180,208,32
v_fma_f32 v170, v170, s63, v238
v_fma_f32 v171, v171, s63, v238
MV 180,212,36
v_fma_f32 v172, v172, s63, v239
MV0 184,208,64
v_fma_f32 v173, v173, s63, v239
v_fma_f32 v174, v174, s63, v239
MV 184,212,68
v_fma_f32 v175, v175, s63, v239
v_fma_f32 v232, v232, s63, v240
MV0 188,208,96
RB 208,249,38912
v_fma_f32 v233, v233, s63, v240
MV 188,212,100
v_fma_f32 v234, v234, s63, v240
v_fma_f32 v235, v235, s63, v240
MV 176,216,8
RB 212,249,39424
v_max3_f32 v246, v160, v161, v162
MV 176,220,12
v_max3_f32 v246, v246, v163, v164
v_max3_f32 v246, v246, v165, v166
MV 180,216,40
v_max3_f32 v246, v246, v167, v168
v_max3_f32 v246, v246, v169, v170
MV 180,220,44
v_max3_f32 v246, v246, v171, v172
MV 184,216,72
v_max3_f32 v246, v246, v173, v174
v_max3_f32 v246, v246, v175, v232
MV 184,220,76
v_max3_f32 v246, v246, v233, v234
v_max_f32 v246, v246, v235
MV 188,216,104
RT 216,250,16384
v_cmp_lt_f32 vcc, s64, v246
MV 188,220,108
s_waitcnt lgkmcnt(3)
MV 176,224,16
RT 218,250,24576
MV 176,228,20
RT 220,251,16384
MV 180,224,48
RT 222,251,24576
MV 180,228,52
s_cbranch_vccnz Lst3_5
Lrt3_5:
v_exp_f32 v160, v160
MV 184,224,80
v_exp_f32 v161, v161
MV 184,228,84
v_exp_f32 v162, v162
MV 188,224,112
RT 224,250,16896
v_add_f32 v241, v241, v160
MV 188,228,116
s_waitcnt lgkmcnt(5)
v_exp_f32 v163, v163
MV 176,208,24
RT 226,250,25088
v_add_f32 v241, v241, v161
MV 176,212,28
RT 228,251,16896
v_add_f32 v241, v241, v162
MV 180,208,56
RT 230,251,25088
v_add_f32 v241, v241, v163
MV 180,212,60
v_exp_f32 v164, v164
MV 184,208,88
v_exp_f32 v165, v165
MV 184,212,92
v_fma_f32 v176, v176, s63, v236
v_fma_f32 v177, v177, s63, v236
MV 188,208,120
s_waitcnt vmcnt(13) lgkmcnt(6)
v_fma_f32 v178, v178, s63, v236
v_fma_f32 v179, v179, s63, v236
MV 188,212,124
s_barrier
v_fma_f32 v182, v182, s63, v237
MA 0,216,192
RT 208,250,18432
v_fma_f32 v180, v180, s63, v237
MA 4,216,196
RT 210,250,26624
v_fma_f32 v181, v181, s63, v237
MA 8,216,200
RT 212,251,18432
v_fma_f32 v183, v183, s63, v237
MA 12,216,204
s_waitcnt lgkmcnt(7)
v_fma_f32 v184, v184, s63, v238
v_fma_f32 v185, v185, s63, v238
MA 16,220,192
RT 214,251,26624
v_fma_f32 v186, v186, s63, v238
MA 20,220,196
RT 216,250,22528
v_fma_f32 v187, v187, s63, v238
MA 24,220,200
RT 218,250,30720
v_fma_f32 v188, v188, s63, v239
MA 28,220,204
s_waitcnt lgkmcnt(8)
v_fma_f32 v189, v189, s63, v239
v_fma_f32 v190, v190, s63, v239
MA 32,224,192
ds_read_b32 v247, v252 offset:29704
v_fma_f32 v191, v191, s63, v239
MA 36,224,196
v_max3_f32 v246, v176, v177, v178
v_max3_f32 v246, v246, v179, v182
MA 40,224,200
RT 220,251,22528
v_max3_f32 v246, v246, v180, v181
MA 44,224,204
s_waitcnt lgkmcnt(8)
v_max3_f32 v246, v246, v183, v184
v_max3_f32 v246, v246, v185, v186
MA 48,228,192
RT 222,251,30720
v_max3_f32 v246, v246, v187, v188
MA 52,228,196
RB 224,252,18432
v_max3_f32 v246, v246, v189, v190
MA 56,228,200
s_mov_b32 m0, 107520
v_exp_f32 v166, v166
MA 60,228,204
s_waitcnt lgkmcnt(3)
v_exp_f32 v167, v167
MA 64,208,192
RB 228,252,19456
v_max3_f32 v246, v246, v191, v247
MA 68,208,196
buffer_load_dwordx4 v253, s[44:47], s61 offen offset:0 nt lds
MA 72,208,200
v_cmp_lt_f32 vcc, s64, v246
v_add_f32 v242, v242, v164
MA 76,208,204
v_add_f32 v242, v242, v165
v_add_f32 v242, v242, v166
MA 80,212,192
RT 208,250,23040
v_add_f32 v242, v242, v167
MA 84,212,196
RT 210,250,31232
MA 88,212,200
s_mov_b32 m0, 109440
v_exp_f32 v168, v168
MA 92,212,204
v_exp_f32 v169, v169
MA 192,216,192
RT 212,251,23040
v_add_f32 v243, v243, v168
MA 196,216,196
RT 214,251,31232
v_add_f32 v243, v243, v169
MA 200,216,200
buffer_load_dwordx4 v253, s[44:47], s61 offen offset:128 nt lds
MA 204,216,204
s_waitcnt lgkmcnt(4)
v_exp_f32 v170, v170
MV 128,216,224
s_cbranch_vccnz Lst3_6
Lrt3_6:
v_exp_f32 v176, v176
MV 132,216,228
v_exp_f32 v177, v177
MA 208,220,192
RT 216,250,18944
v_add_f32 v241, v241, v176
MA 212,220,196
RT 218,250,27136
v_add_f32 v241, v241, v177
MA 216,220,200
s_mov_b32 m0, 111360
v_exp_f32 v178, v178
MA 220,220,204
buffer_load_dwordx4 v253, s[44:47], s61 offen offset:256 nt lds
MV 136,220,224
v_exp_f32 v179, v179
MV 140,220,228
s_waitcnt lgkmcnt(4)
v_exp_f32 v180, v180
MA 224,208,192
RT 220,251,18944
v_add_f32 v241, v241, v178
MA 228,208,196
RT 222,251,27136
v_add_f32 v241, v241, v179
MA 232,208,200
v_exp_f32 v181, v181
MA 236,208,204
v_exp_f32 v182, v182
MV 144,208,224
s_mov_b32 m0, 113280
v_exp_f32 v183, v183
MV 148,208,228
s_waitcnt lgkmcnt(4)
CV 163,162,163
CV 162,160,161
MA 240,212,192
RT 208,250,20480
CV 160,176,177
MA 244,212,196
RT 210,250,28672
CV 161,178,179
MA 248,212,200
buffer_load_dwordx4 v253, s[44:47], s61 offen offset:384 nt lds
s_add_u32 s61, s61, 16384
MA 252,212,204
v_add_f32 v242, v242, v180
v_add_f32 v242, v242, v181
MV 152,212,224
v_add_f32 v242, v242, v182
v_add_f32 v242, v242, v183
MV 156,212,228
s_waitcnt lgkmcnt(4)
CV 167,166,167
MA 96,216,192
RT 212,251,20480
CV 166,164,165
MA 100,216,196
RT 214,251,28672
CV 164,180,181
MA 104,216,200
CV 165,182,183
v_add_f32 v243, v243, v170
MA 108,216,204
s_waitcnt lgkmcnt(4)
v_exp_f32 v232, v232
MA 112,220,192
RT 216,250,20992
v_add_f32 v245, v245, v232
MA 116,220,196
RT 218,250,29184
MA 120,220,200
v_exp_f32 v233, v233
MA 124,220,204
s_waitcnt lgkmcnt(4)
v_exp_f32 v234, v234
MA 128,208,192
RT 220,251,20992
v_add_f32 v245, v245, v233
MA 132,208,196
RB 176,249,57344
v_add_f32 v245, v245, v234
MA 136,208,200
RB 180,249,57856
CV 254,232,233
MA 140,208,204
s_waitcnt lgkmcnt(5)
v_exp_f32 v235, v235
MA 144,212,192
RT 222,251,29184
v_add_f32 v245, v245, v235
MA 148,212,196
RB 224,252,8192
CV 255,234,235
MA 152,212,200
RB 208,249,59392
MA 156,212,204
s_waitcnt lgkmcnt(6)
v_exp_f32 v171, v171
MA 160,216,192
RB 212,249,59904
v_add_f32 v243, v243, v171
MA 164,216,196
RB 228,252,9216
CV 171,170,171
MA 168,216,200
ds_write_b64 v252, v[254:255] offset:17416
CV 170,168,169
MA 172,216,204
s_waitcnt lgkmcnt(5)
v_exp_f32 v172, v172
MA 176,220,192
v_exp_f32 v173, v173
MA 180,220,196
v_exp_f32 v174, v174
MA 184,220,200
v_exp_f32 v175, v175
MA 188,220,204
v_exp_f32 v184, v184
MV0 192,176,0
RB 216,249,61440
v_add_f32 v244, v244, v172
MV 192,180,4
RB 220,249,61952
v_add_f32 v244, v244, v173
MV0 196,176,32
s_cmp_lg_u32 s65, 0
v_exp_f32 v185, v185
MV 196,180,36
s_cbranch_scc1 Lst3_7
Lrt3_7:
v_exp_f32 v186, v186
MV0 200,176,64
s_lshl_b32 s78, s62, 20
v_exp_f32 v187, v187
MV 200,180,68
s_add_u32 s78, s78, Lpeel3-Lbase+8192
v_exp_f32 v188, v188
MV0 204,176,96
s_mov_b32 m0, 41984
v_exp_f32 v189, v189
MV 204,180,100
s_waitcnt lgkmcnt(3)
v_exp_f32 v190, v190
MV0 232,176,224
RB 224,252,10240
v_add_f32 v244, v244, v174
MV 232,180,228
v_exp_f32 v191, v191
MV 192,208,8
RB 176,249,63488
v_add_f32 v244, v244, v175
MV 192,212,12
RB 180,249,64000
v_add_f32 v243, v243, v184
MV 196,208,40
RB 228,252,11264
v_add_f32 v243, v243, v185
MV 196,212,44
buffer_load_dwordx4 v253, s[40:43], s60 offen offset:0 nt lds
MV 200,208,72
s_mov_b32 m0, 43904
v_add_f32 v243, v243, v186
v_add_f32 v243, v243, v187
MV 200,212,76
buffer_load_dwordx4 v253, s[40:43], s60 offen offset:128 nt lds
MV 204,208,104
v_add_f32 v244, v244, v188
v_add_f32 v244, v244, v189
MV 204,212,108
s_waitcnt lgkmcnt(0)
v_add_f32 v244, v244, v190
v_add_f32 v244, v244, v191
MV 232,208,224
RB 224,252,12288
CV 168,184,185
MV 232,212,228
CV 175,174,175
CV 174,172,173
MV 192,216,16
RB 208,249,49152
CV 169,186,187
MV 192,220,20
RB 212,249,49664
CV 172,188,189
MV 196,216,48
RB 228,252,13312
CV 173,190,191
MV 196,220,52
s_mov_b32 m0, 45824
MV 200,216,80
buffer_load_dwordx4 v253, s[40:43], s60 offen offset:256 nt lds
MV 200,220,84
MV 204,216,112
MV 204,220,116
s_waitcnt lgkmcnt(0)
MV 232,216,224
RB 216,249,51200
v_subrev_u32 v248, 0x10000, v252
MV 232,220,228
MV 192,176,24
RB 220,249,51712
v_lshlrev_b32 v248, 3, v248
MV 192,180,28
RB 224,252,14336
MV 196,176,56
RB 228,252,15360
MV 196,180,60
s_mov_b32 m0, 47744
MV 200,176,88
buffer_load_dwordx4 v253, s[40:43], s60 offen offset:384 nt lds
s_add_u32 s60, s60, 16384
MV 200,180,92
s_mov_b32 m0, 98048
v_fma_f32 v192, v192, s63, v236
v_fma_f32 v193, v193, s63, v236
MV 204,176,120
buffer_load_dword v248, s[56:59], s78 offen lds
MV 204,180,124
s_waitcnt lgkmcnt(0)
v_fma_f32 v194, v194, s63, v236
v_fma_f32 v195, v195, s63, v236
MV 232,176,224
RB 224,249,53248
v_fma_f32 v196, v196, s63, v237
MV 232,180,228
v_fma_f32 v197, v197, s63, v237
v_fma_f32 v198, v198, s63, v237
MV0 176,208,0
RB 228,249,53760
v_fma_f32 v199, v199, s63, v237
MV 176,212,4
v_fma_f32 v200, v200, s63, v238
v_fma_f32 v201, v201, s63, v238
MV0 180,208,32
v_fma_f32 v202, v202, s63, v238
v_fma_f32 v203, v203, s63, v238
MV 180,212,36
v_fma_f32 v204, v204, s63, v239
MV0 184,208,64
v_fma_f32 v205, v205, s63, v239
v_fma_f32 v206, v206, s63, v239
MV 184,212,68
v_fma_f32 v207, v207, s63, v239
v_fma_f32 v232, v232, s63, v240
MV0 188,208,96
RB 208,249,55296
v_fma_f32 v233, v233, s63, v240
MV 188,212,100
v_fma_f32 v234, v234, s63, v240
v_fma_f32 v235, v235, s63, v240
MV 176,216,8
RB 212,249,55808
v_max3_f32 v246, v192, v193, v194
MV 176,220,12
v_max3_f32 v246, v246, v195, v196
v_max3_f32 v246, v246, v197, v198
MV 180,216,40
v_max3_f32 v246, v246, v199, v200
v_max3_f32 v246, v246, v201, v202
MV 180,220,44
v_max3_f32 v246, v246, v203, v204
MV 184,216,72
v_max3_f32 v246, v246, v205, v206
v_max3_f32 v246, v246, v207, v232
MV 184,220,76
v_max3_f32 v246, v246, v233, v234
v_max_f32 v246, v246, v235
MV 188,216,104
RT 216,250,32768
v_cmp_lt_f32 vcc, s64, v246
MV 188,220,108
s_waitcnt lgkmcnt(3)
MV 176,224,16
RT 218,250,40960
MV 176,228,20
RT 220,251,32768
MV 180,224,48
RT 222,251,40960
MV 180,228,52
s_cbranch_vccnz Lst3_8
Lrt3_8:
v_exp_f32 v192, v192
MV 184,224,80
v_exp_f32 v193, v193
MV 184,228,84
v_exp_f32 v194, v194
MV 188,224,112
RT 224,250,33280
v_add_f32 v241, v241, v192
MV 188,228,116
s_waitcnt lgkmcnt(5)
v_exp_f32 v195, v195
MV 176,208,24
RT 226,250,41472
v_add_f32 v241, v241, v193
MV 176,212,28
RT 228,251,33280
v_add_f32 v241, v241, v194
MV 180,208,56
RT 230,251,41472
v_add_f32 v241, v241, v195
MV 180,212,60
v_exp_f32 v196, v196
MV 184,208,88
v_exp_f32 v197, v197
MV 184,212,92
v_fma_f32 v176, v176, s63, v236
v_fma_f32 v177, v177, s63, v236
MV 188,208,120
s_waitcnt vmcnt(14) lgkmcnt(6)
v_fma_f32 v178, v178, s63, v236
v_fma_f32 v179, v179, s63, v236
MV 188,212,124
s_barrier
v_fma_f32 v182, v182, s63, v237
MA 0,216,160
RT 208,250,34816
v_fma_f32 v180, v180, s63, v237
MA 4,216,164
RT 210,250,43008
v_fma_f32 v181, v181, s63, v237
MA 8,216,168
RT 212,251,34816
v_fma_f32 v183, v183, s63, v237
MA 12,216,172
s_waitcnt lgkmcnt(7)
v_fma_f32 v184, v184, s63, v238
v_fma_f32 v185, v185, s63, v238
MA 16,220,160
RT 214,251,43008
v_fma_f32 v186, v186, s63, v238
MA 20,220,164
RT 216,250,38912
v_fma_f32 v187, v187, s63, v238
MA 24,220,168
RT 218,250,47104
v_fma_f32 v188, v188, s63, v239
MA 28,220,172
s_waitcnt lgkmcnt(8)
v_fma_f32 v189, v189, s63, v239
v_fma_f32 v190, v190, s63, v239
MA 32,224,160
ds_read_b32 v247, v252 offset:29704
v_fma_f32 v191, v191, s63, v239
MA 36,224,164
v_max3_f32 v246, v176, v177, v178
v_max3_f32 v246, v246, v179, v182
MA 40,224,168
RT 220,251,38912
v_max3_f32 v246, v246, v180, v181
MA 44,224,172
s_waitcnt lgkmcnt(8)
v_max3_f32 v246, v246, v183, v184
v_max3_f32 v246, v246, v185, v186
MA 48,228,160
RT 222,251,47104
v_max3_f32 v246, v246, v187, v188
MA 52,228,164
RB 224,252,16384
v_max3_f32 v246, v246, v189, v190
MA 56,228,168
s_mov_b32 m0, 123904
v_exp_f32 v198, v198
MA 60,228,172
s_waitcnt lgkmcnt(3)
v_exp_f32 v199, v199
MA 64,208,160
RB 228,252,17408
v_max3_f32 v246, v246, v191, v247
MA 68,208,164
buffer_load_dwordx4 v253, s[44:47], s61 offen offset:0 nt lds
MA 72,208,168
v_cmp_lt_f32 vcc, s64, v246
v_add_f32 v242, v242, v196
MA 76,208,172
v_add_f32 v242, v242, v197
v_add_f32 v242, v242, v198
MA 80,212,160
RT 208,250,39424
v_add_f32 v242, v242, v199
MA 84,212,164
RT 210,250,47616
MA 88,212,168
s_mov_b32 m0, 125824
v_exp_f32 v200, v200
MA 92,212,172
v_exp_f32 v201, v201
MA 192,216,160
RT 212,251,39424
v_add_f32 v243, v243, v200
MA 196,216,164
RT 214,251,47616
v_add_f32 v243, v243, v201
MA 200,216,168
buffer_load_dwordx4 v253, s[44:47], s61 offen offset:128 nt lds
MA 204,216,172
s_waitcnt lgkmcnt(4)
v_exp_f32 v202, v202
MV 128,216,224
s_cbranch_vccnz Lst3_9
Lrt3_9:
v_exp_f32 v176, v176
MV 132,216,228
v_exp_f32 v177, v177
MA 208,220,160
RT 216,250,35328
v_add_f32 v241, v241, v176
MA 212,220,164
RT 218,250,43520
v_add_f32 v241, v241, v177
MA 216,220,168
s_mov_b32 m0, 127744
v_exp_f32 v178, v178
MA 220,220,172
buffer_load_dwordx4 v253, s[44:47], s61 offen offset:256 nt lds
MV 136,220,224
v_exp_f32 v179, v179
MV 140,220,228
s_waitcnt lgkmcnt(4)
v_exp_f32 v180, v180
MA 224,208,160
RT 220,251,35328
v_add_f32 v241, v241, v178
MA 228,208,164
RT 222,251,43520
v_add_f32 v241, v241, v179
MA 232,208,168
v_exp_f32 v181, v181
MA 236,208,172
v_exp_f32 v182, v182
MV 144,208,224
s_mov_b32 m0, 129664
v_exp_f32 v183, v183
MV 148,208,228
s_waitcnt lgkmcnt(4)
CV 195,194,195
CV 194,192,193
MA 240,212,160
RT 208,250,36864
CV 192,176,177
MA 244,212,164
RT 210,250,45056
CV 193,178,179
MA 248,212,168
buffer_load_dwordx4 v253, s[44:47], s61 offen offset:384 nt lds
s_add_u32 s61, s61, 16384
MA 252,212,172
v_add_f32 v242, v242, v180
v_add_f32 v242, v242, v181
MV 152,212,224
v_add_f32 v242, v242, v182
v_add_f32 v242, v242, v183
MV 156,212,228
s_waitcnt lgkmcnt(4)
CV 199,198,199
MA 96,216,160
RT 212,251,36864
CV 198,196,197
MA 100,216,164
RT 214,251,45056
CV 196,180,181
MA 104,216,168
CV 197,182,183
v_add_f32 v243, v243, v202
MA 108,216,172
s_waitcnt lgkmcnt(4)
v_exp_f32 v232, v232
MA 112,220,160
RT 216,250,37376
v_add_f32 v245, v245, v232
MA 116,220,164
RT 218,250,45568
MA 120,220,168
v_exp_f32 v233, v233
MA 124,220,172
s_waitcnt lgkmcnt(4)
v_exp_f32 v234, v234
MA 128,208,160
RT 220,251,37376
v_add_f32 v245, v245, v233
MA 132,208,164
RB 176,249,8192
v_add_f32 v245, v245, v234
MA 136,208,168
RB 180,249,8704
CV 254,232,233
MA 140,208,172
s_waitcnt lgkmcnt(5)
v_exp_f32 v235, v235
MA 144,212,160
RT 222,251,45568
v_add_f32 v245, v245, v235
MA 148,212,164
RB 224,252,8192
CV 255,234,235
MA 152,212,168
RB 208,249,10240
MA 156,212,172
s_waitcnt lgkmcnt(6)
v_exp_f32 v203, v203
MA 160,216,160
RB 212,249,10752
v_add_f32 v243, v243, v203
MA 164,216,164
RB 228,252,9216
CV 203,202,203
MA 168,216,168
ds_write_b64 v252, v[254:255] offset:19464
CV 202,200,201
MA 172,216,172
s_waitcnt lgkmcnt(5)
v_exp_f32 v204, v204
MA 176,220,160
v_exp_f32 v205, v205
MA 180,220,164
v_exp_f32 v206, v206
MA 184,220,168
v_exp_f32 v207, v207
MA 188,220,172
v_exp_f32 v184, v184
MV0 160,176,0
RB 216,249,12288
v_add_f32 v244, v244, v204
MV 160,180,4
RB 220,249,12800
v_add_f32 v244, v244, v205
MV0 164,176,32
s_cmp_lg_u32 s65, 0
v_exp_f32 v185, v185
MV 164,180,36
s_cbranch_scc1 Lst3_10
Lrt3_10:
v_exp_f32 v186, v186
MV0 168,176,64
s_lshl_b32 s78, s62, 20
v_exp_f32 v187, v187
MV 168,180,68
s_add_u32 s78, s78, LT-Lbase+24576
v_exp_f32 v188, v188
MV0 172,176,96
s_mov_b32 m0, 58368
v_exp_f32 v189, v189
MV 172,180,100
s_waitcnt lgkmcnt(3)
v_exp_f32 v190, v190
MV0 232,176,224
RB 224,252,10240
v_add_f32 v244, v244, v206
MV 232,180,228
v_exp_f32 v191, v191
MV 160,208,8
RB 176,249,14336
v_add_f32 v244, v244, v207
MV 160,212,12
RB 180,249,14848
v_add_f32 v243, v243, v184
MV 164,208,40
RB 228,252,11264
v_add_f32 v243, v243, v185
MV 164,212,44
buffer_load_dwordx4 v253, s[40:43], s60 offen offset:0 nt lds
MV 168,208,72
s_mov_b32 m0, 60288
v_add_f32 v243, v243, v186
v_add_f32 v243, v243, v187
MV 168,212,76
buffer_load_dwordx4 v253, s[40:43], s60 offen offset:128 nt lds
MV 172,208,104
v_add_f32 v244, v244, v188
v_add_f32 v244, v244, v189
MV 172,212,108
s_waitcnt lgkmcnt(0)
v_add_f32 v244, v244, v190
v_add_f32 v244, v244, v191
MV 232,208,224
RB 224,252,12288
CV 200,184,185
MV 232,212,228
CV 207,206,207
CV 206,204,205
MV 160,216,16
RB 208,249,0
CV 201,186,187
MV 160,220,20
RB 212,249,512
CV 204,188,189
MV 164,216,48
RB 228,252,13312
CV 205,190,191
MV 164,220,52
s_mov_b32 m0, 62208
MV 168,216,80
buffer_load_dwordx4 v253, s[40:43], s60 offen offset:256 nt lds
MV 168,220,84
MV 172,216,112
MV 172,220,116
s_waitcnt lgkmcnt(0)
MV 232,216,224
RB 216,249,2048
v_subrev_u32 v248, 0x10000, v252
MV 232,220,228
MV 160,176,24
RB 220,249,2560
v_lshlrev_b32 v248, 3, v248
MV 160,180,28
RB 224,252,14336
MV 164,176,56
RB 228,252,15360
MV 164,180,60
s_mov_b32 m0, 64128
MV 168,176,88
buffer_load_dwordx4 v253, s[40:43], s60 offen offset:384 nt lds
s_add_u32 s60, s60, 16384
MV 168,180,92
s_mov_b32 m0, 98048
v_fma_f32 v160, v160, s63, v236
v_fma_f32 v161, v161, s63, v236
MV 172,176,120
buffer_load_dword v248, s[56:59], s78 offen lds
MV 172,180,124
s_waitcnt lgkmcnt(0)
v_fma_f32 v162, v162, s63, v236
v_fma_f32 v163, v163, s63, v236
MV 232,176,224
RB 224,249,4096
v_fma_f32 v164, v164, s63, v237
MV 232,180,228
v_fma_f32 v165, v165, s63, v237
v_fma_f32 v166, v166, s63, v237
MV0 176,208,0
RB 228,249,4608
v_fma_f32 v167, v167, s63, v237
MV 176,212,4
v_fma_f32 v168, v168, s63, v238
v_fma_f32 v169, v169, s63, v238
MV0 180,208,32
v_fma_f32 v170, v170, s63, v238
v_fma_f32 v171, v171, s63, v238
MV 180,212,36
v_fma_f32 v172, v172, s63, v239
MV0 184,208,64
v_fma_f32 v173, v173, s63, v239
v_fma_f32 v174, v174, s63, v239
MV 184,212,68
v_fma_f32 v175, v175, s63, v239
v_fma_f32 v232, v232, s63, v240
MV0 188,208,96
RB 208,249,6144
v_fma_f32 v233, v233, s63, v240
MV 188,212,100
v_fma_f32 v234, v234, s63, v240
v_fma_f32 v235, v235, s63, v240
MV 176,216,8
RB 212,249,6656
v_max3_f32 v246, v160, v161, v162
MV 176,220,12
v_max3_f32 v246, v246, v163, v164
v_max3_f32 v246, v246, v165, v166
MV 180,216,40
v_max3_f32 v246, v246, v167, v168
v_max3_f32 v246, v246, v169, v170
MV 180,220,44
v_max3_f32 v246, v246, v171, v172
MV 184,216,72
v_max3_f32 v246, v246, v173, v174
v_max3_f32 v246, v246, v175, v232
MV 184,220,76
v_max3_f32 v246, v246, v233, v234
v_max_f32 v246, v246, v235
MV 188,216,104
RT 216,250,49152
v_cmp_lt_f32 vcc, s64, v246
MV 188,220,108
s_waitcnt lgkmcnt(3)
MV 176,224,16
RT 218,250,57344
MV 176,228,20
RT 220,251,49152
MV 180,224,48
RT 222,251,57344
MV 180,228,52
s_cbranch_vccnz Lst3_11
Lrt3_11:
v_exp_f32 v160, v160
MV 184,224,80
v_exp_f32 v161, v161
MV 184,228,84
v_exp_f32 v162, v162
MV 188,224,112
RT 224,250,49664
v_add_f32 v241, v241, v160
MV 188,228,116
s_waitcnt lgkmcnt(5)
v_exp_f32 v163, v163
MV 176,208,24
RT 226,250,57856
v_add_f32 v241, v241, v161
MV 176,212,28
RT 228,251,49664
v_add_f32 v241, v241, v162
MV 180,208,56
RT 230,251,57856
v_add_f32 v241, v241, v163
MV 180,212,60
v_exp_f32 v164, v164
MV 184,208,88
v_exp_f32 v165, v165
MV 184,212,92
v_fma_f32 v176, v176, s63, v236
v_fma_f32 v177, v177, s63, v236
MV 188,208,120
s_waitcnt vmcnt(14) lgkmcnt(6)
v_fma_f32 v178, v178, s63, v236
v_fma_f32 v179, v179, s63, v236
MV 188,212,124
s_barrier
v_fma_f32 v182, v182, s63, v237
MA 0,216,192
RT 208,250,51200
v_fma_f32 v180, v180, s63, v237
MA 4,216,196
RT 210,250,59392
v_fma_f32 v181, v181, s63, v237
MA 8,216,200
RT 212,251,51200
v_fma_f32 v183, v183, s63, v237
MA 12,216,204
s_waitcnt lgkmcnt(7)
v_fma_f32 v184, v184, s63, v238
v_fma_f32 v185, v185, s63, v238
MA 16,220,192
RT 214,251,59392
v_fma_f32 v186, v186, s63, v238
MA 20,220,196
RT 216,250,55296
v_fma_f32 v187, v187, s63, v238
MA 24,220,200
RT 218,250,63488
v_fma_f32 v188, v188, s63, v239
MA 28,220,204
s_waitcnt lgkmcnt(8)
v_fma_f32 v189, v189, s63, v239
v_fma_f32 v190, v190, s63, v239
MA 32,224,192
ds_read_b32 v247, v252 offset:29704
v_fma_f32 v191, v191, s63, v239
MA 36,224,196
v_max3_f32 v246, v176, v177, v178
v_max3_f32 v246, v246, v179, v182
MA 40,224,200
RT 220,251,55296
v_max3_f32 v246, v246, v180, v181
MA 44,224,204
s_waitcnt lgkmcnt(8)
v_max3_f32 v246, v246, v183, v184
v_max3_f32 v246, v246, v185, v186
MA 48,228,192
RT 222,251,63488
v_max3_f32 v246, v246, v187, v188
MA 52,228,196
RB 224,252,18432
v_max3_f32 v246, v246, v189, v190
MA 56,228,200
s_mov_b32 m0, 140288
v_exp_f32 v166, v166
MA 60,228,204
s_waitcnt lgkmcnt(3)
v_exp_f32 v167, v167
MA 64,208,192
RB 228,252,19456
v_max3_f32 v246, v246, v191, v247
MA 68,208,196
buffer_load_dwordx4 v253, s[44:47], s61 offen offset:0 nt lds
MA 72,208,200
v_cmp_lt_f32 vcc, s64, v246
v_add_f32 v242, v242, v164
MA 76,208,204
v_add_f32 v242, v242, v165
v_add_f32 v242, v242, v166
MA 80,212,192
RT 208,250,55808
v_add_f32 v242, v242, v167
MA 84,212,196
RT 210,250,64000
MA 88,212,200
s_mov_b32 m0, 142208
v_exp_f32 v168, v168
MA 92,212,204
v_exp_f32 v169, v169
MA 192,216,192
RT 212,251,55808
v_add_f32 v243, v243, v168
MA 196,216,196
RT 214,251,64000
v_add_f32 v243, v243, v169
MA 200,216,200
buffer_load_dwordx4 v253, s[44:47], s61 offen offset:128 nt lds
MA 204,216,204
s_waitcnt lgkmcnt(4)
v_exp_f32 v170, v170
MV 128,216,224
s_cbranch_vccnz Lst3_12
Lrt3_12:
v_exp_f32 v176, v176
MV 132,216,228
v_exp_f32 v177, v177
MA 208,220,192
RT 216,250,51712
v_add_f32 v241, v241, v176
MA 212,220,196
RT 218,250,59904
v_add_f32 v241, v241, v177
MA 216,220,200
s_mov_b32 m0, 144128
v_exp_f32 v178, v178
MA 220,220,204
buffer_load_dwordx4 v253, s[44:47], s61 offen offset:256 nt lds
MV 136,220,224
v_exp_f32 v179, v179
MV 140,220,228
s_waitcnt lgkmcnt(4)
v_exp_f32 v180, v180
MA 224,208,192
RT 220,251,51712
v_add_f32 v241, v241, v178
MA 228,208,196
RT 222,251,59904
v_add_f32 v241, v241, v179
MA 232,208,200
v_exp_f32 v181, v181
MA 236,208,204
v_exp_f32 v182, v182
MV 144,208,224
s_mov_b32 m0, 146048
v_exp_f32 v183, v183
MV 148,208,228
s_waitcnt lgkmcnt(4)
CV 163,162,163
CV 162,160,161
MA 240,212,192
RT 208,250,53248
CV 160,176,177
MA 244,212,196
RT 210,250,61440
CV 161,178,179
MA 248,212,200
buffer_load_dwordx4 v253, s[44:47], s61 offen offset:384 nt lds
s_add_u32 s61, s61, 16384
MA 252,212,204
v_add_f32 v242, v242, v180
v_add_f32 v242, v242, v181
MV 152,212,224
v_add_f32 v242, v242, v182
v_add_f32 v242, v242, v183
MV 156,212,228
s_waitcnt lgkmcnt(4)
CV 167,166,167
MA 96,216,192
RT 212,251,53248
CV 166,164,165
MA 100,216,196
RT 214,251,61440
CV 164,180,181
MA 104,216,200
CV 165,182,183
v_add_f32 v243, v243, v170
MA 108,216,204
s_waitcnt lgkmcnt(4)
v_exp_f32 v232, v232
MA 112,220,192
RT 216,250,53760
v_add_f32 v245, v245, v232
MA 116,220,196
RT 218,250,61952
MA 120,220,200
v_exp_f32 v233, v233
MA 124,220,204
s_waitcnt lgkmcnt(4)
v_exp_f32 v234, v234
MA 128,208,192
RT 220,251,53760
v_add_f32 v245, v245, v233
MA 132,208,196
RB 176,249,24576
v_add_f32 v245, v245, v234
MA 136,208,200
RB 180,249,25088
CV 254,232,233
MA 140,208,204
s_waitcnt lgkmcnt(5)
v_exp_f32 v235, v235
MA 144,212,192
RT 222,251,61952
v_add_f32 v245, v245, v235
MA 148,212,196
RB 224,252,8192
CV 255,234,235
MA 152,212,200
RB 208,249,26624
MA 156,212,204
s_waitcnt lgkmcnt(6)
v_exp_f32 v171, v171
MA 160,216,192
RB 212,249,27136
v_add_f32 v243, v243, v171
MA 164,216,196
RB 228,252,9216
CV 171,170,171
MA 168,216,200
ds_write_b64 v252, v[254:255] offset:17416
CV 170,168,169
MA 172,216,204
s_waitcnt lgkmcnt(5)
v_exp_f32 v172, v172
MA 176,220,192
v_exp_f32 v173, v173
MA 180,220,196
v_exp_f32 v174, v174
MA 184,220,200
v_exp_f32 v175, v175
MA 188,220,204
v_exp_f32 v184, v184
MV0 192,176,0
RB 216,249,28672
v_add_f32 v244, v244, v172
MV 192,180,4
RB 220,249,29184
v_add_f32 v244, v244, v173
MV0 196,176,32
s_cmp_lg_u32 s65, 0
v_exp_f32 v185, v185
MV 196,180,36
s_cbranch_scc1 Lst3_13
Lrt3_13:
v_exp_f32 v186, v186
MV0 200,176,64
s_lshr_b32 s78, s76, 2
s_lshl_b32 s78, s78, 7
v_exp_f32 v187, v187
MV 200,180,68
s_and_b32 s67, s76, 3
s_add_u32 s78, s78, s67
v_exp_f32 v188, v188
MV0 204,176,96
s_mov_b32 m0, 9216
v_exp_f32 v189, v189
MV 204,180,100
s_waitcnt lgkmcnt(3)
v_exp_f32 v190, v190
MV0 232,176,224
RB 224,252,10240
v_add_f32 v244, v244, v174
MV 232,180,228
v_exp_f32 v191, v191
MV 192,208,8
RB 176,249,30720
v_add_f32 v244, v244, v175
MV 192,212,12
RB 180,249,31232
v_add_f32 v243, v243, v184
MV 196,208,40
RB 228,252,11264
v_add_f32 v243, v243, v185
MV 196,212,44
buffer_load_dwordx4 v253, s[40:43], s60 offen offset:0 nt lds
MV 200,208,72
s_mov_b32 m0, 11136
v_add_f32 v243, v243, v186
v_add_f32 v243, v243, v187
MV 200,212,76
buffer_load_dwordx4 v253, s[40:43], s60 offen offset:128 nt lds
MV 204,208,104
s_lshl_b32 s78, s78, 7
s_add_u32 s76, s76, 1
v_add_f32 v244, v244, v188
v_add_f32 v244, v244, v189
MV 204,212,108
s_waitcnt lgkmcnt(0)
v_add_f32 v244, v244, v190
v_add_f32 v244, v244, v191
MV 232,208,224
RB 224,252,12288
CV 168,184,185
MV 232,212,228
CV 175,174,175
CV 174,172,173
MV 192,216,16
RB 208,249,16384
CV 169,186,187
MV 192,220,20
RB 212,249,16896
CV 172,188,189
MV 196,216,48
RB 228,252,13312
CV 173,190,191
MV 196,220,52
s_mov_b32 m0, 13056
MV 200,216,80
buffer_load_dwordx4 v253, s[40:43], s60 offen offset:256 nt lds
MV 200,220,84
MV 204,216,112
MV 204,220,116
s_waitcnt lgkmcnt(0)
MV 232,216,224
RB 216,249,18432
MV 232,220,228
MV 192,176,24
RB 220,249,18944
MV 192,180,28
RB 224,252,14336
MV 196,176,56
RB 228,252,15360
MV 196,180,60
s_mov_b32 m0, 14976
MV 200,176,88
buffer_load_dwordx4 v253, s[40:43], s60 offen offset:384 nt lds
s_add_u32 s60, s60, 16384
MV 200,180,92
s_mov_b32 m0, 98048
v_fma_f32 v192, v192, s63, v236
v_fma_f32 v193, v193, s63, v236
MV 204,176,120
buffer_load_dword v253, s[48:51], s78 offen lds
MV 204,180,124
s_waitcnt lgkmcnt(0)
v_fma_f32 v194, v194, s63, v236
v_fma_f32 v195, v195, s63, v236
MV 232,176,224
RB 224,249,20480
v_fma_f32 v196, v196, s63, v237
MV 232,180,228
v_fma_f32 v197, v197, s63, v237
v_fma_f32 v198, v198, s63, v237
MV0 176,208,0
RB 228,249,20992
v_fma_f32 v199, v199, s63, v237
MV 176,212,4
v_fma_f32 v200, v200, s63, v238
v_fma_f32 v201, v201, s63, v238
MV0 180,208,32
v_fma_f32 v202, v202, s63, v238
v_fma_f32 v203, v203, s63, v238
MV 180,212,36
v_fma_f32 v204, v204, s63, v239
MV0 184,208,64
v_fma_f32 v205, v205, s63, v239
v_fma_f32 v206, v206, s63, v239
MV 184,212,68
v_fma_f32 v207, v207, s63, v239
v_fma_f32 v232, v232, s63, v240
MV0 188,208,96
RB 208,249,22528
v_fma_f32 v233, v233, s63, v240
MV 188,212,100
v_fma_f32 v234, v234, s63, v240
v_fma_f32 v235, v235, s63, v240
MV 176,216,8
RB 212,249,23040
v_max3_f32 v246, v192, v193, v194
MV 176,220,12
v_max3_f32 v246, v246, v195, v196
v_max3_f32 v246, v246, v197, v198
MV 180,216,40
v_max3_f32 v246, v246, v199, v200
v_max3_f32 v246, v246, v201, v202
MV 180,220,44
v_max3_f32 v246, v246, v203, v204
MV 184,216,72
v_max3_f32 v246, v246, v205, v206
v_max3_f32 v246, v246, v207, v232
MV 184,220,76
v_max3_f32 v246, v246, v233, v234
v_max_f32 v246, v246, v235
MV 188,216,104
RT 216,250,0
v_cmp_lt_f32 vcc, s64, v246
MV 188,220,108
s_waitcnt lgkmcnt(3)
MV 176,224,16
RT 218,250,8192
MV 176,228,20
RT 220,251,0
MV 180,224,48
RT 222,251,8192
MV 180,228,52
s_cbranch_vccnz Lst3_14
Lrt3_14:
v_exp_f32 v192, v192
MV 184,224,80
v_exp_f32 v193, v193
MV 184,228,84
v_exp_f32 v194, v194
MV 188,224,112
RT 224,250,512
v_add_f32 v241, v241, v192
MV 188,228,116
s_waitcnt lgkmcnt(5)
v_exp_f32 v195, v195
MV 176,208,24
RT 226,250,8704
v_add_f32 v241, v241, v193
MV 176,212,28
RT 228,251,512
v_add_f32 v241, v241, v194
MV 180,208,56
RT 230,251,8704
v_add_f32 v241, v241, v195
MV 180,212,60
v_exp_f32 v196, v196
MV 184,208,88
v_exp_f32 v197, v197
MV 184,212,92
v_fma_f32 v176, v176, s63, v236
v_fma_f32 v177, v177, s63, v236
MV 188,208,120
s_waitcnt vmcnt(14) lgkmcnt(6)
v_fma_f32 v178, v178, s63, v236
v_fma_f32 v179, v179, s63, v236
MV 188,212,124
s_barrier
v_fma_f32 v182, v182, s63, v237
MA 0,216,160
RT 208,250,2048
v_fma_f32 v180, v180, s63, v237
MA 4,216,164
RT 210,250,10240
v_fma_f32 v181, v181, s63, v237
MA 8,216,168
RT 212,251,2048
v_fma_f32 v183, v183, s63, v237
MA 12,216,172
s_waitcnt lgkmcnt(7)
v_fma_f32 v184, v184, s63, v238
v_fma_f32 v185, v185, s63, v238
MA 16,220,160
RT 214,251,10240
v_fma_f32 v186, v186, s63, v238
MA 20,220,164
RT 216,250,6144
v_fma_f32 v187, v187, s63, v238
MA 24,220,168
RT 218,250,14336
v_fma_f32 v188, v188, s63, v239
MA 28,220,172
s_waitcnt lgkmcnt(8)
v_fma_f32 v189, v189, s63, v239
v_fma_f32 v190, v190, s63, v239
MA 32,224,160
ds_read_b32 v247, v252 offset:29704
v_fma_f32 v191, v191, s63, v239
MA 36,224,164
v_max3_f32 v246, v176, v177, v178
v_max3_f32 v246, v246, v179, v182
MA 40,224,168
RT 220,251,6144
v_max3_f32 v246, v246, v180, v181
MA 44,224,172
s_waitcnt lgkmcnt(8)
v_max3_f32 v246, v246, v183, v184
v_max3_f32 v246, v246, v185, v186
MA 48,228,160
RT 222,251,14336
v_max3_f32 v246, v246, v187, v188
MA 52,228,164
RB 224,252,16384
v_max3_f32 v246, v246, v189, v190
MA 56,228,168
s_mov_b32 m0, 156672
v_exp_f32 v198, v198
MA 60,228,172
s_waitcnt lgkmcnt(3)
v_exp_f32 v199, v199
MA 64,208,160
RB 228,252,17408
v_max3_f32 v246, v246, v191, v247
MA 68,208,164
buffer_load_dwordx4 v253, s[44:47], s61 offen offset:0 nt lds
MA 72,208,168
v_cmp_lt_f32 vcc, s64, v246
v_add_f32 v242, v242, v196
MA 76,208,172
v_add_f32 v242, v242, v197
v_add_f32 v242, v242, v198
MA 80,212,160
RT 208,250,6656
v_add_f32 v242, v242, v199
MA 84,212,164
RT 210,250,14848
MA 88,212,168
s_mov_b32 m0, 158592
v_exp_f32 v200, v200
MA 92,212,172
v_exp_f32 v201, v201
MA 192,216,160
RT 212,251,6656
v_add_f32 v243, v243, v200
MA 196,216,164
RT 214,251,14848
v_add_f32 v243, v243, v201
MA 200,216,168
buffer_load_dwordx4 v253, s[44:47], s61 offen offset:128 nt lds
MA 204,216,172
s_waitcnt lgkmcnt(4)
v_exp_f32 v202, v202
MV 128,216,224
s_cbranch_vccnz Lst3_15
Lrt3_15:
v_exp_f32 v176, v176
MV 132,216,228
v_exp_f32 v177, v177
MA 208,220,160
RT 216,250,2560
v_add_f32 v241, v241, v176
MA 212,220,164
RT 218,250,10752
v_add_f32 v241, v241, v177
MA 216,220,168
s_mov_b32 m0, 160512
v_exp_f32 v178, v178
MA 220,220,172
buffer_load_dwordx4 v253, s[44:47], s61 offen offset:256 nt lds
MV 136,220,224
v_exp_f32 v179, v179
MV 140,220,228
s_waitcnt lgkmcnt(4)
v_exp_f32 v180, v180
MA 224,208,160
RT 220,251,2560
v_add_f32 v241, v241, v178
MA 228,208,164
RT 222,251,10752
v_add_f32 v241, v241, v179
MA 232,208,168
v_exp_f32 v181, v181
MA 236,208,172
v_exp_f32 v182, v182
MV 144,208,224
s_mov_b32 m0, 162432
v_exp_f32 v183, v183
MV 148,208,228
s_waitcnt lgkmcnt(4)
CV 195,194,195
CV 194,192,193
MA 240,212,160
RT 208,250,4096
CV 192,176,177
MA 244,212,164
RT 210,250,12288
CV 193,178,179
MA 248,212,168
buffer_load_dwordx4 v253, s[44:47], s61 offen offset:384 nt lds
s_add_u32 s61, s61, 16384
MA 252,212,172
v_add_f32 v242, v242, v180
v_add_f32 v242, v242, v181
MV 152,212,224
v_add_f32 v242, v242, v182
v_add_f32 v242, v242, v183
MV 156,212,228
s_waitcnt lgkmcnt(4)
CV 199,198,199
MA 96,216,160
RT 212,251,4096
CV 198,196,197
MA 100,216,164
RT 214,251,12288
CV 196,180,181
MA 104,216,168
CV 197,182,183
v_add_f32 v243, v243, v202
MA 108,216,172
s_waitcnt lgkmcnt(4)
v_exp_f32 v232, v232
MA 112,220,160
RT 216,250,4608
v_add_f32 v245, v245, v232
MA 116,220,164
RT 218,250,12800
MA 120,220,168
v_exp_f32 v233, v233
MA 124,220,172
s_waitcnt lgkmcnt(4)
v_exp_f32 v234, v234
MA 128,208,160
RT 220,251,4608
v_add_f32 v245, v245, v233
MA 132,208,164
RB 176,249,40960
v_add_f32 v245, v245, v234
MA 136,208,168
RB 180,249,41472
CV 254,232,233
MA 140,208,172
s_waitcnt lgkmcnt(5)
v_exp_f32 v235, v235
MA 144,212,160
RT 222,251,12800
v_add_f32 v245, v245, v235
MA 148,212,164
RB 224,252,8192
CV 255,234,235
MA 152,212,168
RB 208,249,43008
MA 156,212,172
s_waitcnt lgkmcnt(6)
v_exp_f32 v203, v203
MA 160,216,160
RB 212,249,43520
v_add_f32 v243, v243, v203
MA 164,216,164
RB 228,252,9216
CV 203,202,203
MA 168,216,168
ds_write_b64 v252, v[254:255] offset:19464
CV 202,200,201
MA 172,216,172
s_waitcnt lgkmcnt(5)
v_exp_f32 v204, v204
MA 176,220,160
v_exp_f32 v205, v205
MA 180,220,164
v_exp_f32 v206, v206
MA 184,220,168
v_exp_f32 v207, v207
MA 188,220,172
v_exp_f32 v184, v184
s_sub_u32 s62, s62, 1
s_cbranch_scc0 Lloop3
Lpeel3:
s_mov_b32 s48, s24
s_and_b32 s49, s25, 0xffff
s_mov_b32 s50, 3670016
s_and_b32 s77, s26, 15
s_mul_i32 s67, s77, 229376
s_xor_b32 s77, s77, 1
s_mul_i32 s69, s77, 229376
s_mov_b32 s68, 3072
v_bfe_u32 v248, v252, 6, 3
v_add_u32 v248, 24, v248
v_mul_u32_u24 v248, 0x380, v248
v_and_b32 v253, 0x7f, v253
v_add_u32 v253, v253, v248
MV0 160,176,0
RB 216,249,45056
v_add_f32 v244, v244, v204
MV 160,180,4
RB 220,249,45568
v_add_f32 v244, v244, v205
MV0 164,176,32
s_cmp_lg_u32 s65, 0
v_exp_f32 v185, v185
MV 164,180,36
s_cbranch_scc1 Lst3_16
Lrt3_16:
v_exp_f32 v186, v186
MV0 168,176,64
v_exp_f32 v187, v187
MV 168,180,68
v_exp_f32 v188, v188
MV0 172,176,96
v_exp_f32 v189, v189
MV 172,180,100
s_waitcnt lgkmcnt(3)
v_exp_f32 v190, v190
MV0 232,176,224
RB 224,252,10240
v_add_f32 v244, v244, v206
MV 232,180,228
v_exp_f32 v191, v191
MV 160,208,8
RB 176,249,47104
v_add_f32 v244, v244, v207
MV 160,212,12
RB 180,249,47616
v_add_f32 v243, v243, v184
MV 164,208,40
RB 228,252,11264
v_add_f32 v243, v243, v185
MV 164,212,44
MV 168,208,72
v_add_f32 v243, v243, v186
v_add_f32 v243, v243, v187
MV 168,212,76
MV 172,208,104
v_add_f32 v244, v244, v188
v_add_f32 v244, v244, v189
MV 172,212,108
s_waitcnt lgkmcnt(0)
v_add_f32 v244, v244, v190
v_add_f32 v244, v244, v191
MV 232,208,224
RB 224,252,12288
CV 200,184,185
MV 232,212,228
CV 207,206,207
CV 206,204,205
MV 160,216,16
RB 208,249,32768
CV 201,186,187
MV 160,220,20
RB 212,249,33280
CV 204,188,189
MV 164,216,48
RB 228,252,13312
CV 205,190,191
MV 164,220,52
MV 168,216,80
MV 168,220,84
MV 172,216,112
MV 172,220,116
s_waitcnt lgkmcnt(0)
MV 232,216,224
RB 216,249,34816
MV 232,220,228
MV 160,176,24
RB 220,249,35328
MV 160,180,28
RB 224,252,14336
MV 164,176,56
RB 228,252,15360
MV 164,180,60
MV 168,176,88
MV 168,180,92
s_nop 1
v_fma_f32 v160, v160, s63, v236
v_fma_f32 v161, v161, s63, v236
MV 172,176,120
MV 172,180,124
s_waitcnt lgkmcnt(0)
v_fma_f32 v162, v162, s63, v236
v_fma_f32 v163, v163, s63, v236
MV 232,176,224
RB 224,249,36864
v_fma_f32 v164, v164, s63, v237
MV 232,180,228
v_fma_f32 v165, v165, s63, v237
v_fma_f32 v166, v166, s63, v237
MV0 176,208,0
RB 228,249,37376
v_fma_f32 v167, v167, s63, v237
MV 176,212,4
v_fma_f32 v168, v168, s63, v238
v_fma_f32 v169, v169, s63, v238
MV0 180,208,32
v_fma_f32 v170, v170, s63, v238
v_fma_f32 v171, v171, s63, v238
MV 180,212,36
v_fma_f32 v172, v172, s63, v239
MV0 184,208,64
v_fma_f32 v173, v173, s63, v239
v_fma_f32 v174, v174, s63, v239
MV 184,212,68
v_fma_f32 v175, v175, s63, v239
v_fma_f32 v232, v232, s63, v240
MV0 188,208,96
RB 208,249,38912
v_fma_f32 v233, v233, s63, v240
MV 188,212,100
v_fma_f32 v234, v234, s63, v240
v_fma_f32 v235, v235, s63, v240
MV 176,216,8
RB 212,249,39424
v_max3_f32 v246, v160, v161, v162
MV 176,220,12
v_max3_f32 v246, v246, v163, v164
v_max3_f32 v246, v246, v165, v166
MV 180,216,40
v_max3_f32 v246, v246, v167, v168
v_max3_f32 v246, v246, v169, v170
MV 180,220,44
v_max3_f32 v246, v246, v171, v172
MV 184,216,72
v_max3_f32 v246, v246, v173, v174
v_max3_f32 v246, v246, v175, v232
MV 184,220,76
v_max3_f32 v246, v246, v233, v234
v_max_f32 v246, v246, v235
MV 188,216,104
RT 216,250,16384
v_cmp_lt_f32 vcc, s64, v246
MV 188,220,108
s_waitcnt lgkmcnt(3)
MV 176,224,16
RT 218,250,24576
MV 176,228,20
RT 220,251,16384
MV 180,224,48
RT 222,251,24576
MV 180,228,52
s_cbranch_vccnz Lst3_17
Lrt3_17:
v_exp_f32 v160, v160
MV 184,224,80
v_exp_f32 v161, v161
MV 184,228,84
v_exp_f32 v162, v162
MV 188,224,112
RT 224,250,16896
v_add_f32 v241, v241, v160
MV 188,228,116
s_waitcnt lgkmcnt(5)
v_exp_f32 v163, v163
MV 176,208,24
RT 226,250,25088
v_add_f32 v241, v241, v161
MV 176,212,28
RT 228,251,16896
v_add_f32 v241, v241, v162
MV 180,208,56
RT 230,251,25088
v_add_f32 v241, v241, v163
MV 180,212,60
v_exp_f32 v164, v164
MV 184,208,88
v_exp_f32 v165, v165
MV 184,212,92
v_fma_f32 v176, v176, s63, v236
v_fma_f32 v177, v177, s63, v236
MV 188,208,120
s_waitcnt vmcnt(5) lgkmcnt(6)
v_fma_f32 v178, v178, s63, v236
v_fma_f32 v179, v179, s63, v236
MV 188,212,124
s_barrier
v_fma_f32 v182, v182, s63, v237
MA 0,216,192
RT 208,250,18432
v_fma_f32 v180, v180, s63, v237
MA 4,216,196
RT 210,250,26624
v_fma_f32 v181, v181, s63, v237
MA 8,216,200
RT 212,251,18432
v_fma_f32 v183, v183, s63, v237
MA 12,216,204
s_waitcnt lgkmcnt(7)
v_fma_f32 v184, v184, s63, v238
v_fma_f32 v185, v185, s63, v238
MA 16,220,192
RT 214,251,26624
v_fma_f32 v186, v186, s63, v238
MA 20,220,196
RT 216,250,22528
v_fma_f32 v187, v187, s63, v238
MA 24,220,200
RT 218,250,30720
v_fma_f32 v188, v188, s63, v239
MA 28,220,204
s_waitcnt lgkmcnt(8)
v_fma_f32 v189, v189, s63, v239
v_fma_f32 v190, v190, s63, v239
MA 32,224,192
ds_read_b32 v247, v252 offset:29704
v_fma_f32 v191, v191, s63, v239
MA 36,224,196
v_max3_f32 v246, v176, v177, v178
v_max3_f32 v246, v246, v179, v182
MA 40,224,200
RT 220,251,22528
v_max3_f32 v246, v246, v180, v181
MA 44,224,204
s_waitcnt lgkmcnt(8)
v_max3_f32 v246, v246, v183, v184
v_max3_f32 v246, v246, v185, v186
MA 48,228,192
RT 222,251,30720
v_max3_f32 v246, v246, v187, v188
MA 52,228,196
RB 224,252,18432
v_max3_f32 v246, v246, v189, v190
MA 56,228,200
s_mov_b32 m0, 3072
v_exp_f32 v166, v166
MA 60,228,204
s_waitcnt lgkmcnt(3)
v_exp_f32 v167, v167
MA 64,208,192
RB 228,252,19456
v_max3_f32 v246, v246, v191, v247
MA 68,208,196
buffer_load_dwordx4 v253, s[48:51], s67 offen offset:0 lds
MA 72,208,200
v_cmp_lt_f32 vcc, s64, v246
v_add_f32 v242, v242, v164
MA 76,208,204
v_add_f32 v242, v242, v165
v_add_f32 v242, v242, v166
MA 80,212,192
RT 208,250,23040
v_add_f32 v242, v242, v167
MA 84,212,196
RT 210,250,31232
MA 88,212,200
s_mov_b32 m0, 7040
v_exp_f32 v168, v168
MA 92,212,204
v_exp_f32 v169, v169
MA 192,216,192
RT 212,251,23040
v_add_f32 v243, v243, v168
MA 196,216,196
RT 214,251,31232
v_add_f32 v243, v243, v169
MA 200,216,200
buffer_load_dwordx4 v253, s[48:51], s67 offen offset:128 lds
MA 204,216,204
s_waitcnt lgkmcnt(4)
v_exp_f32 v170, v170
MV 128,216,224
s_cbranch_vccnz Lst3_18
Lrt3_18:
v_exp_f32 v176, v176
MV 132,216,228
v_exp_f32 v177, v177
MA 208,220,192
RT 216,250,18944
v_add_f32 v241, v241, v176
MA 212,220,196
RT 218,250,27136
v_add_f32 v241, v241, v177
MA 216,220,200
s_mov_b32 m0, 11008
v_exp_f32 v178, v178
MA 220,220,204
buffer_load_dwordx4 v253, s[48:51], s67 offen offset:256 lds
MV 136,220,224
v_exp_f32 v179, v179
MV 140,220,228
s_waitcnt lgkmcnt(4)
v_exp_f32 v180, v180
MA 224,208,192
RT 220,251,18944
v_add_f32 v241, v241, v178
MA 228,208,196
RT 222,251,27136
v_add_f32 v241, v241, v179
MA 232,208,200
v_exp_f32 v181, v181
MA 236,208,204
v_exp_f32 v182, v182
MV 144,208,224
s_mov_b32 m0, 14976
v_exp_f32 v183, v183
MV 148,208,228
s_waitcnt lgkmcnt(4)
CV 163,162,163
CV 162,160,161
MA 240,212,192
RT 208,250,20480
CV 160,176,177
MA 244,212,196
RT 210,250,28672
CV 161,178,179
MA 248,212,200
buffer_load_dwordx4 v253, s[48:51], s67 offen offset:384 lds
MA 252,212,204
v_add_f32 v242, v242, v180
v_add_f32 v242, v242, v181
MV 152,212,224
v_add_f32 v242, v242, v182
v_add_f32 v242, v242, v183
MV 156,212,228
s_waitcnt lgkmcnt(4)
CV 167,166,167
MA 96,216,192
RT 212,251,20480
CV 166,164,165
MA 100,216,196
RT 214,251,28672
CV 164,180,181
MA 104,216,200
CV 165,182,183
v_add_f32 v243, v243, v170
MA 108,216,204
s_waitcnt lgkmcnt(4)
v_exp_f32 v232, v232
MA 112,220,192
RT 216,250,20992
v_add_f32 v245, v245, v232
MA 116,220,196
RT 218,250,29184
MA 120,220,200
v_exp_f32 v233, v233
MA 124,220,204
s_waitcnt lgkmcnt(4)
v_exp_f32 v234, v234
MA 128,208,192
RT 220,251,20992
v_add_f32 v245, v245, v233
MA 132,208,196
RB 176,249,57344
v_add_f32 v245, v245, v234
MA 136,208,200
RB 180,249,57856
CV 254,232,233
MA 140,208,204
s_waitcnt lgkmcnt(5)
v_exp_f32 v235, v235
MA 144,212,192
RT 222,251,29184
v_add_f32 v245, v245, v235
MA 148,212,196
RB 224,252,8192
CV 255,234,235
MA 152,212,200
RB 208,249,59392
MA 156,212,204
s_waitcnt lgkmcnt(6)
v_exp_f32 v171, v171
MA 160,216,192
RB 212,249,59904
v_add_f32 v243, v243, v171
MA 164,216,196
RB 228,252,9216
CV 171,170,171
MA 168,216,200
ds_write_b64 v252, v[254:255] offset:17416
CV 170,168,169
MA 172,216,204
s_waitcnt lgkmcnt(5)
v_exp_f32 v172, v172
MA 176,220,192
v_exp_f32 v173, v173
MA 180,220,196
v_exp_f32 v174, v174
MA 184,220,200
v_exp_f32 v175, v175
MA 188,220,204
v_exp_f32 v184, v184
MV0 192,176,0
RB 216,249,61440
v_add_f32 v244, v244, v172
MV 192,180,4
RB 220,249,61952
v_add_f32 v244, v244, v173
MV0 196,176,32
s_cmp_lg_u32 s65, 0
v_exp_f32 v185, v185
MV 196,180,36
s_cbranch_scc1 Lst3_19
Lrt3_19:
v_exp_f32 v186, v186
MV0 200,176,64
v_exp_f32 v187, v187
MV 200,180,68
v_exp_f32 v188, v188
MV0 204,176,96
s_mov_b32 m0, 18944
v_exp_f32 v189, v189
MV 204,180,100
s_waitcnt lgkmcnt(3)
v_exp_f32 v190, v190
MV0 232,176,224
RB 224,252,10240
v_add_f32 v244, v244, v174
MV 232,180,228
v_exp_f32 v191, v191
MV 192,208,8
RB 176,249,63488
v_add_f32 v244, v244, v175
MV 192,212,12
RB 180,249,64000
v_add_f32 v243, v243, v184
MV 196,208,40
RB 228,252,11264
v_add_f32 v243, v243, v185
MV 196,212,44
buffer_load_dwordx4 v253, s[48:51], s67 offen offset:512 lds
MV 200,208,72
s_mov_b32 m0, 22912
v_add_f32 v243, v243, v186
v_add_f32 v243, v243, v187
MV 200,212,76
buffer_load_dwordx4 v253, s[48:51], s67 offen offset:640 lds
MV 204,208,104
v_add_f32 v244, v244, v188
v_add_f32 v244, v244, v189
MV 204,212,108
s_waitcnt lgkmcnt(0)
v_add_f32 v244, v244, v190
v_add_f32 v244, v244, v191
MV 232,208,224
RB 224,252,12288
CV 168,184,185
MV 232,212,228
CV 175,174,175
CV 174,172,173
MV 192,216,16
RB 208,249,49152
CV 169,186,187
MV 192,220,20
RB 212,249,49664
CV 172,188,189
MV 196,216,48
RB 228,252,13312
CV 173,190,191
MV 196,220,52
s_mov_b32 m0, 26880
MV 200,216,80
buffer_load_dwordx4 v253, s[48:51], s67 offen offset:768 lds
s_add_u32 s67, s67, 28672
MV 200,220,84
MV 204,216,112
MV 204,220,116
s_waitcnt lgkmcnt(0)
MV 232,216,224
RB 216,249,51200
MV 232,220,228
MV 192,176,24
RB 220,249,51712
MV 192,180,28
RB 224,252,14336
MV 196,176,56
RB 228,252,15360
MV 196,180,60
s_mov_b32 m0, 31744
MV 200,176,88
buffer_load_dwordx4 v253, s[48:51], s67 offen offset:0 lds
MV 200,180,92
v_fma_f32 v192, v192, s63, v236
v_fma_f32 v193, v193, s63, v236
MV 204,176,120
MV 204,180,124
s_waitcnt lgkmcnt(0)
v_fma_f32 v194, v194, s63, v236
v_fma_f32 v195, v195, s63, v236
MV 232,176,224
RB 224,249,53248
v_fma_f32 v196, v196, s63, v237
MV 232,180,228
v_fma_f32 v197, v197, s63, v237
v_fma_f32 v198, v198, s63, v237
MV0 176,208,0
RB 228,249,53760
v_fma_f32 v199, v199, s63, v237
MV 176,212,4
v_fma_f32 v200, v200, s63, v238
v_fma_f32 v201, v201, s63, v238
MV0 180,208,32
v_fma_f32 v202, v202, s63, v238
v_fma_f32 v203, v203, s63, v238
MV 180,212,36
v_fma_f32 v204, v204, s63, v239
MV0 184,208,64
v_fma_f32 v205, v205, s63, v239
v_fma_f32 v206, v206, s63, v239
MV 184,212,68
v_fma_f32 v207, v207, s63, v239
v_fma_f32 v232, v232, s63, v240
MV0 188,208,96
RB 208,249,55296
v_fma_f32 v233, v233, s63, v240
MV 188,212,100
v_fma_f32 v234, v234, s63, v240
v_fma_f32 v235, v235, s63, v240
MV 176,216,8
RB 212,249,55808
v_max3_f32 v246, v192, v193, v194
MV 176,220,12
v_max3_f32 v246, v246, v195, v196
v_max3_f32 v246, v246, v197, v198
MV 180,216,40
v_max3_f32 v246, v246, v199, v200
v_max3_f32 v246, v246, v201, v202
MV 180,220,44
v_max3_f32 v246, v246, v203, v204
MV 184,216,72
v_max3_f32 v246, v246, v205, v206
v_max3_f32 v246, v246, v207, v232
MV 184,220,76
v_max3_f32 v246, v246, v233, v234
v_max_f32 v246, v246, v235
MV 188,216,104
RT 216,250,32768
v_cmp_lt_f32 vcc, s64, v246
MV 188,220,108
s_waitcnt lgkmcnt(3)
MV 176,224,16
RT 218,250,40960
MV 176,228,20
RT 220,251,32768
MV 180,224,48
RT 222,251,40960
MV 180,228,52
s_cbranch_vccnz Lst3_20
Lrt3_20:
v_exp_f32 v192, v192
MV 184,224,80
v_exp_f32 v193, v193
MV 184,228,84
v_exp_f32 v194, v194
MV 188,224,112
RT 224,250,33280
v_add_f32 v241, v241, v192
MV 188,228,116
s_waitcnt lgkmcnt(5)
v_exp_f32 v195, v195
MV 176,208,24
RT 226,250,41472
v_add_f32 v241, v241, v193
MV 176,212,28
RT 228,251,33280
v_add_f32 v241, v241, v194
MV 180,208,56
RT 230,251,41472
v_add_f32 v241, v241, v195
MV 180,212,60
v_exp_f32 v196, v196
MV 184,208,88
v_exp_f32 v197, v197
MV 184,212,92
v_fma_f32 v176, v176, s63, v236
v_fma_f32 v177, v177, s63, v236
MV 188,208,120
s_waitcnt vmcnt(8) lgkmcnt(6)
v_fma_f32 v178, v178, s63, v236
v_fma_f32 v179, v179, s63, v236
MV 188,212,124
s_barrier
v_fma_f32 v182, v182, s63, v237
MA 0,216,160
RT 208,250,34816
v_fma_f32 v180, v180, s63, v237
MA 4,216,164
RT 210,250,43008
v_fma_f32 v181, v181, s63, v237
MA 8,216,168
RT 212,251,34816
v_fma_f32 v183, v183, s63, v237
MA 12,216,172
s_waitcnt lgkmcnt(7)
v_fma_f32 v184, v184, s63, v238
v_fma_f32 v185, v185, s63, v238
MA 16,220,160
RT 214,251,43008
v_fma_f32 v186, v186, s63, v238
MA 20,220,164
RT 216,250,38912
v_fma_f32 v187, v187, s63, v238
MA 24,220,168
RT 218,250,47104
v_fma_f32 v188, v188, s63, v239
MA 28,220,172
s_waitcnt lgkmcnt(8)
v_fma_f32 v189, v189, s63, v239
v_fma_f32 v190, v190, s63, v239
MA 32,224,160
ds_read_b32 v247, v252 offset:29704
v_fma_f32 v191, v191, s63, v239
MA 36,224,164
v_max3_f32 v246, v176, v177, v178
v_max3_f32 v246, v246, v179, v182
MA 40,224,168
RT 220,251,38912
v_max3_f32 v246, v246, v180, v181
MA 44,224,172
s_waitcnt lgkmcnt(8)
v_max3_f32 v246, v246, v183, v184
v_max3_f32 v246, v246, v185, v186
MA 48,228,160
RT 222,251,47104
v_max3_f32 v246, v246, v187, v188
MA 52,228,164
RB 224,252,16384
v_max3_f32 v246, v246, v189, v190
MA 56,228,168
s_mov_b32 m0, 35712
v_exp_f32 v198, v198
MA 60,228,172
s_waitcnt lgkmcnt(3)
v_exp_f32 v199, v199
MA 64,208,160
RB 228,252,17408
v_max3_f32 v246, v246, v191, v247
MA 68,208,164
buffer_load_dwordx4 v253, s[48:51], s67 offen offset:128 lds
MA 72,208,168
v_cmp_lt_f32 vcc, s64, v246
v_add_f32 v242, v242, v196
MA 76,208,172
v_add_f32 v242, v242, v197
v_add_f32 v242, v242, v198
MA 80,212,160
RT 208,250,39424
v_add_f32 v242, v242, v199
MA 84,212,164
RT 210,250,47616
MA 88,212,168
s_mov_b32 m0, 39680
v_exp_f32 v200, v200
MA 92,212,172
v_exp_f32 v201, v201
MA 192,216,160
RT 212,251,39424
v_add_f32 v243, v243, v200
MA 196,216,164
RT 214,251,47616
v_add_f32 v243, v243, v201
MA 200,216,168
buffer_load_dwordx4 v253, s[48:51], s67 offen offset:256 lds
MA 204,216,172
s_waitcnt lgkmcnt(4)
v_exp_f32 v202, v202
MV 128,216,224
s_cbranch_vccnz Lst3_21
Lrt3_21:
v_exp_f32 v176, v176
MV 132,216,228
v_exp_f32 v177, v177
MA 208,220,160
RT 216,250,35328
v_add_f32 v241, v241, v176
MA 212,220,164
RT 218,250,43520
v_add_f32 v241, v241, v177
MA 216,220,168
s_mov_b32 m0, 43648
v_exp_f32 v178, v178
MA 220,220,172
buffer_load_dwordx4 v253, s[48:51], s67 offen offset:384 lds
MV 136,220,224
v_exp_f32 v179, v179
MV 140,220,228
s_waitcnt lgkmcnt(4)
v_exp_f32 v180, v180
MA 224,208,160
RT 220,251,35328
v_add_f32 v241, v241, v178
MA 228,208,164
RT 222,251,43520
v_add_f32 v241, v241, v179
MA 232,208,168
v_exp_f32 v181, v181
MA 236,208,172
v_exp_f32 v182, v182
MV 144,208,224
s_mov_b32 m0, 47616
v_exp_f32 v183, v183
MV 148,208,228
s_waitcnt lgkmcnt(4)
CV 195,194,195
CV 194,192,193
MA 240,212,160
RT 208,250,36864
CV 192,176,177
MA 244,212,164
RT 210,250,45056
CV 193,178,179
MA 248,212,168
buffer_load_dwordx4 v253, s[48:51], s67 offen offset:512 lds
MA 252,212,172
v_add_f32 v242, v242, v180
v_add_f32 v242, v242, v181
MV 152,212,224
v_add_f32 v242, v242, v182
v_add_f32 v242, v242, v183
MV 156,212,228
s_waitcnt lgkmcnt(4)
CV 199,198,199
MA 96,216,160
RT 212,251,36864
CV 198,196,197
MA 100,216,164
RT 214,251,45056
CV 196,180,181
MA 104,216,168
CV 197,182,183
v_add_f32 v243, v243, v202
MA 108,216,172
s_waitcnt lgkmcnt(4)
v_exp_f32 v232, v232
MA 112,220,160
RT 216,250,37376
v_add_f32 v245, v245, v232
MA 116,220,164
RT 218,250,45568
MA 120,220,168
v_exp_f32 v233, v233
MA 124,220,172
s_waitcnt lgkmcnt(4)
v_exp_f32 v234, v234
MA 128,208,160
RT 220,251,37376
v_add_f32 v245, v245, v233
MA 132,208,164
RB 176,249,8192
v_add_f32 v245, v245, v234
)ASM"
R"ASM(
MA 136,208,168
RB 180,249,8704
CV 254,232,233
MA 140,208,172
s_waitcnt lgkmcnt(5)
v_exp_f32 v235, v235
MA 144,212,160
RT 222,251,45568
v_add_f32 v245, v245, v235
MA 148,212,164
RB 224,252,8192
CV 255,234,235
MA 152,212,168
RB 208,249,10240
MA 156,212,172
s_waitcnt lgkmcnt(6)
v_exp_f32 v203, v203
MA 160,216,160
RB 212,249,10752
v_add_f32 v243, v243, v203
MA 164,216,164
RB 228,252,9216
CV 203,202,203
MA 168,216,168
ds_write_b64 v252, v[254:255] offset:19464
CV 202,200,201
MA 172,216,172
s_waitcnt lgkmcnt(5)
v_exp_f32 v204, v204
MA 176,220,160
v_exp_f32 v205, v205
MA 180,220,164
v_exp_f32 v206, v206
MA 184,220,168
v_exp_f32 v207, v207
MA 188,220,172
s_waitcnt lgkmcnt(1)
v_exp_f32 v184, v184
s_mov_b32 m0, 51584
s_nop 0
buffer_load_dwordx4 v253, s[48:51], s67 offen offset:640 lds
s_mov_b32 m0, 55552
s_nop 0
buffer_load_dwordx4 v253, s[48:51], s67 offen offset:768 lds
s_add_u32 s67, s67, 28672
v_add_f32 v244, v244, v204
v_add_f32 v244, v244, v205
s_cmp_lg_u32 s65, 0
v_exp_f32 v185, v185
s_cbranch_scc1 Lst3_22
Lrt3_22:
v_exp_f32 v186, v186
v_exp_f32 v187, v187
v_exp_f32 v188, v188
v_exp_f32 v189, v189
v_exp_f32 v190, v190
v_add_f32 v244, v244, v206
v_exp_f32 v191, v191
v_add_f32 v244, v244, v207
v_add_f32 v243, v243, v184
v_add_f32 v243, v243, v185
v_add_f32 v243, v243, v186
v_add_f32 v243, v243, v187
v_add_f32 v244, v244, v188
v_add_f32 v244, v244, v189
v_add_f32 v244, v244, v190
v_add_f32 v244, v244, v191
CV 200,184,185
CV 207,206,207
CV 206,204,205
CV 201,186,187
CV 204,188,189
CV 205,190,191
RT 216,250,49152
RT 218,250,57344
RT 220,251,49152
RT 222,251,57344
RT 224,250,49664
RT 226,250,57856
RT 228,251,49664
RT 230,251,57856
v_mov_b32 v176, v241
v_mov_b32 v177, v242
v_mov_b32 v178, v243
v_mov_b32 v179, v244
v_mov_b32 v180, v245
v_permlane16_swap_b32 v241, v176
v_permlane16_swap_b32 v242, v177
v_permlane16_swap_b32 v243, v178
v_permlane16_swap_b32 v244, v179
v_permlane16_swap_b32 v245, v180
v_add_f32 v241, v241, v176
v_add_f32 v242, v242, v177
v_add_f32 v243, v243, v178
v_add_f32 v244, v244, v179
v_add_f32 v245, v245, v180
v_mov_b32 v176, v241
v_mov_b32 v177, v242
v_mov_b32 v178, v243
v_mov_b32 v179, v244
v_mov_b32 v180, v245
v_permlane32_swap_b32 v241, v176
v_permlane32_swap_b32 v242, v177
v_permlane32_swap_b32 v243, v178
v_permlane32_swap_b32 v244, v179
v_permlane32_swap_b32 v245, v180
v_add_f32 v241, v241, v176
v_add_f32 v242, v242, v177
v_add_f32 v243, v243, v178
v_add_f32 v244, v244, v179
v_add_f32 v245, v245, v180
ds_write_b32 v252, v245 offset:30732
v_rcp_f32 v241, v241
v_rcp_f32 v242, v242
v_rcp_f32 v243, v243
v_rcp_f32 v244, v244
s_waitcnt lgkmcnt(0)
s_barrier
RB 236,252,30720
MA 0,216,192
RT 208,250,51200
MA 4,216,196
RT 210,250,59392
MA 8,216,200
RT 212,251,51200
MA 12,216,204
MA 16,220,192
RT 214,251,59392
MA 20,220,196
RT 216,250,55296
MA 24,220,200
RT 218,250,63488
MA 28,220,204
MA 32,224,192
MA 36,224,196
MA 40,224,200
RT 220,251,55296
MA 44,224,204
MA 48,228,192
RT 222,251,63488
MA 52,228,196
RB 224,252,18432
MA 56,228,200
s_waitcnt lgkmcnt(9)
v_add_f32 v236, v236, v237
MA 60,228,204
s_waitcnt lgkmcnt(7)
v_add_f32 v237, v238, v239
MA 64,208,192
RB 228,252,19456
MA 68,208,196
v_rcp_f32 v236, v236
MA 72,208,200
v_rcp_f32 v237, v237
MA 76,208,204
s_waitcnt lgkmcnt(6)
MA 80,212,192
RT 208,250,55808
MA 84,212,196
RT 210,250,64000
MA 88,212,200
MA 92,212,204
s_waitcnt lgkmcnt(6)
MA 192,216,192
RT 212,251,55808
MA 196,216,196
RT 214,251,64000
MA 200,216,200
MA 204,216,204
s_waitcnt lgkmcnt(5)
MV 128,216,224
s_waitcnt lgkmcnt(4)
MV 132,216,228
MA 208,220,192
RT 216,250,51712
MA 212,220,196
RT 218,250,59904
MA 216,220,200
MA 220,220,204
MV 136,220,224
MV 140,220,228
s_waitcnt lgkmcnt(4)
MA 224,208,192
RT 220,251,51712
MA 228,208,196
RT 222,251,59904
MA 232,208,200
MA 236,208,204
MV 144,208,224
MV 148,208,228
s_waitcnt lgkmcnt(4)
MA 240,212,192
RT 208,250,53248
MA 244,212,196
RT 210,250,61440
MA 248,212,200
MA 252,212,204
MV 152,212,224
MV 156,212,228
s_waitcnt lgkmcnt(4)
MA 96,216,192
RT 212,251,53248
MA 100,216,196
RT 214,251,61440
MA 104,216,200
MA 108,216,204
s_waitcnt lgkmcnt(4)
MA 112,220,192
RT 216,250,53760
MA 116,220,196
RT 218,250,61952
MA 120,220,200
MA 124,220,204
s_waitcnt lgkmcnt(4)
MA 128,208,192
RT 220,251,53760
MA 132,208,196
MA 136,208,200
MA 140,208,204
s_waitcnt lgkmcnt(3)
MA 144,212,192
RT 222,251,61952
MA 148,212,196
MA 152,212,200
MA 156,212,204
s_waitcnt lgkmcnt(2)
MA 160,216,192
MA 164,216,196
MA 168,216,200
MA 172,216,204
s_waitcnt lgkmcnt(0)
MA 176,220,192
MA 180,220,196
MA 184,220,200
MA 188,220,204
s_waitcnt vmcnt(0)
s_waitcnt lgkmcnt(0)
s_barrier
s_getpc_b64 s[80:81]
Ljt3:
s_add_u32 s80, s80, LT-Ljt3
s_addc_u32 s81, s81, 0
s_setpc_b64 s[80:81]
Lst3_0:
s_call_b64 s[80:81], LR2f_3
s_branch Lrt3_0
Lst3_1:
s_call_b64 s[80:81], LH_3
s_branch Lrt3_1
Lst3_2:
s_call_b64 s[80:81], LR1_3_1
s_branch Lrt3_2
Lst3_3:
s_call_b64 s[80:81], LR2_3_1
s_branch Lrt3_3
Lst3_4:
s_call_b64 s[80:81], LH_3
s_branch Lrt3_4
Lst3_5:
s_call_b64 s[80:81], LR1_3_0
s_branch Lrt3_5
Lst3_6:
s_call_b64 s[80:81], LR2_3_0
s_branch Lrt3_6
Lst3_7:
s_call_b64 s[80:81], LH_3
s_branch Lrt3_7
Lst3_8:
s_call_b64 s[80:81], LR1_3_1
s_branch Lrt3_8
Lst3_9:
s_call_b64 s[80:81], LR2_3_1
s_branch Lrt3_9
Lst3_10:
s_call_b64 s[80:81], LH_3
s_branch Lrt3_10
Lst3_11:
s_call_b64 s[80:81], LR1_3_0
s_branch Lrt3_11
Lst3_12:
s_call_b64 s[80:81], LR2_3_0
s_branch Lrt3_12
Lst3_13:
s_call_b64 s[80:81], LH_3
s_branch Lrt3_13
Lst3_14:
s_call_b64 s[80:81], LR1_3_1
s_branch Lrt3_14
Lst3_15:
s_call_b64 s[80:81], LR2_3_1
s_branch Lrt3_15
Lst3_16:
s_call_b64 s[80:81], LH_3
s_branch Lrt3_16
Lst3_17:
s_call_b64 s[80:81], LR1_3_0
s_branch Lrt3_17
Lst3_18:
s_call_b64 s[80:81], LR2_3_0
s_branch Lrt3_18
Lst3_19:
s_call_b64 s[80:81], LH_3
s_branch Lrt3_19
Lst3_20:
s_call_b64 s[80:81], LR1_3_1
s_branch Lrt3_20
Lst3_21:
s_call_b64 s[80:81], LR2_3_1
s_branch Lrt3_21
Lst3_22:
s_call_b64 s[80:81], LH_3
s_branch Lrt3_22
LR1_3_0:
s_nop 8
s_waitcnt lgkmcnt(0)
s_bitcmp1_b32 s65, 0
s_cbranch_scc1 Lsv3_10
ds_write_b128 v252, v[236:239] offset:26624
s_or_b32 s65, s65, 1
s_waitcnt lgkmcnt(0)
Lsv3_10:
v_max3_f32 v246, v160, v161, v162
v_max_f32 v246, v246, v163
v_mov_b32 v248, v246
s_nop 1
v_permlane16_swap_b32 v246, v248
v_max_f32 v246, v246, v248
v_mov_b32 v248, v246
s_nop 1
v_permlane32_swap_b32 v246, v248
v_max_f32 v246, v246, v248
v_max_f32 v246, 0, v246
v_sub_f32 v236, v236, v246
v_sub_f32 v160, v160, v246
v_sub_f32 v161, v161, v246
v_sub_f32 v162, v162, v246
v_sub_f32 v163, v163, v246
v_exp_f32 v248, -v246
s_nop 0
v_mul_f32 v241, v241, v248
v_max3_f32 v246, v164, v165, v166
v_max_f32 v246, v246, v167
v_mov_b32 v248, v246
s_nop 1
v_permlane16_swap_b32 v246, v248
v_max_f32 v246, v246, v248
v_mov_b32 v248, v246
s_nop 1
v_permlane32_swap_b32 v246, v248
v_max_f32 v246, v246, v248
v_max_f32 v246, 0, v246
v_sub_f32 v237, v237, v246
v_sub_f32 v164, v164, v246
v_sub_f32 v165, v165, v246
v_sub_f32 v166, v166, v246
v_sub_f32 v167, v167, v246
v_exp_f32 v248, -v246
s_nop 0
v_mul_f32 v242, v242, v248
v_max3_f32 v246, v168, v169, v170
v_max_f32 v246, v246, v171
v_mov_b32 v248, v246
s_nop 1
v_permlane16_swap_b32 v246, v248
v_max_f32 v246, v246, v248
v_mov_b32 v248, v246
s_nop 1
v_permlane32_swap_b32 v246, v248
v_max_f32 v246, v246, v248
v_max_f32 v246, 0, v246
v_sub_f32 v238, v238, v246
v_sub_f32 v168, v168, v246
v_sub_f32 v169, v169, v246
v_sub_f32 v170, v170, v246
v_sub_f32 v171, v171, v246
v_exp_f32 v248, -v246
s_nop 0
v_mul_f32 v243, v243, v248
v_max3_f32 v246, v172, v173, v174
v_max_f32 v246, v246, v175
v_mov_b32 v248, v246
s_nop 1
v_permlane16_swap_b32 v246, v248
v_max_f32 v246, v246, v248
v_mov_b32 v248, v246
s_nop 1
v_permlane32_swap_b32 v246, v248
v_max_f32 v246, v246, v248
v_max_f32 v246, 0, v246
v_sub_f32 v239, v239, v246
v_sub_f32 v172, v172, v246
v_sub_f32 v173, v173, v246
v_sub_f32 v174, v174, v246
v_sub_f32 v175, v175, v246
v_exp_f32 v248, -v246
s_nop 0
v_mul_f32 v244, v244, v248
v_max3_f32 v246, v232, v233, v234
v_max_f32 v246, v246, v235
v_mov_b32 v248, v246
s_nop 1
v_permlane16_swap_b32 v246, v248
v_max_f32 v246, v246, v248
v_mov_b32 v248, v246
s_nop 1
v_permlane32_swap_b32 v246, v248
v_max_f32 v246, v246, v248
v_max_f32 v246, 0, v246
v_sub_f32 v246, v240, v246
ds_write_b32 v252, v246 offset:28684
v_mov_b32 v248, 0x7149f2ca
ds_write_b32 v252, v248 offset:29704
s_waitcnt lgkmcnt(0)
s_nop 2
s_setpc_b64 s[80:81]
LR2_3_0:
s_nop 8
s_waitcnt lgkmcnt(0)
v_readfirstlane_b32 s77, v247
s_bitcmp1_b32 s65, 0
s_cbranch_scc1 Lsv3_200
ds_write_b128 v252, v[236:239] offset:26624
s_or_b32 s65, s65, 1
s_waitcnt lgkmcnt(0)
Lsv3_200:
v_max3_f32 v246, v176, v177, v178
v_max_f32 v246, v246, v179
v_mov_b32 v248, v246
s_nop 1
v_permlane16_swap_b32 v246, v248
v_max_f32 v246, v246, v248
v_mov_b32 v248, v246
s_nop 1
v_permlane32_swap_b32 v246, v248
v_max_f32 v246, v246, v248
v_max_f32 v246, 0, v246
v_sub_f32 v236, v236, v246
v_sub_f32 v176, v176, v246
v_sub_f32 v177, v177, v246
v_sub_f32 v178, v178, v246
v_sub_f32 v179, v179, v246
v_exp_f32 v248, -v246
s_nop 0
v_mul_f32 v241, v241, v248
v_mul_f32 v160, v160, v248
v_mul_f32 v161, v161, v248
v_mul_f32 v162, v162, v248
v_mul_f32 v163, v163, v248
v_max3_f32 v246, v180, v181, v182
v_max_f32 v246, v246, v183
v_mov_b32 v248, v246
s_nop 1
v_permlane16_swap_b32 v246, v248
v_max_f32 v246, v246, v248
v_mov_b32 v248, v246
s_nop 1
v_permlane32_swap_b32 v246, v248
v_max_f32 v246, v246, v248
v_max_f32 v246, 0, v246
v_sub_f32 v237, v237, v246
v_sub_f32 v180, v180, v246
v_sub_f32 v181, v181, v246
v_sub_f32 v182, v182, v246
v_sub_f32 v183, v183, v246
v_exp_f32 v248, -v246
s_nop 0
v_mul_f32 v242, v242, v248
v_mul_f32 v164, v164, v248
v_mul_f32 v165, v165, v248
v_mul_f32 v166, v166, v248
v_mul_f32 v167, v167, v248
v_max3_f32 v246, v184, v185, v186
v_max_f32 v246, v246, v187
v_mov_b32 v248, v246
s_nop 1
v_permlane16_swap_b32 v246, v248
v_max_f32 v246, v246, v248
v_mov_b32 v248, v246
s_nop 1
v_permlane32_swap_b32 v246, v248
v_max_f32 v246, v246, v248
v_max_f32 v246, 0, v246
v_sub_f32 v238, v238, v246
v_sub_f32 v184, v184, v246
v_sub_f32 v185, v185, v246
v_sub_f32 v186, v186, v246
v_sub_f32 v187, v187, v246
v_exp_f32 v248, -v246
s_nop 0
v_mul_f32 v243, v243, v248
v_mul_f32 v168, v168, v248
v_mul_f32 v169, v169, v248
v_mul_f32 v170, v170, v248
v_sub_f32 v171, v171, v246
v_max3_f32 v246, v188, v189, v190
v_max_f32 v246, v246, v191
v_mov_b32 v248, v246
s_nop 1
v_permlane16_swap_b32 v246, v248
v_max_f32 v246, v246, v248
v_mov_b32 v248, v246
s_nop 1
v_permlane32_swap_b32 v246, v248
v_max_f32 v246, v246, v248
v_max_f32 v246, 0, v246
v_sub_f32 v239, v239, v246
v_sub_f32 v188, v188, v246
v_sub_f32 v189, v189, v246
v_sub_f32 v190, v190, v246
v_sub_f32 v191, v191, v246
v_exp_f32 v248, -v246
s_nop 0
v_mul_f32 v244, v244, v248
v_sub_f32 v172, v172, v246
v_sub_f32 v173, v173, v246
v_sub_f32 v174, v174, v246
v_sub_f32 v175, v175, v246
s_cmp_eq_u32 s77, 0
s_cbranch_scc1 LR2s_3_0
ds_read_b32 v246, v252 offset:29696
ds_read_b32 v247, v252 offset:28672
ds_read_b32 v248, v252 offset:28676
s_waitcnt lgkmcnt(0)
v_min3_f32 v254, v246, v247, v248
v_sub_f32 v246, v254, v246
v_exp_f32 v246, v246
s_nop 0
ds_write_b32 v252, v246 offset:27648
s_waitcnt lgkmcnt(0)
ds_read_b32 v246, v252 offset:29700
ds_read_b32 v247, v252 offset:28680
ds_read_b32 v248, v252 offset:28684
s_waitcnt lgkmcnt(0)
v_min3_f32 v255, v246, v247, v248
v_sub_f32 v246, v255, v246
v_exp_f32 v246, v246
s_nop 0
ds_write_b32 v252, v246 offset:27652
s_waitcnt lgkmcnt(0)
v_sub_f32 v246, v240, v255
v_mov_b32 v240, v255
v_sub_f32 v232, v232, v246
v_sub_f32 v233, v233, v246
v_sub_f32 v234, v234, v246
v_sub_f32 v235, v235, v246
v_exp_f32 v248, -v246
s_nop 0
v_mul_f32 v245, v245, v248
s_or_b32 s65, s65, 2
s_waitcnt lgkmcnt(0)
s_barrier
s_barrier
LR2s_3_0:
s_waitcnt lgkmcnt(0)
s_nop 2
s_setpc_b64 s[80:81]
LR2f_3:
s_nop 8
s_waitcnt lgkmcnt(0)
v_readfirstlane_b32 s77, v247
s_bitcmp1_b32 s65, 0
s_cbranch_scc1 Lsv3_201
ds_write_b128 v252, v[236:239] offset:26624
s_or_b32 s65, s65, 1
s_waitcnt lgkmcnt(0)
Lsv3_201:
v_max3_f32 v246, v176, v177, v178
v_max_f32 v246, v246, v179
v_mov_b32 v248, v246
s_nop 1
v_permlane16_swap_b32 v246, v248
v_max_f32 v246, v246, v248
v_mov_b32 v248, v246
s_nop 1
v_permlane32_swap_b32 v246, v248
v_max_f32 v246, v246, v248
v_max_f32 v246, 0, v246
v_sub_f32 v236, v236, v246
v_sub_f32 v176, v176, v246
v_sub_f32 v177, v177, v246
v_sub_f32 v178, v178, v246
v_sub_f32 v179, v179, v246
v_exp_f32 v248, -v246
s_nop 0
v_mul_f32 v241, v241, v248
v_mul_f32 v160, v160, v248
v_mul_f32 v161, v161, v248
v_mul_f32 v162, v162, v248
v_mul_f32 v163, v163, v248
v_max3_f32 v246, v180, v181, v182
v_max_f32 v246, v246, v183
v_mov_b32 v248, v246
s_nop 1
v_permlane16_swap_b32 v246, v248
v_max_f32 v246, v246, v248
v_mov_b32 v248, v246
s_nop 1
v_permlane32_swap_b32 v246, v248
v_max_f32 v246, v246, v248
v_max_f32 v246, 0, v246
v_sub_f32 v237, v237, v246
v_sub_f32 v180, v180, v246
v_sub_f32 v181, v181, v246
v_sub_f32 v182, v182, v246
v_sub_f32 v183, v183, v246
v_exp_f32 v248, -v246
s_nop 0
v_mul_f32 v242, v242, v248
v_mul_f32 v164, v164, v248
v_mul_f32 v165, v165, v248
v_mul_f32 v166, v166, v248
v_mul_f32 v167, v167, v248
v_max3_f32 v246, v184, v185, v186
v_max_f32 v246, v246, v187
v_mov_b32 v248, v246
s_nop 1
v_permlane16_swap_b32 v246, v248
v_max_f32 v246, v246, v248
v_mov_b32 v248, v246
s_nop 1
v_permlane32_swap_b32 v246, v248
v_max_f32 v246, v246, v248
v_max_f32 v246, 0, v246
v_sub_f32 v238, v238, v246
v_sub_f32 v184, v184, v246
v_sub_f32 v185, v185, v246
v_sub_f32 v186, v186, v246
v_sub_f32 v187, v187, v246
v_exp_f32 v248, -v246
s_nop 0
v_mul_f32 v243, v243, v248
v_mul_f32 v168, v168, v248
v_mul_f32 v169, v169, v248
v_mul_f32 v170, v170, v248
v_mul_f32 v171, v171, v248
v_max3_f32 v246, v188, v189, v190
v_max_f32 v246, v246, v191
v_mov_b32 v248, v246
s_nop 1
v_permlane16_swap_b32 v246, v248
v_max_f32 v246, v246, v248
v_mov_b32 v248, v246
s_nop 1
v_permlane32_swap_b32 v246, v248
v_max_f32 v246, v246, v248
v_max_f32 v246, 0, v246
v_sub_f32 v239, v239, v246
v_sub_f32 v188, v188, v246
v_sub_f32 v189, v189, v246
v_sub_f32 v190, v190, v246
v_sub_f32 v191, v191, v246
v_exp_f32 v248, -v246
s_nop 0
v_mul_f32 v244, v244, v248
v_mul_f32 v172, v172, v248
v_mul_f32 v173, v173, v248
v_mul_f32 v174, v174, v248
v_mul_f32 v175, v175, v248
s_waitcnt lgkmcnt(0)
s_nop 2
s_setpc_b64 s[80:81]
LR1_3_1:
s_nop 8
s_waitcnt lgkmcnt(0)
s_bitcmp1_b32 s65, 0
s_cbranch_scc1 Lsv3_11
ds_write_b128 v252, v[236:239] offset:26624
s_or_b32 s65, s65, 1
s_waitcnt lgkmcnt(0)
Lsv3_11:
v_max3_f32 v246, v192, v193, v194
v_max_f32 v246, v246, v195
v_mov_b32 v248, v246
s_nop 1
v_permlane16_swap_b32 v246, v248
v_max_f32 v246, v246, v248
v_mov_b32 v248, v246
s_nop 1
v_permlane32_swap_b32 v246, v248
v_max_f32 v246, v246, v248
v_max_f32 v246, 0, v246
v_sub_f32 v236, v236, v246
v_sub_f32 v192, v192, v246
v_sub_f32 v193, v193, v246
v_sub_f32 v194, v194, v246
v_sub_f32 v195, v195, v246
v_exp_f32 v248, -v246
s_nop 0
v_mul_f32 v241, v241, v248
v_max3_f32 v246, v196, v197, v198
v_max_f32 v246, v246, v199
v_mov_b32 v248, v246
s_nop 1
v_permlane16_swap_b32 v246, v248
v_max_f32 v246, v246, v248
v_mov_b32 v248, v246
s_nop 1
v_permlane32_swap_b32 v246, v248
v_max_f32 v246, v246, v248
v_max_f32 v246, 0, v246
v_sub_f32 v237, v237, v246
v_sub_f32 v196, v196, v246
v_sub_f32 v197, v197, v246
v_sub_f32 v198, v198, v246
v_sub_f32 v199, v199, v246
v_exp_f32 v248, -v246
s_nop 0
v_mul_f32 v242, v242, v248
v_max3_f32 v246, v200, v201, v202
v_max_f32 v246, v246, v203
v_mov_b32 v248, v246
s_nop 1
v_permlane16_swap_b32 v246, v248
v_max_f32 v246, v246, v248
v_mov_b32 v248, v246
s_nop 1
v_permlane32_swap_b32 v246, v248
v_max_f32 v246, v246, v248
v_max_f32 v246, 0, v246
v_sub_f32 v238, v238, v246
v_sub_f32 v200, v200, v246
v_sub_f32 v201, v201, v246
v_sub_f32 v202, v202, v246
v_sub_f32 v203, v203, v246
v_exp_f32 v248, -v246
s_nop 0
v_mul_f32 v243, v243, v248
v_max3_f32 v246, v204, v205, v206
v_max_f32 v246, v246, v207
v_mov_b32 v248, v246
s_nop 1
v_permlane16_swap_b32 v246, v248
v_max_f32 v246, v246, v248
v_mov_b32 v248, v246
s_nop 1
v_permlane32_swap_b32 v246, v248
v_max_f32 v246, v246, v248
v_max_f32 v246, 0, v246
v_sub_f32 v239, v239, v246
v_sub_f32 v204, v204, v246
v_sub_f32 v205, v205, v246
v_sub_f32 v206, v206, v246
v_sub_f32 v207, v207, v246
v_exp_f32 v248, -v246
s_nop 0
v_mul_f32 v244, v244, v248
v_max3_f32 v246, v232, v233, v234
v_max_f32 v246, v246, v235
v_mov_b32 v248, v246
s_nop 1
v_permlane16_swap_b32 v246, v248
v_max_f32 v246, v246, v248
v_mov_b32 v248, v246
s_nop 1
v_permlane32_swap_b32 v246, v248
v_max_f32 v246, v246, v248
v_max_f32 v246, 0, v246
v_sub_f32 v246, v240, v246
ds_write_b32 v252, v246 offset:28684
v_mov_b32 v248, 0x7149f2ca
ds_write_b32 v252, v248 offset:29704
s_waitcnt lgkmcnt(0)
s_nop 2
s_setpc_b64 s[80:81]
LR2_3_1:
s_nop 8
s_waitcnt lgkmcnt(0)
v_readfirstlane_b32 s77, v247
s_bitcmp1_b32 s65, 0
s_cbranch_scc1 Lsv3_210
ds_write_b128 v252, v[236:239] offset:26624
s_or_b32 s65, s65, 1
s_waitcnt lgkmcnt(0)
Lsv3_210:
v_max3_f32 v246, v176, v177, v178
v_max_f32 v246, v246, v179
v_mov_b32 v248, v246
s_nop 1
v_permlane16_swap_b32 v246, v248
v_max_f32 v246, v246, v248
v_mov_b32 v248, v246
s_nop 1
v_permlane32_swap_b32 v246, v248
v_max_f32 v246, v246, v248
v_max_f32 v246, 0, v246
v_sub_f32 v236, v236, v246
v_sub_f32 v176, v176, v246
v_sub_f32 v177, v177, v246
v_sub_f32 v178, v178, v246
v_sub_f32 v179, v179, v246
v_exp_f32 v248, -v246
s_nop 0
v_mul_f32 v241, v241, v248
v_mul_f32 v192, v192, v248
v_mul_f32 v193, v193, v248
v_mul_f32 v194, v194, v248
v_mul_f32 v195, v195, v248
v_max3_f32 v246, v180, v181, v182
v_max_f32 v246, v246, v183
v_mov_b32 v248, v246
s_nop 1
v_permlane16_swap_b32 v246, v248
v_max_f32 v246, v246, v248
v_mov_b32 v248, v246
s_nop 1
v_permlane32_swap_b32 v246, v248
v_max_f32 v246, v246, v248
v_max_f32 v246, 0, v246
v_sub_f32 v237, v237, v246
v_sub_f32 v180, v180, v246
v_sub_f32 v181, v181, v246
v_sub_f32 v182, v182, v246
v_sub_f32 v183, v183, v246
v_exp_f32 v248, -v246
s_nop 0
v_mul_f32 v242, v242, v248
v_mul_f32 v196, v196, v248
v_mul_f32 v197, v197, v248
v_mul_f32 v198, v198, v248
v_mul_f32 v199, v199, v248
v_max3_f32 v246, v184, v185, v186
v_max_f32 v246, v246, v187
v_mov_b32 v248, v246
s_nop 1
v_permlane16_swap_b32 v246, v248
v_max_f32 v246, v246, v248
v_mov_b32 v248, v246
s_nop 1
v_permlane32_swap_b32 v246, v248
v_max_f32 v246, v246, v248
v_max_f32 v246, 0, v246
v_sub_f32 v238, v238, v246
v_sub_f32 v184, v184, v246
v_sub_f32 v185, v185, v246
v_sub_f32 v186, v186, v246
v_sub_f32 v187, v187, v246
v_exp_f32 v248, -v246
s_nop 0
v_mul_f32 v243, v243, v248
v_mul_f32 v200, v200, v248
v_mul_f32 v201, v201, v248
v_mul_f32 v202, v202, v248
v_sub_f32 v203, v203, v246
v_max3_f32 v246, v188, v189, v190
v_max_f32 v246, v246, v191
v_mov_b32 v248, v246
s_nop 1
v_permlane16_swap_b32 v246, v248
v_max_f32 v246, v246, v248
v_mov_b32 v248, v246
s_nop 1
v_permlane32_swap_b32 v246, v248
v_max_f32 v246, v246, v248
v_max_f32 v246, 0, v246
v_sub_f32 v239, v239, v246
v_sub_f32 v188, v188, v246
v_sub_f32 v189, v189, v246
v_sub_f32 v190, v190, v246
v_sub_f32 v191, v191, v246
v_exp_f32 v248, -v246
s_nop 0
v_mul_f32 v244, v244, v248
v_sub_f32 v204, v204, v246
v_sub_f32 v205, v205, v246
v_sub_f32 v206, v206, v246
v_sub_f32 v207, v207, v246
s_cmp_eq_u32 s77, 0
s_cbranch_scc1 LR2s_3_1
ds_read_b32 v246, v252 offset:29696
ds_read_b32 v247, v252 offset:28672
ds_read_b32 v248, v252 offset:28676
s_waitcnt lgkmcnt(0)
v_min3_f32 v254, v246, v247, v248
v_sub_f32 v246, v254, v246
v_exp_f32 v246, v246
s_nop 0
ds_write_b32 v252, v246 offset:27648
s_waitcnt lgkmcnt(0)
ds_read_b32 v246, v252 offset:29700
ds_read_b32 v247, v252 offset:28680
ds_read_b32 v248, v252 offset:28684
s_waitcnt lgkmcnt(0)
v_min3_f32 v255, v246, v247, v248
v_sub_f32 v246, v255, v246
v_exp_f32 v246, v246
s_nop 0
ds_write_b32 v252, v246 offset:27652
s_waitcnt lgkmcnt(0)
v_sub_f32 v246, v240, v255
v_mov_b32 v240, v255
v_sub_f32 v232, v232, v246
v_sub_f32 v233, v233, v246
v_sub_f32 v234, v234, v246
v_sub_f32 v235, v235, v246
v_exp_f32 v248, -v246
s_nop 0
v_mul_f32 v245, v245, v248
s_or_b32 s65, s65, 2
s_waitcnt lgkmcnt(0)
s_barrier
s_barrier
LR2s_3_1:
s_waitcnt lgkmcnt(0)
s_nop 2
s_setpc_b64 s[80:81]
LH_3:
s_nop 15
s_waitcnt lgkmcnt(0)
s_bitcmp1_b32 s65, 0
s_cbranch_scc0 LH2_3
ds_read_b32 v246, v252 offset:26624
s_waitcnt lgkmcnt(0)
v_sub_f32 v246, v236, v246
v_exp_f32 v246, v246
s_nop 1
.set ci_, 0
.rept 16
v_accvgpr_read_b32 v247, a[ci_+0]
v_accvgpr_read_b32 v248, a[ci_+1]
v_accvgpr_read_b32 v254, a[ci_+2]
v_accvgpr_read_b32 v255, a[ci_+3]
v_mul_f32 v247, v247, v246
v_mul_f32 v248, v248, v246
v_mul_f32 v254, v254, v246
v_mul_f32 v255, v255, v246
v_accvgpr_write_b32 a[ci_+0], v247
v_accvgpr_write_b32 a[ci_+1], v248
v_accvgpr_write_b32 a[ci_+2], v254
v_accvgpr_write_b32 a[ci_+3], v255
.set ci_, ci_+16
.endr
ds_read_b32 v246, v252 offset:26628
s_waitcnt lgkmcnt(0)
v_sub_f32 v246, v237, v246
v_exp_f32 v246, v246
s_nop 1
.set ci_, 4
.rept 16
v_accvgpr_read_b32 v247, a[ci_+0]
v_accvgpr_read_b32 v248, a[ci_+1]
v_accvgpr_read_b32 v254, a[ci_+2]
v_accvgpr_read_b32 v255, a[ci_+3]
v_mul_f32 v247, v247, v246
v_mul_f32 v248, v248, v246
v_mul_f32 v254, v254, v246
v_mul_f32 v255, v255, v246
v_accvgpr_write_b32 a[ci_+0], v247
v_accvgpr_write_b32 a[ci_+1], v248
v_accvgpr_write_b32 a[ci_+2], v254
v_accvgpr_write_b32 a[ci_+3], v255
.set ci_, ci_+16
.endr
ds_read_b32 v246, v252 offset:26632
s_waitcnt lgkmcnt(0)
v_sub_f32 v246, v238, v246
v_exp_f32 v246, v246
s_nop 1
.set ci_, 8
.rept 16
v_accvgpr_read_b32 v247, a[ci_+0]
v_accvgpr_read_b32 v248, a[ci_+1]
v_accvgpr_read_b32 v254, a[ci_+2]
v_accvgpr_read_b32 v255, a[ci_+3]
v_mul_f32 v247, v247, v246
v_mul_f32 v248, v248, v246
v_mul_f32 v254, v254, v246
v_mul_f32 v255, v255, v246
v_accvgpr_write_b32 a[ci_+0], v247
v_accvgpr_write_b32 a[ci_+1], v248
v_accvgpr_write_b32 a[ci_+2], v254
v_accvgpr_write_b32 a[ci_+3], v255
.set ci_, ci_+16
.endr
ds_read_b32 v246, v252 offset:26636
s_waitcnt lgkmcnt(0)
v_sub_f32 v246, v239, v246
v_exp_f32 v246, v246
s_nop 1
.set ci_, 12
.rept 16
v_accvgpr_read_b32 v247, a[ci_+0]
v_accvgpr_read_b32 v248, a[ci_+1]
v_accvgpr_read_b32 v254, a[ci_+2]
v_accvgpr_read_b32 v255, a[ci_+3]
v_mul_f32 v247, v247, v246
v_mul_f32 v248, v248, v246
v_mul_f32 v254, v254, v246
v_mul_f32 v255, v255, v246
v_accvgpr_write_b32 a[ci_+0], v247
v_accvgpr_write_b32 a[ci_+1], v248
v_accvgpr_write_b32 a[ci_+2], v254
v_accvgpr_write_b32 a[ci_+3], v255
.set ci_, ci_+16
.endr
LH2_3:
s_bitcmp1_b32 s65, 1
s_cbranch_scc0 LH3_3
ds_read_b32 v246, v252 offset:27648
s_waitcnt lgkmcnt(0)
v_mul_f32 v128, v128, v246
v_mul_f32 v129, v129, v246
v_mul_f32 v130, v130, v246
v_mul_f32 v131, v131, v246
v_mul_f32 v136, v136, v246
v_mul_f32 v137, v137, v246
v_mul_f32 v138, v138, v246
v_mul_f32 v139, v139, v246
v_mul_f32 v144, v144, v246
v_mul_f32 v145, v145, v246
v_mul_f32 v146, v146, v246
v_mul_f32 v147, v147, v246
v_mul_f32 v152, v152, v246
v_mul_f32 v153, v153, v246
v_mul_f32 v154, v154, v246
v_mul_f32 v155, v155, v246
ds_read_b32 v246, v252 offset:27652
s_waitcnt lgkmcnt(0)
v_mul_f32 v132, v132, v246
v_mul_f32 v133, v133, v246
v_mul_f32 v134, v134, v246
v_mul_f32 v135, v135, v246
v_mul_f32 v140, v140, v246
v_mul_f32 v141, v141, v246
v_mul_f32 v142, v142, v246
v_mul_f32 v143, v143, v246
v_mul_f32 v148, v148, v246
v_mul_f32 v149, v149, v246
v_mul_f32 v150, v150, v246
v_mul_f32 v151, v151, v246
v_mul_f32 v156, v156, v246
v_mul_f32 v157, v157, v246
v_mul_f32 v158, v158, v246
v_mul_f32 v159, v159, v246
LH3_3:
s_mov_b32 s65, 0
s_waitcnt lgkmcnt(0)
s_nop 2
s_setpc_b64 s[80:81]
LT:
s_nop 8
s_add_u32 m0, s68, 57344
s_nop 0
buffer_load_dwordx4 v253, s[48:51], s67 offen offset:0 lds
s_add_u32 m0, s68, 61312
s_nop 0
buffer_load_dwordx4 v253, s[48:51], s67 offen offset:128 lds
s_add_u32 m0, s68, 65280
s_nop 0
buffer_load_dwordx4 v253, s[48:51], s67 offen offset:256 lds
s_add_u32 m0, s68, 69248
s_nop 0
buffer_load_dwordx4 v253, s[48:51], s67 offen offset:384 lds
s_add_u32 m0, s68, 73216
s_nop 0
buffer_load_dwordx4 v253, s[48:51], s67 offen offset:512 lds
s_add_u32 m0, s68, 77184
s_nop 0
buffer_load_dwordx4 v253, s[48:51], s67 offen offset:640 lds
s_add_u32 m0, s68, 81152
s_nop 0
buffer_load_dwordx4 v253, s[48:51], s67 offen offset:768 lds
s_add_u32 s67, s67, 28672
v_mov_b32 v102, v241
v_mov_b32 v104, v242
v_mov_b32 v106, v243
v_mov_b32 v108, v244
v_mov_b32 v224, v236
v_mov_b32 v226, v237
v_mbcnt_lo_u32_b32 v96, -1, 0
v_mbcnt_hi_u32_b32 v96, -1, v96
v_lshlrev_b32 v248, 4, v96
s_mul_i32 s77, s26, 73728
s_add_u32 s77, s77, 0
s_add_u32 s40, s28, s77
s_addc_u32 s41, s29, 0
s_and_b32 s41, s41, 0xffff
s_mov_b32 s42, 73728
s_mov_b32 s43, 0x20000
s_lshl_b32 s70, %[wid], 11
v_add_u32 v240, s70, v248
s_mul_i32 s70, %[wid], 18432
v_add_u32 v98, s70, v248
v_add_u32 v99, 0x16000, v240
v_add_u32 v100, 0x20000, v240
v_add_u32 v101, 0x16000, v98
s_mov_b32 s70, 8192
s_mov_b32 s71, 17408
v_pk_mul_f32 v[132:133], v[132:133], v[226:227] op_sel_hi:[1,0]
v_pk_mul_f32 v[134:135], v[134:135], v[226:227] op_sel_hi:[1,0]
v_pk_mul_f32 v[140:141], v[140:141], v[226:227] op_sel_hi:[1,0]
v_pk_mul_f32 v[142:143], v[142:143], v[226:227] op_sel_hi:[1,0]
CV 132,132,133
CV 133,134,135
CV 134,140,141
CV 135,142,143
buffer_store_dwordx4 v[132:135], v98, s[40:43], s70 offen offset:0 sc1
v_pk_mul_f32 v[148:149], v[148:149], v[226:227] op_sel_hi:[1,0]
v_pk_mul_f32 v[150:151], v[150:151], v[226:227] op_sel_hi:[1,0]
v_pk_mul_f32 v[156:157], v[156:157], v[226:227] op_sel_hi:[1,0]
v_pk_mul_f32 v[158:159], v[158:159], v[226:227] op_sel_hi:[1,0]
CV 148,148,149
CV 149,150,151
CV 150,156,157
CV 151,158,159
buffer_store_dwordx4 v[148:151], v98, s[40:43], s71 offen offset:0 sc1
v_pk_mul_f32 v[128:129], v[128:129], v[224:225] op_sel_hi:[1,0]
v_pk_mul_f32 v[130:131], v[130:131], v[224:225] op_sel_hi:[1,0]
v_pk_mul_f32 v[136:137], v[136:137], v[224:225] op_sel_hi:[1,0]
v_pk_mul_f32 v[138:139], v[138:139], v[224:225] op_sel_hi:[1,0]
CV 128,128,129
CV 129,130,131
CV 130,136,137
CV 131,138,139
ds_write_b128 v101, v[128:131] offset:8192
v_pk_mul_f32 v[144:145], v[144:145], v[224:225] op_sel_hi:[1,0]
v_pk_mul_f32 v[146:147], v[146:147], v[224:225] op_sel_hi:[1,0]
v_pk_mul_f32 v[152:153], v[152:153], v[224:225] op_sel_hi:[1,0]
v_pk_mul_f32 v[154:155], v[154:155], v[224:225] op_sel_hi:[1,0]
CV 144,144,145
CV 145,146,147
CV 146,152,153
CV 147,154,155
ds_write_b128 v101, v[144:147] offset:17408
AR 0,8
AR 1,9
AR 2,10
AR 3,11
AR 4,24
AR 5,25
AR 6,26
AR 7,27
v_pk_mul_f32 v[0:1], v[0:1], v[106:107] op_sel_hi:[1,0]
v_pk_mul_f32 v[2:3], v[2:3], v[106:107] op_sel_hi:[1,0]
v_pk_mul_f32 v[4:5], v[4:5], v[106:107] op_sel_hi:[1,0]
v_pk_mul_f32 v[6:7], v[6:7], v[106:107] op_sel_hi:[1,0]
CV 0,0,1
CV 1,2,3
CV 2,4,5
CV 3,6,7
s_mov_b32 s56, 0
buffer_store_dwordx4 v[0:3], v240, s[40:43], s56 offen offset:0 sc1
AR 8,0
AR 9,1
AR 10,2
AR 11,3
AR 12,16
AR 13,17
AR 14,18
AR 15,19
v_pk_mul_f32 v[8:9], v[8:9], v[102:103] op_sel_hi:[1,0]
v_pk_mul_f32 v[10:11], v[10:11], v[102:103] op_sel_hi:[1,0]
v_pk_mul_f32 v[12:13], v[12:13], v[102:103] op_sel_hi:[1,0]
v_pk_mul_f32 v[14:15], v[14:15], v[102:103] op_sel_hi:[1,0]
CV 8,8,9
CV 9,10,11
CV 10,12,13
CV 11,14,15
ds_write_b128 v99, v[8:11] offset:0
AR 16,12
AR 17,13
AR 18,14
AR 19,15
AR 20,28
AR 21,29
AR 22,30
AR 23,31
v_pk_mul_f32 v[16:17], v[16:17], v[108:109] op_sel_hi:[1,0]
v_pk_mul_f32 v[18:19], v[18:19], v[108:109] op_sel_hi:[1,0]
v_pk_mul_f32 v[20:21], v[20:21], v[108:109] op_sel_hi:[1,0]
v_pk_mul_f32 v[22:23], v[22:23], v[108:109] op_sel_hi:[1,0]
CV 16,16,17
CV 17,18,19
CV 18,20,21
CV 19,22,23
s_mov_b32 s56, 0
buffer_store_dwordx4 v[16:19], v240, s[40:43], s56 offen offset:1024 sc1
AR 24,4
AR 25,5
AR 26,6
AR 27,7
AR 28,20
AR 29,21
AR 30,22
AR 31,23
v_pk_mul_f32 v[24:25], v[24:25], v[104:105] op_sel_hi:[1,0]
v_pk_mul_f32 v[26:27], v[26:27], v[104:105] op_sel_hi:[1,0]
v_pk_mul_f32 v[28:29], v[28:29], v[104:105] op_sel_hi:[1,0]
v_pk_mul_f32 v[30:31], v[30:31], v[104:105] op_sel_hi:[1,0]
CV 24,24,25
CV 25,26,27
CV 26,28,29
CV 27,30,31
ds_write_b128 v99, v[24:27] offset:1024
AR 32,40
AR 33,41
AR 34,42
AR 35,43
AR 36,56
AR 37,57
AR 38,58
AR 39,59
v_pk_mul_f32 v[32:33], v[32:33], v[106:107] op_sel_hi:[1,0]
v_pk_mul_f32 v[34:35], v[34:35], v[106:107] op_sel_hi:[1,0]
v_pk_mul_f32 v[36:37], v[36:37], v[106:107] op_sel_hi:[1,0]
v_pk_mul_f32 v[38:39], v[38:39], v[106:107] op_sel_hi:[1,0]
CV 32,32,33
CV 33,34,35
CV 34,36,37
CV 35,38,39
s_mov_b32 s56, 9216
buffer_store_dwordx4 v[32:35], v240, s[40:43], s56 offen offset:0 sc1
AR 40,32
AR 41,33
AR 42,34
AR 43,35
AR 44,48
AR 45,49
AR 46,50
AR 47,51
v_pk_mul_f32 v[40:41], v[40:41], v[102:103] op_sel_hi:[1,0]
v_pk_mul_f32 v[42:43], v[42:43], v[102:103] op_sel_hi:[1,0]
v_pk_mul_f32 v[44:45], v[44:45], v[102:103] op_sel_hi:[1,0]
v_pk_mul_f32 v[46:47], v[46:47], v[102:103] op_sel_hi:[1,0]
CV 40,40,41
CV 41,42,43
CV 42,44,45
CV 43,46,47
ds_write_b128 v99, v[40:43] offset:9216
AR 48,44
AR 49,45
AR 50,46
AR 51,47
AR 52,60
AR 53,61
AR 54,62
AR 55,63
v_pk_mul_f32 v[48:49], v[48:49], v[108:109] op_sel_hi:[1,0]
v_pk_mul_f32 v[50:51], v[50:51], v[108:109] op_sel_hi:[1,0]
v_pk_mul_f32 v[52:53], v[52:53], v[108:109] op_sel_hi:[1,0]
v_pk_mul_f32 v[54:55], v[54:55], v[108:109] op_sel_hi:[1,0]
CV 48,48,49
CV 49,50,51
CV 50,52,53
CV 51,54,55
s_mov_b32 s56, 9216
buffer_store_dwordx4 v[48:51], v240, s[40:43], s56 offen offset:1024 sc1
AR 56,36
AR 57,37
AR 58,38
AR 59,39
AR 60,52
AR 61,53
AR 62,54
AR 63,55
v_pk_mul_f32 v[56:57], v[56:57], v[104:105] op_sel_hi:[1,0]
v_pk_mul_f32 v[58:59], v[58:59], v[104:105] op_sel_hi:[1,0]
v_pk_mul_f32 v[60:61], v[60:61], v[104:105] op_sel_hi:[1,0]
v_pk_mul_f32 v[62:63], v[62:63], v[104:105] op_sel_hi:[1,0]
CV 56,56,57
CV 57,58,59
CV 58,60,61
CV 59,62,63
ds_write_b128 v99, v[56:59] offset:10240
AR 64,72
AR 65,73
AR 66,74
AR 67,75
AR 68,88
AR 69,89
AR 70,90
AR 71,91
v_pk_mul_f32 v[64:65], v[64:65], v[106:107] op_sel_hi:[1,0]
v_pk_mul_f32 v[66:67], v[66:67], v[106:107] op_sel_hi:[1,0]
v_pk_mul_f32 v[68:69], v[68:69], v[106:107] op_sel_hi:[1,0]
v_pk_mul_f32 v[70:71], v[70:71], v[106:107] op_sel_hi:[1,0]
CV 64,64,65
CV 65,66,67
CV 66,68,69
CV 67,70,71
s_mov_b32 s56, 18432
buffer_store_dwordx4 v[64:67], v240, s[40:43], s56 offen offset:0 sc1
AR 72,64
AR 73,65
AR 74,66
AR 75,67
AR 76,80
AR 77,81
AR 78,82
AR 79,83
v_pk_mul_f32 v[72:73], v[72:73], v[102:103] op_sel_hi:[1,0]
v_pk_mul_f32 v[74:75], v[74:75], v[102:103] op_sel_hi:[1,0]
v_pk_mul_f32 v[76:77], v[76:77], v[102:103] op_sel_hi:[1,0]
v_pk_mul_f32 v[78:79], v[78:79], v[102:103] op_sel_hi:[1,0]
CV 72,72,73
CV 73,74,75
CV 74,76,77
CV 75,78,79
ds_write_b128 v99, v[72:75] offset:18432
AR 80,76
AR 81,77
AR 82,78
AR 83,79
AR 84,92
AR 85,93
AR 86,94
AR 87,95
v_pk_mul_f32 v[80:81], v[80:81], v[108:109] op_sel_hi:[1,0]
v_pk_mul_f32 v[82:83], v[82:83], v[108:109] op_sel_hi:[1,0]
v_pk_mul_f32 v[84:85], v[84:85], v[108:109] op_sel_hi:[1,0]
v_pk_mul_f32 v[86:87], v[86:87], v[108:109] op_sel_hi:[1,0]
CV 80,80,81
CV 81,82,83
CV 82,84,85
CV 83,86,87
s_mov_b32 s56, 18432
buffer_store_dwordx4 v[80:83], v240, s[40:43], s56 offen offset:1024 sc1
AR 88,68
AR 89,69
AR 90,70
AR 91,71
AR 92,84
AR 93,85
AR 94,86
AR 95,87
v_pk_mul_f32 v[88:89], v[88:89], v[104:105] op_sel_hi:[1,0]
v_pk_mul_f32 v[90:91], v[90:91], v[104:105] op_sel_hi:[1,0]
v_pk_mul_f32 v[92:93], v[92:93], v[104:105] op_sel_hi:[1,0]
v_pk_mul_f32 v[94:95], v[94:95], v[104:105] op_sel_hi:[1,0]
CV 88,88,89
CV 89,90,91
CV 90,92,93
CV 91,94,95
ds_write_b128 v99, v[88:91] offset:19456
AR 0,104
AR 1,105
AR 2,106
AR 3,107
AR 4,120
AR 5,121
AR 6,122
AR 7,123
v_pk_mul_f32 v[0:1], v[0:1], v[106:107] op_sel_hi:[1,0]
v_pk_mul_f32 v[2:3], v[2:3], v[106:107] op_sel_hi:[1,0]
v_pk_mul_f32 v[4:5], v[4:5], v[106:107] op_sel_hi:[1,0]
v_pk_mul_f32 v[6:7], v[6:7], v[106:107] op_sel_hi:[1,0]
CV 0,0,1
CV 1,2,3
CV 2,4,5
CV 3,6,7
s_mov_b32 s56, 27648
buffer_store_dwordx4 v[0:3], v240, s[40:43], s56 offen offset:0 sc1
AR 8,96
AR 9,97
AR 10,98
AR 11,99
AR 12,112
AR 13,113
AR 14,114
AR 15,115
v_pk_mul_f32 v[8:9], v[8:9], v[102:103] op_sel_hi:[1,0]
v_pk_mul_f32 v[10:11], v[10:11], v[102:103] op_sel_hi:[1,0]
v_pk_mul_f32 v[12:13], v[12:13], v[102:103] op_sel_hi:[1,0]
v_pk_mul_f32 v[14:15], v[14:15], v[102:103] op_sel_hi:[1,0]
CV 8,8,9
CV 9,10,11
CV 10,12,13
CV 11,14,15
ds_write_b128 v99, v[8:11] offset:27648
AR 16,108
AR 17,109
AR 18,110
AR 19,111
AR 20,124
AR 21,125
AR 22,126
AR 23,127
v_pk_mul_f32 v[16:17], v[16:17], v[108:109] op_sel_hi:[1,0]
v_pk_mul_f32 v[18:19], v[18:19], v[108:109] op_sel_hi:[1,0]
v_pk_mul_f32 v[20:21], v[20:21], v[108:109] op_sel_hi:[1,0]
v_pk_mul_f32 v[22:23], v[22:23], v[108:109] op_sel_hi:[1,0]
CV 16,16,17
CV 17,18,19
CV 18,20,21
CV 19,22,23
s_mov_b32 s56, 27648
buffer_store_dwordx4 v[16:19], v240, s[40:43], s56 offen offset:1024 sc1
AR 24,100
AR 25,101
AR 26,102
AR 27,103
AR 28,116
AR 29,117
AR 30,118
AR 31,119
v_pk_mul_f32 v[24:25], v[24:25], v[104:105] op_sel_hi:[1,0]
v_pk_mul_f32 v[26:27], v[26:27], v[104:105] op_sel_hi:[1,0]
v_pk_mul_f32 v[28:29], v[28:29], v[104:105] op_sel_hi:[1,0]
v_pk_mul_f32 v[30:31], v[30:31], v[104:105] op_sel_hi:[1,0]
CV 24,24,25
CV 25,26,27
CV 26,28,29
CV 27,30,31
ds_write_b128 v99, v[24:27] offset:28672
AR 32,136
AR 33,137
AR 34,138
AR 35,139
AR 36,152
AR 37,153
AR 38,154
AR 39,155
v_pk_mul_f32 v[32:33], v[32:33], v[106:107] op_sel_hi:[1,0]
v_pk_mul_f32 v[34:35], v[34:35], v[106:107] op_sel_hi:[1,0]
v_pk_mul_f32 v[36:37], v[36:37], v[106:107] op_sel_hi:[1,0]
v_pk_mul_f32 v[38:39], v[38:39], v[106:107] op_sel_hi:[1,0]
CV 32,32,33
CV 33,34,35
CV 34,36,37
CV 35,38,39
s_mov_b32 s56, 36864
buffer_store_dwordx4 v[32:35], v240, s[40:43], s56 offen offset:0 sc1
AR 40,128
AR 41,129
AR 42,130
AR 43,131
AR 44,144
AR 45,145
AR 46,146
AR 47,147
v_pk_mul_f32 v[40:41], v[40:41], v[102:103] op_sel_hi:[1,0]
v_pk_mul_f32 v[42:43], v[42:43], v[102:103] op_sel_hi:[1,0]
v_pk_mul_f32 v[44:45], v[44:45], v[102:103] op_sel_hi:[1,0]
v_pk_mul_f32 v[46:47], v[46:47], v[102:103] op_sel_hi:[1,0]
CV 40,40,41
CV 41,42,43
CV 42,44,45
CV 43,46,47
ds_write_b128 v99, v[40:43] offset:36864
AR 48,140
AR 49,141
AR 50,142
AR 51,143
AR 52,156
AR 53,157
AR 54,158
AR 55,159
v_pk_mul_f32 v[48:49], v[48:49], v[108:109] op_sel_hi:[1,0]
v_pk_mul_f32 v[50:51], v[50:51], v[108:109] op_sel_hi:[1,0]
v_pk_mul_f32 v[52:53], v[52:53], v[108:109] op_sel_hi:[1,0]
v_pk_mul_f32 v[54:55], v[54:55], v[108:109] op_sel_hi:[1,0]
CV 48,48,49
CV 49,50,51
CV 50,52,53
CV 51,54,55
s_mov_b32 s56, 36864
buffer_store_dwordx4 v[48:51], v240, s[40:43], s56 offen offset:1024 sc1
AR 56,132
AR 57,133
AR 58,134
AR 59,135
AR 60,148
AR 61,149
AR 62,150
AR 63,151
v_pk_mul_f32 v[56:57], v[56:57], v[104:105] op_sel_hi:[1,0]
v_pk_mul_f32 v[58:59], v[58:59], v[104:105] op_sel_hi:[1,0]
v_pk_mul_f32 v[60:61], v[60:61], v[104:105] op_sel_hi:[1,0]
v_pk_mul_f32 v[62:63], v[62:63], v[104:105] op_sel_hi:[1,0]
CV 56,56,57
CV 57,58,59
CV 58,60,61
CV 59,62,63
ds_write_b128 v99, v[56:59] offset:37888
AR 64,168
AR 65,169
AR 66,170
AR 67,171
AR 68,184
AR 69,185
AR 70,186
AR 71,187
v_pk_mul_f32 v[64:65], v[64:65], v[106:107] op_sel_hi:[1,0]
v_pk_mul_f32 v[66:67], v[66:67], v[106:107] op_sel_hi:[1,0]
v_pk_mul_f32 v[68:69], v[68:69], v[106:107] op_sel_hi:[1,0]
v_pk_mul_f32 v[70:71], v[70:71], v[106:107] op_sel_hi:[1,0]
CV 64,64,65
CV 65,66,67
CV 66,68,69
CV 67,70,71
s_mov_b32 s56, 46080
buffer_store_dwordx4 v[64:67], v240, s[40:43], s56 offen offset:0 sc1
AR 72,160
AR 73,161
AR 74,162
AR 75,163
AR 76,176
AR 77,177
AR 78,178
AR 79,179
v_pk_mul_f32 v[72:73], v[72:73], v[102:103] op_sel_hi:[1,0]
v_pk_mul_f32 v[74:75], v[74:75], v[102:103] op_sel_hi:[1,0]
v_pk_mul_f32 v[76:77], v[76:77], v[102:103] op_sel_hi:[1,0]
v_pk_mul_f32 v[78:79], v[78:79], v[102:103] op_sel_hi:[1,0]
CV 72,72,73
CV 73,74,75
CV 74,76,77
CV 75,78,79
ds_write_b128 v100, v[72:75] offset:5120
AR 80,172
AR 81,173
AR 82,174
AR 83,175
AR 84,188
AR 85,189
AR 86,190
AR 87,191
v_pk_mul_f32 v[80:81], v[80:81], v[108:109] op_sel_hi:[1,0]
v_pk_mul_f32 v[82:83], v[82:83], v[108:109] op_sel_hi:[1,0]
v_pk_mul_f32 v[84:85], v[84:85], v[108:109] op_sel_hi:[1,0]
v_pk_mul_f32 v[86:87], v[86:87], v[108:109] op_sel_hi:[1,0]
CV 80,80,81
CV 81,82,83
CV 82,84,85
CV 83,86,87
s_mov_b32 s56, 46080
buffer_store_dwordx4 v[80:83], v240, s[40:43], s56 offen offset:1024 sc1
AR 88,164
AR 89,165
AR 90,166
AR 91,167
AR 92,180
AR 93,181
AR 94,182
AR 95,183
v_pk_mul_f32 v[88:89], v[88:89], v[104:105] op_sel_hi:[1,0]
v_pk_mul_f32 v[90:91], v[90:91], v[104:105] op_sel_hi:[1,0]
v_pk_mul_f32 v[92:93], v[92:93], v[104:105] op_sel_hi:[1,0]
v_pk_mul_f32 v[94:95], v[94:95], v[104:105] op_sel_hi:[1,0]
CV 88,88,89
CV 89,90,91
CV 90,92,93
CV 91,94,95
ds_write_b128 v100, v[88:91] offset:6144
AR 0,200
AR 1,201
AR 2,202
AR 3,203
AR 4,216
AR 5,217
AR 6,218
AR 7,219
v_pk_mul_f32 v[0:1], v[0:1], v[106:107] op_sel_hi:[1,0]
v_pk_mul_f32 v[2:3], v[2:3], v[106:107] op_sel_hi:[1,0]
v_pk_mul_f32 v[4:5], v[4:5], v[106:107] op_sel_hi:[1,0]
v_pk_mul_f32 v[6:7], v[6:7], v[106:107] op_sel_hi:[1,0]
CV 0,0,1
CV 1,2,3
CV 2,4,5
CV 3,6,7
s_mov_b32 s56, 55296
buffer_store_dwordx4 v[0:3], v240, s[40:43], s56 offen offset:0 sc1
AR 8,192
AR 9,193
AR 10,194
AR 11,195
AR 12,208
AR 13,209
AR 14,210
AR 15,211
v_pk_mul_f32 v[8:9], v[8:9], v[102:103] op_sel_hi:[1,0]
v_pk_mul_f32 v[10:11], v[10:11], v[102:103] op_sel_hi:[1,0]
v_pk_mul_f32 v[12:13], v[12:13], v[102:103] op_sel_hi:[1,0]
v_pk_mul_f32 v[14:15], v[14:15], v[102:103] op_sel_hi:[1,0]
CV 8,8,9
CV 9,10,11
CV 10,12,13
CV 11,14,15
ds_write_b128 v100, v[8:11] offset:14336
AR 16,204
AR 17,205
AR 18,206
AR 19,207
AR 20,220
AR 21,221
AR 22,222
AR 23,223
v_pk_mul_f32 v[16:17], v[16:17], v[108:109] op_sel_hi:[1,0]
v_pk_mul_f32 v[18:19], v[18:19], v[108:109] op_sel_hi:[1,0]
v_pk_mul_f32 v[20:21], v[20:21], v[108:109] op_sel_hi:[1,0]
v_pk_mul_f32 v[22:23], v[22:23], v[108:109] op_sel_hi:[1,0]
CV 16,16,17
CV 17,18,19
CV 18,20,21
CV 19,22,23
s_mov_b32 s56, 55296
buffer_store_dwordx4 v[16:19], v240, s[40:43], s56 offen offset:1024 sc1
AR 24,196
AR 25,197
AR 26,198
AR 27,199
AR 28,212
AR 29,213
AR 30,214
AR 31,215
v_pk_mul_f32 v[24:25], v[24:25], v[104:105] op_sel_hi:[1,0]
v_pk_mul_f32 v[26:27], v[26:27], v[104:105] op_sel_hi:[1,0]
v_pk_mul_f32 v[28:29], v[28:29], v[104:105] op_sel_hi:[1,0]
v_pk_mul_f32 v[30:31], v[30:31], v[104:105] op_sel_hi:[1,0]
CV 24,24,25
CV 25,26,27
CV 26,28,29
CV 27,30,31
ds_write_b128 v100, v[24:27] offset:15360
AR 32,232
AR 33,233
AR 34,234
AR 35,235
AR 36,248
AR 37,249
AR 38,250
AR 39,251
v_pk_mul_f32 v[32:33], v[32:33], v[106:107] op_sel_hi:[1,0]
v_pk_mul_f32 v[34:35], v[34:35], v[106:107] op_sel_hi:[1,0]
v_pk_mul_f32 v[36:37], v[36:37], v[106:107] op_sel_hi:[1,0]
v_pk_mul_f32 v[38:39], v[38:39], v[106:107] op_sel_hi:[1,0]
CV 32,32,33
CV 33,34,35
CV 34,36,37
CV 35,38,39
s_mov_b32 s56, 64512
buffer_store_dwordx4 v[32:35], v240, s[40:43], s56 offen offset:0 sc1
AR 40,224
AR 41,225
AR 42,226
AR 43,227
AR 44,240
AR 45,241
AR 46,242
AR 47,243
v_pk_mul_f32 v[40:41], v[40:41], v[102:103] op_sel_hi:[1,0]
v_pk_mul_f32 v[42:43], v[42:43], v[102:103] op_sel_hi:[1,0]
v_pk_mul_f32 v[44:45], v[44:45], v[102:103] op_sel_hi:[1,0]
v_pk_mul_f32 v[46:47], v[46:47], v[102:103] op_sel_hi:[1,0]
CV 40,40,41
CV 41,42,43
CV 42,44,45
CV 43,46,47
ds_write_b128 v100, v[40:43] offset:23552
AR 48,236
AR 49,237
AR 50,238
AR 51,239
AR 52,252
AR 53,253
AR 54,254
AR 55,255
v_pk_mul_f32 v[48:49], v[48:49], v[108:109] op_sel_hi:[1,0]
v_pk_mul_f32 v[50:51], v[50:51], v[108:109] op_sel_hi:[1,0]
v_pk_mul_f32 v[52:53], v[52:53], v[108:109] op_sel_hi:[1,0]
v_pk_mul_f32 v[54:55], v[54:55], v[108:109] op_sel_hi:[1,0]
CV 48,48,49
CV 49,50,51
CV 50,52,53
CV 51,54,55
s_mov_b32 s56, 64512
buffer_store_dwordx4 v[48:51], v240, s[40:43], s56 offen offset:1024 sc1
AR 56,228
AR 57,229
AR 58,230
AR 59,231
AR 60,244
AR 61,245
AR 62,246
AR 63,247
v_pk_mul_f32 v[56:57], v[56:57], v[104:105] op_sel_hi:[1,0]
v_pk_mul_f32 v[58:59], v[58:59], v[104:105] op_sel_hi:[1,0]
v_pk_mul_f32 v[60:61], v[60:61], v[104:105] op_sel_hi:[1,0]
v_pk_mul_f32 v[62:63], v[62:63], v[104:105] op_sel_hi:[1,0]
CV 56,56,57
CV 57,58,59
CV 58,60,61
CV 59,62,63
ds_write_b128 v100, v[56:59] offset:24576
v_lshrrev_b32 v97, 4, v96
v_bfe_u32 v98, v96, 2, 2
v_and_b32 v100, 1, v97
v_lshl_add_u32 v99, v100, 2, v98
v_lshlrev_b32 v99, 6, v99
v_lshrrev_b32 v100, 1, v97
v_lshl_add_u32 v99, v100, 10, v99
v_sub_u32 v102, 4, v97
v_and_b32 v102, 3, v102
v_and_b32 v100, 3, v96
v_xor_b32 v101, v100, v102
v_lshl_add_u32 v244, v101, 4, v99
s_lshr_b32 s70, %[wid], 1
s_lshl_b32 s71, s70, 12
s_and_b32 s77, %[wid], 1
s_lshl_b32 s77, s77, 9
s_add_u32 s71, s71, s77
v_add_u32 v244, s71, v244
v_add_u32 v245, 0xe000, v244
v_lshrrev_b32 v101, 1, v100
s_and_b32 s77, %[wid], 1
s_lshl_b32 s77, s77, 1
v_or_b32 v101, s77, v101
v_xor_b32 v101, v101, v102
v_lshl_add_u32 v101, v101, 4, v99
v_and_b32 v100, 1, v100
v_lshl_add_u32 v246, v100, 3, v101
s_lshl_b32 s71, s70, 9
s_add_u32 s71, s71, 24576
v_add_u32 v246, s71, v246
v_add_u32 v247, 0xe000, v246
v_add_u32 v249, 0x16000, v248
v_and_b32 v99, 15, v96
v_lshlrev_b32 v250, 2, v99
v_lshlrev_b32 v251, 8, v99
v_add_u32 v251, 0x100, v251
s_xor_b32 s77, s26, 1
s_mul_i32 s77, s77, 73728
s_add_u32 s77, s77, 0
s_add_u32 s44, s28, s77
s_addc_u32 s45, s29, 0
s_and_b32 s45, s45, 0xffff
s_mov_b32 s46, 73728
s_mov_b32 s47, 0x20000
RU 0,244,0
RU 2,244,2048
RU 4,244,8
RU 6,244,2056
RU 8,244,8192
RU 10,244,10240
RU 12,244,8200
RU 14,244,10248
RU 16,244,16384
RU 18,244,18432
RU 20,244,16392
RU 22,244,18440
RU 24,246,0
RU 26,246,2048
RU 28,244,28672
RU 30,244,30720
RU 32,244,28680
RU 34,244,30728
RU 36,244,36864
RU 38,244,38912
RU 40,244,36872
RU 42,244,38920
RU 44,244,45056
RU 46,244,47104
RU 48,244,45064
RU 50,244,47112
RU 52,246,28672
RU 54,246,30720
s_and_b32 s77, s26, 15
s_lshl_b32 s77, s77, 2
s_sub_u32 s80, s38, s77
s_subb_u32 s81, s39, 0
global_load_dword v252, v250, s[80:81] sc1
v_add_u32 v243, 0x10000, v249
s_waitcnt lgkmcnt(0)
s_barrier
ds_read_b128 v[224:227], v249 offset:0
ds_read_b128 v[228:231], v249 offset:9216
ds_read_b128 v[232:235], v249 offset:1024
ds_read_b128 v[236:239], v249 offset:10240
ds_read_b128 a[252:255], v249 offset:2048
s_waitcnt lgkmcnt(4)
MG0 v,0,a,0,v,224
s_waitcnt lgkmcnt(3)
MG v,0,a,28,v,228
MG0 v,4,a,4,v,224
MG v,4,a,32,v,228
MG0 v,8,a,8,v,224
MG v,8,a,36,v,228
MG0 v,12,a,12,v,224
MG v,12,a,40,v,228
s_waitcnt vmcnt(0)
MG0 v,16,a,16,v,224
s_barrier
s_cmp_lg_u32 %[wid], 0
s_cbranch_scc1 LTp1
global_store_dword v251, v252, s[38:39] sc1
LTp1:
s_and_b32 s77, s26, 15
v_readlane_b32 s79, v252, s77
MG v,16,a,44,v,228
s_add_u32 m0, s68, 0
RU 56,245,0
MG0 v,20,a,20,v,224
buffer_load_dwordx4 v253, s[48:51], s67 offen offset:0 lds
MG v,20,a,48,v,228
RU 58,245,2048
MG0 v,24,a,24,v,224
RU 60,245,8
MG v,24,a,52,v,228
s_add_u32 m0, s68, 3968
ds_read_b128 v[224:227], v249 offset:11264
s_waitcnt lgkmcnt(6)
MG0 v,28,a,0,v,232
buffer_load_dwordx4 v253, s[48:51], s67 offen offset:128 lds
s_waitcnt lgkmcnt(5)
MG v,28,a,28,v,236
ds_read_b128 v[228:231], v249 offset:3072
MG0 v,32,a,4,v,232
RU 62,245,2056
MG v,32,a,32,v,236
s_add_u32 m0, s68, 7936
RU 64,245,8192
MG0 v,36,a,8,v,232
buffer_load_dwordx4 v253, s[48:51], s67 offen offset:256 lds
MG v,36,a,36,v,236
RU 66,245,10240
MG0 v,40,a,12,v,232
RU 68,245,8200
MG v,40,a,40,v,236
s_add_u32 m0, s68, 11904
RU 70,245,10248
MG0 v,44,a,16,v,232
buffer_load_dwordx4 v253, s[48:51], s67 offen offset:384 lds
MG v,44,a,44,v,236
RU 72,245,16384
MG0 v,48,a,20,v,232
RU 74,245,18432
MG v,48,a,48,v,236
s_add_u32 m0, s68, 15872
RU 76,245,16392
MG0 v,52,a,24,v,232
buffer_load_dwordx4 v253, s[48:51], s67 offen offset:512 lds
MG v,52,a,52,v,236
ds_read_b128 v[232:235], v249 offset:12288
s_waitcnt lgkmcnt(14)
MG0 v,56,a,0,a,252
ds_read_b128 v[236:239], v249 offset:4096
s_waitcnt lgkmcnt(11)
MG v,56,a,28,v,224
s_add_u32 m0, s68, 19840
RU 78,245,18440
MG0 v,60,a,4,a,252
buffer_load_dwordx4 v253, s[48:51], s67 offen offset:640 lds
MG v,60,a,32,v,224
RU 80,247,0
MG0 v,64,a,8,a,252
RU 82,247,2048
MG v,64,a,36,v,224
s_add_u32 m0, s68, 23808
MG0 v,68,a,12,a,252
buffer_load_dwordx4 v253, s[48:51], s67 offen offset:768 lds
s_add_u32 s67, s67, 28672
MG v,68,a,40,v,224
MG0 v,72,a,16,a,252
MG v,72,a,44,v,224
s_add_u32 m0, s68, 28672
MG0 v,76,a,20,a,252
buffer_load_dwordx4 v253, s[48:51], s67 offen offset:0 lds
MG v,76,a,48,v,224
MG0 v,80,a,24,a,252
s_waitcnt lgkmcnt(4)
MG v,80,a,52,v,224
s_add_u32 m0, s68, 32640
ds_read_b128 a[252:255], v249 offset:13312
MG0 v,84,a,0,v,228
buffer_load_dwordx4 v253, s[48:51], s67 offen offset:128 lds
MG v,84,a,28,v,232
ds_read_b128 v[224:227], v249 offset:5120
MG0 v,88,a,4,v,228
MG v,88,a,32,v,232
s_add_u32 m0, s68, 36608
MG0 v,92,a,8,v,228
buffer_load_dwordx4 v253, s[48:51], s67 offen offset:256 lds
MG v,92,a,36,v,232
MG0 v,96,a,12,v,228
MG v,96,a,40,v,232
s_add_u32 m0, s68, 40576
MG0 v,100,a,16,v,228
buffer_load_dwordx4 v253, s[48:51], s67 offen offset:384 lds
MG v,100,a,44,v,232
MG0 v,104,a,20,v,228
MG v,104,a,48,v,232
s_add_u32 m0, s68, 44544
MG0 v,108,a,24,v,228
buffer_load_dwordx4 v253, s[48:51], s67 offen offset:512 lds
MG v,108,a,52,v,232
ds_read_b128 v[228:231], v249 offset:14336
s_waitcnt lgkmcnt(6)
MG0 v,112,a,0,v,236
ds_read_b128 v[232:235], v249 offset:6144
s_waitcnt lgkmcnt(3)
MG v,112,a,28,a,252
s_add_u32 m0, s68, 48512
MG0 v,116,a,4,v,236
buffer_load_dwordx4 v253, s[48:51], s67 offen offset:640 lds
MG v,116,a,32,a,252
MG0 v,120,a,8,v,236
MG v,120,a,36,a,252
s_add_u32 m0, s68, 52480
MG0 v,124,a,12,v,236
buffer_load_dwordx4 v253, s[48:51], s67 offen offset:768 lds
s_add_u32 s67, s67, 28672
MG v,124,a,40,a,252
MG0 v,128,a,16,v,236
MG v,128,a,44,a,252
MG0 v,132,a,20,v,236
MG v,132,a,48,a,252
MG0 v,136,a,24,v,236
s_waitcnt lgkmcnt(1)
MG v,136,a,52,a,252
ds_read_b128 v[236:239], v249 offset:15360
MG0 v,140,a,0,v,224
ds_read_b128 a[252:255], v249 offset:7168
MG v,140,a,28,v,228
MG0 v,144,a,4,v,224
MG v,144,a,32,v,228
MG0 v,148,a,8,v,224
MG v,148,a,36,v,228
MG0 v,152,a,12,v,224
MG v,152,a,40,v,228
MG0 v,156,a,16,v,224
MG v,156,a,44,v,228
MG0 v,160,a,20,v,224
MG v,160,a,48,v,228
MG0 v,164,a,24,v,224
MG v,164,a,52,v,228
s_waitcnt vmcnt(7) lgkmcnt(1)
MG0 v,168,a,0,v,232
s_barrier
MG v,168,a,28,v,236
s_add_u32 m0, s68, 57344
ds_read_b128 v[224:227], v249 offset:16384
MG0 v,172,a,4,v,232
buffer_load_dwordx4 v253, s[48:51], s67 offen offset:0 lds
MG v,172,a,32,v,236
ds_read_b128 v[228:231], v249 offset:8192
MG0 v,176,a,8,v,232
RU 84,244,0
MG v,176,a,36,v,236
s_add_u32 m0, s68, 61312
RU 86,244,2048
MG0 v,180,a,12,v,232
buffer_load_dwordx4 v253, s[48:51], s67 offen offset:128 lds
MG v,180,a,40,v,236
RU 88,244,8
MG0 v,184,a,16,v,232
RU 90,244,2056
MG v,184,a,44,v,236
s_add_u32 m0, s68, 65280
RU 92,244,8192
MG0 v,188,a,20,v,232
buffer_load_dwordx4 v253, s[48:51], s67 offen offset:256 lds
MG v,188,a,48,v,236
RU 94,244,10240
MG0 v,192,a,24,v,232
RU 96,244,8200
MG v,192,a,52,v,236
s_add_u32 m0, s68, 69248
ds_read_b128 v[232:235], v249 offset:17408
s_waitcnt lgkmcnt(10)
MG0 v,196,a,0,a,252
buffer_load_dwordx4 v253, s[48:51], s67 offen offset:384 lds
s_waitcnt lgkmcnt(9)
MG v,196,a,28,v,224
ds_read_b128 v[236:239], v249 offset:18432
MG0 v,200,a,4,a,252
RU 98,244,10248
MG v,200,a,32,v,224
s_add_u32 m0, s68, 73216
RU 100,244,16384
MG0 v,204,a,8,a,252
buffer_load_dwordx4 v253, s[48:51], s67 offen offset:512 lds
MG v,204,a,36,v,224
RU 102,244,18432
MG0 v,208,a,12,a,252
RU 104,244,16392
MG v,208,a,40,v,224
s_add_u32 m0, s68, 77184
RU 106,244,18440
MG0 v,212,a,16,a,252
buffer_load_dwordx4 v253, s[48:51], s67 offen offset:640 lds
MG v,212,a,44,v,224
RU 108,246,0
MG0 v,216,a,20,a,252
RU 110,246,2048
MG v,216,a,48,v,224
s_add_u32 m0, s68, 81152
MG0 v,220,a,24,a,252
buffer_load_dwordx4 v253, s[48:51], s67 offen offset:768 lds
s_add_u32 s67, s67, 28672
MG v,220,a,52,v,224
ds_read_b128 a[252:255], v249 offset:27648
s_waitcnt lgkmcnt(15)
MG0 a,224,a,0,v,228
ds_read_b128 v[224:227], v249 offset:19456
s_waitcnt lgkmcnt(10)
MG a,224,a,28,v,232
MG0 a,228,a,4,v,228
MG a,228,a,32,v,232
MG0 a,232,a,8,v,228
MG a,232,a,36,v,232
MG0 a,236,a,12,v,228
MG a,236,a,40,v,232
MG0 a,240,a,16,v,228
MG a,240,a,44,v,232
MG0 a,244,a,20,v,228
MG a,244,a,48,v,232
MG0 a,248,a,24,v,228
s_waitcnt lgkmcnt(1)
MG a,248,a,52,v,232
ds_read_b128 v[228:231], v249 offset:28672
MG v,0,a,56,v,236
ds_read_b128 v[232:235], v249 offset:20480
MG v,0,a,84,a,252
MG v,4,a,60,v,236
MG v,4,a,88,a,252
MG v,8,a,64,v,236
MG v,8,a,92,a,252
MG v,12,a,68,v,236
MG v,12,a,96,a,252
MG v,16,a,72,v,236
s_waitcnt vmcnt(7)
MG v,16,a,100,a,252
s_barrier
MG v,20,a,76,v,236
s_add_u32 m0, s68, 0
RU 112,244,28672
MG v,20,a,104,a,252
buffer_load_dwordx4 v253, s[48:51], s67 offen offset:0 lds
MG v,24,a,80,v,236
RU 114,244,30720
MG v,24,a,108,a,252
ds_read_b128 v[236:239], v249 offset:29696
s_waitcnt lgkmcnt(5)
MG v,28,a,56,v,224
s_add_u32 m0, s68, 3968
ds_read_b128 a[252:255], v249 offset:21504
s_waitcnt lgkmcnt(5)
MG v,28,a,84,v,228
buffer_load_dwordx4 v253, s[48:51], s67 offen offset:128 lds
MG v,32,a,60,v,224
RU 116,244,28680
MG v,32,a,88,v,228
RU 118,244,30728
MG v,36,a,64,v,224
s_add_u32 m0, s68, 7936
RU 120,244,36864
MG v,36,a,92,v,228
buffer_load_dwordx4 v253, s[48:51], s67 offen offset:256 lds
MG v,40,a,68,v,224
RU 122,244,38912
MG v,40,a,96,v,228
RU 124,244,36872
MG v,44,a,72,v,224
s_add_u32 m0, s68, 11904
RU 126,244,38920
MG v,44,a,100,v,228
buffer_load_dwordx4 v253, s[48:51], s67 offen offset:384 lds
MG v,48,a,76,v,224
RU 128,244,45056
MG v,48,a,104,v,228
RU 130,244,47104
MG v,52,a,80,v,224
s_add_u32 m0, s68, 15872
RU 132,244,45064
MG v,52,a,108,v,228
buffer_load_dwordx4 v253, s[48:51], s67 offen offset:512 lds
s_waitcnt lgkmcnt(13)
MG v,56,a,56,v,232
ds_read_b128 v[224:227], v249 offset:30720
s_waitcnt lgkmcnt(11)
MG v,56,a,84,v,236
ds_read_b128 v[228:231], v249 offset:22528
MG v,60,a,60,v,232
s_add_u32 m0, s68, 19840
RU 134,244,47112
MG v,60,a,88,v,236
buffer_load_dwordx4 v253, s[48:51], s67 offen offset:640 lds
MG v,64,a,64,v,232
RU 136,246,28672
MG v,64,a,92,v,236
RU 138,246,30720
MG v,68,a,68,v,232
s_add_u32 m0, s68, 23808
MG v,68,a,96,v,236
buffer_load_dwordx4 v253, s[48:51], s67 offen offset:768 lds
s_add_u32 s67, s67, 28672
MG v,72,a,72,v,232
MG v,72,a,100,v,236
MG v,76,a,76,v,232
MG v,76,a,104,v,236
MG v,80,a,80,v,232
s_waitcnt lgkmcnt(4)
MG v,80,a,108,v,236
ds_read_b128 v[232:235], v249 offset:31744
MG v,84,a,56,a,252
ds_read_b128 v[236:239], v249 offset:23552
MG v,84,a,84,v,224
MG v,88,a,60,a,252
MG v,88,a,88,v,224
MG v,92,a,64,a,252
MG v,92,a,92,v,224
MG v,96,a,68,a,252
MG v,96,a,96,v,224
MG v,100,a,72,a,252
MG v,100,a,100,v,224
MG v,104,a,76,a,252
MG v,104,a,104,v,224
MG v,108,a,80,a,252
s_waitcnt lgkmcnt(1)
MG v,108,a,108,v,224
ds_read_b128 a[252:255], v249 offset:32768
MG v,112,a,56,v,228
ds_read_b128 v[224:227], v249 offset:24576
MG v,112,a,84,v,232
MG v,116,a,60,v,228
s_waitcnt vmcnt(7)
MG v,116,a,88,v,232
s_barrier
MG v,120,a,64,v,228
s_add_u32 m0, s68, 28672
RU 140,245,0
MG v,120,a,92,v,232
buffer_load_dwordx4 v253, s[48:51], s67 offen offset:0 lds
MG v,124,a,68,v,228
RU 142,245,2048
MG v,124,a,96,v,232
RU 144,245,8
MG v,128,a,72,v,228
s_add_u32 m0, s68, 32640
RU 146,245,2056
MG v,128,a,100,v,232
buffer_load_dwordx4 v253, s[48:51], s67 offen offset:128 lds
MG v,132,a,76,v,228
RU 148,245,8192
MG v,132,a,104,v,232
RU 150,245,10240
MG v,136,a,80,v,228
s_add_u32 m0, s68, 36608
RU 152,245,8200
MG v,136,a,108,v,232
buffer_load_dwordx4 v253, s[48:51], s67 offen offset:256 lds
s_waitcnt lgkmcnt(9)
MG v,140,a,56,v,236
ds_read_b128 v[228:231], v249 offset:33792
s_waitcnt lgkmcnt(9)
MG v,140,a,84,a,252
ds_read_b128 v[232:235], v249 offset:25600
MG v,144,a,60,v,236
s_add_u32 m0, s68, 40576
RU 154,245,10248
MG v,144,a,88,a,252
buffer_load_dwordx4 v253, s[48:51], s67 offen offset:384 lds
MG v,148,a,64,v,236
RU 156,245,16384
v_lshl_add_u32 v240, %[wid], 10, v248
MG v,148,a,92,a,252
RU 158,245,18432
v_add_u32 v241, 0x16000, v240
MG v,152,a,68,v,236
s_add_u32 m0, s68, 44544
RU 160,245,16392
v_add_u32 v242, 0x20000, v240
MG v,152,a,96,a,252
buffer_load_dwordx4 v253, s[48:51], s67 offen offset:512 lds
MG v,156,a,72,v,236
RU 162,245,18440
MG v,156,a,100,a,252
RU 164,247,0
MG v,160,a,76,v,236
s_add_u32 m0, s68, 48512
RU 166,247,2048
MG v,160,a,104,a,252
buffer_load_dwordx4 v253, s[48:51], s67 offen offset:640 lds
MG v,164,a,80,v,236
s_waitcnt lgkmcnt(8)
MG v,164,a,108,a,252
ds_read_b128 v[236:239], v249 offset:34816
MG v,168,a,56,v,224
s_add_u32 m0, s68, 52480
ds_read_b128 a[252:255], v249 offset:26624
MG v,168,a,84,v,228
buffer_load_dwordx4 v253, s[48:51], s67 offen offset:768 lds
s_mov_b32 s67, s69
MG v,172,a,60,v,224
MG v,172,a,88,v,228
MG v,176,a,64,v,224
MG v,176,a,92,v,228
MG v,180,a,68,v,224
MG v,180,a,96,v,228
MG v,184,a,72,v,224
MG v,184,a,100,v,228
MG v,188,a,76,v,224
global_load_dword v254, v250, s[36:37] sc1
MG v,188,a,104,v,228
MG v,192,a,80,v,224
s_waitcnt lgkmcnt(1)
MG v,192,a,108,v,228
ds_read_b128 v[224:227], v249 offset:35840
MG v,196,a,56,v,232
ds_read_b128 v[228:231], v249 offset:36864
MG v,196,a,84,v,236
MG v,200,a,60,v,232
MG v,200,a,88,v,236
MG v,204,a,64,v,232
MG v,204,a,92,v,236
MG v,208,a,68,v,232
MG v,208,a,96,v,236
MG v,212,a,72,v,232
MG v,212,a,100,v,236
MG v,216,a,76,v,232
s_waitcnt vmcnt(8)
MG v,216,a,104,v,236
s_barrier
MG v,220,a,80,v,232
s_add_u32 m0, s68, 57344
RU 168,244,0
MG v,220,a,108,v,236
buffer_load_dwordx4 v253, s[48:51], s67 offen offset:0 lds
s_waitcnt lgkmcnt(3)
MG a,224,a,56,a,252
ds_read_b128 v[232:235], v249 offset:46080
s_waitcnt lgkmcnt(3)
MG a,224,a,84,v,224
ds_read_b128 v[236:239], v249 offset:37888
MG a,228,a,60,a,252
s_add_u32 m0, s68, 61312
RU 170,244,2048
MG a,228,a,88,v,224
buffer_load_dwordx4 v253, s[48:51], s67 offen offset:128 lds
MG a,232,a,64,a,252
RU 172,244,8
MG a,232,a,92,v,224
RU 174,244,2056
MG a,236,a,68,a,252
s_add_u32 m0, s68, 65280
RU 176,244,8192
MG a,236,a,96,v,224
buffer_load_dwordx4 v253, s[48:51], s67 offen offset:256 lds
MG a,240,a,72,a,252
RU 178,244,10240
MG a,240,a,100,v,224
RU 180,244,8200
MG a,244,a,76,a,252
s_add_u32 m0, s68, 69248
RU 182,244,10248
MG a,244,a,104,v,224
buffer_load_dwordx4 v253, s[48:51], s67 offen offset:384 lds
MG a,248,a,80,a,252
RU 184,244,16384
MG a,248,a,108,v,224
ds_read_b128 a[252:255], v249 offset:47104
s_waitcnt lgkmcnt(12)
MG v,0,a,112,v,228
s_add_u32 m0, s68, 73216
ds_read_b128 v[224:227], v249 offset:38912
s_waitcnt lgkmcnt(11)
MG v,0,a,140,v,232
buffer_load_dwordx4 v253, s[48:51], s67 offen offset:512 lds
MG v,4,a,116,v,228
RU 186,244,18432
MG v,4,a,144,v,232
RU 188,244,16392
MG v,8,a,120,v,228
s_add_u32 m0, s68, 77184
RU 190,244,18440
MG v,8,a,148,v,232
buffer_load_dwordx4 v253, s[48:51], s67 offen offset:640 lds
MG v,12,a,124,v,228
RU 192,246,0
MG v,12,a,152,v,232
RU 194,246,2048
MG v,16,a,128,v,228
s_add_u32 m0, s68, 81152
MG v,16,a,156,v,232
buffer_load_dwordx4 v253, s[48:51], s67 offen offset:768 lds
s_add_u32 s67, s67, 28672
MG v,20,a,132,v,228
MG v,20,a,160,v,232
MG v,24,a,136,v,228
s_waitcnt lgkmcnt(6)
MG v,24,a,164,v,232
ds_read_b128 v[228:231], v249 offset:48128
MG v,28,a,112,v,236
ds_read_b128 v[232:235], v249 offset:39936
MG v,28,a,140,a,252
MG v,32,a,116,v,236
MG v,32,a,144,a,252
MG v,36,a,120,v,236
MG v,36,a,148,a,252
MG v,40,a,124,v,236
MG v,40,a,152,a,252
MG v,44,a,128,v,236
s_waitcnt vmcnt(7)
MG v,44,a,156,a,252
s_and_b32 s77, s26, 15
s_xor_b32 s77, s77, 1
s_lshl_b32 s77, 1, s77
v_cmp_ne_u32 vcc, s79, v254
MG v,48,a,132,v,236
s_nop 1
s_and_b32 vcc_lo, vcc_lo, s77
s_cbranch_scc1 LT3s
LT3e:
MG v,48,a,160,a,252
s_mov_b32 s56, 0
buffer_load_dwordx4 a[56:59], v240, s[44:47], s56 offen offset:0 sc1
MG v,52,a,136,v,236
s_waitcnt lgkmcnt(1)
MG v,52,a,164,a,252
ds_read_b128 v[236:239], v249 offset:49152
MG v,56,a,112,v,224
ds_read_b128 a[252:255], v249 offset:40960
MG v,56,a,140,v,228
s_mov_b32 s56, 4096
buffer_load_dwordx4 a[60:63], v240, s[44:47], s56 offen offset:0 sc1
MG v,60,a,116,v,224
MG v,60,a,144,v,228
MG v,64,a,120,v,224
MG v,64,a,148,v,228
s_barrier
MG v,68,a,124,v,224
s_add_u32 m0, s68, 0
RU 196,244,28672
MG v,68,a,152,v,228
buffer_load_dwordx4 v253, s[48:51], s67 offen offset:0 lds
MG v,72,a,128,v,224
RU 198,244,30720
MG v,72,a,156,v,228
RU 200,244,28680
MG v,76,a,132,v,224
s_add_u32 m0, s68, 3968
RU 202,244,30728
MG v,76,a,160,v,228
buffer_load_dwordx4 v253, s[48:51], s67 offen offset:128 lds
MG v,80,a,136,v,224
RU 204,244,36864
MG v,80,a,164,v,228
ds_read_b128 v[224:227], v249 offset:50176
s_waitcnt lgkmcnt(8)
MG v,84,a,112,v,232
s_add_u32 m0, s68, 7936
ds_read_b128 v[228:231], v249 offset:41984
s_waitcnt lgkmcnt(8)
MG v,84,a,140,v,236
buffer_load_dwordx4 v253, s[48:51], s67 offen offset:256 lds
MG v,88,a,116,v,232
RU 206,244,38912
MG v,88,a,144,v,236
RU 208,244,36872
MG v,92,a,120,v,232
s_add_u32 m0, s68, 11904
RU 210,244,38920
MG v,92,a,148,v,236
buffer_load_dwordx4 v253, s[48:51], s67 offen offset:384 lds
MG v,96,a,124,v,232
RU 212,244,45056
MG v,96,a,152,v,236
RU 214,244,47104
MG v,100,a,128,v,232
s_add_u32 m0, s68, 15872
RU 216,244,45064
MG v,100,a,156,v,236
buffer_load_dwordx4 v253, s[48:51], s67 offen offset:512 lds
MG v,104,a,132,v,232
RU 218,244,47112
MG v,104,a,160,v,236
RU 220,246,28672
MG v,108,a,136,v,232
s_add_u32 m0, s68, 19840
RU 222,246,30720
MG v,108,a,164,v,236
buffer_load_dwordx4 v253, s[48:51], s67 offen offset:640 lds
s_waitcnt lgkmcnt(15)
MG v,112,a,112,a,252
ds_read_b128 v[232:235], v249 offset:51200
s_waitcnt lgkmcnt(11)
MG v,112,a,140,v,224
ds_read_b128 v[236:239], v249 offset:43008
MG v,116,a,116,a,252
s_add_u32 m0, s68, 23808
MG v,116,a,144,v,224
buffer_load_dwordx4 v253, s[48:51], s67 offen offset:768 lds
s_add_u32 s67, s67, 28672
MG v,120,a,120,a,252
MG v,120,a,148,v,224
MG v,124,a,124,a,252
MG v,124,a,152,v,224
s_mov_b32 s56, 8192
buffer_load_dwordx4 a[64:67], v240, s[44:47], s56 offen offset:0 sc1
MG v,128,a,128,a,252
MG v,128,a,156,v,224
MG v,132,a,132,a,252
MG v,132,a,160,v,224
s_mov_b32 s56, 12288
buffer_load_dwordx4 a[68:71], v240, s[44:47], s56 offen offset:0 sc1
MG v,136,a,136,a,252
s_waitcnt lgkmcnt(1)
MG v,136,a,164,v,224
ds_read_b128 a[252:255], v249 offset:52224
MG v,140,a,112,v,228
ds_read_b128 v[224:227], v249 offset:44032
MG v,140,a,140,v,232
s_mov_b32 s56, 16384
buffer_load_dwordx4 a[72:75], v240, s[44:47], s56 offen offset:0 sc1
MG v,144,a,116,v,228
MG v,144,a,144,v,232
MG v,148,a,120,v,228
MG v,148,a,148,v,232
s_mov_b32 s56, 20480
buffer_load_dwordx4 a[76:79], v240, s[44:47], s56 offen offset:0 sc1
MG v,152,a,124,v,228
MG v,152,a,152,v,232
MG v,156,a,128,v,228
MG v,156,a,156,v,232
s_mov_b32 s56, 24576
buffer_load_dwordx4 a[80:83], v240, s[44:47], s56 offen offset:0 sc1
MG v,160,a,132,v,228
MG v,160,a,160,v,232
MG v,164,a,136,v,228
s_waitcnt vmcnt(14) lgkmcnt(1)
MG v,164,a,164,v,232
s_barrier
MG v,168,a,112,v,236
s_add_u32 m0, s68, 28672
ds_read_b128 v[228:231], v249 offset:53248
MG v,168,a,140,a,252
buffer_load_dwordx4 v253, s[48:51], s67 offen offset:0 lds
MG v,172,a,116,v,236
ds_read_b128 v[232:235], v249 offset:45056
MG v,172,a,144,a,252
RU 0,245,0
MG v,176,a,120,v,236
s_add_u32 m0, s68, 32640
RU 2,245,2048
MG v,176,a,148,a,252
buffer_load_dwordx4 v253, s[48:51], s67 offen offset:128 lds
MG v,180,a,124,v,236
RU 4,245,8
MG v,180,a,152,a,252
RU 6,245,2056
MG v,184,a,128,v,236
s_add_u32 m0, s68, 36608
RU 8,245,8192
MG v,184,a,156,a,252
buffer_load_dwordx4 v253, s[48:51], s67 offen offset:256 lds
MG v,188,a,132,v,236
RU 10,245,10240
MG v,188,a,160,a,252
RU 12,245,8200
MG v,192,a,136,v,236
s_add_u32 m0, s68, 40576
RU 14,245,10248
MG v,192,a,164,a,252
buffer_load_dwordx4 v253, s[48:51], s67 offen offset:384 lds
s_waitcnt lgkmcnt(10)
MG v,196,a,112,v,224
ds_read_b128 v[236:239], v249 offset:54272
s_waitcnt lgkmcnt(10)
MG v,196,a,140,v,228
ds_read_b128 a[252:255], v249 offset:55296
MG v,200,a,116,v,224
s_add_u32 m0, s68, 44544
RU 16,245,16384
MG v,200,a,144,v,228
buffer_load_dwordx4 v253, s[48:51], s67 offen offset:512 lds
MG v,204,a,120,v,224
RU 18,245,18432
MG v,204,a,148,v,228
RU 20,245,16392
MG v,208,a,124,v,224
s_add_u32 m0, s68, 48512
RU 22,245,18440
MG v,208,a,152,v,228
buffer_load_dwordx4 v253, s[48:51], s67 offen offset:640 lds
MG v,212,a,128,v,224
RU 24,247,0
MG v,212,a,156,v,228
RU 26,247,2048
MG v,216,a,132,v,224
s_add_u32 m0, s68, 52480
MG v,216,a,160,v,228
buffer_load_dwordx4 v253, s[48:51], s67 offen offset:768 lds
s_add_u32 s67, s67, 28672
MG v,220,a,136,v,224
s_waitcnt lgkmcnt(7)
MG v,220,a,164,v,228
ds_read_b128 v[224:227], v249 offset:64512
MG a,224,a,112,v,232
ds_read_b128 v[228:231], v249 offset:56320
)ASM"
R"ASM(
MG a,224,a,140,v,236
s_mov_b32 s56, 28672
buffer_load_dwordx4 a[84:87], v240, s[44:47], s56 offen offset:0 sc1
MG a,228,a,116,v,232
MG a,228,a,144,v,236
MG a,232,a,120,v,232
MG a,232,a,148,v,236
s_mov_b32 s56, 32768
buffer_load_dwordx4 a[88:91], v240, s[44:47], s56 offen offset:0 sc1
MG a,236,a,124,v,232
MG a,236,a,152,v,236
MG a,240,a,128,v,232
MG a,240,a,156,v,236
s_mov_b32 s56, 36864
buffer_load_dwordx4 a[92:95], v240, s[44:47], s56 offen offset:0 sc1
MG a,244,a,132,v,232
MG a,244,a,160,v,236
MG a,248,a,136,v,232
s_waitcnt lgkmcnt(1)
MG a,248,a,164,v,236
ds_read_b128 v[232:235], v243 offset:0
MG v,0,a,168,a,252
ds_read_b128 v[236:239], v249 offset:57344
MG v,0,a,196,v,224
s_mov_b32 s56, 40960
buffer_load_dwordx4 a[96:99], v240, s[44:47], s56 offen offset:0 sc1
MG v,4,a,172,a,252
MG v,4,a,200,v,224
MG v,8,a,176,a,252
MG v,8,a,204,v,224
s_mov_b32 s56, 45056
buffer_load_dwordx4 a[100:103], v240, s[44:47], s56 offen offset:0 sc1
MG v,12,a,180,a,252
s_waitcnt vmcnt(17)
MG v,12,a,208,v,224
s_barrier
MG v,16,a,184,a,252
s_add_u32 m0, s68, 57344
RU 28,244,0
MG v,16,a,212,v,224
buffer_load_dwordx4 v253, s[48:51], s67 offen offset:0 lds
MG v,20,a,188,a,252
RU 30,244,2048
MG v,20,a,216,v,224
RU 32,244,8
MG v,24,a,192,a,252
s_add_u32 m0, s68, 61312
RU 34,244,2056
MG v,24,a,220,v,224
buffer_load_dwordx4 v253, s[48:51], s67 offen offset:128 lds
s_waitcnt lgkmcnt(6)
MG v,28,a,168,v,228
ds_read_b128 a[252:255], v243 offset:1024
s_waitcnt lgkmcnt(6)
MG v,28,a,196,v,232
ds_read_b128 v[224:227], v249 offset:58368
MG v,32,a,172,v,228
s_add_u32 m0, s68, 65280
RU 36,244,8192
MG v,32,a,200,v,232
buffer_load_dwordx4 v253, s[48:51], s67 offen offset:256 lds
MG v,36,a,176,v,228
RU 38,244,10240
MG v,36,a,204,v,232
RU 40,244,8200
MG v,40,a,180,v,228
s_add_u32 m0, s68, 69248
RU 42,244,10248
MG v,40,a,208,v,232
buffer_load_dwordx4 v253, s[48:51], s67 offen offset:384 lds
MG v,44,a,184,v,228
RU 44,244,16384
MG v,44,a,212,v,232
RU 46,244,18432
MG v,48,a,188,v,228
s_add_u32 m0, s68, 73216
RU 48,244,16392
MG v,48,a,216,v,232
buffer_load_dwordx4 v253, s[48:51], s67 offen offset:512 lds
MG v,52,a,192,v,228
RU 50,244,18440
MG v,52,a,220,v,232
ds_read_b128 v[228:231], v243 offset:2048
s_waitcnt lgkmcnt(15)
MG v,56,a,168,v,236
s_add_u32 m0, s68, 77184
ds_read_b128 v[232:235], v249 offset:59392
s_waitcnt lgkmcnt(11)
MG v,56,a,196,a,252
buffer_load_dwordx4 v253, s[48:51], s67 offen offset:640 lds
MG v,60,a,172,v,236
RU 52,246,0
MG v,60,a,200,a,252
RU 54,246,2048
MG v,64,a,176,v,236
s_add_u32 m0, s68, 81152
ds_write_b128 v241, a[56:59] offset:0
MG v,64,a,204,a,252
buffer_load_dwordx4 v253, s[48:51], s67 offen offset:768 lds
s_add_u32 s67, s67, 28672
MG v,68,a,180,v,236
ds_write_b128 v241, a[60:63] offset:4096
MG v,68,a,208,a,252
MG v,72,a,184,v,236
MG v,72,a,212,a,252
s_mov_b32 s56, 49152
buffer_load_dwordx4 a[104:107], v240, s[44:47], s56 offen offset:0 sc1
MG v,76,a,188,v,236
MG v,76,a,216,a,252
MG v,80,a,192,v,236
s_waitcnt lgkmcnt(5)
MG v,80,a,220,a,252
ds_read_b128 v[236:239], v243 offset:3072
MG v,84,a,168,v,224
ds_read_b128 a[252:255], v249 offset:60416
MG v,84,a,196,v,228
s_mov_b32 s56, 53248
buffer_load_dwordx4 a[108:111], v240, s[44:47], s56 offen offset:0 sc1
MG v,88,a,172,v,224
MG v,88,a,200,v,228
MG v,92,a,176,v,224
MG v,92,a,204,v,228
s_mov_b32 s56, 57344
buffer_load_dwordx4 a[56:59], v240, s[44:47], s56 offen offset:0 sc1
MG v,96,a,180,v,224
MG v,96,a,208,v,228
MG v,100,a,184,v,224
MG v,100,a,212,v,228
s_mov_b32 s56, 61440
buffer_load_dwordx4 a[60:63], v240, s[44:47], s56 offen offset:0 sc1
MG v,104,a,188,v,224
MG v,104,a,216,v,228
MG v,108,a,192,v,224
s_waitcnt lgkmcnt(6)
MG v,108,a,220,v,228
ds_read_b128 v[224:227], v243 offset:4096
MG v,112,a,168,v,232
s_waitcnt vmcnt(16) lgkmcnt(2)
MG v,112,a,196,v,236
s_barrier
MG v,116,a,172,v,232
s_add_u32 m0, s68, 0
ds_read_b128 v[228:231], v249 offset:61440
MG v,116,a,200,v,236
buffer_load_dwordx4 v253, s[48:51], s67 offen offset:0 lds
MG v,120,a,176,v,232
RU 112,244,28672
MG v,120,a,204,v,236
RU 114,244,30720
MG v,124,a,180,v,232
s_add_u32 m0, s68, 3968
RU 116,244,28680
MG v,124,a,208,v,236
buffer_load_dwordx4 v253, s[48:51], s67 offen offset:128 lds
MG v,128,a,184,v,232
RU 118,244,30728
MG v,128,a,212,v,236
RU 120,244,36864
MG v,132,a,188,v,232
s_add_u32 m0, s68, 7936
RU 122,244,38912
MG v,132,a,216,v,236
buffer_load_dwordx4 v253, s[48:51], s67 offen offset:256 lds
MG v,136,a,192,v,232
RU 124,244,36872
MG v,136,a,220,v,236
ds_read_b128 v[232:235], v243 offset:5120
s_waitcnt lgkmcnt(10)
MG v,140,a,168,a,252
s_add_u32 m0, s68, 11904
ds_read_b128 v[236:239], v249 offset:62464
s_waitcnt lgkmcnt(10)
MG v,140,a,196,v,224
buffer_load_dwordx4 v253, s[48:51], s67 offen offset:384 lds
MG v,144,a,172,a,252
RU 126,244,38920
MG v,144,a,200,v,224
RU 128,244,45056
MG v,148,a,176,a,252
s_add_u32 m0, s68, 15872
RU 130,244,47104
MG v,148,a,204,v,224
buffer_load_dwordx4 v253, s[48:51], s67 offen offset:512 lds
MG v,152,a,180,a,252
RU 132,244,45064
MG v,152,a,208,v,224
RU 134,244,47112
MG v,156,a,184,a,252
s_add_u32 m0, s68, 19840
RU 136,246,28672
MG v,156,a,212,v,224
buffer_load_dwordx4 v253, s[48:51], s67 offen offset:640 lds
MG v,160,a,188,a,252
RU 138,246,30720
MG v,160,a,216,v,224
ds_write_b128 v241, a[64:67] offset:8192
MG v,164,a,192,a,252
s_add_u32 m0, s68, 23808
ds_write_b128 v241, a[68:71] offset:12288
MG v,164,a,220,v,224
buffer_load_dwordx4 v253, s[48:51], s67 offen offset:768 lds
s_add_u32 s67, s67, 28672
s_waitcnt lgkmcnt(15)
MG v,168,a,168,v,228
ds_read_b128 a[252:255], v243 offset:6144
s_waitcnt lgkmcnt(11)
MG v,168,a,196,v,232
ds_read_b128 v[224:227], v249 offset:63488
MG v,172,a,172,v,228
ds_write_b128 v241, a[72:75] offset:16384
MG v,172,a,200,v,232
ds_write_b128 v241, a[76:79] offset:20480
MG v,176,a,176,v,228
ds_write_b128 v241, a[80:83] offset:24576
MG v,176,a,204,v,232
s_mov_b32 s56, 65536
buffer_load_dwordx4 a[64:67], v240, s[44:47], s56 offen offset:0 sc1
MG v,180,a,180,v,228
MG v,180,a,208,v,232
MG v,184,a,184,v,228
MG v,184,a,212,v,232
s_mov_b32 s56, 69632
buffer_load_dwordx4 a[68:71], v240, s[44:47], s56 offen offset:0 sc1
MG v,188,a,188,v,228
MG v,188,a,216,v,232
MG v,192,a,192,v,228
s_waitcnt lgkmcnt(14)
MG v,192,a,220,v,232
ds_read_b128 v[228:231], v243 offset:7168
MG v,196,a,168,v,236
s_waitcnt lgkmcnt(5)
MG v,196,a,196,a,252
MG v,200,a,172,v,236
MG v,200,a,200,a,252
MG v,204,a,176,v,236
MG v,204,a,204,a,252
MG v,208,a,180,v,236
MG v,208,a,208,a,252
MG v,212,a,184,v,236
MG v,212,a,212,a,252
MG v,216,a,188,v,236
MG v,216,a,216,a,252
s_waitcnt vmcnt(13) lgkmcnt(3)
MG v,220,a,192,v,236
s_barrier
MG v,220,a,220,a,252
s_add_u32 m0, s68, 28672
ds_read_b128 v[232:235], v249 offset:0
MG a,224,a,168,v,224
buffer_load_dwordx4 v253, s[48:51], s67 offen offset:0 lds
s_waitcnt lgkmcnt(1)
MG a,224,a,196,v,228
ds_read_b128 v[236:239], v249 offset:9216
MG a,228,a,172,v,224
ds_read_b128 a[252:255], v249 offset:1024
MG a,228,a,200,v,228
s_add_u32 m0, s68, 32640
RU 140,245,0
MG a,232,a,176,v,224
buffer_load_dwordx4 v253, s[48:51], s67 offen offset:128 lds
MG a,232,a,204,v,228
RU 142,245,2048
MG a,236,a,180,v,224
RU 144,245,8
MG a,236,a,208,v,228
s_add_u32 m0, s68, 36608
RU 146,245,2056
MG a,240,a,184,v,224
buffer_load_dwordx4 v253, s[48:51], s67 offen offset:256 lds
MG a,240,a,212,v,228
RU 148,245,8192
MG a,244,a,188,v,224
RU 150,245,10240
MG a,244,a,216,v,228
s_add_u32 m0, s68, 40576
RU 152,245,8200
MG a,248,a,192,v,224
buffer_load_dwordx4 v253, s[48:51], s67 offen offset:384 lds
MG a,248,a,220,v,228
ds_read_b128 v[224:227], v249 offset:10240
s_waitcnt lgkmcnt(10)
MG v,0,a,0,v,232
ds_read_b128 v[228:231], v249 offset:2048
s_waitcnt lgkmcnt(10)
MG v,0,a,28,v,236
s_add_u32 m0, s68, 44544
RU 154,245,10248
MG v,4,a,4,v,232
buffer_load_dwordx4 v253, s[48:51], s67 offen offset:512 lds
MG v,4,a,32,v,236
RU 156,245,16384
MG v,8,a,8,v,232
RU 158,245,18432
MG v,8,a,36,v,236
s_add_u32 m0, s68, 48512
RU 160,245,16392
MG v,12,a,12,v,232
buffer_load_dwordx4 v253, s[48:51], s67 offen offset:640 lds
MG v,12,a,40,v,236
RU 162,245,18440
MG v,16,a,16,v,232
RU 164,247,0
MG v,16,a,44,v,236
s_add_u32 m0, s68, 52480
RU 166,247,2048
MG v,20,a,20,v,232
buffer_load_dwordx4 v253, s[48:51], s67 offen offset:768 lds
s_add_u32 s67, s67, 28672
MG v,20,a,48,v,236
ds_write_b128 v241, a[84:87] offset:28672
MG v,24,a,24,v,232
ds_write_b128 v241, a[88:91] offset:32768
MG v,24,a,52,v,236
ds_read_b128 v[232:235], v249 offset:11264
s_waitcnt lgkmcnt(15)
MG v,28,a,0,a,252
ds_read_b128 v[236:239], v249 offset:3072
s_waitcnt lgkmcnt(12)
MG v,28,a,28,v,224
ds_write_b128 v241, a[92:95] offset:36864
MG v,32,a,4,a,252
ds_write_b128 v242, a[96:99] offset:0
MG v,32,a,32,v,224
ds_write_b128 v242, a[100:103] offset:4096
MG v,36,a,8,a,252
MG v,36,a,36,v,224
MG v,40,a,12,a,252
MG v,40,a,40,v,224
MG v,44,a,16,a,252
MG v,44,a,44,v,224
s_waitcnt vmcnt(9) lgkmcnt(7)
MG v,48,a,20,a,252
s_barrier
MG v,48,a,48,v,224
s_add_u32 m0, s68, 57344
RU 168,244,0
MG v,52,a,24,a,252
buffer_load_dwordx4 v253, s[48:51], s67 offen offset:0 lds
MG v,52,a,52,v,224
ds_read_b128 a[252:255], v249 offset:12288
MG v,56,a,0,v,228
ds_read_b128 v[224:227], v249 offset:4096
s_waitcnt lgkmcnt(7)
MG v,56,a,28,v,232
s_add_u32 m0, s68, 61312
RU 170,244,2048
MG v,60,a,4,v,228
buffer_load_dwordx4 v253, s[48:51], s67 offen offset:128 lds
MG v,60,a,32,v,232
RU 172,244,8
MG v,64,a,8,v,228
RU 174,244,2056
MG v,64,a,36,v,232
s_add_u32 m0, s68, 65280
RU 176,244,8192
MG v,68,a,12,v,228
buffer_load_dwordx4 v253, s[48:51], s67 offen offset:256 lds
MG v,68,a,40,v,232
RU 178,244,10240
MG v,72,a,16,v,228
RU 180,244,8200
MG v,72,a,44,v,232
s_add_u32 m0, s68, 69248
RU 182,244,10248
MG v,76,a,20,v,228
buffer_load_dwordx4 v253, s[48:51], s67 offen offset:384 lds
MG v,76,a,48,v,232
RU 184,244,16384
MG v,80,a,24,v,228
RU 186,244,18432
MG v,80,a,52,v,232
s_add_u32 m0, s68, 73216
ds_read_b128 v[228:231], v249 offset:13312
s_waitcnt lgkmcnt(15)
MG v,84,a,0,v,236
buffer_load_dwordx4 v253, s[48:51], s67 offen offset:512 lds
s_waitcnt lgkmcnt(11)
MG v,84,a,28,a,252
ds_read_b128 v[232:235], v249 offset:5120
MG v,88,a,4,v,236
RU 188,244,16392
MG v,88,a,32,a,252
s_add_u32 m0, s68, 77184
RU 190,244,18440
MG v,92,a,8,v,236
buffer_load_dwordx4 v253, s[48:51], s67 offen offset:640 lds
MG v,92,a,36,a,252
RU 192,246,0
MG v,96,a,12,v,236
RU 194,246,2048
MG v,96,a,40,a,252
s_add_u32 m0, s68, 81152
ds_write_b128 v242, a[104:107] offset:8192
MG v,100,a,16,v,236
buffer_load_dwordx4 v253, s[48:51], s67 offen offset:768 lds
s_add_u32 s67, s67, 28672
MG v,100,a,44,a,252
ds_write_b128 v242, a[108:111] offset:12288
MG v,104,a,20,v,236
ds_write_b128 v242, a[56:59] offset:16384
MG v,104,a,48,a,252
ds_write_b128 v242, a[60:63] offset:20480
MG v,108,a,24,v,236
s_waitcnt lgkmcnt(9)
MG v,108,a,52,a,252
ds_read_b128 v[236:239], v249 offset:14336
MG v,112,a,0,v,224
ds_read_b128 a[252:255], v249 offset:6144
MG v,112,a,28,v,228
MG v,116,a,4,v,224
MG v,116,a,32,v,228
MG v,120,a,8,v,224
s_waitcnt vmcnt(7) lgkmcnt(6)
MG v,120,a,36,v,228
s_barrier
MG v,124,a,12,v,224
s_add_u32 m0, s68, 0
RU 196,244,28672
MG v,124,a,40,v,228
buffer_load_dwordx4 v253, s[48:51], s67 offen offset:0 lds
MG v,128,a,16,v,224
RU 198,244,30720
MG v,128,a,44,v,228
RU 200,244,28680
MG v,132,a,20,v,224
s_add_u32 m0, s68, 3968
RU 202,244,30728
MG v,132,a,48,v,228
buffer_load_dwordx4 v253, s[48:51], s67 offen offset:128 lds
MG v,136,a,24,v,224
RU 204,244,36864
MG v,136,a,52,v,228
ds_read_b128 v[224:227], v249 offset:15360
MG v,140,a,0,v,232
s_add_u32 m0, s68, 7936
ds_read_b128 v[228:231], v249 offset:7168
s_waitcnt lgkmcnt(8)
MG v,140,a,28,v,236
buffer_load_dwordx4 v253, s[48:51], s67 offen offset:256 lds
MG v,144,a,4,v,232
RU 206,244,38912
MG v,144,a,32,v,236
RU 208,244,36872
MG v,148,a,8,v,232
s_add_u32 m0, s68, 11904
RU 210,244,38920
MG v,148,a,36,v,236
buffer_load_dwordx4 v253, s[48:51], s67 offen offset:384 lds
MG v,152,a,12,v,232
RU 212,244,45056
MG v,152,a,40,v,236
RU 214,244,47104
MG v,156,a,16,v,232
s_add_u32 m0, s68, 15872
RU 216,244,45064
MG v,156,a,44,v,236
buffer_load_dwordx4 v253, s[48:51], s67 offen offset:512 lds
MG v,160,a,20,v,232
RU 218,244,47112
MG v,160,a,48,v,236
RU 220,246,28672
MG v,164,a,24,v,232
s_add_u32 m0, s68, 19840
RU 222,246,30720
MG v,164,a,52,v,236
buffer_load_dwordx4 v253, s[48:51], s67 offen offset:640 lds
s_waitcnt lgkmcnt(15)
MG v,168,a,0,a,252
ds_read_b128 v[232:235], v249 offset:16384
s_waitcnt lgkmcnt(11)
MG v,168,a,28,v,224
ds_read_b128 v[236:239], v249 offset:8192
MG v,172,a,4,a,252
s_add_u32 m0, s68, 23808
MG v,172,a,32,v,224
buffer_load_dwordx4 v253, s[48:51], s67 offen offset:768 lds
s_add_u32 s67, s67, 28672
MG v,176,a,8,a,252
ds_write_b128 v242, a[64:67] offset:24576
MG v,176,a,36,v,224
MG v,180,a,12,a,252
MG v,180,a,40,v,224
MG v,184,a,16,a,252
ds_write_b128 v242, a[68:71] offset:28672
MG v,184,a,44,v,224
MG v,188,a,20,a,252
MG v,188,a,48,v,224
s_waitcnt vmcnt(7) lgkmcnt(0)
MG v,192,a,24,a,252
s_barrier
MG v,192,a,52,v,224
ds_read_b128 a[252:255], v249 offset:17408
MG v,196,a,0,v,228
ds_read_b128 v[224:227], v249 offset:18432
MG v,196,a,28,v,232
ds_read_b128 a[84:87], v249 offset:27648
MG v,200,a,4,v,228
ds_read_b128 a[88:91], v249 offset:36864
MG v,200,a,32,v,232
ds_read_b128 a[92:95], v249 offset:46080
MG v,204,a,8,v,228
ds_read_b128 a[96:99], v249 offset:55296
MG v,204,a,36,v,232
ds_read_b128 a[100:103], v249 offset:64512
MG v,208,a,12,v,228
ds_read_b128 a[104:107], v249 offset:19456
MG v,208,a,40,v,232
ds_read_b128 a[108:111], v249 offset:28672
MG v,212,a,16,v,228
RU 56,245,0
MG v,212,a,44,v,232
RU 58,245,2048
MG v,216,a,20,v,228
RU 60,245,8
MG v,216,a,48,v,232
RU 62,245,2056
MG v,220,a,24,v,228
RU 64,245,8192
MG v,220,a,52,v,232
ds_read_b128 v[228:231], v249 offset:37888
MG a,224,a,0,v,236
ds_read_b128 v[232:235], v249 offset:47104
s_waitcnt lgkmcnt(15)
MG a,224,a,28,a,252
RU 66,245,10240
MG a,228,a,4,v,236
RU 68,245,8200
MG a,228,a,32,a,252
RU 70,245,10248
MG a,232,a,8,v,236
RU 72,245,16384
MG a,232,a,36,a,252
RU 74,245,18432
MG a,236,a,12,v,236
RU 76,245,16392
MG a,236,a,40,a,252
RU 78,245,18440
MG a,240,a,16,v,236
RU 80,247,0
MG a,240,a,44,a,252
RU 82,247,2048
MG a,244,a,20,v,236
MG a,244,a,48,a,252
MG a,248,a,24,v,236
MG a,248,a,52,a,252
s_waitcnt vmcnt(0) lgkmcnt(0)
MG v,0,a,112,v,224
s_barrier
MG v,0,a,140,a,84
RU 0,244,0
MG v,4,a,116,v,224
RU 2,244,2048
MG v,4,a,144,a,84
RU 4,244,8
MG v,8,a,120,v,224
RU 6,244,2056
MG v,8,a,148,a,84
RU 8,244,8192
MG v,12,a,124,v,224
RU 10,244,10240
MG v,12,a,152,a,84
RU 12,244,8200
MG v,16,a,128,v,224
RU 14,244,10248
MG v,16,a,156,a,84
RU 16,244,16384
MG v,20,a,132,v,224
RU 18,244,18432
MG v,20,a,160,a,84
RU 20,244,16392
MG v,24,a,136,v,224
RU 22,244,18440
MG v,24,a,164,a,84
RU 24,246,0
MG v,0,a,168,a,88
RU 26,246,2048
MG v,0,a,196,a,92
ds_read_b128 v[236:239], v249 offset:56320
MG v,4,a,172,a,88
ds_read_b128 a[252:255], v243 offset:0
MG v,4,a,200,a,92
ds_read_b128 a[28:31], v249 offset:20480
MG v,8,a,176,a,88
ds_read_b128 a[32:35], v249 offset:29696
MG v,8,a,204,a,92
ds_read_b128 a[36:39], v249 offset:38912
MG v,12,a,180,a,88
ds_read_b128 a[40:43], v249 offset:48128
MG v,12,a,208,a,92
ds_read_b128 a[44:47], v249 offset:57344
MG v,16,a,184,a,88
ds_read_b128 a[48:51], v243 offset:1024
MG v,16,a,212,a,92
ds_read_b128 a[52:55], v249 offset:21504
MG v,20,a,188,a,88
ds_read_b128 v[224:227], v249 offset:30720
MG v,20,a,216,a,92
ds_read_b128 a[84:87], v249 offset:39936
MG v,24,a,192,a,88
s_waitcnt lgkmcnt(15)
MG v,24,a,220,a,92
ds_read_b128 a[88:91], v249 offset:49152
MG v,0,a,56,a,96
ds_read_b128 a[92:95], v249 offset:58368
s_and_b32 s69, s26, 1
s_lshl_b32 s69, s69, 3
s_bfe_u32 s77, s26, 0x30001
s_add_u32 s77, s77, s69
s_and_b32 s69, s26, 0xf0
s_add_u32 s77, s77, s69
s_mul_i32 s77, s77, 129024
s_add_u32 s77, s77, 33554432
s_add_u32 s52, s28, s77
s_addc_u32 s53, s29, 0
s_and_b32 s53, s53, 0xffff
s_mov_b32 s54, 129024
s_mov_b32 s55, 0x20000
s_mov_b32 s67, 0
MG v,0,a,0,a,100
MG v,4,a,60,a,96
s_waitcnt lgkmcnt(15)
v_lshrrev_b32 v245, 8, v248
MG v,4,a,4,a,100
v_and_b32 v247, 0xf0, v248
MG v,8,a,64,a,96
s_waitcnt lgkmcnt(15)
v_lshlrev_b32 v247, 1, v247
MG v,8,a,8,a,100
v_lshl_add_u32 v247, v245, 3, v247
MG v,12,a,68,a,96
s_waitcnt lgkmcnt(15)
v_lshl_add_u32 v247, %[wid], 9, v247
MG v,12,a,12,a,100
MG v,16,a,72,a,96
s_waitcnt lgkmcnt(15)
MG v,16,a,16,a,100
MG v,20,a,76,a,96
s_waitcnt lgkmcnt(15)
MG v,20,a,20,a,100
MG v,24,a,80,a,96
s_waitcnt lgkmcnt(13)
MG v,24,a,24,a,100
ds_read_b128 a[96:99], v243 offset:2048
MG v,28,a,112,a,104
ds_read_b128 a[100:103], v249 offset:22528
MG v,28,a,140,a,108
MG v,32,a,116,a,104
MG v,32,a,144,a,108
CV 0,0,1
MG v,36,a,120,a,104
CV 1,2,3
MG v,36,a,148,a,108
CV 2,4,5
MG v,40,a,124,a,104
CV 3,6,7
MG v,40,a,152,a,108
buffer_store_dwordx4 v[0:3], v240, s[52:55], s67 offen offset:0 sc1
s_add_u32 s67, s67, 4096
CV 8,8,9
MG v,44,a,128,a,104
CV 9,10,11
MG v,44,a,156,a,108
CV 10,12,13
MG v,48,a,132,a,104
CV 11,14,15
MG v,48,a,160,a,108
buffer_store_dwordx4 v[8:11], v240, s[52:55], s67 offen offset:0 sc1
s_add_u32 s67, s67, 4096
CV 16,16,17
MG v,52,a,136,a,104
CV 17,18,19
MG v,52,a,164,a,108
ds_read_b128 a[104:107], v249 offset:31744
CV 18,20,21
MG v,28,a,168,v,228
ds_read_b128 a[108:111], v249 offset:40960
CV 19,22,23
MG v,28,a,196,v,232
buffer_store_dwordx4 v[16:19], v240, s[52:55], s67 offen offset:0 sc1
s_add_u32 s67, s67, 4096
CV 24,24,25
MG v,32,a,172,v,228
CV 25,26,27
MG v,32,a,200,v,232
MG v,36,a,176,v,228
MG v,36,a,204,v,232
buffer_store_dwordx2 v[24:25], v247, s[52:55], s67 offen offset:0 sc1
s_add_u32 s67, s67, 2048
MG v,40,a,180,v,228
MG v,40,a,208,v,232
MG v,44,a,184,v,228
MG v,44,a,212,v,232
MG v,48,a,188,v,228
MG v,48,a,216,v,232
MG v,52,a,192,v,228
s_waitcnt lgkmcnt(15)
MG v,52,a,220,v,232
ds_read_b128 v[228:231], v249 offset:50176
MG v,28,a,56,v,236
ds_read_b128 v[232:235], v249 offset:59392
MG v,28,a,0,a,252
MG v,32,a,60,v,236
MG v,32,a,4,a,252
MG v,36,a,64,v,236
MG v,36,a,8,a,252
MG v,40,a,68,v,236
MG v,40,a,12,a,252
MG v,44,a,72,v,236
MG v,44,a,16,a,252
MG v,48,a,76,v,236
MG v,48,a,20,a,252
MG v,52,a,80,v,236
s_waitcnt lgkmcnt(15)
MG v,52,a,24,a,252
ds_read_b128 v[236:239], v243 offset:3072
MG v,56,a,112,a,28
ds_read_b128 a[252:255], v249 offset:23552
MG v,56,a,140,a,32
s_waitcnt lgkmcnt(15)
MG v,56,a,168,a,36
s_waitcnt lgkmcnt(15)
MG v,56,a,196,a,40
s_waitcnt lgkmcnt(14)
CV 28,28,29
MG v,56,a,56,a,44
s_waitcnt lgkmcnt(13)
CV 29,30,31
MG v,56,a,0,a,48
CV 30,32,33
MG v,60,a,116,a,28
CV 31,34,35
MG v,60,a,144,a,32
buffer_store_dwordx4 v[28:31], v240, s[52:55], s67 offen offset:0 sc1
s_add_u32 s67, s67, 4096
CV 36,36,37
MG v,60,a,172,a,36
CV 37,38,39
MG v,60,a,200,a,40
CV 38,40,41
MG v,60,a,60,a,44
CV 39,42,43
MG v,60,a,4,a,48
buffer_store_dwordx4 v[36:39], v240, s[52:55], s67 offen offset:0 sc1
s_add_u32 s67, s67, 4096
CV 44,44,45
MG v,64,a,120,a,28
CV 45,46,47
MG v,64,a,148,a,32
CV 46,48,49
MG v,64,a,176,a,36
CV 47,50,51
MG v,64,a,204,a,40
buffer_store_dwordx4 v[44:47], v240, s[52:55], s67 offen offset:0 sc1
s_add_u32 s67, s67, 4096
CV 52,52,53
MG v,64,a,64,a,44
CV 53,54,55
MG v,64,a,8,a,48
MG v,68,a,124,a,28
MG v,68,a,152,a,32
buffer_store_dwordx2 v[52:53], v247, s[52:55], s67 offen offset:0 sc1
s_add_u32 s67, s67, 2048
MG v,68,a,180,a,36
MG v,68,a,208,a,40
MG v,68,a,68,a,44
MG v,68,a,12,a,48
MG v,72,a,128,a,28
MG v,72,a,156,a,32
MG v,72,a,184,a,36
MG v,72,a,212,a,40
MG v,72,a,72,a,44
MG v,72,a,16,a,48
MG v,76,a,132,a,28
MG v,76,a,160,a,32
MG v,76,a,188,a,36
MG v,76,a,216,a,40
MG v,76,a,76,a,44
MG v,76,a,20,a,48
MG v,80,a,136,a,28
MG v,80,a,164,a,32
ds_read_b128 a[28:31], v249 offset:32768
MG v,80,a,192,a,36
ds_read_b128 a[32:35], v249 offset:41984
MG v,80,a,220,a,40
ds_read_b128 a[36:39], v249 offset:51200
MG v,80,a,80,a,44
ds_read_b128 a[40:43], v249 offset:60416
MG v,80,a,24,a,48
ds_read_b128 a[44:47], v243 offset:4096
s_waitcnt lgkmcnt(15)
MG v,84,a,112,a,52
ds_read_b128 a[48:51], v249 offset:24576
MG v,84,a,140,v,224
MG v,84,a,168,a,84
s_waitcnt lgkmcnt(15)
MG v,84,a,196,a,88
s_waitcnt lgkmcnt(14)
CV 56,56,57
MG v,84,a,56,a,92
s_waitcnt lgkmcnt(13)
CV 57,58,59
MG v,84,a,0,a,96
CV 58,60,61
MG v,88,a,116,a,52
CV 59,62,63
MG v,88,a,144,v,224
buffer_store_dwordx4 v[56:59], v240, s[52:55], s67 offen offset:0 sc1
s_add_u32 s67, s67, 4096
CV 64,64,65
MG v,88,a,172,a,84
CV 65,66,67
MG v,88,a,200,a,88
CV 66,68,69
MG v,88,a,60,a,92
CV 67,70,71
MG v,88,a,4,a,96
buffer_store_dwordx4 v[64:67], v240, s[52:55], s67 offen offset:0 sc1
s_add_u32 s67, s67, 4096
CV 72,72,73
MG v,92,a,120,a,52
CV 73,74,75
MG v,92,a,148,v,224
CV 74,76,77
MG v,92,a,176,a,84
CV 75,78,79
MG v,92,a,204,a,88
buffer_store_dwordx4 v[72:75], v240, s[52:55], s67 offen offset:0 sc1
s_add_u32 s67, s67, 4096
CV 80,80,81
MG v,92,a,64,a,92
CV 81,82,83
MG v,92,a,8,a,96
MG v,96,a,124,a,52
MG v,96,a,152,v,224
buffer_store_dwordx2 v[80:81], v247, s[52:55], s67 offen offset:0 sc1
s_add_u32 s67, s67, 2048
MG v,96,a,180,a,84
MG v,96,a,208,a,88
MG v,96,a,68,a,92
MG v,96,a,12,a,96
MG v,100,a,128,a,52
MG v,100,a,156,v,224
MG v,100,a,184,a,84
MG v,100,a,212,a,88
MG v,100,a,72,a,92
MG v,100,a,16,a,96
MG v,104,a,132,a,52
MG v,104,a,160,v,224
MG v,104,a,188,a,84
MG v,104,a,216,a,88
MG v,104,a,76,a,92
MG v,104,a,20,a,96
MG v,108,a,136,a,52
MG v,108,a,164,v,224
ds_read_b128 a[52:55], v249 offset:33792
MG v,108,a,192,a,84
ds_read_b128 v[224:227], v249 offset:43008
MG v,108,a,220,a,88
ds_read_b128 a[84:87], v249 offset:52224
MG v,108,a,80,a,92
ds_read_b128 a[88:91], v249 offset:61440
MG v,108,a,24,a,96
ds_read_b128 a[92:95], v243 offset:5120
s_waitcnt lgkmcnt(15)
MG v,112,a,112,a,100
ds_read_b128 a[96:99], v249 offset:25600
MG v,112,a,140,a,104
MG v,112,a,168,a,108
s_waitcnt lgkmcnt(15)
MG v,112,a,196,v,228
s_waitcnt lgkmcnt(14)
CV 84,84,85
MG v,112,a,56,v,232
s_waitcnt lgkmcnt(13)
CV 85,86,87
MG v,112,a,0,v,236
CV 86,88,89
MG v,116,a,116,a,100
CV 87,90,91
MG v,116,a,144,a,104
buffer_store_dwordx4 v[84:87], v240, s[52:55], s67 offen offset:0 sc1
s_add_u32 s67, s67, 4096
CV 92,92,93
MG v,116,a,172,a,108
CV 93,94,95
MG v,116,a,200,v,228
CV 94,96,97
MG v,116,a,60,v,232
CV 95,98,99
MG v,116,a,4,v,236
buffer_store_dwordx4 v[92:95], v240, s[52:55], s67 offen offset:0 sc1
s_add_u32 s67, s67, 4096
CV 100,100,101
MG v,120,a,120,a,100
CV 101,102,103
MG v,120,a,148,a,104
CV 102,104,105
MG v,120,a,176,a,108
CV 103,106,107
MG v,120,a,204,v,228
buffer_store_dwordx4 v[100:103], v240, s[52:55], s67 offen offset:0 sc1
s_add_u32 s67, s67, 4096
CV 108,108,109
MG v,120,a,64,v,232
CV 109,110,111
MG v,120,a,8,v,236
MG v,124,a,124,a,100
MG v,124,a,152,a,104
buffer_store_dwordx2 v[108:109], v247, s[52:55], s67 offen offset:0 sc1
s_add_u32 s67, s67, 2048
MG v,124,a,180,a,108
MG v,124,a,208,v,228
MG v,124,a,68,v,232
MG v,124,a,12,v,236
MG v,128,a,128,a,100
MG v,128,a,156,a,104
MG v,128,a,184,a,108
MG v,128,a,212,v,228
MG v,128,a,72,v,232
MG v,128,a,16,v,236
MG v,132,a,132,a,100
MG v,132,a,160,a,104
MG v,132,a,188,a,108
MG v,132,a,216,v,228
MG v,132,a,76,v,232
MG v,132,a,20,v,236
MG v,136,a,136,a,100
MG v,136,a,164,a,104
ds_read_b128 a[100:103], v249 offset:34816
MG v,136,a,192,a,108
ds_read_b128 a[104:107], v249 offset:44032
MG v,136,a,220,v,228
ds_read_b128 a[108:111], v249 offset:53248
MG v,136,a,80,v,232
ds_read_b128 v[228:231], v249 offset:62464
MG v,136,a,24,v,236
ds_read_b128 v[232:235], v243 offset:6144
s_waitcnt lgkmcnt(15)
MG v,140,a,112,a,252
ds_read_b128 v[236:239], v249 offset:26624
MG v,140,a,140,a,28
MG v,140,a,168,a,32
s_waitcnt lgkmcnt(15)
MG v,140,a,196,a,36
s_waitcnt lgkmcnt(14)
CV 112,112,113
MG v,140,a,56,a,40
s_waitcnt lgkmcnt(13)
CV 113,114,115
MG v,140,a,0,a,44
CV 114,116,117
MG v,144,a,116,a,252
CV 115,118,119
MG v,144,a,144,a,28
buffer_store_dwordx4 v[112:115], v240, s[52:55], s67 offen offset:0 sc1
s_add_u32 s67, s67, 4096
CV 120,120,121
MG v,144,a,172,a,32
CV 121,122,123
MG v,144,a,200,a,36
CV 122,124,125
MG v,144,a,60,a,40
CV 123,126,127
MG v,144,a,4,a,44
buffer_store_dwordx4 v[120:123], v240, s[52:55], s67 offen offset:0 sc1
s_add_u32 s67, s67, 4096
CV 128,128,129
MG v,148,a,120,a,252
CV 129,130,131
MG v,148,a,148,a,28
CV 130,132,133
MG v,148,a,176,a,32
CV 131,134,135
MG v,148,a,204,a,36
buffer_store_dwordx4 v[128:131], v240, s[52:55], s67 offen offset:0 sc1
s_add_u32 s67, s67, 4096
CV 136,136,137
MG v,148,a,64,a,40
CV 137,138,139
MG v,148,a,8,a,44
MG v,152,a,124,a,252
MG v,152,a,152,a,28
buffer_store_dwordx2 v[136:137], v247, s[52:55], s67 offen offset:0 sc1
s_add_u32 s67, s67, 2048
MG v,152,a,180,a,32
MG v,152,a,208,a,36
MG v,152,a,68,a,40
MG v,152,a,12,a,44
MG v,156,a,128,a,252
MG v,156,a,156,a,28
MG v,156,a,184,a,32
MG v,156,a,212,a,36
MG v,156,a,72,a,40
MG v,156,a,16,a,44
MG v,160,a,132,a,252
MG v,160,a,160,a,28
MG v,160,a,188,a,32
MG v,160,a,216,a,36
MG v,160,a,76,a,40
MG v,160,a,20,a,44
MG v,164,a,136,a,252
MG v,164,a,164,a,28
ds_read_b128 a[252:255], v249 offset:35840
MG v,164,a,192,a,32
ds_read_b128 a[28:31], v249 offset:45056
MG v,164,a,220,a,36
ds_read_b128 a[32:35], v249 offset:54272
MG v,164,a,80,a,40
ds_read_b128 a[36:39], v249 offset:63488
MG v,164,a,24,a,44
ds_read_b128 a[40:43], v243 offset:7168
s_waitcnt lgkmcnt(15)
MG v,168,a,112,a,48
MG v,168,a,140,a,52
MG v,168,a,168,v,224
s_waitcnt lgkmcnt(14)
MG v,168,a,196,a,84
s_waitcnt lgkmcnt(13)
CV 140,140,141
MG v,168,a,56,a,88
s_waitcnt lgkmcnt(12)
CV 141,142,143
MG v,168,a,0,a,92
CV 142,144,145
MG v,172,a,116,a,48
CV 143,146,147
MG v,172,a,144,a,52
buffer_store_dwordx4 v[140:143], v240, s[52:55], s67 offen offset:0 sc1
s_add_u32 s67, s67, 4096
CV 148,148,149
MG v,172,a,172,v,224
CV 149,150,151
MG v,172,a,200,a,84
CV 150,152,153
MG v,172,a,60,a,88
CV 151,154,155
MG v,172,a,4,a,92
buffer_store_dwordx4 v[148:151], v240, s[52:55], s67 offen offset:0 sc1
s_add_u32 s67, s67, 4096
CV 156,156,157
MG v,176,a,120,a,48
CV 157,158,159
MG v,176,a,148,a,52
CV 158,160,161
MG v,176,a,176,v,224
CV 159,162,163
MG v,176,a,204,a,84
buffer_store_dwordx4 v[156:159], v240, s[52:55], s67 offen offset:0 sc1
s_add_u32 s67, s67, 4096
CV 164,164,165
MG v,176,a,64,a,88
CV 165,166,167
MG v,176,a,8,a,92
MG v,180,a,124,a,48
MG v,180,a,152,a,52
buffer_store_dwordx2 v[164:165], v247, s[52:55], s67 offen offset:0 sc1
s_add_u32 s67, s67, 2048
MG v,180,a,180,v,224
MG v,180,a,208,a,84
MG v,180,a,68,a,88
MG v,180,a,12,a,92
MG v,184,a,128,a,48
MG v,184,a,156,a,52
MG v,184,a,184,v,224
MG v,184,a,212,a,84
s_waitcnt vmcnt(4)
MG v,184,a,72,a,88
s_barrier
s_cmp_lg_u32 %[wid], 0
s_cbranch_scc1 LTp3
global_store_dword v251, v252, s[38:39] offset:64 sc1
LTp3:
MG v,184,a,16,a,92
MG v,188,a,132,a,48
MG v,188,a,160,a,52
MG v,188,a,188,v,224
MG v,188,a,216,a,84
MG v,188,a,76,a,88
MG v,188,a,20,a,92
MG v,192,a,136,a,48
MG v,192,a,164,a,52
MG v,192,a,192,v,224
MG v,192,a,220,a,84
MG v,192,a,80,a,88
MG v,192,a,24,a,92
s_waitcnt lgkmcnt(11)
MG v,196,a,112,a,96
s_waitcnt lgkmcnt(10)
MG v,196,a,140,a,100
s_waitcnt lgkmcnt(9)
MG v,196,a,168,a,104
s_waitcnt lgkmcnt(8)
MG v,196,a,196,a,108
s_waitcnt lgkmcnt(7)
CV 168,168,169
MG v,196,a,56,v,228
s_waitcnt lgkmcnt(6)
CV 169,170,171
MG v,196,a,0,v,232
CV 170,172,173
MG v,200,a,116,a,96
CV 171,174,175
MG v,200,a,144,a,100
buffer_store_dwordx4 v[168:171], v240, s[52:55], s67 offen offset:0 sc1
s_add_u32 s67, s67, 4096
CV 176,176,177
MG v,200,a,172,a,104
CV 177,178,179
MG v,200,a,200,a,108
CV 178,180,181
MG v,200,a,60,v,228
CV 179,182,183
MG v,200,a,4,v,232
buffer_store_dwordx4 v[176:179], v240, s[52:55], s67 offen offset:0 sc1
s_add_u32 s67, s67, 4096
CV 184,184,185
MG v,204,a,120,a,96
CV 185,186,187
MG v,204,a,148,a,100
CV 186,188,189
MG v,204,a,176,a,104
CV 187,190,191
MG v,204,a,204,a,108
buffer_store_dwordx4 v[184:187], v240, s[52:55], s67 offen offset:0 sc1
s_add_u32 s67, s67, 4096
CV 192,192,193
MG v,204,a,64,v,228
CV 193,194,195
MG v,204,a,8,v,232
MG v,208,a,124,a,96
MG v,208,a,152,a,100
buffer_store_dwordx2 v[192:193], v247, s[52:55], s67 offen offset:0 sc1
s_add_u32 s67, s67, 2048
MG v,208,a,180,a,104
MG v,208,a,208,a,108
MG v,208,a,68,v,228
MG v,208,a,12,v,232
MG v,212,a,128,a,96
MG v,212,a,156,a,100
MG v,212,a,184,a,104
MG v,212,a,212,a,108
MG v,212,a,72,v,228
MG v,212,a,16,v,232
MG v,216,a,132,a,96
MG v,216,a,160,a,100
MG v,216,a,188,a,104
MG v,216,a,216,a,108
MG v,216,a,76,v,228
MG v,216,a,20,v,232
MG v,220,a,136,a,96
MG v,220,a,164,a,100
MG v,220,a,192,a,104
MG v,220,a,220,a,108
MG v,220,a,80,v,228
MG v,220,a,24,v,232
s_waitcnt lgkmcnt(5)
MG a,224,a,112,v,236
s_waitcnt lgkmcnt(4)
MG a,224,a,140,a,252
s_waitcnt lgkmcnt(3)
MG a,224,a,168,a,28
s_waitcnt lgkmcnt(2)
MG a,224,a,196,a,32
s_waitcnt lgkmcnt(1)
CV 196,196,197
MG a,224,a,56,a,36
s_waitcnt lgkmcnt(0)
CV 197,198,199
MG a,224,a,0,a,40
CV 198,200,201
MG a,228,a,116,v,236
CV 199,202,203
MG a,228,a,144,a,252
buffer_store_dwordx4 v[196:199], v240, s[52:55], s67 offen offset:0 sc1
s_add_u32 s67, s67, 4096
CV 204,204,205
MG a,228,a,172,a,28
CV 205,206,207
MG a,228,a,200,a,32
CV 206,208,209
MG a,228,a,60,a,36
CV 207,210,211
MG a,228,a,4,a,40
buffer_store_dwordx4 v[204:207], v240, s[52:55], s67 offen offset:0 sc1
s_add_u32 s67, s67, 4096
CV 212,212,213
MG a,232,a,120,v,236
CV 213,214,215
MG a,232,a,148,a,252
CV 214,216,217
MG a,232,a,176,a,28
CV 215,218,219
MG a,232,a,204,a,32
buffer_store_dwordx4 v[212:215], v240, s[52:55], s67 offen offset:0 sc1
s_add_u32 s67, s67, 4096
CV 220,220,221
MG a,232,a,64,a,36
CV 221,222,223
MG a,232,a,8,a,40
MG a,236,a,124,v,236
MG a,236,a,152,a,252
buffer_store_dwordx2 v[220:221], v247, s[52:55], s67 offen offset:0 sc1
s_add_u32 s67, s67, 2048
MG a,236,a,180,a,28
MG a,236,a,208,a,32
MG a,236,a,68,a,36
MG a,236,a,12,a,40
MG a,240,a,128,v,236
MG a,240,a,156,a,252
MG a,240,a,184,a,28
MG a,240,a,212,a,32
MG a,240,a,72,a,36
MG a,240,a,16,a,40
MG a,244,a,132,v,236
MG a,244,a,160,a,252
MG a,244,a,188,a,28
MG a,244,a,216,a,32
MG a,244,a,76,a,36
MG a,244,a,20,a,40
MG a,248,a,136,v,236
MG a,248,a,164,a,252
MG a,248,a,192,a,28
MG a,248,a,220,a,32
MG a,248,a,80,a,36
MG a,248,a,24,a,40
AR 0,224
AR 1,225
AR 2,226
AR 3,227
AR 4,228
AR 5,229
AR 6,230
AR 7,231
AR 8,232
AR 9,233
AR 10,234
AR 11,235
AR 12,236
AR 13,237
AR 14,238
AR 15,239
AR 16,240
AR 17,241
AR 18,242
AR 19,243
AR 20,244
AR 21,245
AR 22,246
AR 23,247
AR 24,248
AR 25,249
AR 26,250
AR 27,251
CV 0,0,1
CV 1,2,3
CV 2,4,5
CV 3,6,7
buffer_store_dwordx4 v[0:3], v240, s[52:55], s67 offen offset:0 sc1
s_add_u32 s67, s67, 4096
CV 8,8,9
CV 9,10,11
CV 10,12,13
CV 11,14,15
buffer_store_dwordx4 v[8:11], v240, s[52:55], s67 offen offset:0 sc1
s_add_u32 s67, s67, 4096
CV 16,16,17
CV 17,18,19
CV 18,20,21
CV 19,22,23
buffer_store_dwordx4 v[16:19], v240, s[52:55], s67 offen offset:0 sc1
s_add_u32 s67, s67, 4096
CV 24,24,25
CV 25,26,27
buffer_store_dwordx2 v[24:25], v247, s[52:55], s67 offen offset:0 sc1
s_add_u32 s67, s67, 2048
s_and_b32 s69, s26, 1
s_lshl_b32 s69, s69, 3
s_and_b32 s77, s26, 0xf0
s_add_u32 s77, s77, s69
s_mul_i32 s77, s77, 129024
s_add_u32 s77, s77, 33554432
s_add_u32 s44, s28, s77
s_addc_u32 s45, s29, 0
s_and_b32 s45, s45, 0xffff
s_mov_b32 s46, 1032192
s_mov_b32 s47, 0x20000
s_mov_b32 s77, s27
s_add_u32 s77, s77, 0
s_add_u32 s56, s30, s77
s_addc_u32 s57, s31, 0
s_and_b32 s57, s57, 0xffff
s_mov_b32 s58, 257152
s_mov_b32 s59, 0x20000
v_bfe_u32 v234, v248, 4, 4
v_mul_u32_u24 v232, 0x700, v234
v_lshrrev_b32 v234, 8, v248
v_lshl_add_u32 v232, v234, 4, v232
v_bfe_u32 v234, v248, 5, 4
v_mul_u32_u24 v233, 0x700, v234
v_and_b32 v234, 16, v248
v_add_u32 v233, v233, v234
v_lshrrev_b32 v234, 9, v248
v_lshl_add_u32 v233, v234, 5, v233
s_bfe_u32 s69, s26, 0x30001
s_lshl_b32 s69, s69, 2
s_add_u32 s69, s69, %[wid]
s_lshl_b32 s70, s69, 10
s_cmp_lt_u32 s69, 126
s_cselect_b32 s70, s70, 0x7fff0000
v_add_u32 v224, s70, v248
s_mul_i32 s70, s69, 147
s_lshr_b32 s70, s70, 11
s_mul_i32 s71, s70, 14
s_sub_u32 s71, s69, s71
s_mul_i32 s70, s70, 28672
s_lshl_b32 s77, s71, 6
s_add_u32 s70, s70, s77
s_cmp_lt_u32 s69, 126
s_cselect_b32 s70, s70, 0x7fff0000
s_cmp_lt_u32 s71, 12
s_cselect_b64 vcc, -1, 0
v_cndmask_b32 v228, v233, v232, vcc
v_add_u32 v228, s70, v228
s_add_u32 s69, s69, 32
s_lshl_b32 s70, s69, 10
s_cmp_lt_u32 s69, 126
s_cselect_b32 s70, s70, 0x7fff0000
v_add_u32 v225, s70, v248
s_mul_i32 s70, s69, 147
s_lshr_b32 s70, s70, 11
s_mul_i32 s71, s70, 14
s_sub_u32 s71, s69, s71
s_mul_i32 s70, s70, 28672
s_lshl_b32 s77, s71, 6
s_add_u32 s70, s70, s77
s_cmp_lt_u32 s69, 126
s_cselect_b32 s70, s70, 0x7fff0000
s_cmp_lt_u32 s71, 12
s_cselect_b64 vcc, -1, 0
v_cndmask_b32 v229, v233, v232, vcc
v_add_u32 v229, s70, v229
s_add_u32 s69, s69, 32
s_lshl_b32 s70, s69, 10
s_cmp_lt_u32 s69, 126
s_cselect_b32 s70, s70, 0x7fff0000
v_add_u32 v226, s70, v248
s_mul_i32 s70, s69, 147
s_lshr_b32 s70, s70, 11
s_mul_i32 s71, s70, 14
s_sub_u32 s71, s69, s71
s_mul_i32 s70, s70, 28672
s_lshl_b32 s77, s71, 6
s_add_u32 s70, s70, s77
s_cmp_lt_u32 s69, 126
s_cselect_b32 s70, s70, 0x7fff0000
s_cmp_lt_u32 s71, 12
s_cselect_b64 vcc, -1, 0
v_cndmask_b32 v230, v233, v232, vcc
v_add_u32 v230, s70, v230
s_add_u32 s69, s69, 32
s_lshl_b32 s70, s69, 10
s_cmp_lt_u32 s69, 126
s_cselect_b32 s70, s70, 0x7fff0000
v_add_u32 v227, s70, v248
s_mul_i32 s70, s69, 147
s_lshr_b32 s70, s70, 11
s_mul_i32 s71, s70, 14
s_sub_u32 s71, s69, s71
s_mul_i32 s70, s70, 28672
s_lshl_b32 s77, s71, 6
s_add_u32 s70, s70, s77
s_cmp_lt_u32 s69, 126
s_cselect_b32 s70, s70, 0x7fff0000
s_cmp_lt_u32 s71, 12
s_cselect_b64 vcc, -1, 0
v_cndmask_b32 v231, v233, v232, vcc
v_add_u32 v231, s70, v231
s_add_u32 s69, s69, 32
s_mov_b32 s40, 0
s_mov_b32 s41, 129024
s_mov_b32 s42, 258048
s_mov_b32 s43, 387072
s_mov_b32 s48, 516096
s_mov_b32 s49, 645120
s_mov_b32 s50, 774144
s_mov_b32 s51, 903168
v_bfe_u32 v234, v248, 2, 7
global_load_dword v254, v234, s[36:37] offset:64 sc1
s_and_b32 s77, s26, 1
s_lshl_b32 s77, 0x5555, s77
s_and_b32 s69, s26, 15
s_bitset0_b32 s77, s69
s_waitcnt vmcnt(0)
v_cmp_ne_u32 vcc, s79, v254
s_barrier
s_cmp_lg_u32 %[wid], 0
s_cbranch_scc1 LTp2
global_store_dword v251, v252, s[38:39] offset:128 sc1
LTp2:
s_mov_b32 s70, 0xffff0000
s_lshr_b32 s69, vcc_lo, 16
s_and_b32 s69, s69, s77
s_cbranch_scc0 LT7
s_and_b32 vcc_lo, vcc_lo, s77
s_cbranch_scc1 LT4s
LT4e:
buffer_load_dwordx4 v[32:35], v224, s[44:47], s40 offen offset:0 sc1
buffer_load_dwordx4 v[36:39], v224, s[44:47], s41 offen offset:0 sc1
buffer_load_dwordx4 v[40:43], v224, s[44:47], s42 offen offset:0 sc1
buffer_load_dwordx4 v[44:47], v224, s[44:47], s43 offen offset:0 sc1
buffer_load_dwordx4 v[48:51], v224, s[44:47], s48 offen offset:0 sc1
buffer_load_dwordx4 v[52:55], v224, s[44:47], s49 offen offset:0 sc1
buffer_load_dwordx4 v[56:59], v224, s[44:47], s50 offen offset:0 sc1
buffer_load_dwordx4 v[60:63], v224, s[44:47], s51 offen offset:0 sc1
buffer_load_dwordx4 v[64:67], v225, s[44:47], s40 offen offset:0 sc1
buffer_load_dwordx4 v[68:71], v225, s[44:47], s41 offen offset:0 sc1
buffer_load_dwordx4 v[72:75], v225, s[44:47], s42 offen offset:0 sc1
buffer_load_dwordx4 v[76:79], v225, s[44:47], s43 offen offset:0 sc1
buffer_load_dwordx4 v[80:83], v225, s[44:47], s48 offen offset:0 sc1
buffer_load_dwordx4 v[84:87], v225, s[44:47], s49 offen offset:0 sc1
buffer_load_dwordx4 v[88:91], v225, s[44:47], s50 offen offset:0 sc1
buffer_load_dwordx4 v[92:95], v225, s[44:47], s51 offen offset:0 sc1
s_waitcnt vmcnt(15)
v_lshlrev_b32 v160, 16, v32
v_and_b32 v161, s70, v32
v_lshlrev_b32 v162, 16, v33
v_and_b32 v163, s70, v33
v_lshlrev_b32 v164, 16, v34
v_and_b32 v165, s70, v34
v_lshlrev_b32 v166, 16, v35
v_and_b32 v167, s70, v35
s_waitcnt vmcnt(14)
v_lshlrev_b32 v192, 16, v36
v_and_b32 v193, s70, v36
v_lshlrev_b32 v194, 16, v37
v_and_b32 v195, s70, v37
v_lshlrev_b32 v196, 16, v38
v_and_b32 v197, s70, v38
v_lshlrev_b32 v198, 16, v39
v_and_b32 v199, s70, v39
v_pk_add_f32 v[160:161], v[160:161], v[192:193]
v_pk_add_f32 v[162:163], v[162:163], v[194:195]
v_pk_add_f32 v[164:165], v[164:165], v[196:197]
v_pk_add_f32 v[166:167], v[166:167], v[198:199]
s_waitcnt vmcnt(13)
v_lshlrev_b32 v192, 16, v40
v_and_b32 v193, s70, v40
v_lshlrev_b32 v194, 16, v41
v_and_b32 v195, s70, v41
v_lshlrev_b32 v196, 16, v42
v_and_b32 v197, s70, v42
v_lshlrev_b32 v198, 16, v43
v_and_b32 v199, s70, v43
v_pk_add_f32 v[160:161], v[160:161], v[192:193]
v_pk_add_f32 v[162:163], v[162:163], v[194:195]
v_pk_add_f32 v[164:165], v[164:165], v[196:197]
v_pk_add_f32 v[166:167], v[166:167], v[198:199]
s_waitcnt vmcnt(12)
v_lshlrev_b32 v192, 16, v44
v_and_b32 v193, s70, v44
v_lshlrev_b32 v194, 16, v45
v_and_b32 v195, s70, v45
v_lshlrev_b32 v196, 16, v46
v_and_b32 v197, s70, v46
v_lshlrev_b32 v198, 16, v47
v_and_b32 v199, s70, v47
v_pk_add_f32 v[160:161], v[160:161], v[192:193]
v_pk_add_f32 v[162:163], v[162:163], v[194:195]
v_pk_add_f32 v[164:165], v[164:165], v[196:197]
v_pk_add_f32 v[166:167], v[166:167], v[198:199]
s_waitcnt vmcnt(11)
v_lshlrev_b32 v192, 16, v48
v_and_b32 v193, s70, v48
v_lshlrev_b32 v194, 16, v49
v_and_b32 v195, s70, v49
v_lshlrev_b32 v196, 16, v50
v_and_b32 v197, s70, v50
v_lshlrev_b32 v198, 16, v51
v_and_b32 v199, s70, v51
v_pk_add_f32 v[160:161], v[160:161], v[192:193]
v_pk_add_f32 v[162:163], v[162:163], v[194:195]
v_pk_add_f32 v[164:165], v[164:165], v[196:197]
v_pk_add_f32 v[166:167], v[166:167], v[198:199]
s_waitcnt vmcnt(10)
v_lshlrev_b32 v192, 16, v52
v_and_b32 v193, s70, v52
v_lshlrev_b32 v194, 16, v53
v_and_b32 v195, s70, v53
v_lshlrev_b32 v196, 16, v54
v_and_b32 v197, s70, v54
v_lshlrev_b32 v198, 16, v55
v_and_b32 v199, s70, v55
v_pk_add_f32 v[160:161], v[160:161], v[192:193]
v_pk_add_f32 v[162:163], v[162:163], v[194:195]
v_pk_add_f32 v[164:165], v[164:165], v[196:197]
v_pk_add_f32 v[166:167], v[166:167], v[198:199]
s_waitcnt vmcnt(9)
v_lshlrev_b32 v192, 16, v56
v_and_b32 v193, s70, v56
v_lshlrev_b32 v194, 16, v57
v_and_b32 v195, s70, v57
v_lshlrev_b32 v196, 16, v58
v_and_b32 v197, s70, v58
v_lshlrev_b32 v198, 16, v59
v_and_b32 v199, s70, v59
v_pk_add_f32 v[160:161], v[160:161], v[192:193]
v_pk_add_f32 v[162:163], v[162:163], v[194:195]
v_pk_add_f32 v[164:165], v[164:165], v[196:197]
v_pk_add_f32 v[166:167], v[166:167], v[198:199]
s_waitcnt vmcnt(8)
v_lshlrev_b32 v192, 16, v60
v_and_b32 v193, s70, v60
v_lshlrev_b32 v194, 16, v61
v_and_b32 v195, s70, v61
v_lshlrev_b32 v196, 16, v62
v_and_b32 v197, s70, v62
v_lshlrev_b32 v198, 16, v63
v_and_b32 v199, s70, v63
v_pk_add_f32 v[160:161], v[160:161], v[192:193]
v_pk_add_f32 v[162:163], v[162:163], v[194:195]
v_pk_add_f32 v[164:165], v[164:165], v[196:197]
v_pk_add_f32 v[166:167], v[166:167], v[198:199]
CV 160,160,161
CV 161,162,163
CV 162,164,165
CV 163,166,167
s_waitcnt vmcnt(7)
v_lshlrev_b32 v168, 16, v64
v_and_b32 v169, s70, v64
v_lshlrev_b32 v170, 16, v65
v_and_b32 v171, s70, v65
v_lshlrev_b32 v172, 16, v66
v_and_b32 v173, s70, v66
v_lshlrev_b32 v174, 16, v67
v_and_b32 v175, s70, v67
s_waitcnt vmcnt(6)
v_lshlrev_b32 v192, 16, v68
v_and_b32 v193, s70, v68
v_lshlrev_b32 v194, 16, v69
v_and_b32 v195, s70, v69
v_lshlrev_b32 v196, 16, v70
v_and_b32 v197, s70, v70
v_lshlrev_b32 v198, 16, v71
v_and_b32 v199, s70, v71
v_pk_add_f32 v[168:169], v[168:169], v[192:193]
v_pk_add_f32 v[170:171], v[170:171], v[194:195]
v_pk_add_f32 v[172:173], v[172:173], v[196:197]
v_pk_add_f32 v[174:175], v[174:175], v[198:199]
s_waitcnt vmcnt(5)
v_lshlrev_b32 v192, 16, v72
v_and_b32 v193, s70, v72
v_lshlrev_b32 v194, 16, v73
v_and_b32 v195, s70, v73
v_lshlrev_b32 v196, 16, v74
v_and_b32 v197, s70, v74
v_lshlrev_b32 v198, 16, v75
v_and_b32 v199, s70, v75
v_pk_add_f32 v[168:169], v[168:169], v[192:193]
v_pk_add_f32 v[170:171], v[170:171], v[194:195]
v_pk_add_f32 v[172:173], v[172:173], v[196:197]
v_pk_add_f32 v[174:175], v[174:175], v[198:199]
s_waitcnt vmcnt(4)
v_lshlrev_b32 v192, 16, v76
v_and_b32 v193, s70, v76
v_lshlrev_b32 v194, 16, v77
v_and_b32 v195, s70, v77
v_lshlrev_b32 v196, 16, v78
v_and_b32 v197, s70, v78
v_lshlrev_b32 v198, 16, v79
v_and_b32 v199, s70, v79
v_pk_add_f32 v[168:169], v[168:169], v[192:193]
v_pk_add_f32 v[170:171], v[170:171], v[194:195]
v_pk_add_f32 v[172:173], v[172:173], v[196:197]
v_pk_add_f32 v[174:175], v[174:175], v[198:199]
s_waitcnt vmcnt(3)
v_lshlrev_b32 v192, 16, v80
v_and_b32 v193, s70, v80
v_lshlrev_b32 v194, 16, v81
v_and_b32 v195, s70, v81
v_lshlrev_b32 v196, 16, v82
v_and_b32 v197, s70, v82
v_lshlrev_b32 v198, 16, v83
v_and_b32 v199, s70, v83
v_pk_add_f32 v[168:169], v[168:169], v[192:193]
v_pk_add_f32 v[170:171], v[170:171], v[194:195]
v_pk_add_f32 v[172:173], v[172:173], v[196:197]
v_pk_add_f32 v[174:175], v[174:175], v[198:199]
s_waitcnt vmcnt(2)
v_lshlrev_b32 v192, 16, v84
v_and_b32 v193, s70, v84
v_lshlrev_b32 v194, 16, v85
v_and_b32 v195, s70, v85
v_lshlrev_b32 v196, 16, v86
v_and_b32 v197, s70, v86
v_lshlrev_b32 v198, 16, v87
v_and_b32 v199, s70, v87
v_pk_add_f32 v[168:169], v[168:169], v[192:193]
v_pk_add_f32 v[170:171], v[170:171], v[194:195]
v_pk_add_f32 v[172:173], v[172:173], v[196:197]
v_pk_add_f32 v[174:175], v[174:175], v[198:199]
s_waitcnt vmcnt(1)
v_lshlrev_b32 v192, 16, v88
v_and_b32 v193, s70, v88
v_lshlrev_b32 v194, 16, v89
v_and_b32 v195, s70, v89
v_lshlrev_b32 v196, 16, v90
v_and_b32 v197, s70, v90
v_lshlrev_b32 v198, 16, v91
v_and_b32 v199, s70, v91
v_pk_add_f32 v[168:169], v[168:169], v[192:193]
v_pk_add_f32 v[170:171], v[170:171], v[194:195]
v_pk_add_f32 v[172:173], v[172:173], v[196:197]
v_pk_add_f32 v[174:175], v[174:175], v[198:199]
s_waitcnt vmcnt(0)
v_lshlrev_b32 v192, 16, v92
v_and_b32 v193, s70, v92
v_lshlrev_b32 v194, 16, v93
v_and_b32 v195, s70, v93
v_lshlrev_b32 v196, 16, v94
v_and_b32 v197, s70, v94
v_lshlrev_b32 v198, 16, v95
v_and_b32 v199, s70, v95
v_pk_add_f32 v[168:169], v[168:169], v[192:193]
v_pk_add_f32 v[170:171], v[170:171], v[194:195]
v_pk_add_f32 v[172:173], v[172:173], v[196:197]
v_pk_add_f32 v[174:175], v[174:175], v[198:199]
CV 168,168,169
CV 169,170,171
CV 170,172,173
CV 171,174,175
s_mov_b32 s71, 30000
LT5:
global_load_dword v254, v250, s[36:37] offset:128 sc1
global_load_dword v255, v250, s[80:81] sc1
s_waitcnt vmcnt(0)
s_cmp_lg_u32 %[wid], 0
s_cbranch_scc1 LT5a
v_cmp_ne_u32 vcc, v255, v252
s_cbranch_vccz LT5a
v_mov_b32 v252, v255
global_store_dword v251, v252, s[38:39] sc1
global_store_dword v251, v252, s[38:39] offset:64 sc1
global_store_dword v251, v252, s[38:39] offset:128 sc1
LT5a:
v_cmp_ne_u32 vcc, s79, v254
s_nop 3
s_and_b32 vcc_lo, vcc_lo, s77
s_cbranch_scc0 LT5e
s_sleep 1
s_sub_u32 s71, s71, 1
s_cbranch_scc0 LT5
LT5e:
s_waitcnt vmcnt(0) lgkmcnt(0)
buffer_load_dwordx4 v[96:99], v226, s[44:47], s40 offen offset:0 sc1
buffer_load_dwordx4 v[100:103], v226, s[44:47], s41 offen offset:0 sc1
buffer_load_dwordx4 v[104:107], v226, s[44:47], s42 offen offset:0 sc1
buffer_load_dwordx4 v[108:111], v226, s[44:47], s43 offen offset:0 sc1
buffer_load_dwordx4 v[112:115], v226, s[44:47], s48 offen offset:0 sc1
buffer_load_dwordx4 v[116:119], v226, s[44:47], s49 offen offset:0 sc1
buffer_load_dwordx4 v[120:123], v226, s[44:47], s50 offen offset:0 sc1
buffer_load_dwordx4 v[124:127], v226, s[44:47], s51 offen offset:0 sc1
buffer_load_dwordx4 v[128:131], v227, s[44:47], s40 offen offset:0 sc1
buffer_load_dwordx4 v[132:135], v227, s[44:47], s41 offen offset:0 sc1
buffer_load_dwordx4 v[136:139], v227, s[44:47], s42 offen offset:0 sc1
buffer_load_dwordx4 v[140:143], v227, s[44:47], s43 offen offset:0 sc1
buffer_load_dwordx4 v[144:147], v227, s[44:47], s48 offen offset:0 sc1
buffer_load_dwordx4 v[148:151], v227, s[44:47], s49 offen offset:0 sc1
buffer_load_dwordx4 v[152:155], v227, s[44:47], s50 offen offset:0 sc1
buffer_load_dwordx4 v[156:159], v227, s[44:47], s51 offen offset:0 sc1
global_load_dword v255, v250, s[80:81] sc1
buffer_store_dwordx4 v[160:163], v228, s[56:59], 0 offen offset:0
buffer_store_dwordx4 v[168:171], v229, s[56:59], 0 offen offset:0
s_waitcnt vmcnt(18)
v_lshlrev_b32 v176, 16, v96
v_and_b32 v177, s70, v96
v_lshlrev_b32 v178, 16, v97
v_and_b32 v179, s70, v97
v_lshlrev_b32 v180, 16, v98
v_and_b32 v181, s70, v98
v_lshlrev_b32 v182, 16, v99
v_and_b32 v183, s70, v99
s_waitcnt vmcnt(17)
v_lshlrev_b32 v192, 16, v100
v_and_b32 v193, s70, v100
v_lshlrev_b32 v194, 16, v101
v_and_b32 v195, s70, v101
v_lshlrev_b32 v196, 16, v102
v_and_b32 v197, s70, v102
v_lshlrev_b32 v198, 16, v103
v_and_b32 v199, s70, v103
v_pk_add_f32 v[176:177], v[176:177], v[192:193]
v_pk_add_f32 v[178:179], v[178:179], v[194:195]
v_pk_add_f32 v[180:181], v[180:181], v[196:197]
v_pk_add_f32 v[182:183], v[182:183], v[198:199]
s_waitcnt vmcnt(16)
v_lshlrev_b32 v192, 16, v104
v_and_b32 v193, s70, v104
v_lshlrev_b32 v194, 16, v105
v_and_b32 v195, s70, v105
v_lshlrev_b32 v196, 16, v106
v_and_b32 v197, s70, v106
v_lshlrev_b32 v198, 16, v107
v_and_b32 v199, s70, v107
v_pk_add_f32 v[176:177], v[176:177], v[192:193]
v_pk_add_f32 v[178:179], v[178:179], v[194:195]
v_pk_add_f32 v[180:181], v[180:181], v[196:197]
v_pk_add_f32 v[182:183], v[182:183], v[198:199]
s_waitcnt vmcnt(15)
v_lshlrev_b32 v192, 16, v108
v_and_b32 v193, s70, v108
v_lshlrev_b32 v194, 16, v109
v_and_b32 v195, s70, v109
v_lshlrev_b32 v196, 16, v110
v_and_b32 v197, s70, v110
v_lshlrev_b32 v198, 16, v111
v_and_b32 v199, s70, v111
v_pk_add_f32 v[176:177], v[176:177], v[192:193]
v_pk_add_f32 v[178:179], v[178:179], v[194:195]
v_pk_add_f32 v[180:181], v[180:181], v[196:197]
v_pk_add_f32 v[182:183], v[182:183], v[198:199]
s_waitcnt vmcnt(14)
v_lshlrev_b32 v192, 16, v112
v_and_b32 v193, s70, v112
v_lshlrev_b32 v194, 16, v113
v_and_b32 v195, s70, v113
v_lshlrev_b32 v196, 16, v114
v_and_b32 v197, s70, v114
v_lshlrev_b32 v198, 16, v115
v_and_b32 v199, s70, v115
v_pk_add_f32 v[176:177], v[176:177], v[192:193]
v_pk_add_f32 v[178:179], v[178:179], v[194:195]
v_pk_add_f32 v[180:181], v[180:181], v[196:197]
v_pk_add_f32 v[182:183], v[182:183], v[198:199]
s_waitcnt vmcnt(13)
v_lshlrev_b32 v192, 16, v116
v_and_b32 v193, s70, v116
v_lshlrev_b32 v194, 16, v117
v_and_b32 v195, s70, v117
v_lshlrev_b32 v196, 16, v118
v_and_b32 v197, s70, v118
v_lshlrev_b32 v198, 16, v119
v_and_b32 v199, s70, v119
v_pk_add_f32 v[176:177], v[176:177], v[192:193]
v_pk_add_f32 v[178:179], v[178:179], v[194:195]
v_pk_add_f32 v[180:181], v[180:181], v[196:197]
v_pk_add_f32 v[182:183], v[182:183], v[198:199]
s_waitcnt vmcnt(12)
v_lshlrev_b32 v192, 16, v120
v_and_b32 v193, s70, v120
v_lshlrev_b32 v194, 16, v121
v_and_b32 v195, s70, v121
v_lshlrev_b32 v196, 16, v122
v_and_b32 v197, s70, v122
v_lshlrev_b32 v198, 16, v123
v_and_b32 v199, s70, v123
v_pk_add_f32 v[176:177], v[176:177], v[192:193]
v_pk_add_f32 v[178:179], v[178:179], v[194:195]
v_pk_add_f32 v[180:181], v[180:181], v[196:197]
v_pk_add_f32 v[182:183], v[182:183], v[198:199]
s_waitcnt vmcnt(11)
v_lshlrev_b32 v192, 16, v124
v_and_b32 v193, s70, v124
v_lshlrev_b32 v194, 16, v125
v_and_b32 v195, s70, v125
v_lshlrev_b32 v196, 16, v126
v_and_b32 v197, s70, v126
v_lshlrev_b32 v198, 16, v127
v_and_b32 v199, s70, v127
v_pk_add_f32 v[176:177], v[176:177], v[192:193]
v_pk_add_f32 v[178:179], v[178:179], v[194:195]
v_pk_add_f32 v[180:181], v[180:181], v[196:197]
v_pk_add_f32 v[182:183], v[182:183], v[198:199]
CV 176,176,177
CV 177,178,179
CV 178,180,181
CV 179,182,183
buffer_store_dwordx4 v[176:179], v230, s[56:59], 0 offen offset:0
s_waitcnt vmcnt(11)
v_lshlrev_b32 v184, 16, v128
v_and_b32 v185, s70, v128
v_lshlrev_b32 v186, 16, v129
v_and_b32 v187, s70, v129
v_lshlrev_b32 v188, 16, v130
v_and_b32 v189, s70, v130
v_lshlrev_b32 v190, 16, v131
v_and_b32 v191, s70, v131
s_waitcnt vmcnt(10)
v_lshlrev_b32 v192, 16, v132
v_and_b32 v193, s70, v132
v_lshlrev_b32 v194, 16, v133
v_and_b32 v195, s70, v133
v_lshlrev_b32 v196, 16, v134
v_and_b32 v197, s70, v134
v_lshlrev_b32 v198, 16, v135
v_and_b32 v199, s70, v135
v_pk_add_f32 v[184:185], v[184:185], v[192:193]
v_pk_add_f32 v[186:187], v[186:187], v[194:195]
v_pk_add_f32 v[188:189], v[188:189], v[196:197]
v_pk_add_f32 v[190:191], v[190:191], v[198:199]
s_waitcnt vmcnt(9)
v_lshlrev_b32 v192, 16, v136
v_and_b32 v193, s70, v136
v_lshlrev_b32 v194, 16, v137
v_and_b32 v195, s70, v137
v_lshlrev_b32 v196, 16, v138
v_and_b32 v197, s70, v138
v_lshlrev_b32 v198, 16, v139
v_and_b32 v199, s70, v139
v_pk_add_f32 v[184:185], v[184:185], v[192:193]
v_pk_add_f32 v[186:187], v[186:187], v[194:195]
v_pk_add_f32 v[188:189], v[188:189], v[196:197]
v_pk_add_f32 v[190:191], v[190:191], v[198:199]
s_waitcnt vmcnt(8)
v_lshlrev_b32 v192, 16, v140
v_and_b32 v193, s70, v140
v_lshlrev_b32 v194, 16, v141
v_and_b32 v195, s70, v141
v_lshlrev_b32 v196, 16, v142
v_and_b32 v197, s70, v142
v_lshlrev_b32 v198, 16, v143
v_and_b32 v199, s70, v143
v_pk_add_f32 v[184:185], v[184:185], v[192:193]
v_pk_add_f32 v[186:187], v[186:187], v[194:195]
v_pk_add_f32 v[188:189], v[188:189], v[196:197]
v_pk_add_f32 v[190:191], v[190:191], v[198:199]
s_waitcnt vmcnt(7)
v_lshlrev_b32 v192, 16, v144
v_and_b32 v193, s70, v144
v_lshlrev_b32 v194, 16, v145
v_and_b32 v195, s70, v145
v_lshlrev_b32 v196, 16, v146
v_and_b32 v197, s70, v146
v_lshlrev_b32 v198, 16, v147
v_and_b32 v199, s70, v147
v_pk_add_f32 v[184:185], v[184:185], v[192:193]
v_pk_add_f32 v[186:187], v[186:187], v[194:195]
v_pk_add_f32 v[188:189], v[188:189], v[196:197]
v_pk_add_f32 v[190:191], v[190:191], v[198:199]
s_waitcnt vmcnt(6)
v_lshlrev_b32 v192, 16, v148
v_and_b32 v193, s70, v148
v_lshlrev_b32 v194, 16, v149
v_and_b32 v195, s70, v149
v_lshlrev_b32 v196, 16, v150
v_and_b32 v197, s70, v150
v_lshlrev_b32 v198, 16, v151
v_and_b32 v199, s70, v151
v_pk_add_f32 v[184:185], v[184:185], v[192:193]
v_pk_add_f32 v[186:187], v[186:187], v[194:195]
v_pk_add_f32 v[188:189], v[188:189], v[196:197]
v_pk_add_f32 v[190:191], v[190:191], v[198:199]
s_waitcnt vmcnt(5)
v_lshlrev_b32 v192, 16, v152
v_and_b32 v193, s70, v152
v_lshlrev_b32 v194, 16, v153
v_and_b32 v195, s70, v153
v_lshlrev_b32 v196, 16, v154
v_and_b32 v197, s70, v154
v_lshlrev_b32 v198, 16, v155
v_and_b32 v199, s70, v155
v_pk_add_f32 v[184:185], v[184:185], v[192:193]
v_pk_add_f32 v[186:187], v[186:187], v[194:195]
v_pk_add_f32 v[188:189], v[188:189], v[196:197]
v_pk_add_f32 v[190:191], v[190:191], v[198:199]
s_waitcnt vmcnt(4)
v_lshlrev_b32 v192, 16, v156
v_and_b32 v193, s70, v156
v_lshlrev_b32 v194, 16, v157
v_and_b32 v195, s70, v157
v_lshlrev_b32 v196, 16, v158
v_and_b32 v197, s70, v158
v_lshlrev_b32 v198, 16, v159
v_and_b32 v199, s70, v159
v_pk_add_f32 v[184:185], v[184:185], v[192:193]
v_pk_add_f32 v[186:187], v[186:187], v[194:195]
v_pk_add_f32 v[188:189], v[188:189], v[196:197]
v_pk_add_f32 v[190:191], v[190:191], v[198:199]
CV 184,184,185
CV 185,186,187
CV 186,188,189
CV 187,190,191
buffer_store_dwordx4 v[184:187], v231, s[56:59], 0 offen offset:0
s_waitcnt vmcnt(4)
v_mov_b32 v255, v255
LT8:
v_cmp_ne_u32 vcc, v255, v252
s_cmp_lg_u32 %[wid], 0
s_cbranch_scc1 LT10
s_cbranch_vccz LT10
global_store_dword v251, v255, s[38:39] offset:0 sc1
global_store_dword v251, v255, s[38:39] offset:64 sc1
global_store_dword v251, v255, s[38:39] offset:128 sc1
LT10:
s_endpgm
LT3s:
s_mov_b32 s71, 30000
LT3:
global_load_dword v254, v250, s[36:37] offset:0 sc1
global_load_dword v255, v250, s[80:81] sc1
s_waitcnt vmcnt(0)
s_cmp_lg_u32 %[wid], 0
s_cbranch_scc1 LT3a
v_cmp_ne_u32 vcc, v255, v252
s_cbranch_vccz LT3a
v_mov_b32 v252, v255
global_store_dword v251, v252, s[38:39] sc1
LT3a:
v_cmp_ne_u32 vcc, s79, v254
s_nop 3
s_and_b32 vcc_lo, vcc_lo, s77
s_cbranch_scc0 LT3e
s_sleep 1
s_sub_u32 s71, s71, 1
s_cbranch_scc0 LT3
s_branch LT3e
LT4s:
s_mov_b32 s71, 30000
LT4:
global_load_dword v254, v250, s[36:37] offset:64 sc1
global_load_dword v255, v250, s[80:81] sc1
s_waitcnt vmcnt(0)
s_cmp_lg_u32 %[wid], 0
s_cbranch_scc1 LT4a
v_cmp_ne_u32 vcc, v255, v252
s_cbranch_vccz LT4a
v_mov_b32 v252, v255
global_store_dword v251, v252, s[38:39] sc1
global_store_dword v251, v252, s[38:39] offset:64 sc1
global_store_dword v251, v252, s[38:39] offset:128 sc1
LT4a:
v_cmp_ne_u32 vcc, s79, v254
s_nop 3
s_and_b32 vcc_lo, vcc_lo, s77
s_cbranch_scc0 LT4e
s_sleep 1
s_sub_u32 s71, s71, 1
s_cbranch_scc0 LT4
s_branch LT4e
LT7:
buffer_load_dwordx4 v[32:35], v224, s[44:47], s40 offen offset:0 sc1
buffer_load_dwordx4 v[36:39], v224, s[44:47], s41 offen offset:0 sc1
buffer_load_dwordx4 v[40:43], v224, s[44:47], s42 offen offset:0 sc1
buffer_load_dwordx4 v[44:47], v224, s[44:47], s43 offen offset:0 sc1
buffer_load_dwordx4 v[48:51], v224, s[44:47], s48 offen offset:0 sc1
buffer_load_dwordx4 v[52:55], v224, s[44:47], s49 offen offset:0 sc1
buffer_load_dwordx4 v[56:59], v224, s[44:47], s50 offen offset:0 sc1
buffer_load_dwordx4 v[60:63], v224, s[44:47], s51 offen offset:0 sc1
buffer_load_dwordx4 v[64:67], v225, s[44:47], s40 offen offset:0 sc1
buffer_load_dwordx4 v[68:71], v225, s[44:47], s41 offen offset:0 sc1
buffer_load_dwordx4 v[72:75], v225, s[44:47], s42 offen offset:0 sc1
buffer_load_dwordx4 v[76:79], v225, s[44:47], s43 offen offset:0 sc1
buffer_load_dwordx4 v[80:83], v225, s[44:47], s48 offen offset:0 sc1
buffer_load_dwordx4 v[84:87], v225, s[44:47], s49 offen offset:0 sc1
buffer_load_dwordx4 v[88:91], v225, s[44:47], s50 offen offset:0 sc1
buffer_load_dwordx4 v[92:95], v225, s[44:47], s51 offen offset:0 sc1
buffer_load_dwordx4 v[96:99], v226, s[44:47], s40 offen offset:0 sc1
buffer_load_dwordx4 v[100:103], v226, s[44:47], s41 offen offset:0 sc1
buffer_load_dwordx4 v[104:107], v226, s[44:47], s42 offen offset:0 sc1
buffer_load_dwordx4 v[108:111], v226, s[44:47], s43 offen offset:0 sc1
buffer_load_dwordx4 v[112:115], v226, s[44:47], s48 offen offset:0 sc1
buffer_load_dwordx4 v[116:119], v226, s[44:47], s49 offen offset:0 sc1
buffer_load_dwordx4 v[120:123], v226, s[44:47], s50 offen offset:0 sc1
buffer_load_dwordx4 v[124:127], v226, s[44:47], s51 offen offset:0 sc1
buffer_load_dwordx4 v[128:131], v227, s[44:47], s40 offen offset:0 sc1
buffer_load_dwordx4 v[132:135], v227, s[44:47], s41 offen offset:0 sc1
buffer_load_dwordx4 v[136:139], v227, s[44:47], s42 offen offset:0 sc1
buffer_load_dwordx4 v[140:143], v227, s[44:47], s43 offen offset:0 sc1
buffer_load_dwordx4 v[144:147], v227, s[44:47], s48 offen offset:0 sc1
buffer_load_dwordx4 v[148:151], v227, s[44:47], s49 offen offset:0 sc1
buffer_load_dwordx4 v[152:155], v227, s[44:47], s50 offen offset:0 sc1
buffer_load_dwordx4 v[156:159], v227, s[44:47], s51 offen offset:0 sc1
global_load_dword v255, v250, s[80:81] sc1
s_waitcnt vmcnt(32)
v_lshlrev_b32 v160, 16, v32
v_and_b32 v161, s70, v32
v_lshlrev_b32 v162, 16, v33
v_and_b32 v163, s70, v33
v_lshlrev_b32 v164, 16, v34
v_and_b32 v165, s70, v34
v_lshlrev_b32 v166, 16, v35
v_and_b32 v167, s70, v35
s_waitcnt vmcnt(31)
v_lshlrev_b32 v192, 16, v36
v_and_b32 v193, s70, v36
v_lshlrev_b32 v194, 16, v37
v_and_b32 v195, s70, v37
v_lshlrev_b32 v196, 16, v38
v_and_b32 v197, s70, v38
v_lshlrev_b32 v198, 16, v39
v_and_b32 v199, s70, v39
v_pk_add_f32 v[160:161], v[160:161], v[192:193]
v_pk_add_f32 v[162:163], v[162:163], v[194:195]
v_pk_add_f32 v[164:165], v[164:165], v[196:197]
v_pk_add_f32 v[166:167], v[166:167], v[198:199]
s_waitcnt vmcnt(30)
v_lshlrev_b32 v192, 16, v40
v_and_b32 v193, s70, v40
v_lshlrev_b32 v194, 16, v41
v_and_b32 v195, s70, v41
v_lshlrev_b32 v196, 16, v42
v_and_b32 v197, s70, v42
v_lshlrev_b32 v198, 16, v43
v_and_b32 v199, s70, v43
v_pk_add_f32 v[160:161], v[160:161], v[192:193]
v_pk_add_f32 v[162:163], v[162:163], v[194:195]
v_pk_add_f32 v[164:165], v[164:165], v[196:197]
v_pk_add_f32 v[166:167], v[166:167], v[198:199]
s_waitcnt vmcnt(29)
v_lshlrev_b32 v192, 16, v44
v_and_b32 v193, s70, v44
v_lshlrev_b32 v194, 16, v45
v_and_b32 v195, s70, v45
v_lshlrev_b32 v196, 16, v46
v_and_b32 v197, s70, v46
v_lshlrev_b32 v198, 16, v47
v_and_b32 v199, s70, v47
v_pk_add_f32 v[160:161], v[160:161], v[192:193]
v_pk_add_f32 v[162:163], v[162:163], v[194:195]
v_pk_add_f32 v[164:165], v[164:165], v[196:197]
v_pk_add_f32 v[166:167], v[166:167], v[198:199]
s_waitcnt vmcnt(28)
v_lshlrev_b32 v192, 16, v48
v_and_b32 v193, s70, v48
v_lshlrev_b32 v194, 16, v49
v_and_b32 v195, s70, v49
v_lshlrev_b32 v196, 16, v50
v_and_b32 v197, s70, v50
v_lshlrev_b32 v198, 16, v51
v_and_b32 v199, s70, v51
v_pk_add_f32 v[160:161], v[160:161], v[192:193]
v_pk_add_f32 v[162:163], v[162:163], v[194:195]
v_pk_add_f32 v[164:165], v[164:165], v[196:197]
v_pk_add_f32 v[166:167], v[166:167], v[198:199]
s_waitcnt vmcnt(27)
v_lshlrev_b32 v192, 16, v52
v_and_b32 v193, s70, v52
v_lshlrev_b32 v194, 16, v53
v_and_b32 v195, s70, v53
v_lshlrev_b32 v196, 16, v54
v_and_b32 v197, s70, v54
v_lshlrev_b32 v198, 16, v55
v_and_b32 v199, s70, v55
v_pk_add_f32 v[160:161], v[160:161], v[192:193]
v_pk_add_f32 v[162:163], v[162:163], v[194:195]
v_pk_add_f32 v[164:165], v[164:165], v[196:197]
v_pk_add_f32 v[166:167], v[166:167], v[198:199]
s_waitcnt vmcnt(26)
v_lshlrev_b32 v192, 16, v56
v_and_b32 v193, s70, v56
v_lshlrev_b32 v194, 16, v57
v_and_b32 v195, s70, v57
v_lshlrev_b32 v196, 16, v58
v_and_b32 v197, s70, v58
v_lshlrev_b32 v198, 16, v59
v_and_b32 v199, s70, v59
v_pk_add_f32 v[160:161], v[160:161], v[192:193]
v_pk_add_f32 v[162:163], v[162:163], v[194:195]
v_pk_add_f32 v[164:165], v[164:165], v[196:197]
v_pk_add_f32 v[166:167], v[166:167], v[198:199]
s_waitcnt vmcnt(25)
v_lshlrev_b32 v192, 16, v60
v_and_b32 v193, s70, v60
v_lshlrev_b32 v194, 16, v61
v_and_b32 v195, s70, v61
v_lshlrev_b32 v196, 16, v62
v_and_b32 v197, s70, v62
v_lshlrev_b32 v198, 16, v63
v_and_b32 v199, s70, v63
v_pk_add_f32 v[160:161], v[160:161], v[192:193]
v_pk_add_f32 v[162:163], v[162:163], v[194:195]
v_pk_add_f32 v[164:165], v[164:165], v[196:197]
v_pk_add_f32 v[166:167], v[166:167], v[198:199]
CV 160,160,161
CV 161,162,163
CV 162,164,165
CV 163,166,167
buffer_store_dwordx4 v[160:163], v228, s[56:59], 0 offen offset:0
s_waitcnt vmcnt(25)
v_lshlrev_b32 v168, 16, v64
v_and_b32 v169, s70, v64
v_lshlrev_b32 v170, 16, v65
v_and_b32 v171, s70, v65
v_lshlrev_b32 v172, 16, v66
v_and_b32 v173, s70, v66
v_lshlrev_b32 v174, 16, v67
v_and_b32 v175, s70, v67
s_waitcnt vmcnt(24)
v_lshlrev_b32 v192, 16, v68
v_and_b32 v193, s70, v68
v_lshlrev_b32 v194, 16, v69
v_and_b32 v195, s70, v69
v_lshlrev_b32 v196, 16, v70
v_and_b32 v197, s70, v70
v_lshlrev_b32 v198, 16, v71
v_and_b32 v199, s70, v71
v_pk_add_f32 v[168:169], v[168:169], v[192:193]
v_pk_add_f32 v[170:171], v[170:171], v[194:195]
v_pk_add_f32 v[172:173], v[172:173], v[196:197]
v_pk_add_f32 v[174:175], v[174:175], v[198:199]
s_waitcnt vmcnt(23)
v_lshlrev_b32 v192, 16, v72
v_and_b32 v193, s70, v72
v_lshlrev_b32 v194, 16, v73
v_and_b32 v195, s70, v73
v_lshlrev_b32 v196, 16, v74
v_and_b32 v197, s70, v74
v_lshlrev_b32 v198, 16, v75
v_and_b32 v199, s70, v75
v_pk_add_f32 v[168:169], v[168:169], v[192:193]
v_pk_add_f32 v[170:171], v[170:171], v[194:195]
v_pk_add_f32 v[172:173], v[172:173], v[196:197]
v_pk_add_f32 v[174:175], v[174:175], v[198:199]
s_waitcnt vmcnt(22)
v_lshlrev_b32 v192, 16, v76
v_and_b32 v193, s70, v76
v_lshlrev_b32 v194, 16, v77
v_and_b32 v195, s70, v77
v_lshlrev_b32 v196, 16, v78
v_and_b32 v197, s70, v78
v_lshlrev_b32 v198, 16, v79
v_and_b32 v199, s70, v79
v_pk_add_f32 v[168:169], v[168:169], v[192:193]
v_pk_add_f32 v[170:171], v[170:171], v[194:195]
v_pk_add_f32 v[172:173], v[172:173], v[196:197]
v_pk_add_f32 v[174:175], v[174:175], v[198:199]
s_waitcnt vmcnt(21)
v_lshlrev_b32 v192, 16, v80
v_and_b32 v193, s70, v80
v_lshlrev_b32 v194, 16, v81
v_and_b32 v195, s70, v81
v_lshlrev_b32 v196, 16, v82
v_and_b32 v197, s70, v82
v_lshlrev_b32 v198, 16, v83
v_and_b32 v199, s70, v83
v_pk_add_f32 v[168:169], v[168:169], v[192:193]
v_pk_add_f32 v[170:171], v[170:171], v[194:195]
v_pk_add_f32 v[172:173], v[172:173], v[196:197]
v_pk_add_f32 v[174:175], v[174:175], v[198:199]
s_waitcnt vmcnt(20)
v_lshlrev_b32 v192, 16, v84
v_and_b32 v193, s70, v84
v_lshlrev_b32 v194, 16, v85
v_and_b32 v195, s70, v85
v_lshlrev_b32 v196, 16, v86
v_and_b32 v197, s70, v86
v_lshlrev_b32 v198, 16, v87
v_and_b32 v199, s70, v87
v_pk_add_f32 v[168:169], v[168:169], v[192:193]
v_pk_add_f32 v[170:171], v[170:171], v[194:195]
v_pk_add_f32 v[172:173], v[172:173], v[196:197]
v_pk_add_f32 v[174:175], v[174:175], v[198:199]
s_waitcnt vmcnt(19)
v_lshlrev_b32 v192, 16, v88
v_and_b32 v193, s70, v88
v_lshlrev_b32 v194, 16, v89
v_and_b32 v195, s70, v89
v_lshlrev_b32 v196, 16, v90
v_and_b32 v197, s70, v90
v_lshlrev_b32 v198, 16, v91
v_and_b32 v199, s70, v91
v_pk_add_f32 v[168:169], v[168:169], v[192:193]
v_pk_add_f32 v[170:171], v[170:171], v[194:195]
v_pk_add_f32 v[172:173], v[172:173], v[196:197]
v_pk_add_f32 v[174:175], v[174:175], v[198:199]
s_waitcnt vmcnt(18)
v_lshlrev_b32 v192, 16, v92
v_and_b32 v193, s70, v92
v_lshlrev_b32 v194, 16, v93
v_and_b32 v195, s70, v93
v_lshlrev_b32 v196, 16, v94
v_and_b32 v197, s70, v94
v_lshlrev_b32 v198, 16, v95
)ASM"
R"ASM(
v_and_b32 v199, s70, v95
v_pk_add_f32 v[168:169], v[168:169], v[192:193]
v_pk_add_f32 v[170:171], v[170:171], v[194:195]
v_pk_add_f32 v[172:173], v[172:173], v[196:197]
v_pk_add_f32 v[174:175], v[174:175], v[198:199]
CV 168,168,169
CV 169,170,171
CV 170,172,173
CV 171,174,175
buffer_store_dwordx4 v[168:171], v229, s[56:59], 0 offen offset:0
s_waitcnt vmcnt(18)
v_lshlrev_b32 v176, 16, v96
v_and_b32 v177, s70, v96
v_lshlrev_b32 v178, 16, v97
v_and_b32 v179, s70, v97
v_lshlrev_b32 v180, 16, v98
v_and_b32 v181, s70, v98
v_lshlrev_b32 v182, 16, v99
v_and_b32 v183, s70, v99
s_waitcnt vmcnt(17)
v_lshlrev_b32 v192, 16, v100
v_and_b32 v193, s70, v100
v_lshlrev_b32 v194, 16, v101
v_and_b32 v195, s70, v101
v_lshlrev_b32 v196, 16, v102
v_and_b32 v197, s70, v102
v_lshlrev_b32 v198, 16, v103
v_and_b32 v199, s70, v103
v_pk_add_f32 v[176:177], v[176:177], v[192:193]
v_pk_add_f32 v[178:179], v[178:179], v[194:195]
v_pk_add_f32 v[180:181], v[180:181], v[196:197]
v_pk_add_f32 v[182:183], v[182:183], v[198:199]
s_waitcnt vmcnt(16)
v_lshlrev_b32 v192, 16, v104
v_and_b32 v193, s70, v104
v_lshlrev_b32 v194, 16, v105
v_and_b32 v195, s70, v105
v_lshlrev_b32 v196, 16, v106
v_and_b32 v197, s70, v106
v_lshlrev_b32 v198, 16, v107
v_and_b32 v199, s70, v107
v_pk_add_f32 v[176:177], v[176:177], v[192:193]
v_pk_add_f32 v[178:179], v[178:179], v[194:195]
v_pk_add_f32 v[180:181], v[180:181], v[196:197]
v_pk_add_f32 v[182:183], v[182:183], v[198:199]
s_waitcnt vmcnt(15)
v_lshlrev_b32 v192, 16, v108
v_and_b32 v193, s70, v108
v_lshlrev_b32 v194, 16, v109
v_and_b32 v195, s70, v109
v_lshlrev_b32 v196, 16, v110
v_and_b32 v197, s70, v110
v_lshlrev_b32 v198, 16, v111
v_and_b32 v199, s70, v111
v_pk_add_f32 v[176:177], v[176:177], v[192:193]
v_pk_add_f32 v[178:179], v[178:179], v[194:195]
v_pk_add_f32 v[180:181], v[180:181], v[196:197]
v_pk_add_f32 v[182:183], v[182:183], v[198:199]
s_waitcnt vmcnt(14)
v_lshlrev_b32 v192, 16, v112
v_and_b32 v193, s70, v112
v_lshlrev_b32 v194, 16, v113
v_and_b32 v195, s70, v113
v_lshlrev_b32 v196, 16, v114
v_and_b32 v197, s70, v114
v_lshlrev_b32 v198, 16, v115
v_and_b32 v199, s70, v115
v_pk_add_f32 v[176:177], v[176:177], v[192:193]
v_pk_add_f32 v[178:179], v[178:179], v[194:195]
v_pk_add_f32 v[180:181], v[180:181], v[196:197]
v_pk_add_f32 v[182:183], v[182:183], v[198:199]
s_waitcnt vmcnt(13)
v_lshlrev_b32 v192, 16, v116
v_and_b32 v193, s70, v116
v_lshlrev_b32 v194, 16, v117
v_and_b32 v195, s70, v117
v_lshlrev_b32 v196, 16, v118
v_and_b32 v197, s70, v118
v_lshlrev_b32 v198, 16, v119
v_and_b32 v199, s70, v119
v_pk_add_f32 v[176:177], v[176:177], v[192:193]
v_pk_add_f32 v[178:179], v[178:179], v[194:195]
v_pk_add_f32 v[180:181], v[180:181], v[196:197]
v_pk_add_f32 v[182:183], v[182:183], v[198:199]
s_waitcnt vmcnt(12)
v_lshlrev_b32 v192, 16, v120
v_and_b32 v193, s70, v120
v_lshlrev_b32 v194, 16, v121
v_and_b32 v195, s70, v121
v_lshlrev_b32 v196, 16, v122
v_and_b32 v197, s70, v122
v_lshlrev_b32 v198, 16, v123
v_and_b32 v199, s70, v123
v_pk_add_f32 v[176:177], v[176:177], v[192:193]
v_pk_add_f32 v[178:179], v[178:179], v[194:195]
v_pk_add_f32 v[180:181], v[180:181], v[196:197]
v_pk_add_f32 v[182:183], v[182:183], v[198:199]
s_waitcnt vmcnt(11)
v_lshlrev_b32 v192, 16, v124
v_and_b32 v193, s70, v124
v_lshlrev_b32 v194, 16, v125
v_and_b32 v195, s70, v125
v_lshlrev_b32 v196, 16, v126
v_and_b32 v197, s70, v126
v_lshlrev_b32 v198, 16, v127
v_and_b32 v199, s70, v127
v_pk_add_f32 v[176:177], v[176:177], v[192:193]
v_pk_add_f32 v[178:179], v[178:179], v[194:195]
v_pk_add_f32 v[180:181], v[180:181], v[196:197]
v_pk_add_f32 v[182:183], v[182:183], v[198:199]
CV 176,176,177
CV 177,178,179
CV 178,180,181
CV 179,182,183
buffer_store_dwordx4 v[176:179], v230, s[56:59], 0 offen offset:0
s_waitcnt vmcnt(11)
v_lshlrev_b32 v184, 16, v128
v_and_b32 v185, s70, v128
v_lshlrev_b32 v186, 16, v129
v_and_b32 v187, s70, v129
v_lshlrev_b32 v188, 16, v130
v_and_b32 v189, s70, v130
v_lshlrev_b32 v190, 16, v131
v_and_b32 v191, s70, v131
s_waitcnt vmcnt(10)
v_lshlrev_b32 v192, 16, v132
v_and_b32 v193, s70, v132
v_lshlrev_b32 v194, 16, v133
v_and_b32 v195, s70, v133
v_lshlrev_b32 v196, 16, v134
v_and_b32 v197, s70, v134
v_lshlrev_b32 v198, 16, v135
v_and_b32 v199, s70, v135
v_pk_add_f32 v[184:185], v[184:185], v[192:193]
v_pk_add_f32 v[186:187], v[186:187], v[194:195]
v_pk_add_f32 v[188:189], v[188:189], v[196:197]
v_pk_add_f32 v[190:191], v[190:191], v[198:199]
s_waitcnt vmcnt(9)
v_lshlrev_b32 v192, 16, v136
v_and_b32 v193, s70, v136
v_lshlrev_b32 v194, 16, v137
v_and_b32 v195, s70, v137
v_lshlrev_b32 v196, 16, v138
v_and_b32 v197, s70, v138
v_lshlrev_b32 v198, 16, v139
v_and_b32 v199, s70, v139
v_pk_add_f32 v[184:185], v[184:185], v[192:193]
v_pk_add_f32 v[186:187], v[186:187], v[194:195]
v_pk_add_f32 v[188:189], v[188:189], v[196:197]
v_pk_add_f32 v[190:191], v[190:191], v[198:199]
s_waitcnt vmcnt(8)
v_lshlrev_b32 v192, 16, v140
v_and_b32 v193, s70, v140
v_lshlrev_b32 v194, 16, v141
v_and_b32 v195, s70, v141
v_lshlrev_b32 v196, 16, v142
v_and_b32 v197, s70, v142
v_lshlrev_b32 v198, 16, v143
v_and_b32 v199, s70, v143
v_pk_add_f32 v[184:185], v[184:185], v[192:193]
v_pk_add_f32 v[186:187], v[186:187], v[194:195]
v_pk_add_f32 v[188:189], v[188:189], v[196:197]
v_pk_add_f32 v[190:191], v[190:191], v[198:199]
s_waitcnt vmcnt(7)
v_lshlrev_b32 v192, 16, v144
v_and_b32 v193, s70, v144
v_lshlrev_b32 v194, 16, v145
v_and_b32 v195, s70, v145
v_lshlrev_b32 v196, 16, v146
v_and_b32 v197, s70, v146
v_lshlrev_b32 v198, 16, v147
v_and_b32 v199, s70, v147
v_pk_add_f32 v[184:185], v[184:185], v[192:193]
v_pk_add_f32 v[186:187], v[186:187], v[194:195]
v_pk_add_f32 v[188:189], v[188:189], v[196:197]
v_pk_add_f32 v[190:191], v[190:191], v[198:199]
s_waitcnt vmcnt(6)
v_lshlrev_b32 v192, 16, v148
v_and_b32 v193, s70, v148
v_lshlrev_b32 v194, 16, v149
v_and_b32 v195, s70, v149
v_lshlrev_b32 v196, 16, v150
v_and_b32 v197, s70, v150
v_lshlrev_b32 v198, 16, v151
v_and_b32 v199, s70, v151
v_pk_add_f32 v[184:185], v[184:185], v[192:193]
v_pk_add_f32 v[186:187], v[186:187], v[194:195]
v_pk_add_f32 v[188:189], v[188:189], v[196:197]
v_pk_add_f32 v[190:191], v[190:191], v[198:199]
s_waitcnt vmcnt(5)
v_lshlrev_b32 v192, 16, v152
v_and_b32 v193, s70, v152
v_lshlrev_b32 v194, 16, v153
v_and_b32 v195, s70, v153
v_lshlrev_b32 v196, 16, v154
v_and_b32 v197, s70, v154
v_lshlrev_b32 v198, 16, v155
v_and_b32 v199, s70, v155
v_pk_add_f32 v[184:185], v[184:185], v[192:193]
v_pk_add_f32 v[186:187], v[186:187], v[194:195]
v_pk_add_f32 v[188:189], v[188:189], v[196:197]
v_pk_add_f32 v[190:191], v[190:191], v[198:199]
s_waitcnt vmcnt(4)
v_lshlrev_b32 v192, 16, v156
v_and_b32 v193, s70, v156
v_lshlrev_b32 v194, 16, v157
v_and_b32 v195, s70, v157
v_lshlrev_b32 v196, 16, v158
v_and_b32 v197, s70, v158
v_lshlrev_b32 v198, 16, v159
v_and_b32 v199, s70, v159
v_pk_add_f32 v[184:185], v[184:185], v[192:193]
v_pk_add_f32 v[186:187], v[186:187], v[194:195]
v_pk_add_f32 v[188:189], v[188:189], v[196:197]
v_pk_add_f32 v[190:191], v[190:191], v[198:199]
CV 184,184,185
CV 185,186,187
CV 186,188,189
CV 187,190,191
buffer_store_dwordx4 v[184:187], v231, s[56:59], 0 offen offset:0
s_waitcnt vmcnt(4)
v_mov_b32 v255, v255
s_branch LT8
Lcodeend:
.purgem MG
.purgem MG0
.purgem RU
.purgem MA
.purgem MA0
.purgem MV
.purgem MV0
.purgem RB
.purgem RT
.purgem CV
.purgem AR
)ASM"
    :
    : [ka] "s"(ka), [bid] "s"(bid), [wid] "s"(wid)
    : "v0","v1","v2","v3","v4","v5","v6","v7","v8","v9","v10","v11","v12","v13","v14","v15","v16","v17","v18","v19","v20","v21","v22","v23","v24","v25","v26","v27","v28","v29","v30","v31","v32","v33","v34","v35","v36","v37","v38","v39","v40","v41","v42","v43","v44","v45","v46","v47","v48","v49","v50","v51","v52","v53","v54","v55","v56","v57","v58","v59","v60","v61","v62","v63","v64","v65","v66","v67","v68","v69","v70","v71","v72","v73","v74","v75","v76","v77","v78","v79","v80","v81","v82","v83","v84","v85","v86","v87","v88","v89","v90","v91","v92","v93","v94","v95","v96","v97","v98","v99","v100","v101","v102","v103","v104","v105","v106","v107","v108","v109","v110","v111","v112","v113","v114","v115","v116","v117","v118","v119","v120","v121","v122","v123","v124","v125","v126","v127","v128","v129","v130","v131","v132","v133","v134","v135","v136","v137","v138","v139","v140","v141","v142","v143","v144","v145","v146","v147","v148","v149","v150","v151","v152","v153","v154","v155","v156","v157","v158","v159","v160","v161","v162","v163","v164","v165","v166","v167","v168","v169","v170","v171","v172","v173","v174","v175","v176","v177","v178","v179","v180","v181","v182","v183","v184","v185","v186","v187","v188","v189","v190","v191","v192","v193","v194","v195","v196","v197","v198","v199","v200","v201","v202","v203","v204","v205","v206","v207","v208","v209","v210","v211","v212","v213","v214","v215","v216","v217","v218","v219","v220","v221","v222","v223","v224","v225","v226","v227","v228","v229","v230","v231","v232","v233","v234","v235","v236","v237","v238","v239","v240","v241","v242","v243","v244","v245","v246","v247","v248","v249","v250","v251","v252","v253","v254","v255","a0","a1","a2","a3","a4","a5","a6","a7","a8","a9","a10","a11","a12","a13","a14","a15","a16","a17","a18","a19","a20","a21","a22","a23","a24","a25","a26","a27","a28","a29","a30","a31","a32","a33","a34","a35","a36","a37","a38","a39","a40","a41","a42","a43","a44","a45","a46","a47","a48","a49","a50","a51","a52","a53","a54","a55","a56","a57","a58","a59","a60","a61","a62","a63","a64","a65","a66","a67","a68","a69","a70","a71","a72","a73","a74","a75","a76","a77","a78","a79","a80","a81","a82","a83","a84","a85","a86","a87","a88","a89","a90","a91","a92","a93","a94","a95","a96","a97","a98","a99","a100","a101","a102","a103","a104","a105","a106","a107","a108","a109","a110","a111","a112","a113","a114","a115","a116","a117","a118","a119","a120","a121","a122","a123","a124","a125","a126","a127","a128","a129","a130","a131","a132","a133","a134","a135","a136","a137","a138","a139","a140","a141","a142","a143","a144","a145","a146","a147","a148","a149","a150","a151","a152","a153","a154","a155","a156","a157","a158","a159","a160","a161","a162","a163","a164","a165","a166","a167","a168","a169","a170","a171","a172","a173","a174","a175","a176","a177","a178","a179","a180","a181","a182","a183","a184","a185","a186","a187","a188","a189","a190","a191","a192","a193","a194","a195","a196","a197","a198","a199","a200","a201","a202","a203","a204","a205","a206","a207","a208","a209","a210","a211","a212","a213","a214","a215","a216","a217","a218","a219","a220","a221","a222","a223","a224","a225","a226","a227","a228","a229","a230","a231","a232","a233","a234","a235","a236","a237","a238","a239","a240","a241","a242","a243","a244","a245","a246","a247","a248","a249","a250","a251","a252","a253","a254","a255","s16","s17","s18","s19","s20","s21","s22","s23","s24","s25","s26","s27","s28","s29","s30","s31","s36","s37","s38","s39","s40","s41","s42","s43","s44","s45","s46","s47","s48","s49","s50","s51","s52","s53","s54","s55","s56","s57","s58","s59","s60","s61","s62","s63","s64","s65","s66","s67","s68","s69","s70","s71","s72","s73","s74","s75","s76","s77","s78","s79","s80","s81","vcc","scc","memory");
}

extern "C" void kernel_launch(void* const* d_in, const int* in_sizes, int n_in, void* d_out, int out_size, void* d_ws, size_t ws_size,
                              hipStream_t stream) {
  attn_kernel<<<256, 256, 0, stream>>>((const char*)d_in[0], (const char*)d_in[1], (const char*)d_in[2], (const char*)d_in[3], (const char*)d_in[4],
                                       (const char*)d_in[5], (const char*)d_in[6], (const char*)d_in[7], (const char*)d_in[8], (const char*)d_in[9],
                                       (char*)d_ws, (char*)d_out);
}
```
